```python
import torch

def _softmax(x, axis=-1):
    return torch.softmax(x, dim=axis)
def _gelu(x):
    return torch.nn.functional.gelu(x, approximate="tanh")
def _silu(x):
    return torch.nn.functional.silu(x)
def _sigmoid(x):
    return torch.sigmoid(x)
def _layernorm(x, axis=-1, eps=1e-5):
    m = x.mean(dim=axis, keepdim=True)
    v = x.var(dim=axis, keepdim=True, unbiased=False)
    return (x - m) / torch.sqrt(v + eps)
def _scatter_add(tgt, idx, src, axis=0):
    return tgt.clone().index_add_(axis, idx, src)
def _segment_sum(src, seg, n):
    out = torch.zeros((n,) + src.shape[1:], dtype=src.dtype)
    return out.index_add_(0, seg, src)
def _pad(x, pw):
    flat = tuple(v for pair in reversed(pw) for v in pair)
    return torch.nn.functional.pad(x, flat)

kernel_name = 'recurrent_scan__compressed_kv_cla__moe_arch_mod__prompt'

def setup_inputs():

    _w = (lambda a: (lambda t: t.to(torch.bfloat16) if t.is_floating_point() else t)(torch.as_tensor(a)))
    def _A_setup():
        B, H, Sq, Sk, D, M = 1, 32, 2048, 2048, 128, 512
        Dc = 4 * D
        return {
            "x": _w((lambda *s: torch.randn(*s, dtype=torch.bfloat16) * 0.05)(B, Sq, M)),
            "c_kv": _w((lambda *s: torch.randn(*s, dtype=torch.bfloat16) * 0.2)(B, Sk, Dc)),
            "w_q": _w((lambda *s: torch.randn(*s, dtype=torch.bfloat16) * 3.2)(M, H, D) * (M ** -0.5)),
            "w_uk": _w((lambda *s: torch.randn(*s, dtype=torch.bfloat16) * 3.2)(Dc, H, D) * (Dc ** -0.5)),
            "w_uv": _w((lambda *s: torch.randn(*s, dtype=torch.bfloat16) * 0.2)(Dc, H, D) * (Dc ** -0.5)),
            "w_o": _w((lambda *s: torch.randn(*s, dtype=torch.bfloat16) * 0.2)(H, D, M) * ((H * D) ** -0.5)),
        }
    _A_in = _A_setup()

    def _B_setup():
        B, S, D, F, C = 1, 2048, 512, 1792, 1
        return {
            "x": _w((lambda *s: torch.randn(*s, dtype=torch.bfloat16) * 0.05)(B, S, D)),
            "w_r": _w((lambda *s: torch.randn(*s, dtype=torch.bfloat16) * 1.6)(D)),
            "w_gate": _w((lambda *s: torch.randn(*s, dtype=torch.bfloat16) * 0.2)(D, F)),
            "w_up": _w((lambda *s: torch.randn(*s, dtype=torch.bfloat16) * 0.2)(D, F)),
            "w_down": _w((lambda *s: torch.randn(*s, dtype=torch.bfloat16) * 0.2)(F, D)),
        }
    _B_in = _B_setup()

    return {
        "A_x": _A_in["x"],
        "A_c_kv": _A_in["c_kv"],
        "A_w_q": _A_in["w_q"],
        "A_w_uk": _A_in["w_uk"],
        "A_w_uv": _A_in["w_uv"],
        "A_w_o": _A_in["w_o"],
        "B_x": _B_in["x"],
        "B_w_r": _B_in["w_r"],
        "B_w_gate": _B_in["w_gate"],
        "B_w_up": _B_in["w_up"],
        "B_w_down": _B_in["w_down"],
    }

def reference(A_x, A_c_kv, A_w_q, A_w_uk, A_w_uv, A_w_o, B_x, B_w_r, B_w_gate, B_w_up, B_w_down):
    def _A_ref(x, c_kv, w_q, w_uk, w_uv, w_o):
        B, Sq, M = x.shape[0], x.shape[1], x.shape[2]
        Sk = c_kv.shape[1]
        H, D = w_q.shape[1], w_q.shape[2]
        q = torch.einsum("bqm,mhd->bhqd", x, w_q)
        k = torch.einsum("bkc,chd->bhkd", c_kv, w_uk)
        v = torch.einsum("bkc,chd->bhkd", c_kv, w_uv)
        scores = torch.einsum("bhqd,bhkd->bhqk", q, k) * (D ** -0.5)
        off = Sk - Sq
        qi = torch.arange(Sq)[:, None]
        kj = torch.arange(Sk)[None, :]
        cmask = kj <= (qi + off)
        scores = torch.where(cmask[None, None], scores, -1e9)
        p = _softmax(scores, axis=-1)
        ctx = torch.einsum("bhqk,bhkd->bhqd", p, v)
        return torch.einsum("bhqd,hdm->bqm", ctx, w_o)

    def _B_ref(x, w_r, w_gate, w_up, w_down):
        B, S, D = x.shape
        C = 1
        r = x @ w_r
        idx = torch.argsort(-r, axis=-1)[:, :C]
        idx_e = (lambda x, n, axis: torch.repeat_interleave(x, n, dim=axis))(idx.reshape(B, C, 1), D, axis=2)
        x_sel = (lambda x, i, axis: torch.take_along_dim(x, i, dim=axis))(x, idx_e, axis=1)
        g = _silu(x_sel @ w_gate)
        u = x_sel @ w_up
        y = (g * u) @ w_down
        r_sel = _sigmoid((lambda x, i, axis: torch.take_along_dim(x, i, dim=axis))(r, idx, axis=1))
        y = y * r_sel.reshape(B, C, 1)
        b_off = (torch.arange(B) * S).reshape(B, 1)
        flat_idx = (idx + b_off).reshape(B * C)
        zero = (x * 0.0).reshape(B * S, D)
        upd = _scatter_add(zero, flat_idx, y.reshape(B * C, D), axis=0)
        return x + upd.reshape(B, S, D)

    x = A_x
    xn = _layernorm(x, axis=-1)
    s = _A_ref(xn, A_c_kv, A_w_q, A_w_uk, A_w_uv, A_w_o)
    g = _silu(_B_ref(xn, B_w_r, B_w_gate, B_w_up, B_w_down))
    return x + s * g
```

```cpp
#include <hip/hip_runtime.h>
#include <hip/hip_bf16.h>
#include <stdint.h>
typedef unsigned short u16;
__device__ __forceinline__ float b2f(u16 x){ return __uint_as_float(((unsigned)x)<<16); }
__device__ __forceinline__ u16 f2b(float f){ unsigned u=__float_as_uint(f); return (u16)((u + 0x7FFF + ((u>>16)&1))>>16); }
__device__ __forceinline__ float rb(float f){ return b2f(f2b(f)); }
constexpr int S=2048, M=512, H=32, D=128, F=1792;

__global__ void k_ln(const u16* x, const u16* wr, u16* xn, float* r){
  int row=blockIdx.x; int l=threadIdx.x;
  float v[8]; float s=0;
  for(int i=0;i<8;i++){ v[i]=b2f(x[row*M+l*8+i]); s+=v[i]; }
  for(int o=32;o;o>>=1) s+=__shfl_xor(s,o);
  float mean=s/M; float q=0;
  for(int i=0;i<8;i++){ float d=v[i]-mean; q+=d*d; }
  for(int o=32;o;o>>=1) q+=__shfl_xor(q,o);
  float var=rb(q/M); float mb=rb(mean);
  float sd=rb(sqrtf(rb(var+1e-5f)));
  float acc=0;
  for(int i=0;i<8;i++){ float t=rb(rb(v[i]-mb)/sd); xn[row*M+l*8+i]=f2b(t); acc+=t*b2f(wr[l*8+i]); }
  for(int o=32;o;o>>=1) acc+=__shfl_xor(acc,o);
  if(l==0) r[row]=rb(acc);
}
__global__ void k_gemm(const u16* A,const u16* B,u16* C,int Mm,int N,int K){
  __shared__ float As[16][65], Bs[16][65];
  int tx=threadIdx.x&15, ty=threadIdx.x>>4; int bm=blockIdx.y*64, bn=blockIdx.x*64;
  float acc[4][4]={};
  for(int k0=0;k0<K;k0+=16){
    for(int i=threadIdx.x;i<64*16;i+=256){ int r=i>>4,c=i&15; As[c][r]=b2f(A[(size_t)(bm+r)*K+k0+c]); }
    for(int i=threadIdx.x;i<64*16;i+=256){ int r=i>>6,c=i&63; Bs[r][c]=b2f(B[(size_t)(k0+r)*N+bn+c]); }
    __syncthreads();
    for(int k=0;k<16;k++){ float a[4],b[4]; for(int i=0;i<4;i++){a[i]=As[k][ty*4+i]; b[i]=Bs[k][tx*4+i];}
      for(int i=0;i<4;i++)for(int j=0;j<4;j++)acc[i][j]+=a[i]*b[j]; }
    __syncthreads();
  }
  for(int i=0;i<4;i++)for(int j=0;j<4;j++) C[(size_t)(bm+ty*4+i)*N+bn+tx*4+j]=f2b(acc[i][j]);
}
__global__ void k_attn(const u16* Q,const u16* Kk,const u16* V,u16* ctx){
  int row=blockIdx.x, h=blockIdx.y, t=threadIdx.x;
  __shared__ float q[D]; __shared__ float sc[S]; __shared__ float red[128];
  q[t]=b2f(Q[(size_t)row*H*D+h*D+t]); __syncthreads();
  float mx=-1e30f;
  for(int j=t;j<=row;j+=128){ float s=0; const u16* kp=Kk+(size_t)j*H*D+h*D; for(int d=0;d<D;d++) s+=q[d]*b2f(kp[d]); s*=0.08838834764831845f; sc[j]=s; mx=fmaxf(mx,s);}
  red[t]=mx; __syncthreads(); for(int o=64;o;o>>=1){ if(t<o) red[t]=fmaxf(red[t],red[t+o]); __syncthreads(); } mx=red[0]; __syncthreads();
  float sum=0; for(int j=t;j<=row;j+=128){ float p=__expf(sc[j]-mx); sc[j]=p; sum+=p; }
  red[t]=sum; __syncthreads(); for(int o=64;o;o>>=1){ if(t<o) red[t]+=red[t+o]; __syncthreads(); } sum=red[0];
  float o=0; for(int j=0;j<=row;j++) o+=sc[j]*b2f(V[(size_t)j*H*D+h*D+t]);
  ctx[(size_t)row*H*D+h*D+t]=f2b(o/sum);
}
__global__ void k_argmax(const float* r,int* idx){
  __shared__ float bv[256]; __shared__ int bi[256]; int t=threadIdx.x; float v=-1e30f; int ii=0;
  for(int j=t;j<S;j+=256){ if(r[j]>v){v=r[j];ii=j;} } bv[t]=v;bi[t]=ii; __syncthreads();
  for(int o=128;o;o>>=1){ if(t<o){ if(bv[t+o]>bv[t]||(bv[t+o]==bv[t]&&bi[t+o]<bi[t])){bv[t]=bv[t+o];bi[t]=bi[t+o];} } __syncthreads(); }
  if(t==0) idx[0]=bi[0];
}
__global__ void k_ffn1(const u16* xn,const int* idx,const u16* wg,const u16* wu,float* hbuf){
  int f=blockIdx.x*blockDim.x+threadIdx.x; if(f>=F) return; const u16* xs=xn+idx[0]*M; float g=0,u=0;
  for(int k=0;k<M;k++){ float xv=b2f(xs[k]); g+=xv*b2f(wg[(size_t)k*F+f]); u+=xv*b2f(wu[(size_t)k*F+f]); }
  g=rb(g); u=rb(u); float sg=rb(g/(1.f+__expf(-g))); hbuf[f]=rb(sg*u);
}
__global__ void k_ffn2(const float* hbuf,const u16* wd,const float* r,const int* idx,float* y){
  int c=blockIdx.x*blockDim.x+threadIdx.x; if(c>=M) return; float a=0; for(int f=0;f<F;f++) a+=hbuf[f]*b2f(wd[(size_t)f*M+c]);
  float rs=rb(1.f/(1.f+__expf(-r[idx[0]]))); y[c]=rb(rb(a)*rs);
}
__global__ void k_final(const u16* x,const u16* xn,const u16* s,const float* y,const int* idx,u16* out){
  int i=blockIdx.x*blockDim.x+threadIdx.x; int row=i/M,c=i%M; float b=b2f(xn[i]); if(row==idx[0]) b=rb(b+y[c]);
  float g=rb(b/(1.f+__expf(-b))); out[i]=f2b(b2f(x[i])+rb(b2f(s[i])*g));
}
extern "C" void kernel_launch(void* const* d_in, const int* in_sizes, int n_in, void* d_out, int out_size, void* d_ws, size_t ws_size, hipStream_t stream){
  const u16* x=(const u16*)d_in[0]; const u16* ckv=(const u16*)d_in[1]; const u16* wq=(const u16*)d_in[2]; const u16* wk=(const u16*)d_in[3]; const u16* wv=(const u16*)d_in[4]; const u16* wo=(const u16*)d_in[5];
  const u16* wr=(const u16*)d_in[7]; const u16* wg=(const u16*)d_in[8]; const u16* wu=(const u16*)d_in[9]; const u16* wd=(const u16*)d_in[10];
  char* ws=(char*)d_ws; size_t o=0;
  u16* xn=(u16*)(ws+o); o+=(size_t)S*M*2; u16* q=(u16*)(ws+o); o+=(size_t)S*H*D*2; u16* k=(u16*)(ws+o); o+=(size_t)S*H*D*2; u16* v=(u16*)(ws+o); o+=(size_t)S*H*D*2; u16* ctx=(u16*)(ws+o); o+=(size_t)S*H*D*2;
  u16* s=(u16*)(ws+o); o+=(size_t)S*M*2; float* r=(float*)(ws+o); o+=S*4; float* hb=(float*)(ws+o); o+=F*4; float* y=(float*)(ws+o); o+=M*4; int* idx=(int*)(ws+o);
  k_ln<<<S,64,0,stream>>>(x,wr,xn,r);
  k_gemm<<<dim3(H*D/64,S/64),256,0,stream>>>(xn,wq,q,S,H*D,M);
  k_gemm<<<dim3(H*D/64,S/64),256,0,stream>>>(ckv,wk,k,S,H*D,M);
  k_gemm<<<dim3(H*D/64,S/64),256,0,stream>>>(ckv,wv,v,S,H*D,M);
  k_attn<<<dim3(S,H),128,0,stream>>>(q,k,v,ctx);
  k_gemm<<<dim3(M/64,S/64),256,0,stream>>>(ctx,wo,s,S,M,H*D);
  k_argmax<<<1,256,0,stream>>>(r,idx);
  k_ffn1<<<F/256,256,0,stream>>>(xn,idx,wg,wu,hb);
  k_ffn2<<<2,256,0,stream>>>(hb,wd,r,idx,y);
  k_final<<<S*M/256,256,0,stream>>>(x,xn,s,y,idx,(u16*)d_out);
}
```

```cpp
#include <hip/hip_runtime.h>
#include <stdint.h>
__global__ __launch_bounds__(256, 1) void fused(const void* x, const void* ckv, const void* wq, const void* wk, const void* wv, const void* wo,
    const void* wr, const void* wg, const void* wu, const void* wd, void* out, void* ws) {
  __shared__ __attribute__((aligned(16))) char lds[163840];
  unsigned ldsb = (unsigned)(uintptr_t)lds;
  asm volatile(
"L_start_%=:\n"
"v_and_b32 v21, 63, v0\n"
"v_lshrrev_b32 v24, 6, v0\n"
"s_nop 0\n"
"v_readfirstlane_b32 s64, v24\n"
"v_lshlrev_b32 v1, 4, v21\n"
"s_lshr_b32 s97, s32, 3\n"
"s_lshl_b32 s97, s97, 2\n"
"s_add_u32 s97, s97, s64\n"
"s_getpc_b64 s[4:5]\n"
"LT_pc0_%=:\n"
"s_sub_u32 s4, s4, LT_pc0_%=-L_start_%=\n"
"s_subb_u32 s5, s5, 0\n"
"s_and_b32 s5, s5, 0xffff\n"
"s_mov_b32 s6, L_end_%=-L_start_%=\n"
"s_mov_b32 s7, 0x20000\n"
"s_lshl_b32 s34, s97, 10\n"
"buffer_load_dwordx4 a[252:255], v1, s[4:7], s34 offen offset:0\n"
"s_and_b32 s65, s32, 7\n"
"s_lshl_b32 s65, s65, 2\n"
"s_lshr_b32 s73, s32, 3\n"
"s_and_b32 s73, s73, 3\n"
"s_add_u32 s65, s65, s73\n"
"s_lshr_b32 s66, s32, 5\n"
"s_add_u32 s3, s66, 0\n"
"s_and_b32 s3, s3, 7\n"
"s_mov_b32 s36, s10\n"
"s_and_b32 s37, s11, 0xffff\n"
"s_mov_b32 s38, 0x200000\n"
"s_mov_b32 s39, 0x20000\n"
"s_mov_b32 s40, s14\n"
"s_and_b32 s41, s15, 0xffff\n"
"s_mov_b32 s42, 0x400000\n"
"s_mov_b32 s43, 0x20000\n"
"s_mov_b32 s44, s16\n"
"s_and_b32 s45, s17, 0xffff\n"
"s_mov_b32 s46, 0x400000\n"
"s_mov_b32 s47, 0x20000\n"
"s_lshl_b32 s67, s64, 13\n"
"s_lshl_b32 s70, s64, 12\n"
"v_lshrrev_b32 v24, 4, v21\n"
"v_and_b32 v25, 7, v21\n"
"v_xor_b32 v25, v25, v24\n"
"v_lshlrev_b32 v25, 4, v25\n"
"v_lshrrev_b32 v8, 3, v21\n"
"v_lshl_add_u32 v8, v8, 10, v25\n"
"v_xor_b32 v9, 64, v8\n"
"v_and_b32 v25, 15, v21\n"
"v_lshlrev_b32 v10, 1, v24\n"
"v_xor_b32 v25, v25, v10\n"
"v_lshlrev_b32 v25, 4, v25\n"
"v_lshl_add_u32 v10, v24, 13, v25\n"
"v_xor_b32 v11, 128, v10\n"
"s_sub_u32 s73, 15, s66\n"
"s_cmp_lt_u32 s64, 2\n"
"s_cselect_b32 s73, s66, s73\n"
"s_lshl_b32 s73, s73, 1\n"
"s_and_b32 s74, s64, 1\n"
"s_add_u32 s73, s73, s74\n"
"s_lshl_b32 s71, s73, 16\n"
"s_lshl_b32 s72, s64, 17\n"
"s_lshl_b32 s73, s65, 8\n"
"s_add_u32 s72, s72, s73\n"
"s_add_u32 m0, s67, 0x0\n"
"s_add_u32 s75, s71, 0x0\n"
"buffer_load_dwordx4 v8, s[36:39], s75 offen offset:0 lds\n"
"s_add_u32 s75, s71, 0x1c00\n"
"buffer_load_dwordx4 v9, s[36:39], s75 offen offset:1024 lds\n"
"s_add_u32 s75, s71, 0x3800\n"
"buffer_load_dwordx4 v8, s[36:39], s75 offen offset:2048 lds\n"
"s_add_u32 s75, s71, 0x5400\n"
"buffer_load_dwordx4 v9, s[36:39], s75 offen offset:3072 lds\n"
"s_add_u32 m0, s67, 0x1000\n"
"s_add_u32 s75, s71, 0x8000\n"
"buffer_load_dwordx4 v8, s[36:39], s75 offen offset:0 lds\n"
"s_add_u32 s75, s71, 0x9c00\n"
"buffer_load_dwordx4 v9, s[36:39], s75 offen offset:1024 lds\n"
"s_add_u32 s75, s71, 0xb800\n"
"buffer_load_dwordx4 v8, s[36:39], s75 offen offset:2048 lds\n"
"s_add_u32 s75, s71, 0xd400\n"
"buffer_load_dwordx4 v9, s[36:39], s75 offen offset:3072 lds\n"
"s_add_u32 m0, s70, 0x8000\n"
"s_add_u32 s95, s72, 0x0\n"
"buffer_load_dwordx4 v10, s[40:43], s95 offen offset:0 lds\n"
"s_add_u32 s95, s72, 0x7c00\n"
"buffer_load_dwordx4 v10, s[40:43], s95 offen offset:1024 lds\n"
"s_add_u32 s95, s72, 0xf800\n"
"buffer_load_dwordx4 v11, s[40:43], s95 offen offset:2048 lds\n"
"s_add_u32 s95, s72, 0x17400\n"
"buffer_load_dwordx4 v11, s[40:43], s95 offen offset:3072 lds\n"
"s_add_u32 m0, s70, 0xc000\n"
"s_add_u32 s95, s72, 0x0\n"
"buffer_load_dwordx4 v10, s[44:47], s95 offen offset:0 lds\n"
"s_add_u32 s95, s72, 0x7c00\n"
"buffer_load_dwordx4 v10, s[44:47], s95 offen offset:1024 lds\n"
"s_add_u32 s95, s72, 0xf800\n"
"buffer_load_dwordx4 v11, s[44:47], s95 offen offset:2048 lds\n"
"s_add_u32 s95, s72, 0x17400\n"
"buffer_load_dwordx4 v11, s[44:47], s95 offen offset:3072 lds\n"
"s_mov_b32 s48, s30\n"
"s_and_b32 s49, s31, 0xffff\n"
"s_mov_b32 s50, 0x6000000\n"
"s_mov_b32 s51, 0x20000\n"
"s_mov_b32 s52, s8\n"
"s_and_b32 s53, s9, 0xffff\n"
"s_mov_b32 s54, 0x200000\n"
"s_mov_b32 s55, 0x20000\n"
"s_mov_b32 s56, s20\n"
"s_and_b32 s57, s21, 0xffff\n"
"s_mov_b32 s58, 0x400\n"
"s_mov_b32 s59, 0x20000\n"
"v_mov_b32 v25, 0\n"
"buffer_load_dwordx2 v[30:31], v25, s[48:51], 0 offen sc1\n"
"v_lshrrev_b32 v25, 1, v21\n"
"v_lshlrev_b32 v25, 4, v25\n"
"v_and_b32 v24, 1, v21\n"
"v_lshl_add_u32 v24, v24, 10, v25\n"
"s_lshl_b32 s96, s32, 2\n"
"s_add_u32 s96, s96, s64\n"
"s_lshl_b32 s73, s96, 11\n"
"buffer_load_dwordx4 v[32:35], v24, s[52:55], s73 offen\n"
"buffer_load_dwordx4 v[36:39], v24, s[52:55], s73 offen offset:512\n"
"buffer_load_dwordx4 v[40:43], v25, s[56:59], 0 offen\n"
"buffer_load_dwordx4 v[44:47], v25, s[56:59], 0 offen offset:512\n"
"s_mov_b32 s56, s22\n"
"s_and_b32 s57, s23, 0xffff\n"
"s_mov_b32 s58, 0x1c0000\n"
"s_mov_b32 s59, 0x20000\n"
"s_mov_b32 s60, s24\n"
"s_and_b32 s61, s25, 0xffff\n"
"s_mov_b32 s62, 0x1c0000\n"
"s_mov_b32 s63, 0x20000\n"
"s_and_b32 s92, s32, 7\n"
"s_mul_i32 s92, s92, 28\n"
"s_lshr_b32 s84, s32, 3\n"
"s_add_u32 s92, s92, s84\n"
"s_and_b32 s85, s32, 31\n"
"s_add_u32 s85, s85, 224\n"
"s_cmp_lt_u32 s84, 28\n"
"s_cselect_b32 s92, s92, s85\n"
"v_lshl_or_b32 v29, s64, 6, v21\n"
"v_mul_u32_u24 v28, 7168, v29\n"
"v_lshlrev_b32 v29, 2, v29\n"
"s_lshl_b32 s84, s92, 4\n"
"s_cmp_lt_u32 s92, 224\n"
"s_cselect_b32 s88, s84, 0x10000000\n"
"s_add_u32 m0, s67, 0x10000\n"
"s_add_u32 s75, s71, 0x80\n"
"buffer_load_dwordx4 v8, s[36:39], s75 offen offset:0 lds\n"
"s_add_u32 s75, s71, 0x1c80\n"
"buffer_load_dwordx4 v9, s[36:39], s75 offen offset:1024 lds\n"
"s_add_u32 s75, s71, 0x3880\n"
"buffer_load_dwordx4 v8, s[36:39], s75 offen offset:2048 lds\n"
"s_add_u32 s75, s71, 0x5480\n"
"buffer_load_dwordx4 v9, s[36:39], s75 offen offset:3072 lds\n"
"s_add_u32 m0, s67, 0x11000\n"
"s_add_u32 s75, s71, 0x8080\n"
"buffer_load_dwordx4 v8, s[36:39], s75 offen offset:0 lds\n"
"s_add_u32 s75, s71, 0x9c80\n"
"buffer_load_dwordx4 v9, s[36:39], s75 offen offset:1024 lds\n"
"s_add_u32 s75, s71, 0xb880\n"
"buffer_load_dwordx4 v8, s[36:39], s75 offen offset:2048 lds\n"
"s_add_u32 s75, s71, 0xd480\n"
"buffer_load_dwordx4 v9, s[36:39], s75 offen offset:3072 lds\n"
"s_add_u32 m0, s70, 0x18000\n"
"s_add_u32 s95, s72, 0x80000\n"
"buffer_load_dwordx4 v10, s[40:43], s95 offen offset:0 lds\n"
"s_add_u32 s95, s72, 0x87c00\n"
"buffer_load_dwordx4 v10, s[40:43], s95 offen offset:1024 lds\n"
"s_add_u32 s95, s72, 0x8f800\n"
"buffer_load_dwordx4 v11, s[40:43], s95 offen offset:2048 lds\n"
"s_add_u32 s95, s72, 0x97400\n"
"buffer_load_dwordx4 v11, s[40:43], s95 offen offset:3072 lds\n"
"v_lshrrev_b32 v24, 3, v21\n"
"v_and_b32 v23, 7, v21\n"
"v_lshlrev_b32 v23, 4, v23\n"
"v_lshl_add_u32 v23, v24, 10, v23\n"
"s_lshl_b32 s34, s97, 14\n"
"s_cmp_lt_u32 s97, 64\n"
"s_cselect_b32 s4, s40, s44\n"
"s_cselect_b32 s5, s41, s45\n"
"s_mov_b32 s6, 0x400000\n"
"s_mov_b32 s7, 0x20000\n"
"s_and_b32 s35, s97, 63\n"
"s_lshl_b32 s35, s35, 13\n"
"s_and_b32 s76, s32, 7\n"
"s_lshl_b32 s76, s76, 10\n"
"s_add_u32 s35, s35, s76\n"
"v_and_b32 v24, 15, v21\n"
"v_lshrrev_b32 v25, 4, v21\n"
"v_lshrrev_b32 v26, 1, v24\n"
"v_xor_b32 v26, v26, v25\n"
"v_lshlrev_b32 v26, 4, v26\n"
"v_lshl_add_u32 v2, v24, 7, v26\n"
"s_and_b32 s73, s64, 1\n"
"s_lshl_b32 s73, s73, 13\n"
"s_add_u32 s73, s73, 0x0\n"
"v_add_u32 v2, s73, v2\n"
"v_xor_b32 v3, 64, v2\n"
"v_bfe_u32 v27, v21, 2, 2\n"
"v_and_b32 v48, 3, v21\n"
"v_and_b32 v49, 1, v25\n"
"v_lshlrev_b32 v49, 3, v49\n"
"v_lshl_add_u32 v49, v27, 1, v49\n"
"v_lshrrev_b32 v26, 1, v48\n"
"v_xor_b32 v49, v49, v26\n"
"v_lshl_add_u32 v24, v25, 3, v27\n"
"v_lshlrev_b32 v24, 8, v24\n"
"v_and_b32 v48, 1, v48\n"
"v_lshl_add_u32 v24, v48, 3, v24\n"
"v_xor_b32 v26, 0, v49\n"
"v_lshl_add_u32 v12, v26, 4, v24\n"
"v_xor_b32 v26, 2, v49\n"
"v_lshl_add_u32 v13, v26, 4, v24\n"
"v_xor_b32 v26, 4, v49\n"
"v_lshl_add_u32 v14, v26, 4, v24\n"
"v_xor_b32 v26, 6, v49\n"
"v_lshl_add_u32 v15, v26, 4, v24\n"
"v_xor_b32 v26, 8, v49\n"
"v_lshl_add_u32 v16, v26, 4, v24\n"
"v_xor_b32 v26, 10, v49\n"
"v_lshl_add_u32 v17, v26, 4, v24\n"
"v_xor_b32 v26, 12, v49\n"
"v_lshl_add_u32 v18, v26, 4, v24\n"
"v_xor_b32 v26, 14, v49\n"
"v_lshl_add_u32 v19, v26, 4, v24\n"
"s_cmp_lt_u32 s64, 2\n"
"s_cselect_b64 s[84:85], -1, 0\n"
"v_cndmask_b32 v4, v16, v12, s[84:85]\n"
"v_add_u32 v4, 0x8000, v4\n"
"v_cndmask_b32 v5, v17, v13, s[84:85]\n"
"v_add_u32 v5, 0x8000, v5\n"
"v_cndmask_b32 v6, v18, v14, s[84:85]\n"
"v_add_u32 v6, 0x8000, v6\n"
"v_cndmask_b32 v7, v19, v15, s[84:85]\n"
"v_add_u32 v7, 0x8000, v7\n"
"v_add_u32 v12, 0x10000, v12\n"
"v_add_u32 v13, 0x10000, v13\n"
"v_add_u32 v14, 0x10000, v14\n"
"v_add_u32 v15, 0x10000, v15\n"
"v_add_u32 v16, 0x10000, v16\n"
"v_add_u32 v17, 0x10000, v17\n"
"v_add_u32 v18, 0x10000, v18\n"
"v_add_u32 v19, 0x10000, v19\n"
"s_and_b32 s75, s64, 1\n"
"s_lshl_b32 s75, s75, 15\n"
"s_add_u32 s73, s75, 0x8000\n"
"s_lshr_b32 s74, s64, 1\n"
"s_lshl_b32 s74, s74, 11\n"
"s_add_u32 s73, s73, s74\n"
"v_add_u32 v20, s73, v1\n"
"v_add_u32 v22, s74, v20\n"
"s_lshl_b32 s73, s66, 16\n"
"s_add_u32 s73, s73, s75\n"
"s_lshl_b32 s75, s65, 20\n"
"s_add_u32 s73, s73, s75\n"
"s_add_u32 s73, s73, 0x400000\n"
"s_add_u32 s77, s73, s74\n"
"s_add_u32 s94, s77, s74\n"
"s_lshl_b32 s73, s66, 3\n"
"s_lshl_b32 s74, s64, 1\n"
"s_add_u32 s73, s73, s74\n"
"s_sub_u32 s74, 126, s73\n"
"s_lshl_b32 s78, s73, 14\n"
"s_add_u32 s78, s78, 0x100000\n"
"s_add_u32 s79, s78, 0x4000\n"
"s_lshl_b32 s80, s74, 14\n"
"s_add_u32 s80, s80, 0x100000\n"
"s_add_u32 s81, s80, 0x4000\n"
"s_waitcnt vmcnt(12)\n"
"v_readfirstlane_b32 s68, v30\n"
"v_readfirstlane_b32 s69, v31\n"
"v_lshlrev_b32 v48, 16, v32\n"
"v_and_b32 v49, 0xffff0000, v32\n"
"v_lshlrev_b32 v50, 16, v33\n"
"v_and_b32 v51, 0xffff0000, v33\n"
"v_lshlrev_b32 v52, 16, v34\n"
"v_and_b32 v53, 0xffff0000, v34\n"
"v_lshlrev_b32 v54, 16, v35\n"
"v_and_b32 v55, 0xffff0000, v35\n"
"v_lshlrev_b32 v56, 16, v36\n"
"v_and_b32 v57, 0xffff0000, v36\n"
"v_lshlrev_b32 v58, 16, v37\n"
"v_and_b32 v59, 0xffff0000, v37\n"
"v_lshlrev_b32 v60, 16, v38\n"
"v_and_b32 v61, 0xffff0000, v38\n"
"v_lshlrev_b32 v62, 16, v39\n"
"v_and_b32 v63, 0xffff0000, v39\n"
"s_add_u32 s68, s68, 1\n"
"s_waitcnt vmcnt(17)\n"
"s_barrier\n"
"ds_read_b128 a[64:67], v2 offset:0\n"
"ds_read_b128 a[68:71], v3 offset:0\n"
"ds_read_b128 a[72:75], v2 offset:2048\n"
"ds_read_b128 a[76:79], v3 offset:2048\n"
"ds_read_b128 a[80:83], v2 offset:4096\n"
"ds_read_b128 a[84:87], v3 offset:4096\n"
"ds_read_b128 a[88:91], v2 offset:6144\n"
"ds_read_b128 a[92:95], v3 offset:6144\n"
"ds_read_b64_tr_b16 a[96:97], v4 offset:0\n"
"ds_read_b64_tr_b16 a[98:99], v4 offset:1024\n"
"ds_read_b64_tr_b16 a[100:101], v4 offset:8192\n"
"ds_read_b64_tr_b16 a[102:103], v4 offset:9216\n"
"ds_read_b64_tr_b16 a[104:105], v5 offset:0\n"
"ds_read_b64_tr_b16 a[106:107], v5 offset:1024\n"
"ds_read_b64_tr_b16 a[108:109], v5 offset:8192\n"
"ds_read_b64_tr_b16 a[110:111], v5 offset:9216\n"
"ds_read_b64_tr_b16 a[112:113], v6 offset:0\n"
"ds_read_b64_tr_b16 a[114:115], v6 offset:1024\n"
"ds_read_b64_tr_b16 a[116:117], v6 offset:8192\n"
"ds_read_b64_tr_b16 a[118:119], v6 offset:9216\n"
"ds_read_b64_tr_b16 a[120:121], v7 offset:0\n"
"ds_read_b64_tr_b16 a[122:123], v7 offset:1024\n"
"ds_read_b64_tr_b16 a[124:125], v7 offset:8192\n"
"ds_read_b64_tr_b16 a[126:127], v7 offset:9216\n"
"s_waitcnt lgkmcnt(0)\n"
"s_add_u32 m0, s70, 0x1c000\n"
"s_add_u32 s95, s72, 0x80000\n"
"v_mfma_f32_16x16x32_bf16 v[64:67], a[96:99], a[64:67], 0\n"
"buffer_load_dwordx4 v10, s[44:47], s95 offen offset:0 lds\n"
"v_mfma_f32_16x16x32_bf16 v[64:67], a[100:103], a[68:71], v[64:67]\n"
"ds_read_b128 a[128:131], v2 offset:16384\n"
"s_mov_b32 s4, s18\n"
"s_and_b32 s5, s19, 0xffff\n"
"v_mfma_f32_16x16x32_bf16 v[68:71], a[108:111], a[68:71], 0\n"
"s_mov_b32 s6, 0x400000\n"
"s_lshr_b32 s34, s96, 3\n"
"v_mfma_f32_16x16x32_bf16 v[68:71], a[104:107], a[64:67], v[68:71]\n"
"ds_read_b128 a[132:135], v3 offset:16384\n"
"s_add_u32 s95, s72, 0x87c00\n"
"s_lshl_b32 s34, s34, 15\n"
"s_and_b32 s35, s96, 7\n"
"v_mfma_f32_16x16x32_bf16 v[72:75], a[112:115], a[64:67], 0\n"
"buffer_load_dwordx4 v10, s[44:47], s95 offen offset:1024 lds\n"
"s_lshl_b32 s35, s35, 7\n"
"s_add_u32 s34, s34, s35\n"
"v_mfma_f32_16x16x32_bf16 v[72:75], a[116:119], a[68:71], v[72:75]\n"
"v_lshrrev_b32 v31, 3, v21\n"
"v_and_b32 v23, 6, v31\n"
"v_mfma_f32_16x16x32_bf16 v[76:79], a[124:127], a[68:71], 0\n"
"ds_read_b128 a[136:139], v2 offset:18432\n"
"s_add_u32 s95, s72, 0x8f800\n"
"v_xor_b32 v23, v23, v21\n"
"v_and_b32 v23, 7, v23\n"
"v_mfma_f32_16x16x32_bf16 v[76:79], a[120:123], a[64:67], v[76:79]\n"
"buffer_load_dwordx4 v11, s[44:47], s95 offen offset:2048 lds\n"
"v_lshlrev_b32 v23, 4, v23\n"
"v_lshl_add_u32 v23, v31, 10, v23\n"
"v_mfma_f32_16x16x32_bf16 v[92:95], a[120:123], a[72:75], 0\n"
"buffer_load_dwordx4 a[216:219], v23, s[4:7], s34 offen\n"
"v_mfma_f32_16x16x32_bf16 v[92:95], a[124:127], a[76:79], v[92:95]\n"
"ds_read_b128 a[140:143], v3 offset:18432\n"
"s_add_u32 s95, s72, 0x97400\n"
"v_mfma_f32_16x16x32_bf16 v[88:91], a[116:119], a[76:79], 0\n"
"buffer_load_dwordx4 v11, s[44:47], s95 offen offset:3072 lds\n"
"v_mfma_f32_16x16x32_bf16 v[88:91], a[112:115], a[72:75], v[88:91]\n"
"s_addc_u32 s69, s69, 0\n"
"v_add_f32 v32, v48, v49\n"
"v_mfma_f32_16x16x32_bf16 v[84:87], a[104:107], a[72:75], 0\n"
"ds_read_b128 a[144:147], v2 offset:20480\n"
"v_add_f32 v33, v50, v51\n"
"v_mfma_f32_16x16x32_bf16 v[84:87], a[108:111], a[76:79], v[84:87]\n"
"v_add_f32 v32, v32, v52\n"
"v_add_f32 v33, v33, v53\n"
"v_mfma_f32_16x16x32_bf16 v[80:83], a[100:103], a[76:79], 0\n"
"v_add_f32 v32, v32, v54\n"
"v_add_f32 v33, v33, v55\n"
"v_mfma_f32_16x16x32_bf16 v[80:83], a[96:99], a[72:75], v[80:83]\n"
"ds_read_b128 a[148:151], v3 offset:20480\n"
"v_add_f32 v32, v32, v56\n"
"v_mfma_f32_16x16x32_bf16 v[96:99], a[96:99], a[80:83], 0\n"
"v_add_f32 v33, v33, v57\n"
"v_add_f32 v32, v32, v58\n"
"v_mfma_f32_16x16x32_bf16 v[96:99], a[100:103], a[84:87], v[96:99]\n"
"v_add_f32 v33, v33, v59\n"
"v_add_f32 v32, v32, v60\n"
"v_mfma_f32_16x16x32_bf16 v[100:103], a[108:111], a[84:87], 0\n"
"ds_read_b128 a[152:155], v2 offset:22528\n"
"v_add_f32 v33, v33, v61\n"
"v_mfma_f32_16x16x32_bf16 v[100:103], a[104:107], a[80:83], v[100:103]\n"
"v_add_f32 v32, v32, v62\n"
"v_add_f32 v33, v33, v63\n"
"v_mfma_f32_16x16x32_bf16 v[104:107], a[112:115], a[80:83], 0\n"
"v_add_f32 v32, v32, v33\n"
"v_mfma_f32_16x16x32_bf16 v[104:107], a[116:119], a[84:87], v[104:107]\n"
"ds_read_b128 a[156:159], v3 offset:22528\n"
"v_add_f32_dpp v32, v32, v32 row_ror:2 row_mask:0xf bank_mask:0xf\n"
"v_mfma_f32_16x16x32_bf16 v[108:111], a[124:127], a[84:87], 0\n"
"v_mfma_f32_16x16x32_bf16 v[108:111], a[120:123], a[80:83], v[108:111]\n"
"v_add_f32_dpp v32, v32, v32 row_ror:4 row_mask:0xf bank_mask:0xf\n"
"v_mfma_f32_16x16x32_bf16 v[124:127], a[120:123], a[88:91], 0\n"
"v_mfma_f32_16x16x32_bf16 v[124:127], a[124:127], a[92:95], v[124:127]\n"
"v_add_f32_dpp v32, v32, v32 row_ror:8 row_mask:0xf bank_mask:0xf\n"
"v_mov_b32 v33, v32\n"
"v_mfma_f32_16x16x32_bf16 v[120:123], a[116:119], a[92:95], 0\n"
"v_mfma_f32_16x16x32_bf16 v[120:123], a[112:115], a[88:91], v[120:123]\n"
"v_permlane16_swap_b32 v32, v33\n"
"v_add_f32 v32, v32, v33\n"
"v_mfma_f32_16x16x32_bf16 v[116:119], a[104:107], a[88:91], 0\n"
"v_mov_b32 v33, v32\n"
"v_mfma_f32_16x16x32_bf16 v[116:119], a[108:111], a[92:95], v[116:119]\n"
"v_mfma_f32_16x16x32_bf16 v[112:115], a[100:103], a[92:95], 0\n"
"v_permlane32_swap_b32 v32, v33\n"
"v_add_f32 v32, v32, v33\n"
"v_mfma_f32_16x16x32_bf16 v[112:115], a[96:99], a[88:91], v[112:115]\n"
"s_waitcnt lgkmcnt(0)\n"
"v_mul_f32 v34, 0x3b000000, v32\n"
"v_mfma_f32_16x16x32_bf16 v[192:195], a[96:99], a[128:131], 0\n"
"ds_read_b64_tr_b16 a[160:161], v4 offset:16384\n"
"v_sub_f32 v48, v48, v34\n"
"v_mfma_f32_16x16x32_bf16 v[192:195], a[100:103], a[132:135], v[192:195]\n"
"ds_read_b64_tr_b16 a[162:163], v4 offset:17408\n"
"v_sub_f32 v49, v49, v34\n"
"v_mfma_f32_16x16x32_bf16 v[196:199], a[108:111], a[132:135], 0\n"
"ds_read_b64_tr_b16 a[164:165], v4 offset:24576\n"
"v_sub_f32 v50, v50, v34\n"
"v_mfma_f32_16x16x32_bf16 v[196:199], a[104:107], a[128:131], v[196:199]\n"
"v_sub_f32 v51, v51, v34\n"
"v_sub_f32 v52, v52, v34\n"
"v_mfma_f32_16x16x32_bf16 v[200:203], a[112:115], a[128:131], 0\n"
"ds_read_b64_tr_b16 a[166:167], v4 offset:25600\n"
"v_sub_f32 v53, v53, v34\n"
"v_mfma_f32_16x16x32_bf16 v[200:203], a[116:119], a[132:135], v[200:203]\n"
"ds_read_b64_tr_b16 a[168:169], v5 offset:16384\n"
"v_sub_f32 v54, v54, v34\n"
"v_mfma_f32_16x16x32_bf16 v[204:207], a[124:127], a[132:135], 0\n"
"v_sub_f32 v55, v55, v34\n"
"v_sub_f32 v56, v56, v34\n"
"v_mfma_f32_16x16x32_bf16 v[204:207], a[120:123], a[128:131], v[204:207]\n"
"ds_read_b64_tr_b16 a[170:171], v5 offset:17408\n"
"v_sub_f32 v57, v57, v34\n"
"v_mfma_f32_16x16x32_bf16 v[220:223], a[120:123], a[136:139], 0\n"
"ds_read_b64_tr_b16 a[172:173], v5 offset:24576\n"
"v_sub_f32 v58, v58, v34\n"
"v_mfma_f32_16x16x32_bf16 v[220:223], a[124:127], a[140:143], v[220:223]\n"
"v_sub_f32 v59, v59, v34\n"
"v_sub_f32 v60, v60, v34\n"
"v_mfma_f32_16x16x32_bf16 v[216:219], a[116:119], a[140:143], 0\n"
"ds_read_b64_tr_b16 a[174:175], v5 offset:25600\n"
"v_sub_f32 v61, v61, v34\n"
"v_mfma_f32_16x16x32_bf16 v[216:219], a[112:115], a[136:139], v[216:219]\n"
"ds_read_b64_tr_b16 a[176:177], v6 offset:16384\n"
"v_sub_f32 v62, v62, v34\n"
"v_mfma_f32_16x16x32_bf16 v[212:215], a[104:107], a[136:139], 0\n"
"ds_read_b64_tr_b16 a[178:179], v6 offset:17408\n"
"s_add_u32 m0, s67, 0x20000\n"
"s_add_u32 s75, s71, 0x100\n"
"v_mfma_f32_16x16x32_bf16 v[212:215], a[108:111], a[140:143], v[212:215]\n"
"buffer_load_dwordx4 v8, s[36:39], s75 offen offset:0 lds\n"
"v_mfma_f32_16x16x32_bf16 v[208:211], a[100:103], a[140:143], 0\n"
"ds_read_b64_tr_b16 a[180:181], v6 offset:24576\n"
"v_sub_f32 v63, v63, v34\n"
"v_mfma_f32_16x16x32_bf16 v[208:211], a[96:99], a[136:139], v[208:211]\n"
"ds_read_b64_tr_b16 a[182:183], v6 offset:25600\n"
"s_add_u32 s75, s71, 0x1d00\n"
"v_mfma_f32_16x16x32_bf16 v[224:227], a[96:99], a[144:147], 0\n"
"buffer_load_dwordx4 v9, s[36:39], s75 offen offset:1024 lds\n"
"v_mfma_f32_16x16x32_bf16 v[224:227], a[100:103], a[148:151], v[224:227]\n"
"ds_read_b64_tr_b16 a[184:185], v7 offset:16384\n"
"v_mul_f32 v32, v48, v48\n"
"v_mfma_f32_16x16x32_bf16 v[228:231], a[108:111], a[148:151], 0\n"
"ds_read_b64_tr_b16 a[186:187], v7 offset:17408\n"
"s_add_u32 s75, s71, 0x3900\n"
"v_mfma_f32_16x16x32_bf16 v[228:231], a[104:107], a[144:147], v[228:231]\n"
"buffer_load_dwordx4 v8, s[36:39], s75 offen offset:2048 lds\n"
"v_mfma_f32_16x16x32_bf16 v[232:235], a[112:115], a[144:147], 0\n"
"ds_read_b64_tr_b16 a[188:189], v7 offset:24576\n"
"v_mul_f32 v33, v49, v49\n"
"v_mfma_f32_16x16x32_bf16 v[232:235], a[116:119], a[148:151], v[232:235]\n"
"ds_read_b64_tr_b16 a[190:191], v7 offset:25600\n"
"s_add_u32 s75, s71, 0x5500\n"
"v_mfma_f32_16x16x32_bf16 v[236:239], a[124:127], a[148:151], 0\n"
"buffer_load_dwordx4 v9, s[36:39], s75 offen offset:3072 lds\n"
"s_add_u32 m0, s67, 0x21000\n"
"s_add_u32 s75, s71, 0x8100\n"
"v_mfma_f32_16x16x32_bf16 v[236:239], a[120:123], a[144:147], v[236:239]\n"
"buffer_load_dwordx4 v8, s[36:39], s75 offen offset:0 lds\n"
"s_add_u32 s75, s71, 0x9d00\n"
"v_mfma_f32_16x16x32_bf16 v[252:255], a[120:123], a[152:155], 0\n"
"buffer_load_dwordx4 v9, s[36:39], s75 offen offset:1024 lds\n"
"s_add_u32 s75, s71, 0xb900\n"
"v_add_u32 v2, 0x10000, v2\n"
"v_mfma_f32_16x16x32_bf16 v[252:255], a[124:127], a[156:159], v[252:255]\n"
"buffer_load_dwordx4 v8, s[36:39], s75 offen offset:2048 lds\n"
"v_add_u32 v3, 0x10000, v3\n"
"v_mfma_f32_16x16x32_bf16 v[248:251], a[116:119], a[156:159], 0\n"
"s_add_u32 s75, s71, 0xd500\n"
"v_add_u32 v4, 0x10000, v4\n"
"v_mfma_f32_16x16x32_bf16 v[248:251], a[112:115], a[152:155], v[248:251]\n"
"buffer_load_dwordx4 v9, s[36:39], s75 offen offset:3072 lds\n"
"v_add_u32 v5, 0x10000, v5\n"
"v_mfma_f32_16x16x32_bf16 v[244:247], a[104:107], a[152:155], 0\n"
"v_add_u32 v6, 0x10000, v6\n"
"v_fmac_f32 v32, v50, v50\n"
"v_mfma_f32_16x16x32_bf16 v[244:247], a[108:111], a[156:159], v[244:247]\n"
"v_add_u32 v7, 0x10000, v7\n"
"v_fmac_f32 v33, v51, v51\n"
"v_mfma_f32_16x16x32_bf16 v[240:243], a[100:103], a[156:159], 0\n"
"v_fmac_f32 v32, v52, v52\n"
"v_fmac_f32 v33, v53, v53\n"
"v_mfma_f32_16x16x32_bf16 v[240:243], a[96:99], a[152:155], v[240:243]\n"
"s_waitcnt vmcnt(8) lgkmcnt(0)\n"
"s_barrier\n"
"v_mfma_f32_16x16x32_bf16 a[0:3], a[128:131], a[160:163], 0\n"
"v_fmac_f32 v32, v54, v54\n"
"v_fmac_f32 v33, v55, v55\n"
"v_mfma_f32_16x16x32_bf16 a[0:3], a[132:135], a[164:167], a[0:3]\n"
"ds_read_b128 a[96:99], v2 offset:0\n"
"s_add_u32 m0, s70, 0x0\n"
"s_add_u32 s95, s72, 0x100000\n"
"v_mfma_f32_16x16x32_bf16 a[4:7], a[140:143], a[164:167], 0\n"
"buffer_load_dwordx4 v10, s[40:43], s95 offen offset:0 lds\n"
"v_mfma_f32_16x16x32_bf16 a[4:7], a[136:139], a[160:163], a[4:7]\n"
"ds_read_b128 a[100:103], v3 offset:0\n"
"v_fmac_f32 v32, v56, v56\n"
"v_mfma_f32_16x16x32_bf16 a[32:35], a[144:147], a[160:163], 0\n"
"v_fmac_f32 v33, v57, v57\n"
"v_fmac_f32 v32, v58, v58\n"
"v_mfma_f32_16x16x32_bf16 a[32:35], a[148:151], a[164:167], a[32:35]\n"
"v_fmac_f32 v33, v59, v59\n"
"v_fmac_f32 v32, v60, v60\n"
"v_mfma_f32_16x16x32_bf16 a[36:39], a[156:159], a[164:167], 0\n"
"ds_read_b128 a[104:107], v2 offset:2048\n"
"s_add_u32 s95, s72, 0x107c00\n"
"v_mfma_f32_16x16x32_bf16 a[36:39], a[152:155], a[160:163], a[36:39]\n"
"buffer_load_dwordx4 v10, s[40:43], s95 offen offset:1024 lds\n"
"v_mfma_f32_16x16x32_bf16 a[44:47], a[152:155], a[168:171], 0\n"
"v_fmac_f32 v33, v61, v61\n"
"v_fmac_f32 v32, v62, v62\n"
"v_mfma_f32_16x16x32_bf16 a[44:47], a[156:159], a[172:175], a[44:47]\n"
"ds_read_b128 a[108:111], v3 offset:2048\n"
"s_add_u32 s95, s72, 0x10f800\n"
"v_mfma_f32_16x16x32_bf16 a[40:43], a[148:151], a[172:175], 0\n"
"buffer_load_dwordx4 v11, s[40:43], s95 offen offset:2048 lds\n"
"v_mfma_f32_16x16x32_bf16 a[40:43], a[144:147], a[168:171], a[40:43]\n"
"v_fmac_f32 v33, v63, v63\n"
"v_add_f32 v32, v32, v33\n"
"v_mfma_f32_16x16x32_bf16 a[12:15], a[136:139], a[168:171], 0\n"
"ds_read_b128 a[112:115], v2 offset:4096\n"
"v_add_f32_dpp v32, v32, v32 row_ror:2 row_mask:0xf bank_mask:0xf\n"
"v_mfma_f32_16x16x32_bf16 a[12:15], a[140:143], a[172:175], a[12:15]\n"
"s_add_u32 s95, s72, 0x117400\n"
"v_add_f32_dpp v32, v32, v32 row_ror:4 row_mask:0xf bank_mask:0xf\n"
"v_mfma_f32_16x16x32_bf16 a[8:11], a[132:135], a[172:175], 0\n"
"buffer_load_dwordx4 v11, s[40:43], s95 offen offset:3072 lds\n"
"v_mfma_f32_16x16x32_bf16 a[8:11], a[128:131], a[168:171], a[8:11]\n"
"ds_read_b128 a[116:119], v3 offset:4096\n"
"v_add_f32_dpp v32, v32, v32 row_ror:8 row_mask:0xf bank_mask:0xf\n"
"v_mfma_f32_16x16x32_bf16 a[16:19], a[128:131], a[176:179], 0\n"
"v_mov_b32 v33, v32\n"
"v_mfma_f32_16x16x32_bf16 a[16:19], a[132:135], a[180:183], a[16:19]\n"
"v_mfma_f32_16x16x32_bf16 a[20:23], a[140:143], a[180:183], 0\n"
"ds_read_b128 a[120:123], v2 offset:6144\n"
"s_add_u32 m0, s70, 0x4000\n"
"s_add_u32 s95, s72, 0x100000\n"
"v_mfma_f32_16x16x32_bf16 a[20:23], a[136:139], a[176:179], a[20:23]\n"
"buffer_load_dwordx4 v10, s[44:47], s95 offen offset:0 lds\n"
"v_mfma_f32_16x16x32_bf16 a[48:51], a[144:147], a[176:179], 0\n"
"v_permlane16_swap_b32 v32, v33\n"
"v_add_f32 v32, v32, v33\n"
"v_mfma_f32_16x16x32_bf16 a[48:51], a[148:151], a[180:183], a[48:51]\n"
"ds_read_b128 a[124:127], v3 offset:6144\n"
"v_mov_b32 v33, v32\n"
"v_mfma_f32_16x16x32_bf16 a[52:55], a[156:159], a[180:183], 0\n"
"s_add_u32 s95, s72, 0x107c00\n"
"v_permlane32_swap_b32 v32, v33\n"
"v_mfma_f32_16x16x32_bf16 a[52:55], a[152:155], a[176:179], a[52:55]\n"
"buffer_load_dwordx4 v10, s[44:47], s95 offen offset:1024 lds\n"
"v_mfma_f32_16x16x32_bf16 a[60:63], a[152:155], a[184:187], 0\n"
"v_add_f32 v32, v32, v33\n"
"v_mul_f32 v32, 0x3b000000, v32\n"
"v_mfma_f32_16x16x32_bf16 a[60:63], a[156:159], a[188:191], a[60:63]\n"
"v_cvt_pk_bf16_f32 v33, v32, v34\n"
"v_and_b32 v38, 0xffff0000, v33\n"
"v_mfma_f32_16x16x32_bf16 a[56:59], a[148:151], a[188:191], 0\n"
"s_add_u32 s95, s72, 0x10f800\n"
"v_lshlrev_b32 v32, 16, v33\n"
"v_mfma_f32_16x16x32_bf16 a[56:59], a[144:147], a[184:187], a[56:59]\n"
"buffer_load_dwordx4 v11, s[44:47], s95 offen offset:2048 lds\n"
"v_mfma_f32_16x16x32_bf16 a[28:31], a[136:139], a[184:187], 0\n"
"v_add_f32 v32, 0x3727c5ac, v32\n"
"v_cvt_pk_bf16_f32 v32, v32, v32\n"
"v_mfma_f32_16x16x32_bf16 a[28:31], a[140:143], a[188:191], a[28:31]\n"
"v_lshlrev_b32 v32, 16, v32\n"
"v_mfma_f32_16x16x32_bf16 a[24:27], a[132:135], a[188:191], 0\n"
"s_add_u32 s95, s72, 0x117400\n"
"v_mfma_f32_16x16x32_bf16 a[24:27], a[128:131], a[184:187], a[24:27]\n"
"buffer_load_dwordx4 v11, s[44:47], s95 offen offset:3072 lds\n"
"s_waitcnt lgkmcnt(0)\n"
"v_mfma_f32_16x16x32_bf16 v[128:131], a[64:67], a[160:163], 0\n"
"v_sqrt_f32 v32, v32\n"
"v_mfma_f32_16x16x32_bf16 v[128:131], a[68:71], a[164:167], v[128:131]\n"
"ds_read_b64_tr_b16 a[128:129], v4 offset:0\n"
"v_cvt_pk_bf16_f32 v32, v32, v32\n"
"v_mfma_f32_16x16x32_bf16 v[132:135], a[76:79], a[164:167], 0\n"
"ds_read_b64_tr_b16 a[130:131], v4 offset:1024\n"
"v_lshlrev_b32 v32, 16, v32\n"
"v_mfma_f32_16x16x32_bf16 v[132:135], a[72:75], a[160:163], v[132:135]\n"
"ds_read_b64_tr_b16 a[132:133], v4 offset:8192\n"
"v_mfma_f32_16x16x32_bf16 v[160:163], a[80:83], a[160:163], 0\n"
"buffer_load_dwordx4 a[192:195], v28, s[56:59], s88 offen offset:0\n"
"v_mfma_f32_16x16x32_bf16 v[160:163], a[84:87], a[164:167], v[160:163]\n"
"ds_read_b64_tr_b16 a[134:135], v4 offset:9216\n"
"v_mfma_f32_16x16x32_bf16 v[164:167], a[92:95], a[164:167], 0\n"
"ds_read_b64_tr_b16 a[136:137], v5 offset:0\n"
"v_mfma_f32_16x16x32_bf16 v[164:167], a[88:91], a[160:163], v[164:167]\n"
"v_rcp_f32 v39, v32\n"
"v_mfma_f32_16x16x32_bf16 v[172:175], a[88:91], a[168:171], 0\n"
"ds_read_b64_tr_b16 a[138:139], v5 offset:1024\n"
"v_sub_f32 v36, v34, v38\n"
"v_mfma_f32_16x16x32_bf16 v[172:175], a[92:95], a[172:175], v[172:175]\n"
"ds_read_b64_tr_b16 a[140:141], v5 offset:8192\n"
"v_add_f32 v48, v48, v36\n"
"v_mfma_f32_16x16x32_bf16 v[168:171], a[84:87], a[172:175], 0\n"
"v_add_f32 v49, v49, v36\n"
"v_add_f32 v50, v50, v36\n"
"v_mfma_f32_16x16x32_bf16 v[168:171], a[80:83], a[168:171], v[168:171]\n"
"ds_read_b64_tr_b16 a[142:143], v5 offset:9216\n"
"v_add_f32 v51, v51, v36\n"
"v_mfma_f32_16x16x32_bf16 v[140:143], a[72:75], a[168:171], 0\n"
"ds_read_b64_tr_b16 a[144:145], v6 offset:0\n"
"v_add_f32 v52, v52, v36\n"
"v_mfma_f32_16x16x32_bf16 v[140:143], a[76:79], a[172:175], v[140:143]\n"
"ds_read_b64_tr_b16 a[146:147], v6 offset:1024\n"
"v_add_f32 v53, v53, v36\n"
"v_mfma_f32_16x16x32_bf16 v[136:139], a[68:71], a[172:175], 0\n"
"v_add_f32 v54, v54, v36\n"
"v_add_f32 v55, v55, v36\n"
"v_mfma_f32_16x16x32_bf16 v[136:139], a[64:67], a[168:171], v[136:139]\n"
"ds_read_b64_tr_b16 a[148:149], v6 offset:8192\n"
"v_add_f32 v56, v56, v36\n"
"v_mfma_f32_16x16x32_bf16 v[144:147], a[64:67], a[176:179], 0\n"
"ds_read_b64_tr_b16 a[150:151], v6 offset:9216\n"
"v_add_f32 v57, v57, v36\n"
"v_mfma_f32_16x16x32_bf16 v[144:147], a[68:71], a[180:183], v[144:147]\n"
"v_add_f32 v58, v58, v36\n"
"v_add_f32 v59, v59, v36\n"
"v_mfma_f32_16x16x32_bf16 v[148:151], a[76:79], a[180:183], 0\n"
"ds_read_b64_tr_b16 a[152:153], v7 offset:0\n"
"v_add_f32 v60, v60, v36\n"
"v_mfma_f32_16x16x32_bf16 v[148:151], a[72:75], a[176:179], v[148:151]\n"
"ds_read_b64_tr_b16 a[154:155], v7 offset:1024\n"
"v_add_f32 v61, v61, v36\n"
"v_mfma_f32_16x16x32_bf16 v[176:179], a[80:83], a[176:179], 0\n"
"v_add_f32 v62, v62, v36\n"
"v_add_f32 v63, v63, v36\n"
"v_mfma_f32_16x16x32_bf16 v[176:179], a[84:87], a[180:183], v[176:179]\n"
"ds_read_b64_tr_b16 a[156:157], v7 offset:8192\n"
"v_cvt_pk_bf16_f32 v48, v48, v49\n"
"v_mfma_f32_16x16x32_bf16 v[180:183], a[92:95], a[180:183], 0\n"
"ds_read_b64_tr_b16 a[158:159], v7 offset:9216\n"
"v_and_b32 v49, 0xffff0000, v48\n"
"v_mfma_f32_16x16x32_bf16 v[180:183], a[88:91], a[176:179], v[180:183]\n"
"v_lshlrev_b32 v48, 16, v48\n"
"v_cvt_pk_bf16_f32 v50, v50, v51\n"
"v_mfma_f32_16x16x32_bf16 v[188:191], a[88:91], a[184:187], 0\n"
"v_and_b32 v51, 0xffff0000, v50\n"
"v_lshlrev_b32 v50, 16, v50\n"
"v_mfma_f32_16x16x32_bf16 v[188:191], a[92:95], a[188:191], v[188:191]\n"
"v_cvt_pk_bf16_f32 v52, v52, v53\n"
"v_and_b32 v53, 0xffff0000, v52\n"
"v_mfma_f32_16x16x32_bf16 v[184:187], a[84:87], a[188:191], 0\n"
"v_lshlrev_b32 v52, 16, v52\n"
"v_cvt_pk_bf16_f32 v54, v54, v55\n"
"v_mfma_f32_16x16x32_bf16 v[184:187], a[80:83], a[184:187], v[184:187]\n"
"v_and_b32 v55, 0xffff0000, v54\n"
"v_lshlrev_b32 v54, 16, v54\n"
"v_mfma_f32_16x16x32_bf16 v[156:159], a[72:75], a[184:187], 0\n"
"v_cvt_pk_bf16_f32 v56, v56, v57\n"
"v_and_b32 v57, 0xffff0000, v56\n"
"v_mfma_f32_16x16x32_bf16 v[156:159], a[76:79], a[188:191], v[156:159]\n"
"v_lshlrev_b32 v56, 16, v56\n"
"v_cvt_pk_bf16_f32 v58, v58, v59\n"
"v_mfma_f32_16x16x32_bf16 v[152:155], a[68:71], a[188:191], 0\n"
"v_and_b32 v59, 0xffff0000, v58\n"
"v_lshlrev_b32 v58, 16, v58\n"
"v_mfma_f32_16x16x32_bf16 v[152:155], a[64:67], a[184:187], v[152:155]\n"
"s_waitcnt lgkmcnt(0)\n"
"v_cvt_pk_bf16_f32 v60, v60, v61\n"
"s_add_u32 s34, s34, 0x2000\n"
"v_mfma_f32_16x16x32_bf16 v[64:67], a[128:131], a[96:99], v[64:67]\n"
"buffer_load_dwordx4 a[220:223], v23, s[4:7], s34 offen\n"
"v_mfma_f32_16x16x32_bf16 v[64:67], a[132:135], a[100:103], v[64:67]\n"
"ds_read_b128 a[160:163], v2 offset:16384\n"
"v_and_b32 v61, 0xffff0000, v60\n"
"v_mfma_f32_16x16x32_bf16 v[68:71], a[140:143], a[100:103], v[68:71]\n"
"v_lshlrev_b32 v60, 16, v60\n"
"v_cvt_pk_bf16_f32 v62, v62, v63\n"
"v_mfma_f32_16x16x32_bf16 v[68:71], a[136:139], a[96:99], v[68:71]\n"
"ds_read_b128 a[164:167], v3 offset:16384\n"
"v_and_b32 v63, 0xffff0000, v62\n"
"v_mfma_f32_16x16x32_bf16 v[72:75], a[144:147], a[96:99], v[72:75]\n"
"v_lshlrev_b32 v62, 16, v62\n"
"v_mul_f32 v48, v48, v39\n"
"v_mfma_f32_16x16x32_bf16 v[72:75], a[148:151], a[100:103], v[72:75]\n"
"v_mul_f32 v49, v49, v39\n"
"v_mul_f32 v50, v50, v39\n"
"v_mfma_f32_16x16x32_bf16 v[76:79], a[156:159], a[100:103], v[76:79]\n"
"ds_read_b128 a[168:171], v2 offset:18432\n"
"v_mul_f32 v51, v51, v39\n"
"v_mfma_f32_16x16x32_bf16 v[76:79], a[152:155], a[96:99], v[76:79]\n"
"v_mul_f32 v52, v52, v39\n"
"v_mul_f32 v53, v53, v39\n"
"v_mfma_f32_16x16x32_bf16 v[92:95], a[152:155], a[104:107], v[92:95]\n"
"v_mul_f32 v54, v54, v39\n"
"v_mul_f32 v55, v55, v39\n"
"v_mfma_f32_16x16x32_bf16 v[92:95], a[156:159], a[108:111], v[92:95]\n"
"ds_read_b128 a[172:175], v3 offset:18432\n"
"v_mul_f32 v56, v56, v39\n"
"v_mfma_f32_16x16x32_bf16 v[88:91], a[148:151], a[108:111], v[88:91]\n"
"v_mul_f32 v57, v57, v39\n"
"v_mul_f32 v58, v58, v39\n"
"v_mfma_f32_16x16x32_bf16 v[88:91], a[144:147], a[104:107], v[88:91]\n"
"v_mul_f32 v59, v59, v39\n"
"v_mul_f32 v60, v60, v39\n"
"v_mfma_f32_16x16x32_bf16 v[84:87], a[136:139], a[104:107], v[84:87]\n"
"ds_read_b128 a[176:179], v2 offset:20480\n"
"v_mul_f32 v61, v61, v39\n"
"v_mfma_f32_16x16x32_bf16 v[84:87], a[140:143], a[108:111], v[84:87]\n"
"v_mul_f32 v62, v62, v39\n"
"v_mul_f32 v63, v63, v39\n"
"v_mfma_f32_16x16x32_bf16 v[80:83], a[132:135], a[108:111], v[80:83]\n"
"v_mov_b32 v37, 0\n"
"v_mov_b32 v32, 0\n"
"v_mfma_f32_16x16x32_bf16 v[80:83], a[128:131], a[104:107], v[80:83]\n"
"ds_read_b128 a[180:183], v3 offset:20480\n"
"v_cvt_pk_bf16_f32 v35, v48, v49\n"
"v_mfma_f32_16x16x32_bf16 v[96:99], a[128:131], a[112:115], v[96:99]\n"
"v_lshlrev_b32 v33, 16, v40\n"
"v_and_b32 v49, 0xffff0000, v40\n"
"v_mfma_f32_16x16x32_bf16 v[96:99], a[132:135], a[116:119], v[96:99]\n"
"v_lshlrev_b32 v48, 16, v35\n"
"v_mov_b32 v40, v35\n"
"v_mfma_f32_16x16x32_bf16 v[100:103], a[140:143], a[116:119], v[100:103]\n"
"ds_read_b128 a[184:187], v2 offset:22528\n"
"s_add_u32 m0, s67, 0x8000\n"
"s_add_u32 s75, s71, 0x180\n"
"v_mfma_f32_16x16x32_bf16 v[100:103], a[136:139], a[112:115], v[100:103]\n"
"buffer_load_dwordx4 v8, s[36:39], s75 offen offset:0 lds\n"
"v_mfma_f32_16x16x32_bf16 v[104:107], a[144:147], a[112:115], v[104:107]\n"
"v_and_b32 v35, 0xffff0000, v35\n"
"v_fmac_f32 v37, v33, v48\n"
"v_mfma_f32_16x16x32_bf16 v[104:107], a[148:151], a[116:119], v[104:107]\n"
"ds_read_b128 a[188:191], v3 offset:22528\n"
"v_fmac_f32 v32, v35, v49\n"
"v_mfma_f32_16x16x32_bf16 v[108:111], a[156:159], a[116:119], v[108:111]\n"
"s_add_u32 s75, s71, 0x1d80\n"
"v_cvt_pk_bf16_f32 v35, v50, v51\n"
"v_mfma_f32_16x16x32_bf16 v[108:111], a[152:155], a[112:115], v[108:111]\n"
"buffer_load_dwordx4 v9, s[36:39], s75 offen offset:1024 lds\n"
"v_mfma_f32_16x16x32_bf16 v[124:127], a[152:155], a[120:123], v[124:127]\n"
"v_lshlrev_b32 v33, 16, v41\n"
"v_and_b32 v51, 0xffff0000, v41\n"
"v_mfma_f32_16x16x32_bf16 v[124:127], a[156:159], a[124:127], v[124:127]\n"
"v_lshlrev_b32 v50, 16, v35\n"
"v_mov_b32 v41, v35\n"
"v_mfma_f32_16x16x32_bf16 v[120:123], a[148:151], a[124:127], v[120:123]\n"
"v_and_b32 v35, 0xffff0000, v35\n"
"v_fmac_f32 v37, v33, v50\n"
"v_mfma_f32_16x16x32_bf16 v[120:123], a[144:147], a[120:123], v[120:123]\n"
"s_add_u32 s75, s71, 0x3980\n"
"v_fmac_f32 v32, v35, v51\n"
"v_mfma_f32_16x16x32_bf16 v[116:119], a[136:139], a[120:123], v[116:119]\n"
"buffer_load_dwordx4 v8, s[36:39], s75 offen offset:2048 lds\n"
"v_mfma_f32_16x16x32_bf16 v[116:119], a[140:143], a[124:127], v[116:119]\n"
"v_cvt_pk_bf16_f32 v35, v52, v53\n"
"v_lshlrev_b32 v33, 16, v42\n"
"v_mfma_f32_16x16x32_bf16 v[112:115], a[132:135], a[124:127], v[112:115]\n"
"v_and_b32 v53, 0xffff0000, v42\n"
"v_lshlrev_b32 v52, 16, v35\n"
"v_mfma_f32_16x16x32_bf16 v[112:115], a[128:131], a[120:123], v[112:115]\n"
"s_waitcnt lgkmcnt(0)\n"
"v_mov_b32 v42, v35\n"
"v_mfma_f32_16x16x32_bf16 v[192:195], a[128:131], a[160:163], v[192:195]\n"
"ds_read_b64_tr_b16 a[64:65], v4 offset:16384\n"
"v_and_b32 v35, 0xffff0000, v35\n"
"v_mfma_f32_16x16x32_bf16 v[192:195], a[132:135], a[164:167], v[192:195]\n"
"ds_read_b64_tr_b16 a[66:67], v4 offset:17408\n"
"v_fmac_f32 v37, v33, v52\n"
"v_mfma_f32_16x16x32_bf16 v[196:199], a[140:143], a[164:167], v[196:199]\n"
"ds_read_b64_tr_b16 a[68:69], v4 offset:24576\n"
"s_add_u32 s75, s71, 0x5580\n"
"v_mfma_f32_16x16x32_bf16 v[196:199], a[136:139], a[160:163], v[196:199]\n"
"buffer_load_dwordx4 v9, s[36:39], s75 offen offset:3072 lds\n"
"v_mfma_f32_16x16x32_bf16 v[200:203], a[144:147], a[160:163], v[200:203]\n"
"ds_read_b64_tr_b16 a[70:71], v4 offset:25600\n"
"v_fmac_f32 v32, v35, v53\n"
"v_mfma_f32_16x16x32_bf16 v[200:203], a[148:151], a[164:167], v[200:203]\n"
"ds_read_b64_tr_b16 a[72:73], v5 offset:16384\n"
"s_add_u32 m0, s67, 0x9000\n"
"s_add_u32 s75, s71, 0x8180\n"
"v_mfma_f32_16x16x32_bf16 v[204:207], a[156:159], a[164:167], v[204:207]\n"
"buffer_load_dwordx4 v8, s[36:39], s75 offen offset:0 lds\n"
"v_mfma_f32_16x16x32_bf16 v[204:207], a[152:155], a[160:163], v[204:207]\n"
"ds_read_b64_tr_b16 a[74:75], v5 offset:17408\n"
"v_cvt_pk_bf16_f32 v35, v54, v55\n"
"v_mfma_f32_16x16x32_bf16 v[220:223], a[152:155], a[168:171], v[220:223]\n"
"ds_read_b64_tr_b16 a[76:77], v5 offset:24576\n"
"v_lshlrev_b32 v33, 16, v43\n"
"v_mfma_f32_16x16x32_bf16 v[220:223], a[156:159], a[172:175], v[220:223]\n"
"v_and_b32 v55, 0xffff0000, v43\n"
"v_lshlrev_b32 v54, 16, v35\n"
"v_mfma_f32_16x16x32_bf16 v[216:219], a[148:151], a[172:175], v[216:219]\n"
"ds_read_b64_tr_b16 a[78:79], v5 offset:25600\n"
"v_mov_b32 v43, v35\n"
"v_mfma_f32_16x16x32_bf16 v[216:219], a[144:147], a[168:171], v[216:219]\n"
"ds_read_b64_tr_b16 a[80:81], v6 offset:16384\n"
"v_and_b32 v35, 0xffff0000, v35\n"
"v_mfma_f32_16x16x32_bf16 v[212:215], a[136:139], a[168:171], v[212:215]\n"
"ds_read_b64_tr_b16 a[82:83], v6 offset:17408\n"
"s_add_u32 s75, s71, 0x9d80\n"
"v_mfma_f32_16x16x32_bf16 v[212:215], a[140:143], a[172:175], v[212:215]\n"
"buffer_load_dwordx4 v9, s[36:39], s75 offen offset:1024 lds\n"
"v_mfma_f32_16x16x32_bf16 v[208:211], a[132:135], a[172:175], v[208:211]\n"
"ds_read_b64_tr_b16 a[84:85], v6 offset:24576\n"
"v_fmac_f32 v37, v33, v54\n"
"v_mfma_f32_16x16x32_bf16 v[208:211], a[128:131], a[168:171], v[208:211]\n"
"ds_read_b64_tr_b16 a[86:87], v6 offset:25600\n"
"s_add_u32 s75, s71, 0xb980\n"
"v_mfma_f32_16x16x32_bf16 v[224:227], a[128:131], a[176:179], v[224:227]\n"
"buffer_load_dwordx4 v8, s[36:39], s75 offen offset:2048 lds\n"
"v_mfma_f32_16x16x32_bf16 v[224:227], a[132:135], a[180:183], v[224:227]\n"
"ds_read_b64_tr_b16 a[88:89], v7 offset:16384\n"
"v_fmac_f32 v32, v35, v55\n"
"v_mfma_f32_16x16x32_bf16 v[228:231], a[140:143], a[180:183], v[228:231]\n"
"ds_read_b64_tr_b16 a[90:91], v7 offset:17408\n"
"v_cvt_pk_bf16_f32 v35, v56, v57\n"
"v_mfma_f32_16x16x32_bf16 v[228:231], a[136:139], a[176:179], v[228:231]\n"
"v_lshlrev_b32 v33, 16, v44\n"
"v_and_b32 v57, 0xffff0000, v44\n"
"v_mfma_f32_16x16x32_bf16 v[232:235], a[144:147], a[176:179], v[232:235]\n"
"ds_read_b64_tr_b16 a[92:93], v7 offset:24576\n"
"v_lshlrev_b32 v56, 16, v35\n"
"v_mfma_f32_16x16x32_bf16 v[232:235], a[148:151], a[180:183], v[232:235]\n"
"ds_read_b64_tr_b16 a[94:95], v7 offset:25600\n"
"s_add_u32 s75, s71, 0xd580\n"
"v_mfma_f32_16x16x32_bf16 v[236:239], a[156:159], a[180:183], v[236:239]\n"
"buffer_load_dwordx4 v9, s[36:39], s75 offen offset:3072 lds\n"
"v_mfma_f32_16x16x32_bf16 v[236:239], a[152:155], a[176:179], v[236:239]\n"
"v_mov_b32 v44, v35\n"
"v_and_b32 v35, 0xffff0000, v35\n"
"v_mfma_f32_16x16x32_bf16 v[252:255], a[152:155], a[184:187], v[252:255]\n"
"v_add_u32 v2, 0x10000, v2\n"
"v_fmac_f32 v37, v33, v56\n"
"v_mfma_f32_16x16x32_bf16 v[252:255], a[156:159], a[188:191], v[252:255]\n"
"v_add_u32 v3, 0x10000, v3\n"
"v_fmac_f32 v32, v35, v57\n"
"v_mfma_f32_16x16x32_bf16 v[248:251], a[148:151], a[188:191], v[248:251]\n"
"v_add_u32 v4, 0xfffe8000, v4\n"
"v_cvt_pk_bf16_f32 v35, v58, v59\n"
"v_mfma_f32_16x16x32_bf16 v[248:251], a[144:147], a[184:187], v[248:251]\n"
"v_add_u32 v5, 0xfffe8000, v5\n"
"v_lshlrev_b32 v33, 16, v45\n"
"v_mfma_f32_16x16x32_bf16 v[244:247], a[136:139], a[184:187], v[244:247]\n"
"v_add_u32 v6, 0xfffe8000, v6\n"
"v_and_b32 v59, 0xffff0000, v45\n"
"v_mfma_f32_16x16x32_bf16 v[244:247], a[140:143], a[188:191], v[244:247]\n"
"v_add_u32 v7, 0xfffe8000, v7\n"
"v_lshlrev_b32 v58, 16, v35\n"
"v_mfma_f32_16x16x32_bf16 v[240:243], a[132:135], a[188:191], v[240:243]\n"
"v_mov_b32 v45, v35\n"
"v_and_b32 v35, 0xffff0000, v35\n"
"v_mfma_f32_16x16x32_bf16 v[240:243], a[128:131], a[184:187], v[240:243]\n"
"s_waitcnt vmcnt(10) lgkmcnt(0)\n"
"s_barrier\n"
"v_mfma_f32_16x16x32_bf16 a[0:3], a[160:163], a[64:67], a[0:3]\n"
"v_fmac_f32 v37, v33, v58\n"
"v_fmac_f32 v32, v35, v59\n"
"v_mfma_f32_16x16x32_bf16 a[0:3], a[164:167], a[68:71], a[0:3]\n"
"ds_read_b128 a[128:131], v2 offset:0\n"
"s_add_u32 m0, s70, 0x10000\n"
"s_add_u32 s95, s72, 0x180000\n"
"v_mfma_f32_16x16x32_bf16 a[4:7], a[172:175], a[68:71], a[4:7]\n"
"buffer_load_dwordx4 v10, s[40:43], s95 offen offset:0 lds\n"
"v_mfma_f32_16x16x32_bf16 a[4:7], a[168:171], a[64:67], a[4:7]\n"
"ds_read_b128 a[132:135], v3 offset:0\n"
"v_cvt_pk_bf16_f32 v35, v60, v61\n"
"v_mfma_f32_16x16x32_bf16 a[32:35], a[176:179], a[64:67], a[32:35]\n"
"v_lshlrev_b32 v33, 16, v46\n"
"v_and_b32 v61, 0xffff0000, v46\n"
"v_mfma_f32_16x16x32_bf16 a[32:35], a[180:183], a[68:71], a[32:35]\n"
"v_lshlrev_b32 v60, 16, v35\n"
"v_mov_b32 v46, v35\n"
"v_mfma_f32_16x16x32_bf16 a[36:39], a[188:191], a[68:71], a[36:39]\n"
"ds_read_b128 a[136:139], v2 offset:2048\n"
"s_add_u32 s95, s72, 0x187c00\n"
"v_mfma_f32_16x16x32_bf16 a[36:39], a[184:187], a[64:67], a[36:39]\n"
"buffer_load_dwordx4 v10, s[40:43], s95 offen offset:1024 lds\n"
"v_mfma_f32_16x16x32_bf16 a[44:47], a[184:187], a[72:75], a[44:47]\n"
"v_and_b32 v35, 0xffff0000, v35\n"
"v_fmac_f32 v37, v33, v60\n"
"v_mfma_f32_16x16x32_bf16 a[44:47], a[188:191], a[76:79], a[44:47]\n"
"ds_read_b128 a[140:143], v3 offset:2048\n"
"s_add_u32 s95, s72, 0x18f800\n"
"v_mfma_f32_16x16x32_bf16 a[40:43], a[180:183], a[76:79], a[40:43]\n"
"buffer_load_dwordx4 v11, s[40:43], s95 offen offset:2048 lds\n"
"v_mfma_f32_16x16x32_bf16 a[40:43], a[176:179], a[72:75], a[40:43]\n"
"v_fmac_f32 v32, v35, v61\n"
"v_cvt_pk_bf16_f32 v35, v62, v63\n"
"v_mfma_f32_16x16x32_bf16 a[12:15], a[168:171], a[72:75], a[12:15]\n"
"ds_read_b128 a[144:147], v2 offset:4096\n"
"v_lshlrev_b32 v33, 16, v47\n"
"v_mfma_f32_16x16x32_bf16 a[12:15], a[172:175], a[76:79], a[12:15]\n"
"s_add_u32 s95, s72, 0x197400\n"
"v_and_b32 v63, 0xffff0000, v47\n"
"v_mfma_f32_16x16x32_bf16 a[8:11], a[164:167], a[76:79], a[8:11]\n"
"buffer_load_dwordx4 v11, s[40:43], s95 offen offset:3072 lds\n"
"v_mfma_f32_16x16x32_bf16 a[8:11], a[160:163], a[72:75], a[8:11]\n"
"ds_read_b128 a[148:151], v3 offset:4096\n"
"v_lshlrev_b32 v62, 16, v35\n"
"v_mfma_f32_16x16x32_bf16 a[16:19], a[160:163], a[80:83], a[16:19]\n"
"v_mov_b32 v47, v35\n"
"v_and_b32 v35, 0xffff0000, v35\n"
"v_mfma_f32_16x16x32_bf16 a[16:19], a[164:167], a[84:87], a[16:19]\n"
"v_fmac_f32 v37, v33, v62\n"
"v_fmac_f32 v32, v35, v63\n"
"v_mfma_f32_16x16x32_bf16 a[20:23], a[172:175], a[84:87], a[20:23]\n"
"ds_read_b128 a[152:155], v2 offset:6144\n"
"s_add_u32 m0, s70, 0x14000\n"
"s_add_u32 s95, s72, 0x180000\n"
"v_mfma_f32_16x16x32_bf16 a[20:23], a[168:171], a[80:83], a[20:23]\n"
"buffer_load_dwordx4 v10, s[44:47], s95 offen offset:0 lds\n"
"v_mfma_f32_16x16x32_bf16 a[48:51], a[176:179], a[80:83], a[48:51]\n"
"v_lshrrev_b32 v24, 1, v21\n"
"v_and_b32 v25, 1, v21\n"
"v_mfma_f32_16x16x32_bf16 a[48:51], a[180:183], a[84:87], a[48:51]\n"
"ds_read_b128 a[156:159], v3 offset:6144\n"
"v_lshlrev_b32 v25, 4, v25\n"
"v_mfma_f32_16x16x32_bf16 a[52:55], a[188:191], a[84:87], a[52:55]\n"
"s_add_u32 s95, s72, 0x187c00\n"
"v_lshl_add_u32 v24, v24, 7, v25\n"
"v_mfma_f32_16x16x32_bf16 a[52:55], a[184:187], a[80:83], a[52:55]\n"
"buffer_load_dwordx4 v10, s[44:47], s95 offen offset:1024 lds\n"
"v_mfma_f32_16x16x32_bf16 a[60:63], a[184:187], a[88:91], a[60:63]\n"
"s_lshl_b32 s73, s64, 5\n"
"s_add_u32 s73, s73, 0x18000\n"
"v_mfma_f32_16x16x32_bf16 a[60:63], a[188:191], a[92:95], a[60:63]\n"
"v_add_u32 v24, s73, v24\n"
"v_mfma_f32_16x16x32_bf16 a[56:59], a[180:183], a[92:95], a[56:59]\n"
"s_add_u32 s95, s72, 0x18f800\n"
"v_mfma_f32_16x16x32_bf16 a[56:59], a[176:179], a[88:91], a[56:59]\n"
"buffer_load_dwordx4 v11, s[44:47], s95 offen offset:2048 lds\n"
"v_mfma_f32_16x16x32_bf16 a[28:31], a[168:171], a[88:91], a[28:31]\n"
"ds_write_b128 v24, v[40:43] offset:0\n"
"v_mfma_f32_16x16x32_bf16 a[28:31], a[172:175], a[92:95], a[28:31]\n"
"ds_write_b128 v24, v[44:47] offset:4096\n"
"v_mfma_f32_16x16x32_bf16 a[24:27], a[164:167], a[92:95], a[24:27]\n"
"s_add_u32 s95, s72, 0x197400\n"
"s_lshl_b32 s73, s64, 11\n"
"v_mfma_f32_16x16x32_bf16 a[24:27], a[160:163], a[88:91], a[24:27]\n"
"buffer_load_dwordx4 v11, s[44:47], s95 offen offset:3072 lds\n"
"s_waitcnt lgkmcnt(0)\n"
"s_add_u32 s73, s73, 0x18000\n"
"v_add_u32 v24, s73, v1\n"
"s_waitcnt lgkmcnt(0)\n"
"s_barrier\n"
"v_mfma_f32_16x16x32_bf16 v[128:131], a[96:99], a[64:67], v[128:131]\n"
"ds_read_b128 v[40:43], v24 offset:0\n"
"v_mfma_f32_16x16x32_bf16 v[128:131], a[100:103], a[68:71], v[128:131]\n"
"ds_read_b64_tr_b16 a[160:161], v4 offset:0\n"
"v_mfma_f32_16x16x32_bf16 v[132:135], a[108:111], a[68:71], v[132:135]\n"
"ds_read_b64_tr_b16 a[162:163], v4 offset:1024\n"
"v_mfma_f32_16x16x32_bf16 v[132:135], a[104:107], a[64:67], v[132:135]\n"
"ds_read_b64_tr_b16 a[164:165], v4 offset:8192\n"
"s_add_u32 s34, s34, 0x2000\n"
"v_mfma_f32_16x16x32_bf16 v[160:163], a[112:115], a[64:67], v[160:163]\n"
"buffer_load_dwordx4 a[224:227], v23, s[4:7], s34 offen\n"
"v_mfma_f32_16x16x32_bf16 v[160:163], a[116:119], a[68:71], v[160:163]\n"
"ds_read_b64_tr_b16 a[166:167], v4 offset:9216\n"
"v_mfma_f32_16x16x32_bf16 v[164:167], a[124:127], a[68:71], v[164:167]\n"
"ds_read_b64_tr_b16 a[168:169], v5 offset:0\n"
"v_mfma_f32_16x16x32_bf16 v[164:167], a[120:123], a[64:67], v[164:167]\n"
"ds_read_b128 v[44:47], v24 offset:1024\n"
"v_lshrrev_b32 v24, 5, v21\n"
"v_mfma_f32_16x16x32_bf16 v[172:175], a[120:123], a[72:75], v[172:175]\n"
"ds_read_b64_tr_b16 a[170:171], v5 offset:1024\n"
"v_bfe_u32 v25, v21, 3, 2\n"
"v_mfma_f32_16x16x32_bf16 v[172:175], a[124:127], a[76:79], v[172:175]\n"
"ds_read_b64_tr_b16 a[172:173], v5 offset:8192\n"
"v_lshlrev_b32 v25, 8, v25\n"
"v_mfma_f32_16x16x32_bf16 v[168:171], a[116:119], a[76:79], v[168:171]\n"
"v_lshl_add_u32 v24, v24, 10, v25\n"
"v_and_b32 v25, 0x70, v1\n"
"v_mfma_f32_16x16x32_bf16 v[168:171], a[112:115], a[72:75], v[168:171]\n"
"ds_read_b64_tr_b16 a[174:175], v5 offset:9216\n"
"v_add_u32 v24, v24, v25\n"
"v_mfma_f32_16x16x32_bf16 v[140:143], a[104:107], a[72:75], v[140:143]\n"
"ds_read_b64_tr_b16 a[176:177], v6 offset:0\n"
"s_lshr_b32 s73, s32, 1\n"
"v_mfma_f32_16x16x32_bf16 v[140:143], a[108:111], a[76:79], v[140:143]\n"
"ds_read_b64_tr_b16 a[178:179], v6 offset:1024\n"
"s_lshl_b32 s73, s73, 14\n"
"v_mfma_f32_16x16x32_bf16 v[136:139], a[100:103], a[76:79], v[136:139]\n"
"s_and_b32 s74, s32, 1\n"
"s_lshl_b32 s74, s74, 7\n"
"v_mfma_f32_16x16x32_bf16 v[136:139], a[96:99], a[72:75], v[136:139]\n"
"ds_read_b64_tr_b16 a[180:181], v6 offset:8192\n"
"s_add_u32 s73, s73, s74\n"
"v_mfma_f32_16x16x32_bf16 v[144:147], a[96:99], a[80:83], v[144:147]\n"
"ds_read_b64_tr_b16 a[182:183], v6 offset:9216\n"
"s_add_u32 s73, s73, s70\n"
"v_mfma_f32_16x16x32_bf16 v[144:147], a[100:103], a[84:87], v[144:147]\n"
"s_add_u32 s73, s73, 0x100000\n"
"s_waitcnt lgkmcnt(7)\n"
"v_mfma_f32_16x16x32_bf16 v[148:151], a[108:111], a[84:87], v[148:151]\n"
"ds_read_b64_tr_b16 a[184:185], v7 offset:0\n"
"v_mfma_f32_16x16x32_bf16 v[148:151], a[104:107], a[80:83], v[148:151]\n"
"ds_read_b64_tr_b16 a[186:187], v7 offset:1024\n"
"v_mfma_f32_16x16x32_bf16 v[176:179], a[112:115], a[80:83], v[176:179]\n"
"buffer_store_dwordx4 v[40:43], v24, s[48:51], s73 offen sc1\n"
"v_mfma_f32_16x16x32_bf16 v[176:179], a[116:119], a[84:87], v[176:179]\n"
"ds_read_b64_tr_b16 a[188:189], v7 offset:8192\n"
"v_mfma_f32_16x16x32_bf16 v[180:183], a[124:127], a[84:87], v[180:183]\n"
"ds_read_b64_tr_b16 a[190:191], v7 offset:9216\n"
"v_mfma_f32_16x16x32_bf16 v[180:183], a[120:123], a[80:83], v[180:183]\n"
"buffer_store_dwordx4 v[44:47], v24, s[48:51], s73 offen offset:2048 sc1\n"
"v_mfma_f32_16x16x32_bf16 v[188:191], a[120:123], a[88:91], v[188:191]\n"
"v_add_f32 v37, v37, v32\n"
"v_mfma_f32_16x16x32_bf16 v[188:191], a[124:127], a[92:95], v[188:191]\n"
"v_mfma_f32_16x16x32_bf16 v[184:187], a[116:119], a[92:95], v[184:187]\n"
"v_add_f32_dpp v37, v37, v37 row_ror:2 row_mask:0xf bank_mask:0xf\n"
"v_mfma_f32_16x16x32_bf16 v[184:187], a[112:115], a[88:91], v[184:187]\n"
"v_mfma_f32_16x16x32_bf16 v[156:159], a[104:107], a[88:91], v[156:159]\n"
"v_add_f32_dpp v37, v37, v37 row_ror:4 row_mask:0xf bank_mask:0xf\n"
"v_mfma_f32_16x16x32_bf16 v[156:159], a[108:111], a[92:95], v[156:159]\n"
"v_mfma_f32_16x16x32_bf16 v[152:155], a[100:103], a[92:95], v[152:155]\n"
"v_add_f32_dpp v37, v37, v37 row_ror:8 row_mask:0xf bank_mask:0xf\n"
"v_mov_b32 v33, v37\n"
"v_mfma_f32_16x16x32_bf16 v[152:155], a[96:99], a[88:91], v[152:155]\n"
"s_waitcnt lgkmcnt(0)\n"
"s_barrier\n"
"v_mfma_f32_16x16x32_bf16 v[64:67], a[160:163], a[128:131], v[64:67]\n"
"buffer_load_dwordx4 a[196:199], v28, s[56:59], s88 offen offset:3584\n"
"v_mfma_f32_16x16x32_bf16 v[64:67], a[164:167], a[132:135], v[64:67]\n"
"ds_read_b128 a[64:67], v2 offset:16384\n"
"v_permlane16_swap_b32 v37, v33\n"
"v_mfma_f32_16x16x32_bf16 v[68:71], a[172:175], a[132:135], v[68:71]\n"
"v_add_f32 v37, v37, v33\n"
"v_mov_b32 v33, v37\n"
"v_mfma_f32_16x16x32_bf16 v[68:71], a[168:171], a[128:131], v[68:71]\n"
"ds_read_b128 a[68:71], v3 offset:16384\n"
"v_permlane32_swap_b32 v37, v33\n"
"v_mfma_f32_16x16x32_bf16 v[72:75], a[176:179], a[128:131], v[72:75]\n"
"v_add_f32 v37, v37, v33\n"
"v_cvt_pk_bf16_f32 v37, v37, v37\n"
"v_mfma_f32_16x16x32_bf16 v[72:75], a[180:183], a[132:135], v[72:75]\n"
"v_lshlrev_b32 v37, 16, v37\n"
"v_ashrrev_i32 v33, 31, v37\n"
"v_mfma_f32_16x16x32_bf16 v[76:79], a[188:191], a[132:135], v[76:79]\n"
"ds_read_b128 a[72:75], v2 offset:18432\n"
"v_or_b32 v33, 0x80000000, v33\n"
"v_mfma_f32_16x16x32_bf16 v[76:79], a[184:187], a[128:131], v[76:79]\n"
"v_xor_b32 v37, v37, v33\n"
"v_and_b32 v37, 0xffff0000, v37\n"
"v_mfma_f32_16x16x32_bf16 v[92:95], a[184:187], a[136:139], v[92:95]\n"
"s_lshl_b32 s73, s96, 1\n"
"s_sub_u32 s73, 0xffff, s73\n"
"v_mfma_f32_16x16x32_bf16 v[92:95], a[188:191], a[140:143], v[92:95]\n"
"ds_read_b128 a[76:79], v3 offset:18432\n"
"v_and_b32 v33, 1, v21\n"
"v_mfma_f32_16x16x32_bf16 v[88:91], a[180:183], a[140:143], v[88:91]\n"
"v_sub_u32 v33, s73, v33\n"
"v_or_b32 v32, v37, v33\n"
"v_mfma_f32_16x16x32_bf16 v[88:91], a[176:179], a[136:139], v[88:91]\n"
"v_mfma_f32_16x16x32_bf16 v[84:87], a[168:171], a[136:139], v[84:87]\n"
"ds_read_b128 a[80:83], v2 offset:20480\n"
"v_max_u32_dpp v32, v32, v32 quad_perm:[1,0,3,2] row_mask:0xf bank_mask:0xf\n"
"v_mfma_f32_16x16x32_bf16 v[84:87], a[172:175], a[140:143], v[84:87]\n"
"v_mov_b32 v33, s68\n"
"v_cmp_eq_u32 vcc, 0, v21\n"
"v_mfma_f32_16x16x32_bf16 v[80:83], a[164:167], a[140:143], v[80:83]\n"
"s_lshl_b32 s73, s96, 3\n"
"v_mov_b32 v24, -1\n"
"v_mfma_f32_16x16x32_bf16 v[80:83], a[160:163], a[136:139], v[80:83]\n"
"ds_read_b128 a[84:87], v3 offset:20480\n"
"s_add_u32 s73, s73, 0x1000\n"
"v_mfma_f32_16x16x32_bf16 v[96:99], a[160:163], a[144:147], v[96:99]\n"
"v_cndmask_b32 v24, v24, 0, vcc\n"
"v_mfma_f32_16x16x32_bf16 v[96:99], a[164:167], a[148:151], v[96:99]\n"
"v_mfma_f32_16x16x32_bf16 v[100:103], a[172:175], a[148:151], v[100:103]\n"
"ds_read_b128 a[88:91], v2 offset:22528\n"
"s_add_u32 m0, s67, 0x18000\n"
"s_add_u32 s75, s71, 0x200\n"
"v_mfma_f32_16x16x32_bf16 v[100:103], a[168:171], a[144:147], v[100:103]\n"
"buffer_load_dwordx4 v8, s[36:39], s75 offen offset:0 lds\n"
"v_mfma_f32_16x16x32_bf16 v[104:107], a[176:179], a[144:147], v[104:107]\n"
"v_mfma_f32_16x16x32_bf16 v[104:107], a[180:183], a[148:151], v[104:107]\n"
"ds_read_b128 a[92:95], v3 offset:22528\n"
"v_mfma_f32_16x16x32_bf16 v[108:111], a[188:191], a[148:151], v[108:111]\n"
"s_add_u32 s75, s71, 0x1e00\n"
"v_mfma_f32_16x16x32_bf16 v[108:111], a[184:187], a[144:147], v[108:111]\n"
"buffer_load_dwordx4 v9, s[36:39], s75 offen offset:1024 lds\n"
"v_mfma_f32_16x16x32_bf16 v[124:127], a[184:187], a[152:155], v[124:127]\n"
"v_mfma_f32_16x16x32_bf16 v[124:127], a[188:191], a[156:159], v[124:127]\n"
"v_mfma_f32_16x16x32_bf16 v[120:123], a[180:183], a[156:159], v[120:123]\n"
"v_mfma_f32_16x16x32_bf16 v[120:123], a[176:179], a[152:155], v[120:123]\n"
"s_add_u32 s75, s71, 0x3a00\n"
"v_mfma_f32_16x16x32_bf16 v[116:119], a[168:171], a[152:155], v[116:119]\n"
"buffer_load_dwordx4 v8, s[36:39], s75 offen offset:2048 lds\n"
"v_mfma_f32_16x16x32_bf16 v[116:119], a[172:175], a[156:159], v[116:119]\n"
"v_mfma_f32_16x16x32_bf16 v[112:115], a[164:167], a[156:159], v[112:115]\n"
"v_mfma_f32_16x16x32_bf16 v[112:115], a[160:163], a[152:155], v[112:115]\n"
"s_waitcnt lgkmcnt(0)\n"
"v_mfma_f32_16x16x32_bf16 v[192:195], a[160:163], a[64:67], v[192:195]\n"
"ds_read_b64_tr_b16 a[96:97], v4 offset:16384\n"
"v_mfma_f32_16x16x32_bf16 v[192:195], a[164:167], a[68:71], v[192:195]\n"
"ds_read_b64_tr_b16 a[98:99], v4 offset:17408\n"
"v_mfma_f32_16x16x32_bf16 v[196:199], a[172:175], a[68:71], v[196:199]\n"
"ds_read_b64_tr_b16 a[100:101], v4 offset:24576\n"
"s_add_u32 s75, s71, 0x5600\n"
"v_mfma_f32_16x16x32_bf16 v[196:199], a[168:171], a[64:67], v[196:199]\n"
"buffer_load_dwordx4 v9, s[36:39], s75 offen offset:3072 lds\n"
"v_mfma_f32_16x16x32_bf16 v[200:203], a[176:179], a[64:67], v[200:203]\n"
"ds_read_b64_tr_b16 a[102:103], v4 offset:25600\n"
"v_mfma_f32_16x16x32_bf16 v[200:203], a[180:183], a[68:71], v[200:203]\n"
"ds_read_b64_tr_b16 a[104:105], v5 offset:16384\n"
"s_add_u32 m0, s67, 0x19000\n"
"s_add_u32 s75, s71, 0x8200\n"
"v_mfma_f32_16x16x32_bf16 v[204:207], a[188:191], a[68:71], v[204:207]\n"
"buffer_load_dwordx4 v8, s[36:39], s75 offen offset:0 lds\n"
"v_mfma_f32_16x16x32_bf16 v[204:207], a[184:187], a[64:67], v[204:207]\n"
"ds_read_b64_tr_b16 a[106:107], v5 offset:17408\n"
"v_mfma_f32_16x16x32_bf16 v[220:223], a[184:187], a[72:75], v[220:223]\n"
"ds_read_b64_tr_b16 a[108:109], v5 offset:24576\n"
"v_mfma_f32_16x16x32_bf16 v[220:223], a[188:191], a[76:79], v[220:223]\n"
"v_mfma_f32_16x16x32_bf16 v[216:219], a[180:183], a[76:79], v[216:219]\n"
"ds_read_b64_tr_b16 a[110:111], v5 offset:25600\n"
"v_mfma_f32_16x16x32_bf16 v[216:219], a[176:179], a[72:75], v[216:219]\n"
"ds_read_b64_tr_b16 a[112:113], v6 offset:16384\n"
"v_mfma_f32_16x16x32_bf16 v[212:215], a[168:171], a[72:75], v[212:215]\n"
"ds_read_b64_tr_b16 a[114:115], v6 offset:17408\n"
"s_add_u32 s75, s71, 0x9e00\n"
"v_mfma_f32_16x16x32_bf16 v[212:215], a[172:175], a[76:79], v[212:215]\n"
"buffer_load_dwordx4 v9, s[36:39], s75 offen offset:1024 lds\n"
"v_mfma_f32_16x16x32_bf16 v[208:211], a[164:167], a[76:79], v[208:211]\n"
"ds_read_b64_tr_b16 a[116:117], v6 offset:24576\n"
"v_mfma_f32_16x16x32_bf16 v[208:211], a[160:163], a[72:75], v[208:211]\n"
"ds_read_b64_tr_b16 a[118:119], v6 offset:25600\n"
"s_add_u32 s75, s71, 0xba00\n"
"v_mfma_f32_16x16x32_bf16 v[224:227], a[160:163], a[80:83], v[224:227]\n"
"buffer_load_dwordx4 v8, s[36:39], s75 offen offset:2048 lds\n"
"v_mfma_f32_16x16x32_bf16 v[224:227], a[164:167], a[84:87], v[224:227]\n"
"ds_read_b64_tr_b16 a[120:121], v7 offset:16384\n"
"v_mfma_f32_16x16x32_bf16 v[228:231], a[172:175], a[84:87], v[228:231]\n"
"ds_read_b64_tr_b16 a[122:123], v7 offset:17408\n"
"v_mfma_f32_16x16x32_bf16 v[228:231], a[168:171], a[80:83], v[228:231]\n"
"v_mfma_f32_16x16x32_bf16 v[232:235], a[176:179], a[80:83], v[232:235]\n"
"ds_read_b64_tr_b16 a[124:125], v7 offset:24576\n"
"v_mfma_f32_16x16x32_bf16 v[232:235], a[180:183], a[84:87], v[232:235]\n"
"ds_read_b64_tr_b16 a[126:127], v7 offset:25600\n"
"s_add_u32 s75, s71, 0xd600\n"
"v_mfma_f32_16x16x32_bf16 v[236:239], a[188:191], a[84:87], v[236:239]\n"
"buffer_load_dwordx4 v9, s[36:39], s75 offen offset:3072 lds\n"
"v_mfma_f32_16x16x32_bf16 v[236:239], a[184:187], a[80:83], v[236:239]\n"
"v_mfma_f32_16x16x32_bf16 v[252:255], a[184:187], a[88:91], v[252:255]\n"
"v_add_u32 v2, 0xfffe8000, v2\n"
"v_mfma_f32_16x16x32_bf16 v[252:255], a[188:191], a[92:95], v[252:255]\n"
"v_add_u32 v3, 0xfffe8000, v3\n"
"v_mfma_f32_16x16x32_bf16 v[248:251], a[180:183], a[92:95], v[248:251]\n"
"v_add_u32 v4, 0x10000, v4\n"
"v_mfma_f32_16x16x32_bf16 v[248:251], a[176:179], a[88:91], v[248:251]\n"
"v_add_u32 v5, 0x10000, v5\n"
"v_mfma_f32_16x16x32_bf16 v[244:247], a[168:171], a[88:91], v[244:247]\n"
"v_add_u32 v6, 0x10000, v6\n"
"v_mfma_f32_16x16x32_bf16 v[244:247], a[172:175], a[92:95], v[244:247]\n"
"v_add_u32 v7, 0x10000, v7\n"
"v_mfma_f32_16x16x32_bf16 v[240:243], a[164:167], a[92:95], v[240:243]\n"
"v_mfma_f32_16x16x32_bf16 v[240:243], a[160:163], a[88:91], v[240:243]\n"
"s_waitcnt vmcnt(12) lgkmcnt(0)\n"
"s_barrier\n"
"v_mfma_f32_16x16x32_bf16 a[0:3], a[64:67], a[96:99], a[0:3]\n"
"v_mfma_f32_16x16x32_bf16 a[0:3], a[68:71], a[100:103], a[0:3]\n"
"ds_read_b128 a[160:163], v2 offset:0\n"
"s_add_u32 m0, s70, 0x20000\n"
"s_add_u32 s95, s72, 0x200000\n"
"v_mfma_f32_16x16x32_bf16 a[4:7], a[76:79], a[100:103], a[4:7]\n"
"buffer_load_dwordx4 v10, s[40:43], s95 offen offset:0 lds\n"
"v_mfma_f32_16x16x32_bf16 a[4:7], a[72:75], a[96:99], a[4:7]\n"
"ds_read_b128 a[164:167], v3 offset:0\n"
"v_mfma_f32_16x16x32_bf16 a[32:35], a[80:83], a[96:99], a[32:35]\n"
"v_mfma_f32_16x16x32_bf16 a[32:35], a[84:87], a[100:103], a[32:35]\n"
"v_mfma_f32_16x16x32_bf16 a[36:39], a[92:95], a[100:103], a[36:39]\n"
"ds_read_b128 a[168:171], v2 offset:2048\n"
"s_add_u32 s95, s72, 0x207c00\n"
"v_mfma_f32_16x16x32_bf16 a[36:39], a[88:91], a[96:99], a[36:39]\n"
"buffer_load_dwordx4 v10, s[40:43], s95 offen offset:1024 lds\n"
"v_mfma_f32_16x16x32_bf16 a[44:47], a[88:91], a[104:107], a[44:47]\n"
"v_mfma_f32_16x16x32_bf16 a[44:47], a[92:95], a[108:111], a[44:47]\n"
"ds_read_b128 a[172:175], v3 offset:2048\n"
"s_add_u32 s95, s72, 0x20f800\n"
"v_mfma_f32_16x16x32_bf16 a[40:43], a[84:87], a[108:111], a[40:43]\n"
"buffer_load_dwordx4 v11, s[40:43], s95 offen offset:2048 lds\n"
"v_mfma_f32_16x16x32_bf16 a[40:43], a[80:83], a[104:107], a[40:43]\n"
"v_mfma_f32_16x16x32_bf16 a[12:15], a[72:75], a[104:107], a[12:15]\n"
"ds_read_b128 a[176:179], v2 offset:4096\n"
"v_mfma_f32_16x16x32_bf16 a[12:15], a[76:79], a[108:111], a[12:15]\n"
"s_add_u32 s95, s72, 0x217400\n"
"v_mfma_f32_16x16x32_bf16 a[8:11], a[68:71], a[108:111], a[8:11]\n"
"buffer_load_dwordx4 v11, s[40:43], s95 offen offset:3072 lds\n"
"v_mfma_f32_16x16x32_bf16 a[8:11], a[64:67], a[104:107], a[8:11]\n"
"ds_read_b128 a[180:183], v3 offset:4096\n"
"v_mfma_f32_16x16x32_bf16 a[16:19], a[64:67], a[112:115], a[16:19]\n"
"v_mfma_f32_16x16x32_bf16 a[16:19], a[68:71], a[116:119], a[16:19]\n"
"v_mfma_f32_16x16x32_bf16 a[20:23], a[76:79], a[116:119], a[20:23]\n"
"ds_read_b128 a[184:187], v2 offset:6144\n"
"s_add_u32 m0, s70, 0x24000\n"
"s_add_u32 s95, s72, 0x200000\n"
"v_mfma_f32_16x16x32_bf16 a[20:23], a[72:75], a[112:115], a[20:23]\n"
"buffer_load_dwordx4 v10, s[44:47], s95 offen offset:0 lds\n"
"v_mfma_f32_16x16x32_bf16 a[48:51], a[80:83], a[112:115], a[48:51]\n"
"v_mfma_f32_16x16x32_bf16 a[48:51], a[84:87], a[116:119], a[48:51]\n"
"ds_read_b128 a[188:191], v3 offset:6144\n"
"v_mfma_f32_16x16x32_bf16 a[52:55], a[92:95], a[116:119], a[52:55]\n"
"s_add_u32 s95, s72, 0x207c00\n"
"v_mfma_f32_16x16x32_bf16 a[52:55], a[88:91], a[112:115], a[52:55]\n"
"buffer_load_dwordx4 v10, s[44:47], s95 offen offset:1024 lds\n"
"v_mfma_f32_16x16x32_bf16 a[60:63], a[88:91], a[120:123], a[60:63]\n"
"v_mfma_f32_16x16x32_bf16 a[60:63], a[92:95], a[124:127], a[60:63]\n"
"v_mfma_f32_16x16x32_bf16 a[56:59], a[84:87], a[124:127], a[56:59]\n"
"s_add_u32 s95, s72, 0x20f800\n"
"v_mfma_f32_16x16x32_bf16 a[56:59], a[80:83], a[120:123], a[56:59]\n"
"buffer_load_dwordx4 v11, s[44:47], s95 offen offset:2048 lds\n"
"v_mfma_f32_16x16x32_bf16 a[28:31], a[72:75], a[120:123], a[28:31]\n"
"v_mfma_f32_16x16x32_bf16 a[28:31], a[76:79], a[124:127], a[28:31]\n"
"v_mfma_f32_16x16x32_bf16 a[24:27], a[68:71], a[124:127], a[24:27]\n"
"s_add_u32 s95, s72, 0x217400\n"
"v_mfma_f32_16x16x32_bf16 a[24:27], a[64:67], a[120:123], a[24:27]\n"
"buffer_load_dwordx4 v11, s[44:47], s95 offen offset:3072 lds\n"
"s_waitcnt lgkmcnt(0)\n"
"v_mfma_f32_16x16x32_bf16 v[128:131], a[128:131], a[96:99], v[128:131]\n"
"v_mfma_f32_16x16x32_bf16 v[128:131], a[132:135], a[100:103], v[128:131]\n"
"ds_read_b64_tr_b16 a[64:65], v4 offset:0\n"
"v_mfma_f32_16x16x32_bf16 v[132:135], a[140:143], a[100:103], v[132:135]\n"
"ds_read_b64_tr_b16 a[66:67], v4 offset:1024\n"
"v_mfma_f32_16x16x32_bf16 v[132:135], a[136:139], a[96:99], v[132:135]\n"
"ds_read_b64_tr_b16 a[68:69], v4 offset:8192\n"
"s_add_u32 s34, s34, 0x2000\n"
"v_mfma_f32_16x16x32_bf16 v[160:163], a[144:147], a[96:99], v[160:163]\n"
"buffer_load_dwordx4 a[228:231], v23, s[4:7], s34 offen\n"
"v_mfma_f32_16x16x32_bf16 v[160:163], a[148:151], a[100:103], v[160:163]\n"
"ds_read_b64_tr_b16 a[70:71], v4 offset:9216\n"
"v_mfma_f32_16x16x32_bf16 v[164:167], a[156:159], a[100:103], v[164:167]\n"
"ds_read_b64_tr_b16 a[72:73], v5 offset:0\n"
"v_mfma_f32_16x16x32_bf16 v[164:167], a[152:155], a[96:99], v[164:167]\n"
"s_mov_b32 s56, s26\n"
"s_and_b32 s57, s27, 0xffff\n"
"v_mfma_f32_16x16x32_bf16 v[172:175], a[152:155], a[104:107], v[172:175]\n"
"ds_read_b64_tr_b16 a[74:75], v5 offset:1024\n"
"s_mov_b32 s58, 0x1c0000\n"
"s_lshl_b32 s86, s92, 13\n"
"v_mfma_f32_16x16x32_bf16 v[172:175], a[156:159], a[108:111], v[172:175]\n"
"ds_read_b64_tr_b16 a[76:77], v5 offset:8192\n"
"s_add_u32 s87, s86, 0x1000\n"
"v_mfma_f32_16x16x32_bf16 v[168:171], a[148:151], a[108:111], v[168:171]\n"
"buffer_load_dword a208, v29, s[56:59], s86 offen offset:0\n"
"v_mfma_f32_16x16x32_bf16 v[168:171], a[144:147], a[104:107], v[168:171]\n"
"ds_read_b64_tr_b16 a[78:79], v5 offset:9216\n"
"s_waitcnt vmcnt(19)\n"
"v_mfma_f32_16x16x32_bf16 v[140:143], a[136:139], a[104:107], v[140:143]\n"
"ds_read_b64_tr_b16 a[80:81], v6 offset:0\n"
"v_mfma_f32_16x16x32_bf16 v[140:143], a[140:143], a[108:111], v[140:143]\n"
"ds_read_b64_tr_b16 a[82:83], v6 offset:1024\n"
"v_mfma_f32_16x16x32_bf16 v[136:139], a[132:135], a[108:111], v[136:139]\n"
"buffer_load_dword a209, v29, s[56:59], s86 offen offset:1024\n"
"v_mfma_f32_16x16x32_bf16 v[136:139], a[128:131], a[104:107], v[136:139]\n"
"ds_read_b64_tr_b16 a[84:85], v6 offset:8192\n"
"v_mfma_f32_16x16x32_bf16 v[144:147], a[128:131], a[112:115], v[144:147]\n"
"ds_read_b64_tr_b16 a[86:87], v6 offset:9216\n"
"v_mfma_f32_16x16x32_bf16 v[144:147], a[132:135], a[116:119], v[144:147]\n"
"buffer_load_dword a210, v29, s[56:59], s86 offen offset:2048\n"
"v_mfma_f32_16x16x32_bf16 v[148:151], a[140:143], a[116:119], v[148:151]\n"
"ds_read_b64_tr_b16 a[88:89], v7 offset:0\n"
"v_mfma_f32_16x16x32_bf16 v[148:151], a[136:139], a[112:115], v[148:151]\n"
"ds_read_b64_tr_b16 a[90:91], v7 offset:1024\n"
"v_mfma_f32_16x16x32_bf16 v[176:179], a[144:147], a[112:115], v[176:179]\n"
"buffer_load_dword a211, v29, s[56:59], s86 offen offset:3072\n"
"v_mfma_f32_16x16x32_bf16 v[176:179], a[148:151], a[116:119], v[176:179]\n"
"ds_read_b64_tr_b16 a[92:93], v7 offset:8192\n"
"v_mfma_f32_16x16x32_bf16 v[180:183], a[156:159], a[116:119], v[180:183]\n"
"ds_read_b64_tr_b16 a[94:95], v7 offset:9216\n"
"v_mfma_f32_16x16x32_bf16 v[180:183], a[152:155], a[112:115], v[180:183]\n"
"buffer_load_dword a212, v29, s[56:59], s87 offen offset:0\n"
"v_mfma_f32_16x16x32_bf16 v[188:191], a[152:155], a[120:123], v[188:191]\n"
"buffer_load_dword a213, v29, s[56:59], s87 offen offset:1024\n"
"v_mfma_f32_16x16x32_bf16 v[188:191], a[156:159], a[124:127], v[188:191]\n"
"buffer_load_dword a214, v29, s[56:59], s87 offen offset:2048\n"
"v_mfma_f32_16x16x32_bf16 v[184:187], a[148:151], a[124:127], v[184:187]\n"
"buffer_load_dword a215, v29, s[56:59], s87 offen offset:3072\n"
"v_mfma_f32_16x16x32_bf16 v[184:187], a[144:147], a[120:123], v[184:187]\n"
"buffer_store_dwordx2 v[32:33], v24, s[48:51], s73 offen sc1\n"
"v_mfma_f32_16x16x32_bf16 v[156:159], a[136:139], a[120:123], v[156:159]\n"
"s_mov_b32 s73, 0x1000\n"
"s_mov_b32 s74, 0x2000\n"
"v_mfma_f32_16x16x32_bf16 v[156:159], a[140:143], a[124:127], v[156:159]\n"
"v_mfma_f32_16x16x32_bf16 v[152:155], a[132:135], a[124:127], v[152:155]\n"
"v_mfma_f32_16x16x32_bf16 v[152:155], a[128:131], a[120:123], v[152:155]\n"
"s_waitcnt lgkmcnt(0)\n"
"v_mfma_f32_16x16x32_bf16 v[64:67], a[64:67], a[160:163], v[64:67]\n"
"buffer_load_dwordx4 a[200:203], v28, s[60:63], s88 offen offset:0\n"
"v_mfma_f32_16x16x32_bf16 v[64:67], a[68:71], a[164:167], v[64:67]\n"
"ds_read_b128 a[96:99], v2 offset:16384\n"
"v_mfma_f32_16x16x32_bf16 v[68:71], a[76:79], a[164:167], v[68:71]\n"
"v_mfma_f32_16x16x32_bf16 v[68:71], a[72:75], a[160:163], v[68:71]\n"
"ds_read_b128 a[100:103], v3 offset:16384\n"
"v_mfma_f32_16x16x32_bf16 v[72:75], a[80:83], a[160:163], v[72:75]\n"
"v_mfma_f32_16x16x32_bf16 v[72:75], a[84:87], a[164:167], v[72:75]\n"
"v_mfma_f32_16x16x32_bf16 v[76:79], a[92:95], a[164:167], v[76:79]\n"
"ds_read_b128 a[104:107], v2 offset:18432\n"
"v_mfma_f32_16x16x32_bf16 v[76:79], a[88:91], a[160:163], v[76:79]\n"
"v_mfma_f32_16x16x32_bf16 v[92:95], a[88:91], a[168:171], v[92:95]\n"
"v_mfma_f32_16x16x32_bf16 v[92:95], a[92:95], a[172:175], v[92:95]\n"
"ds_read_b128 a[108:111], v3 offset:18432\n"
"v_mfma_f32_16x16x32_bf16 v[88:91], a[84:87], a[172:175], v[88:91]\n"
"v_mfma_f32_16x16x32_bf16 v[88:91], a[80:83], a[168:171], v[88:91]\n"
"v_mfma_f32_16x16x32_bf16 v[84:87], a[72:75], a[168:171], v[84:87]\n"
"ds_read_b128 a[112:115], v2 offset:20480\n"
"v_mfma_f32_16x16x32_bf16 v[84:87], a[76:79], a[172:175], v[84:87]\n"
"v_mfma_f32_16x16x32_bf16 v[80:83], a[68:71], a[172:175], v[80:83]\n"
"v_mfma_f32_16x16x32_bf16 v[80:83], a[64:67], a[168:171], v[80:83]\n"
"ds_read_b128 a[116:119], v3 offset:20480\n"
"v_mfma_f32_16x16x32_bf16 v[96:99], a[64:67], a[176:179], v[96:99]\n"
"v_mfma_f32_16x16x32_bf16 v[96:99], a[68:71], a[180:183], v[96:99]\n"
"v_mfma_f32_16x16x32_bf16 v[100:103], a[76:79], a[180:183], v[100:103]\n"
"ds_read_b128 a[120:123], v2 offset:22528\n"
"s_add_u32 m0, s67, 0x0\n"
"s_add_u32 s75, s71, 0x280\n"
"v_mfma_f32_16x16x32_bf16 v[100:103], a[72:75], a[176:179], v[100:103]\n"
"buffer_load_dwordx4 v8, s[36:39], s75 offen offset:0 lds\n"
"v_mfma_f32_16x16x32_bf16 v[104:107], a[80:83], a[176:179], v[104:107]\n"
"v_mfma_f32_16x16x32_bf16 v[104:107], a[84:87], a[180:183], v[104:107]\n"
"ds_read_b128 a[124:127], v3 offset:22528\n"
"v_mfma_f32_16x16x32_bf16 v[108:111], a[92:95], a[180:183], v[108:111]\n"
"s_add_u32 s75, s71, 0x1e80\n"
"v_mfma_f32_16x16x32_bf16 v[108:111], a[88:91], a[176:179], v[108:111]\n"
"buffer_load_dwordx4 v9, s[36:39], s75 offen offset:1024 lds\n"
"v_mfma_f32_16x16x32_bf16 v[124:127], a[88:91], a[184:187], v[124:127]\n"
"v_mfma_f32_16x16x32_bf16 v[124:127], a[92:95], a[188:191], v[124:127]\n"
"v_mfma_f32_16x16x32_bf16 v[120:123], a[84:87], a[188:191], v[120:123]\n"
"v_mfma_f32_16x16x32_bf16 v[120:123], a[80:83], a[184:187], v[120:123]\n"
"s_add_u32 s75, s71, 0x3a80\n"
"v_mfma_f32_16x16x32_bf16 v[116:119], a[72:75], a[184:187], v[116:119]\n"
"buffer_load_dwordx4 v8, s[36:39], s75 offen offset:2048 lds\n"
"v_mfma_f32_16x16x32_bf16 v[116:119], a[76:79], a[188:191], v[116:119]\n"
"v_mfma_f32_16x16x32_bf16 v[112:115], a[68:71], a[188:191], v[112:115]\n"
"v_mfma_f32_16x16x32_bf16 v[112:115], a[64:67], a[184:187], v[112:115]\n"
"s_waitcnt lgkmcnt(0)\n"
"v_mfma_f32_16x16x32_bf16 v[192:195], a[64:67], a[96:99], v[192:195]\n"
"ds_read_b64_tr_b16 a[128:129], v4 offset:16384\n"
"v_mfma_f32_16x16x32_bf16 v[192:195], a[68:71], a[100:103], v[192:195]\n"
"ds_read_b64_tr_b16 a[130:131], v4 offset:17408\n"
"v_mfma_f32_16x16x32_bf16 v[196:199], a[76:79], a[100:103], v[196:199]\n"
"ds_read_b64_tr_b16 a[132:133], v4 offset:24576\n"
"s_add_u32 s75, s71, 0x5680\n"
"v_mfma_f32_16x16x32_bf16 v[196:199], a[72:75], a[96:99], v[196:199]\n"
"buffer_load_dwordx4 v9, s[36:39], s75 offen offset:3072 lds\n"
"v_mfma_f32_16x16x32_bf16 v[200:203], a[80:83], a[96:99], v[200:203]\n"
"ds_read_b64_tr_b16 a[134:135], v4 offset:25600\n"
"v_mfma_f32_16x16x32_bf16 v[200:203], a[84:87], a[100:103], v[200:203]\n"
"ds_read_b64_tr_b16 a[136:137], v5 offset:16384\n"
"s_add_u32 m0, s67, 0x1000\n"
"s_add_u32 s75, s71, 0x8280\n"
"v_mfma_f32_16x16x32_bf16 v[204:207], a[92:95], a[100:103], v[204:207]\n"
"buffer_load_dwordx4 v8, s[36:39], s75 offen offset:0 lds\n"
"v_mfma_f32_16x16x32_bf16 v[204:207], a[88:91], a[96:99], v[204:207]\n"
"ds_read_b64_tr_b16 a[138:139], v5 offset:17408\n"
"v_mfma_f32_16x16x32_bf16 v[220:223], a[88:91], a[104:107], v[220:223]\n"
"ds_read_b64_tr_b16 a[140:141], v5 offset:24576\n"
"v_mfma_f32_16x16x32_bf16 v[220:223], a[92:95], a[108:111], v[220:223]\n"
"v_mfma_f32_16x16x32_bf16 v[216:219], a[84:87], a[108:111], v[216:219]\n"
"ds_read_b64_tr_b16 a[142:143], v5 offset:25600\n"
"v_mfma_f32_16x16x32_bf16 v[216:219], a[80:83], a[104:107], v[216:219]\n"
"ds_read_b64_tr_b16 a[144:145], v6 offset:16384\n"
"v_mfma_f32_16x16x32_bf16 v[212:215], a[72:75], a[104:107], v[212:215]\n"
"ds_read_b64_tr_b16 a[146:147], v6 offset:17408\n"
"s_add_u32 s75, s71, 0x9e80\n"
"v_mfma_f32_16x16x32_bf16 v[212:215], a[76:79], a[108:111], v[212:215]\n"
"buffer_load_dwordx4 v9, s[36:39], s75 offen offset:1024 lds\n"
"v_mfma_f32_16x16x32_bf16 v[208:211], a[68:71], a[108:111], v[208:211]\n"
"ds_read_b64_tr_b16 a[148:149], v6 offset:24576\n"
"v_mfma_f32_16x16x32_bf16 v[208:211], a[64:67], a[104:107], v[208:211]\n"
"ds_read_b64_tr_b16 a[150:151], v6 offset:25600\n"
"s_add_u32 s75, s71, 0xba80\n"
"v_mfma_f32_16x16x32_bf16 v[224:227], a[64:67], a[112:115], v[224:227]\n"
"buffer_load_dwordx4 v8, s[36:39], s75 offen offset:2048 lds\n"
"v_mfma_f32_16x16x32_bf16 v[224:227], a[68:71], a[116:119], v[224:227]\n"
"ds_read_b64_tr_b16 a[152:153], v7 offset:16384\n"
"v_mfma_f32_16x16x32_bf16 v[228:231], a[76:79], a[116:119], v[228:231]\n"
"ds_read_b64_tr_b16 a[154:155], v7 offset:17408\n"
"v_mfma_f32_16x16x32_bf16 v[228:231], a[72:75], a[112:115], v[228:231]\n"
"v_mfma_f32_16x16x32_bf16 v[232:235], a[80:83], a[112:115], v[232:235]\n"
"ds_read_b64_tr_b16 a[156:157], v7 offset:24576\n"
"v_mfma_f32_16x16x32_bf16 v[232:235], a[84:87], a[116:119], v[232:235]\n"
"ds_read_b64_tr_b16 a[158:159], v7 offset:25600\n"
"s_add_u32 s75, s71, 0xd680\n"
"v_mfma_f32_16x16x32_bf16 v[236:239], a[92:95], a[116:119], v[236:239]\n"
"buffer_load_dwordx4 v9, s[36:39], s75 offen offset:3072 lds\n"
"v_mfma_f32_16x16x32_bf16 v[236:239], a[88:91], a[112:115], v[236:239]\n"
"v_mfma_f32_16x16x32_bf16 v[252:255], a[88:91], a[120:123], v[252:255]\n"
"v_add_u32 v2, 0x10000, v2\n"
"v_mfma_f32_16x16x32_bf16 v[252:255], a[92:95], a[124:127], v[252:255]\n"
"v_add_u32 v3, 0x10000, v3\n"
"v_mfma_f32_16x16x32_bf16 v[248:251], a[84:87], a[124:127], v[248:251]\n"
"v_add_u32 v4, 0x10000, v4\n"
"v_mfma_f32_16x16x32_bf16 v[248:251], a[80:83], a[120:123], v[248:251]\n"
"v_add_u32 v5, 0x10000, v5\n"
"v_mfma_f32_16x16x32_bf16 v[244:247], a[72:75], a[120:123], v[244:247]\n"
"v_add_u32 v6, 0x10000, v6\n"
"v_mfma_f32_16x16x32_bf16 v[244:247], a[76:79], a[124:127], v[244:247]\n"
"v_add_u32 v7, 0x10000, v7\n"
"v_mfma_f32_16x16x32_bf16 v[240:243], a[68:71], a[124:127], v[240:243]\n"
"v_mfma_f32_16x16x32_bf16 v[240:243], a[64:67], a[120:123], v[240:243]\n"
"s_waitcnt vmcnt(19) lgkmcnt(0)\n"
"s_barrier\n"
"v_mfma_f32_16x16x32_bf16 a[0:3], a[96:99], a[128:131], a[0:3]\n"
"v_mfma_f32_16x16x32_bf16 a[0:3], a[100:103], a[132:135], a[0:3]\n"
"ds_read_b128 a[64:67], v2 offset:0\n"
"s_add_u32 m0, s70, 0x8000\n"
"s_add_u32 s95, s72, 0x280000\n"
"v_mfma_f32_16x16x32_bf16 a[4:7], a[108:111], a[132:135], a[4:7]\n"
"buffer_load_dwordx4 v10, s[40:43], s95 offen offset:0 lds\n"
"v_mfma_f32_16x16x32_bf16 a[4:7], a[104:107], a[128:131], a[4:7]\n"
"ds_read_b128 a[68:71], v3 offset:0\n"
"v_mfma_f32_16x16x32_bf16 a[32:35], a[112:115], a[128:131], a[32:35]\n"
"v_mfma_f32_16x16x32_bf16 a[32:35], a[116:119], a[132:135], a[32:35]\n"
"v_mfma_f32_16x16x32_bf16 a[36:39], a[124:127], a[132:135], a[36:39]\n"
"ds_read_b128 a[72:75], v2 offset:2048\n"
"s_add_u32 s95, s72, 0x287c00\n"
"v_mfma_f32_16x16x32_bf16 a[36:39], a[120:123], a[128:131], a[36:39]\n"
"buffer_load_dwordx4 v10, s[40:43], s95 offen offset:1024 lds\n"
"v_mfma_f32_16x16x32_bf16 a[44:47], a[120:123], a[136:139], a[44:47]\n"
"v_mfma_f32_16x16x32_bf16 a[44:47], a[124:127], a[140:143], a[44:47]\n"
"ds_read_b128 a[76:79], v3 offset:2048\n"
"s_add_u32 s95, s72, 0x28f800\n"
"v_mfma_f32_16x16x32_bf16 a[40:43], a[116:119], a[140:143], a[40:43]\n"
"buffer_load_dwordx4 v11, s[40:43], s95 offen offset:2048 lds\n"
"v_mfma_f32_16x16x32_bf16 a[40:43], a[112:115], a[136:139], a[40:43]\n"
"v_mfma_f32_16x16x32_bf16 a[12:15], a[104:107], a[136:139], a[12:15]\n"
"ds_read_b128 a[80:83], v2 offset:4096\n"
"v_mfma_f32_16x16x32_bf16 a[12:15], a[108:111], a[140:143], a[12:15]\n"
"s_add_u32 s95, s72, 0x297400\n"
"v_mfma_f32_16x16x32_bf16 a[8:11], a[100:103], a[140:143], a[8:11]\n"
"buffer_load_dwordx4 v11, s[40:43], s95 offen offset:3072 lds\n"
"v_mfma_f32_16x16x32_bf16 a[8:11], a[96:99], a[136:139], a[8:11]\n"
"ds_read_b128 a[84:87], v3 offset:4096\n"
"v_mfma_f32_16x16x32_bf16 a[16:19], a[96:99], a[144:147], a[16:19]\n"
"v_mfma_f32_16x16x32_bf16 a[16:19], a[100:103], a[148:151], a[16:19]\n"
"v_mfma_f32_16x16x32_bf16 a[20:23], a[108:111], a[148:151], a[20:23]\n"
"ds_read_b128 a[88:91], v2 offset:6144\n"
"s_add_u32 m0, s70, 0xc000\n"
"s_add_u32 s95, s72, 0x280000\n"
"v_mfma_f32_16x16x32_bf16 a[20:23], a[104:107], a[144:147], a[20:23]\n"
"buffer_load_dwordx4 v10, s[44:47], s95 offen offset:0 lds\n"
"v_mfma_f32_16x16x32_bf16 a[48:51], a[112:115], a[144:147], a[48:51]\n"
"v_mfma_f32_16x16x32_bf16 a[48:51], a[116:119], a[148:151], a[48:51]\n"
"ds_read_b128 a[92:95], v3 offset:6144\n"
"v_mfma_f32_16x16x32_bf16 a[52:55], a[124:127], a[148:151], a[52:55]\n"
"s_add_u32 s95, s72, 0x287c00\n"
"v_mfma_f32_16x16x32_bf16 a[52:55], a[120:123], a[144:147], a[52:55]\n"
"buffer_load_dwordx4 v10, s[44:47], s95 offen offset:1024 lds\n"
"v_mfma_f32_16x16x32_bf16 a[60:63], a[120:123], a[152:155], a[60:63]\n"
"v_mfma_f32_16x16x32_bf16 a[60:63], a[124:127], a[156:159], a[60:63]\n"
"v_mfma_f32_16x16x32_bf16 a[56:59], a[116:119], a[156:159], a[56:59]\n"
"s_add_u32 s95, s72, 0x28f800\n"
"v_mfma_f32_16x16x32_bf16 a[56:59], a[112:115], a[152:155], a[56:59]\n"
"buffer_load_dwordx4 v11, s[44:47], s95 offen offset:2048 lds\n"
"v_mfma_f32_16x16x32_bf16 a[28:31], a[104:107], a[152:155], a[28:31]\n"
"v_mfma_f32_16x16x32_bf16 a[28:31], a[108:111], a[156:159], a[28:31]\n"
"v_mfma_f32_16x16x32_bf16 a[24:27], a[100:103], a[156:159], a[24:27]\n"
"s_add_u32 s95, s72, 0x297400\n"
"v_mfma_f32_16x16x32_bf16 a[24:27], a[96:99], a[152:155], a[24:27]\n"
"buffer_load_dwordx4 v11, s[44:47], s95 offen offset:3072 lds\n"
"s_waitcnt lgkmcnt(0)\n"
"v_mfma_f32_16x16x32_bf16 v[128:131], a[160:163], a[128:131], v[128:131]\n"
"v_mfma_f32_16x16x32_bf16 v[128:131], a[164:167], a[132:135], v[128:131]\n"
"ds_read_b64_tr_b16 a[96:97], v4 offset:0\n"
"v_mfma_f32_16x16x32_bf16 v[132:135], a[172:175], a[132:135], v[132:135]\n"
"ds_read_b64_tr_b16 a[98:99], v4 offset:1024\n"
"v_mfma_f32_16x16x32_bf16 v[132:135], a[168:171], a[128:131], v[132:135]\n"
"ds_read_b64_tr_b16 a[100:101], v4 offset:8192\n"
"v_mfma_f32_16x16x32_bf16 v[160:163], a[176:179], a[128:131], v[160:163]\n"
"buffer_load_dwordx4 a[204:207], v28, s[60:63], s88 offen offset:3584\n"
"v_mfma_f32_16x16x32_bf16 v[160:163], a[180:183], a[132:135], v[160:163]\n"
"ds_read_b64_tr_b16 a[102:103], v4 offset:9216\n"
"v_mfma_f32_16x16x32_bf16 v[164:167], a[188:191], a[132:135], v[164:167]\n"
"ds_read_b64_tr_b16 a[104:105], v5 offset:0\n"
"v_mfma_f32_16x16x32_bf16 v[164:167], a[184:187], a[128:131], v[164:167]\n"
"v_mfma_f32_16x16x32_bf16 v[172:175], a[184:187], a[136:139], v[172:175]\n"
"ds_read_b64_tr_b16 a[106:107], v5 offset:1024\n"
"v_mfma_f32_16x16x32_bf16 v[172:175], a[188:191], a[140:143], v[172:175]\n"
"ds_read_b64_tr_b16 a[108:109], v5 offset:8192\n"
"v_mfma_f32_16x16x32_bf16 v[168:171], a[180:183], a[140:143], v[168:171]\n"
"v_mfma_f32_16x16x32_bf16 v[168:171], a[176:179], a[136:139], v[168:171]\n"
"ds_read_b64_tr_b16 a[110:111], v5 offset:9216\n"
"v_mfma_f32_16x16x32_bf16 v[140:143], a[168:171], a[136:139], v[140:143]\n"
"ds_read_b64_tr_b16 a[112:113], v6 offset:0\n"
"v_mfma_f32_16x16x32_bf16 v[140:143], a[172:175], a[140:143], v[140:143]\n"
"ds_read_b64_tr_b16 a[114:115], v6 offset:1024\n"
"v_mfma_f32_16x16x32_bf16 v[136:139], a[164:167], a[140:143], v[136:139]\n"
"v_mfma_f32_16x16x32_bf16 v[136:139], a[160:163], a[136:139], v[136:139]\n"
"ds_read_b64_tr_b16 a[116:117], v6 offset:8192\n"
"v_mfma_f32_16x16x32_bf16 v[144:147], a[160:163], a[144:147], v[144:147]\n"
"ds_read_b64_tr_b16 a[118:119], v6 offset:9216\n"
"v_mfma_f32_16x16x32_bf16 v[144:147], a[164:167], a[148:151], v[144:147]\n"
"v_mfma_f32_16x16x32_bf16 v[148:151], a[172:175], a[148:151], v[148:151]\n"
"ds_read_b64_tr_b16 a[120:121], v7 offset:0\n"
"v_mfma_f32_16x16x32_bf16 v[148:151], a[168:171], a[144:147], v[148:151]\n"
"ds_read_b64_tr_b16 a[122:123], v7 offset:1024\n"
"v_mfma_f32_16x16x32_bf16 v[176:179], a[176:179], a[144:147], v[176:179]\n"
"v_mfma_f32_16x16x32_bf16 v[176:179], a[180:183], a[148:151], v[176:179]\n"
"ds_read_b64_tr_b16 a[124:125], v7 offset:8192\n"
"v_mfma_f32_16x16x32_bf16 v[180:183], a[188:191], a[148:151], v[180:183]\n"
"ds_read_b64_tr_b16 a[126:127], v7 offset:9216\n"
"v_mfma_f32_16x16x32_bf16 v[180:183], a[184:187], a[144:147], v[180:183]\n"
"v_mfma_f32_16x16x32_bf16 v[188:191], a[184:187], a[152:155], v[188:191]\n"
"v_mfma_f32_16x16x32_bf16 v[188:191], a[188:191], a[156:159], v[188:191]\n"
"v_mfma_f32_16x16x32_bf16 v[184:187], a[180:183], a[156:159], v[184:187]\n"
"v_mfma_f32_16x16x32_bf16 v[184:187], a[176:179], a[152:155], v[184:187]\n"
"v_mfma_f32_16x16x32_bf16 v[156:159], a[168:171], a[152:155], v[156:159]\n"
"v_mfma_f32_16x16x32_bf16 v[156:159], a[172:175], a[156:159], v[156:159]\n"
"v_mfma_f32_16x16x32_bf16 v[152:155], a[164:167], a[156:159], v[152:155]\n"
"v_mfma_f32_16x16x32_bf16 v[152:155], a[160:163], a[152:155], v[152:155]\n"
"s_waitcnt lgkmcnt(0)\n"
"v_mfma_f32_16x16x32_bf16 v[64:67], a[96:99], a[64:67], v[64:67]\n"
"buffer_load_dwordx4 v[32:35], v1, s[48:51], s73 offen offset:0 sc1\n"
"v_mfma_f32_16x16x32_bf16 v[64:67], a[100:103], a[68:71], v[64:67]\n"
"ds_read_b128 a[128:131], v2 offset:16384\n"
"v_mfma_f32_16x16x32_bf16 v[68:71], a[108:111], a[68:71], v[68:71]\n"
"buffer_load_dwordx4 v[36:39], v1, s[48:51], s73 offen offset:1024 sc1\n"
"v_mfma_f32_16x16x32_bf16 v[68:71], a[104:107], a[64:67], v[68:71]\n"
"ds_read_b128 a[132:135], v3 offset:16384\n"
"v_mfma_f32_16x16x32_bf16 v[72:75], a[112:115], a[64:67], v[72:75]\n"
"buffer_load_dwordx4 v[40:43], v1, s[48:51], s73 offen offset:2048 sc1\n"
"v_mfma_f32_16x16x32_bf16 v[72:75], a[116:119], a[68:71], v[72:75]\n"
"buffer_load_dwordx4 v[44:47], v1, s[48:51], s73 offen offset:3072 sc1\n"
"v_mfma_f32_16x16x32_bf16 v[76:79], a[124:127], a[68:71], v[76:79]\n"
"ds_read_b128 a[136:139], v2 offset:18432\n"
"v_mfma_f32_16x16x32_bf16 v[76:79], a[120:123], a[64:67], v[76:79]\n"
"buffer_load_dwordx4 v[48:51], v1, s[48:51], s74 offen offset:0 sc1\n"
"v_mfma_f32_16x16x32_bf16 v[92:95], a[120:123], a[72:75], v[92:95]\n"
"buffer_load_dwordx4 v[52:55], v1, s[48:51], s74 offen offset:1024 sc1\n"
"v_mfma_f32_16x16x32_bf16 v[92:95], a[124:127], a[76:79], v[92:95]\n"
"ds_read_b128 a[140:143], v3 offset:18432\n"
"v_mfma_f32_16x16x32_bf16 v[88:91], a[116:119], a[76:79], v[88:91]\n"
"buffer_load_dwordx4 v[56:59], v1, s[48:51], s74 offen offset:2048 sc1\n"
"v_mfma_f32_16x16x32_bf16 v[88:91], a[112:115], a[72:75], v[88:91]\n"
"buffer_load_dwordx4 v[60:63], v1, s[48:51], s74 offen offset:3072 sc1\n"
"v_mfma_f32_16x16x32_bf16 v[84:87], a[104:107], a[72:75], v[84:87]\n"
"ds_read_b128 a[144:147], v2 offset:20480\n"
"v_mfma_f32_16x16x32_bf16 v[84:87], a[108:111], a[76:79], v[84:87]\n"
"v_mfma_f32_16x16x32_bf16 v[80:83], a[100:103], a[76:79], v[80:83]\n"
"v_mfma_f32_16x16x32_bf16 v[80:83], a[96:99], a[72:75], v[80:83]\n"
"ds_read_b128 a[148:151], v3 offset:20480\n"
"v_mfma_f32_16x16x32_bf16 v[96:99], a[96:99], a[80:83], v[96:99]\n"
"v_mfma_f32_16x16x32_bf16 v[96:99], a[100:103], a[84:87], v[96:99]\n"
"v_mfma_f32_16x16x32_bf16 v[100:103], a[108:111], a[84:87], v[100:103]\n"
"ds_read_b128 a[152:155], v2 offset:22528\n"
"s_add_u32 m0, s67, 0x10000\n"
"s_add_u32 s75, s71, 0x300\n"
"v_mfma_f32_16x16x32_bf16 v[100:103], a[104:107], a[80:83], v[100:103]\n"
"buffer_load_dwordx4 v8, s[36:39], s75 offen offset:0 lds\n"
"v_mfma_f32_16x16x32_bf16 v[104:107], a[112:115], a[80:83], v[104:107]\n"
"v_mfma_f32_16x16x32_bf16 v[104:107], a[116:119], a[84:87], v[104:107]\n"
"ds_read_b128 a[156:159], v3 offset:22528\n"
"v_mfma_f32_16x16x32_bf16 v[108:111], a[124:127], a[84:87], v[108:111]\n"
"s_add_u32 s75, s71, 0x1f00\n"
"v_mfma_f32_16x16x32_bf16 v[108:111], a[120:123], a[80:83], v[108:111]\n"
"buffer_load_dwordx4 v9, s[36:39], s75 offen offset:1024 lds\n"
"v_mfma_f32_16x16x32_bf16 v[124:127], a[120:123], a[88:91], v[124:127]\n"
"v_mfma_f32_16x16x32_bf16 v[124:127], a[124:127], a[92:95], v[124:127]\n"
"v_mfma_f32_16x16x32_bf16 v[120:123], a[116:119], a[92:95], v[120:123]\n"
"v_mfma_f32_16x16x32_bf16 v[120:123], a[112:115], a[88:91], v[120:123]\n"
"s_add_u32 s75, s71, 0x3b00\n"
"v_mfma_f32_16x16x32_bf16 v[116:119], a[104:107], a[88:91], v[116:119]\n"
"buffer_load_dwordx4 v8, s[36:39], s75 offen offset:2048 lds\n"
"v_mfma_f32_16x16x32_bf16 v[116:119], a[108:111], a[92:95], v[116:119]\n"
"v_mfma_f32_16x16x32_bf16 v[112:115], a[100:103], a[92:95], v[112:115]\n"
"v_mfma_f32_16x16x32_bf16 v[112:115], a[96:99], a[88:91], v[112:115]\n"
"s_waitcnt lgkmcnt(0)\n"
"v_mfma_f32_16x16x32_bf16 v[192:195], a[96:99], a[128:131], v[192:195]\n"
"ds_read_b64_tr_b16 a[160:161], v4 offset:16384\n"
"v_mfma_f32_16x16x32_bf16 v[192:195], a[100:103], a[132:135], v[192:195]\n"
"ds_read_b64_tr_b16 a[162:163], v4 offset:17408\n"
"v_mfma_f32_16x16x32_bf16 v[196:199], a[108:111], a[132:135], v[196:199]\n"
"ds_read_b64_tr_b16 a[164:165], v4 offset:24576\n"
"s_add_u32 s75, s71, 0x5700\n"
"v_mfma_f32_16x16x32_bf16 v[196:199], a[104:107], a[128:131], v[196:199]\n"
"buffer_load_dwordx4 v9, s[36:39], s75 offen offset:3072 lds\n"
"v_mfma_f32_16x16x32_bf16 v[200:203], a[112:115], a[128:131], v[200:203]\n"
"ds_read_b64_tr_b16 a[166:167], v4 offset:25600\n"
"v_mfma_f32_16x16x32_bf16 v[200:203], a[116:119], a[132:135], v[200:203]\n"
"ds_read_b64_tr_b16 a[168:169], v5 offset:16384\n"
"s_add_u32 m0, s67, 0x11000\n"
"s_add_u32 s75, s71, 0x8300\n"
"v_mfma_f32_16x16x32_bf16 v[204:207], a[124:127], a[132:135], v[204:207]\n"
"buffer_load_dwordx4 v8, s[36:39], s75 offen offset:0 lds\n"
"v_mfma_f32_16x16x32_bf16 v[204:207], a[120:123], a[128:131], v[204:207]\n"
"ds_read_b64_tr_b16 a[170:171], v5 offset:17408\n"
"v_mfma_f32_16x16x32_bf16 v[220:223], a[120:123], a[136:139], v[220:223]\n"
"ds_read_b64_tr_b16 a[172:173], v5 offset:24576\n"
"v_mfma_f32_16x16x32_bf16 v[220:223], a[124:127], a[140:143], v[220:223]\n"
"v_mfma_f32_16x16x32_bf16 v[216:219], a[116:119], a[140:143], v[216:219]\n"
"ds_read_b64_tr_b16 a[174:175], v5 offset:25600\n"
"v_mfma_f32_16x16x32_bf16 v[216:219], a[112:115], a[136:139], v[216:219]\n"
"ds_read_b64_tr_b16 a[176:177], v6 offset:16384\n"
"v_mfma_f32_16x16x32_bf16 v[212:215], a[104:107], a[136:139], v[212:215]\n"
"ds_read_b64_tr_b16 a[178:179], v6 offset:17408\n"
"s_add_u32 s75, s71, 0x9f00\n"
"v_mfma_f32_16x16x32_bf16 v[212:215], a[108:111], a[140:143], v[212:215]\n"
"buffer_load_dwordx4 v9, s[36:39], s75 offen offset:1024 lds\n"
"v_mfma_f32_16x16x32_bf16 v[208:211], a[100:103], a[140:143], v[208:211]\n"
"ds_read_b64_tr_b16 a[180:181], v6 offset:24576\n"
"v_mfma_f32_16x16x32_bf16 v[208:211], a[96:99], a[136:139], v[208:211]\n"
"ds_read_b64_tr_b16 a[182:183], v6 offset:25600\n"
"s_add_u32 s75, s71, 0xbb00\n"
"v_mfma_f32_16x16x32_bf16 v[224:227], a[96:99], a[144:147], v[224:227]\n"
"buffer_load_dwordx4 v8, s[36:39], s75 offen offset:2048 lds\n"
"v_mfma_f32_16x16x32_bf16 v[224:227], a[100:103], a[148:151], v[224:227]\n"
"ds_read_b64_tr_b16 a[184:185], v7 offset:16384\n"
"v_mfma_f32_16x16x32_bf16 v[228:231], a[108:111], a[148:151], v[228:231]\n"
"ds_read_b64_tr_b16 a[186:187], v7 offset:17408\n"
"v_mfma_f32_16x16x32_bf16 v[228:231], a[104:107], a[144:147], v[228:231]\n"
"v_mfma_f32_16x16x32_bf16 v[232:235], a[112:115], a[144:147], v[232:235]\n"
"ds_read_b64_tr_b16 a[188:189], v7 offset:24576\n"
"v_mfma_f32_16x16x32_bf16 v[232:235], a[116:119], a[148:151], v[232:235]\n"
"ds_read_b64_tr_b16 a[190:191], v7 offset:25600\n"
"s_add_u32 s75, s71, 0xd700\n"
"v_mfma_f32_16x16x32_bf16 v[236:239], a[124:127], a[148:151], v[236:239]\n"
"buffer_load_dwordx4 v9, s[36:39], s75 offen offset:3072 lds\n"
"v_mfma_f32_16x16x32_bf16 v[236:239], a[120:123], a[144:147], v[236:239]\n"
"v_mfma_f32_16x16x32_bf16 v[252:255], a[120:123], a[152:155], v[252:255]\n"
"v_add_u32 v2, 0xfffe8000, v2\n"
"v_mfma_f32_16x16x32_bf16 v[252:255], a[124:127], a[156:159], v[252:255]\n"
"v_add_u32 v3, 0xfffe8000, v3\n"
"v_mfma_f32_16x16x32_bf16 v[248:251], a[116:119], a[156:159], v[248:251]\n"
"v_add_u32 v4, 0xfffe8000, v4\n"
"v_mfma_f32_16x16x32_bf16 v[248:251], a[112:115], a[152:155], v[248:251]\n"
"v_add_u32 v5, 0xfffe8000, v5\n"
"v_mfma_f32_16x16x32_bf16 v[244:247], a[104:107], a[152:155], v[244:247]\n"
"v_add_u32 v6, 0xfffe8000, v6\n"
"v_mfma_f32_16x16x32_bf16 v[244:247], a[108:111], a[156:159], v[244:247]\n"
"v_add_u32 v7, 0xfffe8000, v7\n"
"v_mfma_f32_16x16x32_bf16 v[240:243], a[100:103], a[156:159], v[240:243]\n"
"v_mfma_f32_16x16x32_bf16 v[240:243], a[96:99], a[152:155], v[240:243]\n"
"s_waitcnt vmcnt(17) lgkmcnt(0)\n"
"s_barrier\n"
"v_mfma_f32_16x16x32_bf16 a[0:3], a[128:131], a[160:163], a[0:3]\n"
"s_waitcnt vmcnt(8)\n"
"v_min3_u32 v24, v33, v35, v37\n"
"v_mfma_f32_16x16x32_bf16 a[0:3], a[132:135], a[164:167], a[0:3]\n"
"ds_read_b128 a[96:99], v2 offset:0\n"
"s_add_u32 m0, s70, 0x18000\n"
"s_add_u32 s95, s72, 0x300000\n"
"v_mfma_f32_16x16x32_bf16 a[4:7], a[140:143], a[164:167], a[4:7]\n"
"buffer_load_dwordx4 v10, s[40:43], s95 offen offset:0 lds\n"
"v_mfma_f32_16x16x32_bf16 a[4:7], a[136:139], a[160:163], a[4:7]\n"
"ds_read_b128 a[100:103], v3 offset:0\n"
"v_min3_u32 v24, v24, v39, v41\n"
"v_mfma_f32_16x16x32_bf16 a[32:35], a[144:147], a[160:163], a[32:35]\n"
"v_min3_u32 v24, v24, v43, v45\n"
"v_min3_u32 v24, v24, v47, v49\n"
"v_mfma_f32_16x16x32_bf16 a[32:35], a[148:151], a[164:167], a[32:35]\n"
"v_min3_u32 v24, v24, v51, v53\n"
"v_min3_u32 v24, v24, v55, v57\n"
"v_mfma_f32_16x16x32_bf16 a[36:39], a[156:159], a[164:167], a[36:39]\n"
"ds_read_b128 a[104:107], v2 offset:2048\n"
"s_add_u32 s95, s72, 0x307c00\n"
"v_mfma_f32_16x16x32_bf16 a[36:39], a[152:155], a[160:163], a[36:39]\n"
"buffer_load_dwordx4 v10, s[40:43], s95 offen offset:1024 lds\n"
"v_mfma_f32_16x16x32_bf16 a[44:47], a[152:155], a[168:171], a[44:47]\n"
"v_min3_u32 v24, v24, v59, v61\n"
"v_min3_u32 v24, v24, v63, v63\n"
"v_mfma_f32_16x16x32_bf16 a[44:47], a[156:159], a[172:175], a[44:47]\n"
"ds_read_b128 a[108:111], v3 offset:2048\n"
"s_add_u32 s95, s72, 0x30f800\n"
"v_mfma_f32_16x16x32_bf16 a[40:43], a[148:151], a[172:175], a[40:43]\n"
"buffer_load_dwordx4 v11, s[40:43], s95 offen offset:2048 lds\n"
"v_mfma_f32_16x16x32_bf16 a[40:43], a[144:147], a[168:171], a[40:43]\n"
"v_max3_u32 v25, v33, v35, v37\n"
"v_max3_u32 v25, v25, v39, v41\n"
"v_mfma_f32_16x16x32_bf16 a[12:15], a[136:139], a[168:171], a[12:15]\n"
"ds_read_b128 a[112:115], v2 offset:4096\n"
"v_max3_u32 v25, v25, v43, v45\n"
"v_mfma_f32_16x16x32_bf16 a[12:15], a[140:143], a[172:175], a[12:15]\n"
"s_add_u32 s95, s72, 0x317400\n"
"v_max3_u32 v25, v25, v47, v49\n"
"v_mfma_f32_16x16x32_bf16 a[8:11], a[132:135], a[172:175], a[8:11]\n"
"buffer_load_dwordx4 v11, s[40:43], s95 offen offset:3072 lds\n"
"v_mfma_f32_16x16x32_bf16 a[8:11], a[128:131], a[168:171], a[8:11]\n"
"ds_read_b128 a[116:119], v3 offset:4096\n"
"v_max3_u32 v25, v25, v51, v53\n"
"v_mfma_f32_16x16x32_bf16 a[16:19], a[128:131], a[176:179], a[16:19]\n"
"v_max3_u32 v25, v25, v55, v57\n"
"v_max3_u32 v25, v25, v59, v61\n"
"v_mfma_f32_16x16x32_bf16 a[16:19], a[132:135], a[180:183], a[16:19]\n"
"v_max3_u32 v25, v25, v63, v63\n"
"v_max3_u32 v26, v32, v34, v36\n"
"v_mfma_f32_16x16x32_bf16 a[20:23], a[140:143], a[180:183], a[20:23]\n"
"ds_read_b128 a[120:123], v2 offset:6144\n"
"s_add_u32 m0, s70, 0x1c000\n"
"s_add_u32 s95, s72, 0x300000\n"
"v_mfma_f32_16x16x32_bf16 a[20:23], a[136:139], a[176:179], a[20:23]\n"
"buffer_load_dwordx4 v10, s[44:47], s95 offen offset:0 lds\n"
"v_mfma_f32_16x16x32_bf16 a[48:51], a[144:147], a[176:179], a[48:51]\n"
"v_max3_u32 v26, v26, v38, v40\n"
"v_max3_u32 v26, v26, v42, v44\n"
"v_mfma_f32_16x16x32_bf16 a[48:51], a[148:151], a[180:183], a[48:51]\n"
"ds_read_b128 a[124:127], v3 offset:6144\n"
"v_max3_u32 v26, v26, v46, v48\n"
"v_mfma_f32_16x16x32_bf16 a[52:55], a[156:159], a[180:183], a[52:55]\n"
"s_add_u32 s95, s72, 0x307c00\n"
"v_max3_u32 v26, v26, v50, v52\n"
"v_mfma_f32_16x16x32_bf16 a[52:55], a[152:155], a[176:179], a[52:55]\n"
"buffer_load_dwordx4 v10, s[44:47], s95 offen offset:1024 lds\n"
"v_mfma_f32_16x16x32_bf16 a[60:63], a[152:155], a[184:187], a[60:63]\n"
"v_max3_u32 v26, v26, v54, v56\n"
"v_max3_u32 v26, v26, v58, v60\n"
"v_mfma_f32_16x16x32_bf16 a[60:63], a[156:159], a[188:191], a[60:63]\n"
"v_max3_u32 v26, v26, v62, v62\n"
"v_xor_b32 v24, s68, v24\n"
"v_mfma_f32_16x16x32_bf16 a[56:59], a[148:151], a[188:191], a[56:59]\n"
"s_add_u32 s95, s72, 0x30f800\n"
"v_xor_b32 v25, s68, v25\n"
"v_mfma_f32_16x16x32_bf16 a[56:59], a[144:147], a[184:187], a[56:59]\n"
"buffer_load_dwordx4 v11, s[44:47], s95 offen offset:2048 lds\n"
"v_mfma_f32_16x16x32_bf16 a[28:31], a[136:139], a[184:187], a[28:31]\n"
"v_or_b32 v24, v24, v25\n"
"v_cmp_eq_u32 vcc, 0, v24\n"
"v_mfma_f32_16x16x32_bf16 a[28:31], a[140:143], a[188:191], a[28:31]\n"
"s_cmp_eq_u64 vcc, -1\n"
"s_cbranch_scc0 L_ln_spin%=\n"
"LT_ln_ok%=:\n"
"v_mfma_f32_16x16x32_bf16 a[24:27], a[132:135], a[188:191], a[24:27]\n"
"s_add_u32 s95, s72, 0x317400\n"
"v_max_u32_dpp v26, v26, v26 row_ror:1 row_mask:0xf bank_mask:0xf\n"
"v_mfma_f32_16x16x32_bf16 a[24:27], a[128:131], a[184:187], a[24:27]\n"
"buffer_load_dwordx4 v11, s[44:47], s95 offen offset:3072 lds\n"
"s_waitcnt lgkmcnt(0)\n"
"v_mfma_f32_16x16x32_bf16 v[128:131], a[64:67], a[160:163], v[128:131]\n"
"s_mov_b32 s4, s12\n"
"s_and_b32 s5, s13, 0xffff\n"
"v_mfma_f32_16x16x32_bf16 v[128:131], a[68:71], a[164:167], v[128:131]\n"
"ds_read_b64_tr_b16 a[128:129], v4 offset:0\n"
"s_mov_b32 s6, 0x400000\n"
"s_mov_b32 s7, 0x20000\n"
"v_mfma_f32_16x16x32_bf16 v[132:135], a[76:79], a[164:167], v[132:135]\n"
"ds_read_b64_tr_b16 a[130:131], v4 offset:1024\n"
"s_lshl_b32 s34, s97, 15\n"
"s_and_b32 s76, s32, 7\n"
"v_mfma_f32_16x16x32_bf16 v[132:135], a[72:75], a[160:163], v[132:135]\n"
"ds_read_b64_tr_b16 a[132:133], v4 offset:8192\n"
"s_lshl_b32 s76, s76, 10\n"
"s_add_u32 s34, s34, s76\n"
"v_mfma_f32_16x16x32_bf16 v[160:163], a[80:83], a[160:163], v[160:163]\n"
"buffer_load_dwordx4 a[252:255], v1, s[4:7], s34 offen offset:0\n"
"v_mfma_f32_16x16x32_bf16 v[160:163], a[84:87], a[164:167], v[160:163]\n"
"ds_read_b64_tr_b16 a[134:135], v4 offset:9216\n"
"v_max_u32_dpp v26, v26, v26 row_ror:2 row_mask:0xf bank_mask:0xf\n"
"v_mfma_f32_16x16x32_bf16 v[164:167], a[92:95], a[164:167], v[164:167]\n"
"ds_read_b64_tr_b16 a[136:137], v5 offset:0\n"
"v_max_u32_dpp v26, v26, v26 row_ror:4 row_mask:0xf bank_mask:0xf\n"
"v_mfma_f32_16x16x32_bf16 v[164:167], a[88:91], a[160:163], v[164:167]\n"
"v_mfma_f32_16x16x32_bf16 v[172:175], a[88:91], a[168:171], v[172:175]\n"
"ds_read_b64_tr_b16 a[138:139], v5 offset:1024\n"
"v_max_u32_dpp v26, v26, v26 row_ror:8 row_mask:0xf bank_mask:0xf\n"
"v_mfma_f32_16x16x32_bf16 v[172:175], a[92:95], a[172:175], v[172:175]\n"
"ds_read_b64_tr_b16 a[140:141], v5 offset:8192\n"
"v_mov_b32 v27, v26\n"
"v_mfma_f32_16x16x32_bf16 v[168:171], a[84:87], a[172:175], v[168:171]\n"
"v_mfma_f32_16x16x32_bf16 v[168:171], a[80:83], a[168:171], v[168:171]\n"
"ds_read_b64_tr_b16 a[142:143], v5 offset:9216\n"
"v_permlane16_swap_b32 v26, v27\n"
"v_mfma_f32_16x16x32_bf16 v[140:143], a[72:75], a[168:171], v[140:143]\n"
"ds_read_b64_tr_b16 a[144:145], v6 offset:0\n"
"v_max_u32 v26, v26, v27\n"
"v_mfma_f32_16x16x32_bf16 v[140:143], a[76:79], a[172:175], v[140:143]\n"
"ds_read_b64_tr_b16 a[146:147], v6 offset:1024\n"
"v_mov_b32 v27, v26\n"
"v_mfma_f32_16x16x32_bf16 v[136:139], a[68:71], a[172:175], v[136:139]\n"
"v_mfma_f32_16x16x32_bf16 v[136:139], a[64:67], a[168:171], v[136:139]\n"
"ds_read_b64_tr_b16 a[148:149], v6 offset:8192\n"
"v_permlane32_swap_b32 v26, v27\n"
"v_mfma_f32_16x16x32_bf16 v[144:147], a[64:67], a[176:179], v[144:147]\n"
"ds_read_b64_tr_b16 a[150:151], v6 offset:9216\n"
"v_max_u32 v26, v26, v27\n"
"v_mfma_f32_16x16x32_bf16 v[144:147], a[68:71], a[180:183], v[144:147]\n"
"v_readfirstlane_b32 s83, v26\n"
"v_mfma_f32_16x16x32_bf16 v[148:151], a[76:79], a[180:183], v[148:151]\n"
"ds_read_b64_tr_b16 a[152:153], v7 offset:0\n"
"s_and_b32 s82, s83, 0xffff\n"
"v_mfma_f32_16x16x32_bf16 v[148:151], a[72:75], a[176:179], v[148:151]\n"
"ds_read_b64_tr_b16 a[154:155], v7 offset:1024\n"
"s_sub_u32 s82, 0xffff, s82\n"
"v_mfma_f32_16x16x32_bf16 v[176:179], a[80:83], a[176:179], v[176:179]\n"
"s_and_b32 s82, s82, 0x7ff\n"
"s_lshr_b32 s73, s82, 4\n"
"v_mfma_f32_16x16x32_bf16 v[176:179], a[84:87], a[180:183], v[176:179]\n"
"ds_read_b64_tr_b16 a[156:157], v7 offset:8192\n"
"s_lshl_b32 s73, s73, 14\n"
"v_mfma_f32_16x16x32_bf16 v[180:183], a[92:95], a[180:183], v[180:183]\n"
"ds_read_b64_tr_b16 a[158:159], v7 offset:9216\n"
"s_and_b32 s74, s82, 15\n"
"v_mfma_f32_16x16x32_bf16 v[180:183], a[88:91], a[176:179], v[180:183]\n"
"s_lshl_b32 s74, s74, 4\n"
"s_add_u32 s73, s73, s74\n"
"v_mfma_f32_16x16x32_bf16 v[188:191], a[88:91], a[184:187], v[188:191]\n"
"s_add_u32 s73, s73, 0x100000\n"
"v_lshrrev_b32 v27, 4, v29\n"
"v_mfma_f32_16x16x32_bf16 v[188:191], a[92:95], a[188:191], v[188:191]\n"
"v_and_b32 v56, 12, v29\n"
"v_lshl_add_u32 v27, v27, 8, v56\n"
"v_mfma_f32_16x16x32_bf16 v[184:187], a[84:87], a[188:191], v[184:187]\n"
"buffer_load_dword v56, v27, s[48:51], s73 offen sc1\n"
"v_mfma_f32_16x16x32_bf16 v[184:187], a[80:83], a[184:187], v[184:187]\n"
"s_and_b32 s83, s83, 0xffff0000\n"
"s_ashr_i32 s73, s83, 31\n"
"v_mfma_f32_16x16x32_bf16 v[156:159], a[72:75], a[184:187], v[156:159]\n"
"s_not_b32 s73, s73\n"
"s_or_b32 s73, s73, 0x80000000\n"
"v_mfma_f32_16x16x32_bf16 v[156:159], a[76:79], a[188:191], v[156:159]\n"
"s_xor_b32 s83, s83, s73\n"
"s_and_b32 s83, s83, 0xffff0000\n"
"v_mfma_f32_16x16x32_bf16 v[152:155], a[68:71], a[188:191], v[152:155]\n"
"v_lshl_or_b32 v24, s64, 6, v21\n"
"v_or_b32 v24, s32, v24\n"
"v_mfma_f32_16x16x32_bf16 v[152:155], a[64:67], a[184:187], v[152:155]\n"
"s_waitcnt lgkmcnt(0)\n"
"v_cmp_eq_u32 vcc, 0, v24\n"
"s_add_u32 s34, s34, 0x2000\n"
"v_mfma_f32_16x16x32_bf16 v[64:67], a[128:131], a[96:99], v[64:67]\n"
"buffer_load_dwordx4 a[252:255], v1, s[4:7], s34 offen offset:0\n"
"v_mfma_f32_16x16x32_bf16 v[64:67], a[132:135], a[100:103], v[64:67]\n"
"ds_read_b128 a[160:163], v2 offset:16384\n"
"v_mov_b32 v24, -1\n"
"v_mfma_f32_16x16x32_bf16 v[68:71], a[140:143], a[100:103], v[68:71]\n"
"v_cndmask_b32 v24, v24, 0, vcc\n"
"v_mov_b32 v60, s68\n"
"v_mfma_f32_16x16x32_bf16 v[68:71], a[136:139], a[96:99], v[68:71]\n"
"ds_read_b128 a[164:167], v3 offset:16384\n"
"v_mov_b32 v61, s69\n"
"v_mfma_f32_16x16x32_bf16 v[72:75], a[144:147], a[96:99], v[72:75]\n"
"buffer_store_dwordx2 v[60:61], v24, s[48:51], 0 offen sc1\n"
"v_mfma_f32_16x16x32_bf16 v[72:75], a[148:151], a[100:103], v[72:75]\n"
"v_accvgpr_read_b32 v32, a192\n"
"v_accvgpr_read_b32 v33, a193\n"
"v_mfma_f32_16x16x32_bf16 v[76:79], a[156:159], a[100:103], v[76:79]\n"
"ds_read_b128 a[168:171], v2 offset:18432\n"
"v_accvgpr_read_b32 v34, a194\n"
"v_mfma_f32_16x16x32_bf16 v[76:79], a[152:155], a[96:99], v[76:79]\n"
"v_accvgpr_read_b32 v35, a195\n"
"v_accvgpr_read_b32 v36, a196\n"
"v_mfma_f32_16x16x32_bf16 v[92:95], a[152:155], a[104:107], v[92:95]\n"
"v_accvgpr_read_b32 v37, a197\n"
"v_accvgpr_read_b32 v38, a198\n"
"v_mfma_f32_16x16x32_bf16 v[92:95], a[156:159], a[108:111], v[92:95]\n"
"ds_read_b128 a[172:175], v3 offset:18432\n"
"v_accvgpr_read_b32 v39, a199\n"
"v_mfma_f32_16x16x32_bf16 v[88:91], a[148:151], a[108:111], v[88:91]\n"
"v_accvgpr_read_b32 v40, a200\n"
"v_accvgpr_read_b32 v41, a201\n"
"v_mfma_f32_16x16x32_bf16 v[88:91], a[144:147], a[104:107], v[88:91]\n"
"v_accvgpr_read_b32 v42, a202\n"
"v_accvgpr_read_b32 v43, a203\n"
"v_mfma_f32_16x16x32_bf16 v[84:87], a[136:139], a[104:107], v[84:87]\n"
"ds_read_b128 a[176:179], v2 offset:20480\n"
"v_accvgpr_read_b32 v44, a204\n"
"v_mfma_f32_16x16x32_bf16 v[84:87], a[140:143], a[108:111], v[84:87]\n"
"v_accvgpr_read_b32 v45, a205\n"
"v_accvgpr_read_b32 v46, a206\n"
"v_mfma_f32_16x16x32_bf16 v[80:83], a[132:135], a[108:111], v[80:83]\n"
"v_accvgpr_read_b32 v47, a207\n"
"v_accvgpr_read_b32 v48, a208\n"
"v_mfma_f32_16x16x32_bf16 v[80:83], a[128:131], a[104:107], v[80:83]\n"
"ds_read_b128 a[180:183], v3 offset:20480\n"
"v_accvgpr_read_b32 v49, a209\n"
"v_mfma_f32_16x16x32_bf16 v[96:99], a[128:131], a[112:115], v[96:99]\n"
"v_accvgpr_read_b32 v50, a210\n"
"v_accvgpr_read_b32 v51, a211\n"
"v_mfma_f32_16x16x32_bf16 v[96:99], a[132:135], a[116:119], v[96:99]\n"
"v_accvgpr_read_b32 v52, a212\n"
"v_accvgpr_read_b32 v53, a213\n"
"v_mfma_f32_16x16x32_bf16 v[100:103], a[140:143], a[116:119], v[100:103]\n"
"ds_read_b128 a[184:187], v2 offset:22528\n"
"s_add_u32 m0, s67, 0x20000\n"
"s_add_u32 s75, s71, 0x380\n"
"v_mfma_f32_16x16x32_bf16 v[100:103], a[136:139], a[112:115], v[100:103]\n"
"buffer_load_dwordx4 v8, s[36:39], s75 offen offset:0 lds\n"
"v_mfma_f32_16x16x32_bf16 v[104:107], a[144:147], a[112:115], v[104:107]\n"
"v_accvgpr_read_b32 v54, a214\n"
"v_accvgpr_read_b32 v55, a215\n"
"v_mfma_f32_16x16x32_bf16 v[104:107], a[148:151], a[116:119], v[104:107]\n"
"ds_read_b128 a[188:191], v3 offset:22528\n"
"v_mfma_f32_16x16x32_bf16 v[108:111], a[156:159], a[116:119], v[108:111]\n"
"s_add_u32 s75, s71, 0x1f80\n"
"v_mfma_f32_16x16x32_bf16 v[108:111], a[152:155], a[112:115], v[108:111]\n"
"buffer_load_dwordx4 v9, s[36:39], s75 offen offset:1024 lds\n"
"v_mfma_f32_16x16x32_bf16 v[124:127], a[152:155], a[120:123], v[124:127]\n"
"v_mfma_f32_16x16x32_bf16 v[124:127], a[156:159], a[124:127], v[124:127]\n"
"v_mfma_f32_16x16x32_bf16 v[120:123], a[148:151], a[124:127], v[120:123]\n"
"v_mfma_f32_16x16x32_bf16 v[120:123], a[144:147], a[120:123], v[120:123]\n"
"s_add_u32 s75, s71, 0x3b80\n"
"v_mfma_f32_16x16x32_bf16 v[116:119], a[136:139], a[120:123], v[116:119]\n"
"buffer_load_dwordx4 v8, s[36:39], s75 offen offset:2048 lds\n"
"v_mfma_f32_16x16x32_bf16 v[116:119], a[140:143], a[124:127], v[116:119]\n"
"v_mfma_f32_16x16x32_bf16 v[112:115], a[132:135], a[124:127], v[112:115]\n"
"v_mfma_f32_16x16x32_bf16 v[112:115], a[128:131], a[120:123], v[112:115]\n"
"s_waitcnt lgkmcnt(0)\n"
"v_mfma_f32_16x16x32_bf16 v[192:195], a[128:131], a[160:163], v[192:195]\n"
"ds_read_b64_tr_b16 a[64:65], v4 offset:16384\n"
"s_waitcnt vmcnt(5)\n"
"v_mfma_f32_16x16x32_bf16 v[192:195], a[132:135], a[164:167], v[192:195]\n"
"ds_read_b64_tr_b16 a[66:67], v4 offset:17408\n"
"v_and_b32 v57, 0xffff0000, v56\n"
"v_mfma_f32_16x16x32_bf16 v[196:199], a[140:143], a[164:167], v[196:199]\n"
"ds_read_b64_tr_b16 a[68:69], v4 offset:24576\n"
"s_add_u32 s75, s71, 0x5780\n"
"v_mfma_f32_16x16x32_bf16 v[196:199], a[136:139], a[160:163], v[196:199]\n"
"buffer_load_dwordx4 v9, s[36:39], s75 offen offset:3072 lds\n"
"v_mfma_f32_16x16x32_bf16 v[200:203], a[144:147], a[160:163], v[200:203]\n"
"ds_read_b64_tr_b16 a[70:71], v4 offset:25600\n"
"v_lshlrev_b32 v56, 16, v56\n"
"v_mfma_f32_16x16x32_bf16 v[200:203], a[148:151], a[164:167], v[200:203]\n"
"ds_read_b64_tr_b16 a[72:73], v5 offset:16384\n"
"s_add_u32 m0, s67, 0x21000\n"
"s_add_u32 s75, s71, 0x8380\n"
"v_mfma_f32_16x16x32_bf16 v[204:207], a[156:159], a[164:167], v[204:207]\n"
"buffer_load_dwordx4 v8, s[36:39], s75 offen offset:0 lds\n"
"v_mfma_f32_16x16x32_bf16 v[204:207], a[152:155], a[160:163], v[204:207]\n"
"ds_read_b64_tr_b16 a[74:75], v5 offset:17408\n"
"v_lshlrev_b32 v60, 16, v32\n"
"v_mfma_f32_16x16x32_bf16 v[220:223], a[152:155], a[168:171], v[220:223]\n"
"ds_read_b64_tr_b16 a[76:77], v5 offset:24576\n"
"v_lshlrev_b32 v61, 16, v36\n"
"v_mfma_f32_16x16x32_bf16 v[220:223], a[156:159], a[172:175], v[220:223]\n"
"v_and_b32 v32, 0xffff0000, v32\n"
"v_and_b32 v36, 0xffff0000, v36\n"
"v_mfma_f32_16x16x32_bf16 v[216:219], a[148:151], a[172:175], v[216:219]\n"
"ds_read_b64_tr_b16 a[78:79], v5 offset:25600\n"
"v_mul_f32 v32, v32, v56\n"
"v_mfma_f32_16x16x32_bf16 v[216:219], a[144:147], a[168:171], v[216:219]\n"
"ds_read_b64_tr_b16 a[80:81], v6 offset:16384\n"
"v_fmac_f32 v32, v36, v57\n"
"v_mfma_f32_16x16x32_bf16 v[212:215], a[136:139], a[168:171], v[212:215]\n"
"ds_read_b64_tr_b16 a[82:83], v6 offset:17408\n"
"s_add_u32 s75, s71, 0x9f80\n"
"v_mfma_f32_16x16x32_bf16 v[212:215], a[140:143], a[172:175], v[212:215]\n"
"buffer_load_dwordx4 v9, s[36:39], s75 offen offset:1024 lds\n"
"v_mfma_f32_16x16x32_bf16 v[208:211], a[132:135], a[172:175], v[208:211]\n"
"ds_read_b64_tr_b16 a[84:85], v6 offset:24576\n"
"v_mul_f32 v36, v60, v56\n"
"v_mfma_f32_16x16x32_bf16 v[208:211], a[128:131], a[168:171], v[208:211]\n"
"ds_read_b64_tr_b16 a[86:87], v6 offset:25600\n"
"s_add_u32 s75, s71, 0xbb80\n"
"v_mfma_f32_16x16x32_bf16 v[224:227], a[128:131], a[176:179], v[224:227]\n"
"buffer_load_dwordx4 v8, s[36:39], s75 offen offset:2048 lds\n"
"v_mfma_f32_16x16x32_bf16 v[224:227], a[132:135], a[180:183], v[224:227]\n"
"ds_read_b64_tr_b16 a[88:89], v7 offset:16384\n"
"v_fmac_f32 v36, v61, v57\n"
"v_mfma_f32_16x16x32_bf16 v[228:231], a[140:143], a[180:183], v[228:231]\n"
"ds_read_b64_tr_b16 a[90:91], v7 offset:17408\n"
"v_lshlrev_b32 v60, 16, v33\n"
"v_mfma_f32_16x16x32_bf16 v[228:231], a[136:139], a[176:179], v[228:231]\n"
"v_lshlrev_b32 v61, 16, v37\n"
"v_and_b32 v33, 0xffff0000, v33\n"
"v_mfma_f32_16x16x32_bf16 v[232:235], a[144:147], a[176:179], v[232:235]\n"
"ds_read_b64_tr_b16 a[92:93], v7 offset:24576\n"
"v_and_b32 v37, 0xffff0000, v37\n"
"v_mfma_f32_16x16x32_bf16 v[232:235], a[148:151], a[180:183], v[232:235]\n"
"ds_read_b64_tr_b16 a[94:95], v7 offset:25600\n"
"s_add_u32 s75, s71, 0xd780\n"
"v_mfma_f32_16x16x32_bf16 v[236:239], a[156:159], a[180:183], v[236:239]\n"
"buffer_load_dwordx4 v9, s[36:39], s75 offen offset:3072 lds\n"
"v_mfma_f32_16x16x32_bf16 v[236:239], a[152:155], a[176:179], v[236:239]\n"
"v_mul_f32 v33, v33, v56\n"
"v_fmac_f32 v33, v37, v57\n"
"v_mfma_f32_16x16x32_bf16 v[252:255], a[152:155], a[184:187], v[252:255]\n"
"v_add_u32 v2, 0x10000, v2\n"
"v_mul_f32 v37, v60, v56\n"
"v_mfma_f32_16x16x32_bf16 v[252:255], a[156:159], a[188:191], v[252:255]\n"
"v_add_u32 v3, 0x10000, v3\n"
"v_fmac_f32 v37, v61, v57\n"
"v_mfma_f32_16x16x32_bf16 v[248:251], a[148:151], a[188:191], v[248:251]\n"
"v_add_u32 v4, 0x10000, v4\n"
"v_lshlrev_b32 v60, 16, v34\n"
"v_mfma_f32_16x16x32_bf16 v[248:251], a[144:147], a[184:187], v[248:251]\n"
"v_add_u32 v5, 0x10000, v5\n"
"v_lshlrev_b32 v61, 16, v38\n"
"v_mfma_f32_16x16x32_bf16 v[244:247], a[136:139], a[184:187], v[244:247]\n"
"v_add_u32 v6, 0x10000, v6\n"
"v_and_b32 v34, 0xffff0000, v34\n"
"v_mfma_f32_16x16x32_bf16 v[244:247], a[140:143], a[188:191], v[244:247]\n"
"v_add_u32 v7, 0x10000, v7\n"
"v_and_b32 v38, 0xffff0000, v38\n"
"v_mfma_f32_16x16x32_bf16 v[240:243], a[132:135], a[188:191], v[240:243]\n"
"v_mul_f32 v34, v34, v56\n"
"v_fmac_f32 v34, v38, v57\n"
"v_mfma_f32_16x16x32_bf16 v[240:243], a[128:131], a[184:187], v[240:243]\n"
"s_waitcnt vmcnt(12) lgkmcnt(0)\n"
"s_barrier\n"
"v_mfma_f32_16x16x32_bf16 a[0:3], a[160:163], a[64:67], a[0:3]\n"
"v_mul_f32 v38, v60, v56\n"
"v_fmac_f32 v38, v61, v57\n"
"v_mfma_f32_16x16x32_bf16 a[0:3], a[164:167], a[68:71], a[0:3]\n"
"ds_read_b128 a[128:131], v2 offset:0\n"
"s_add_u32 m0, s70, 0x0\n"
"s_add_u32 s95, s72, 0x380000\n"
"v_mfma_f32_16x16x32_bf16 a[4:7], a[172:175], a[68:71], a[4:7]\n"
"buffer_load_dwordx4 v10, s[40:43], s95 offen offset:0 lds\n"
"v_mfma_f32_16x16x32_bf16 a[4:7], a[168:171], a[64:67], a[4:7]\n"
"ds_read_b128 a[132:135], v3 offset:0\n"
"v_lshlrev_b32 v60, 16, v35\n"
"v_mfma_f32_16x16x32_bf16 a[32:35], a[176:179], a[64:67], a[32:35]\n"
"v_lshlrev_b32 v61, 16, v39\n"
"v_and_b32 v35, 0xffff0000, v35\n"
"v_mfma_f32_16x16x32_bf16 a[32:35], a[180:183], a[68:71], a[32:35]\n"
"v_and_b32 v39, 0xffff0000, v39\n"
"v_mul_f32 v35, v35, v56\n"
"v_mfma_f32_16x16x32_bf16 a[36:39], a[188:191], a[68:71], a[36:39]\n"
"ds_read_b128 a[136:139], v2 offset:2048\n"
"s_add_u32 s95, s72, 0x387c00\n"
"v_mfma_f32_16x16x32_bf16 a[36:39], a[184:187], a[64:67], a[36:39]\n"
"buffer_load_dwordx4 v10, s[40:43], s95 offen offset:1024 lds\n"
"v_mfma_f32_16x16x32_bf16 a[44:47], a[184:187], a[72:75], a[44:47]\n"
"v_fmac_f32 v35, v39, v57\n"
"v_mul_f32 v39, v60, v56\n"
"v_mfma_f32_16x16x32_bf16 a[44:47], a[188:191], a[76:79], a[44:47]\n"
"ds_read_b128 a[140:143], v3 offset:2048\n"
"s_add_u32 s95, s72, 0x38f800\n"
"v_mfma_f32_16x16x32_bf16 a[40:43], a[180:183], a[76:79], a[40:43]\n"
"buffer_load_dwordx4 v11, s[40:43], s95 offen offset:2048 lds\n"
"v_mfma_f32_16x16x32_bf16 a[40:43], a[176:179], a[72:75], a[40:43]\n"
"v_fmac_f32 v39, v61, v57\n"
"v_lshlrev_b32 v60, 16, v40\n"
"v_mfma_f32_16x16x32_bf16 a[12:15], a[168:171], a[72:75], a[12:15]\n"
"ds_read_b128 a[144:147], v2 offset:4096\n"
"v_lshlrev_b32 v61, 16, v44\n"
"v_mfma_f32_16x16x32_bf16 a[12:15], a[172:175], a[76:79], a[12:15]\n"
"s_add_u32 s95, s72, 0x397400\n"
"v_and_b32 v40, 0xffff0000, v40\n"
"v_mfma_f32_16x16x32_bf16 a[8:11], a[164:167], a[76:79], a[8:11]\n"
"buffer_load_dwordx4 v11, s[40:43], s95 offen offset:3072 lds\n"
"v_mfma_f32_16x16x32_bf16 a[8:11], a[160:163], a[72:75], a[8:11]\n"
"ds_read_b128 a[148:151], v3 offset:4096\n"
"v_and_b32 v44, 0xffff0000, v44\n"
"v_mfma_f32_16x16x32_bf16 a[16:19], a[160:163], a[80:83], a[16:19]\n"
"v_mul_f32 v40, v40, v56\n"
"v_fmac_f32 v40, v44, v57\n"
"v_mfma_f32_16x16x32_bf16 a[16:19], a[164:167], a[84:87], a[16:19]\n"
"v_mul_f32 v44, v60, v56\n"
"v_fmac_f32 v44, v61, v57\n"
"v_mfma_f32_16x16x32_bf16 a[20:23], a[172:175], a[84:87], a[20:23]\n"
"ds_read_b128 a[152:155], v2 offset:6144\n"
"s_add_u32 m0, s70, 0x4000\n"
"s_add_u32 s95, s72, 0x380000\n"
"v_mfma_f32_16x16x32_bf16 a[20:23], a[168:171], a[80:83], a[20:23]\n"
"buffer_load_dwordx4 v10, s[44:47], s95 offen offset:0 lds\n"
"v_mfma_f32_16x16x32_bf16 a[48:51], a[176:179], a[80:83], a[48:51]\n"
"v_lshlrev_b32 v60, 16, v41\n"
"v_lshlrev_b32 v61, 16, v45\n"
"v_mfma_f32_16x16x32_bf16 a[48:51], a[180:183], a[84:87], a[48:51]\n"
"ds_read_b128 a[156:159], v3 offset:6144\n"
"v_and_b32 v41, 0xffff0000, v41\n"
"v_mfma_f32_16x16x32_bf16 a[52:55], a[188:191], a[84:87], a[52:55]\n"
"s_add_u32 s95, s72, 0x387c00\n"
"v_and_b32 v45, 0xffff0000, v45\n"
"v_mfma_f32_16x16x32_bf16 a[52:55], a[184:187], a[80:83], a[52:55]\n"
"buffer_load_dwordx4 v10, s[44:47], s95 offen offset:1024 lds\n"
"v_mfma_f32_16x16x32_bf16 a[60:63], a[184:187], a[88:91], a[60:63]\n"
"v_mul_f32 v41, v41, v56\n"
"v_fmac_f32 v41, v45, v57\n"
"v_mfma_f32_16x16x32_bf16 a[60:63], a[188:191], a[92:95], a[60:63]\n"
"v_mul_f32 v45, v60, v56\n"
"v_fmac_f32 v45, v61, v57\n"
"v_mfma_f32_16x16x32_bf16 a[56:59], a[180:183], a[92:95], a[56:59]\n"
"s_add_u32 s95, s72, 0x38f800\n"
"v_lshlrev_b32 v60, 16, v42\n"
"v_mfma_f32_16x16x32_bf16 a[56:59], a[176:179], a[88:91], a[56:59]\n"
"buffer_load_dwordx4 v11, s[44:47], s95 offen offset:2048 lds\n"
"v_mfma_f32_16x16x32_bf16 a[28:31], a[168:171], a[88:91], a[28:31]\n"
"v_lshlrev_b32 v61, 16, v46\n"
"v_and_b32 v42, 0xffff0000, v42\n"
"v_mfma_f32_16x16x32_bf16 a[28:31], a[172:175], a[92:95], a[28:31]\n"
"v_and_b32 v46, 0xffff0000, v46\n"
"v_mul_f32 v42, v42, v56\n"
"v_mfma_f32_16x16x32_bf16 a[24:27], a[164:167], a[92:95], a[24:27]\n"
"s_add_u32 s95, s72, 0x397400\n"
"v_fmac_f32 v42, v46, v57\n"
"v_mfma_f32_16x16x32_bf16 a[24:27], a[160:163], a[88:91], a[24:27]\n"
"buffer_load_dwordx4 v11, s[44:47], s95 offen offset:3072 lds\n"
"s_waitcnt lgkmcnt(0)\n"
"v_mfma_f32_16x16x32_bf16 v[128:131], a[96:99], a[64:67], v[128:131]\n"
"v_mul_f32 v46, v60, v56\n"
"v_fmac_f32 v46, v61, v57\n"
"v_mfma_f32_16x16x32_bf16 v[128:131], a[100:103], a[68:71], v[128:131]\n"
"ds_read_b64_tr_b16 a[160:161], v4 offset:0\n"
"v_lshlrev_b32 v60, 16, v43\n"
"v_mfma_f32_16x16x32_bf16 v[132:135], a[108:111], a[68:71], v[132:135]\n"
"ds_read_b64_tr_b16 a[162:163], v4 offset:1024\n"
"v_lshlrev_b32 v61, 16, v47\n"
"v_mfma_f32_16x16x32_bf16 v[132:135], a[104:107], a[64:67], v[132:135]\n"
"ds_read_b64_tr_b16 a[164:165], v4 offset:8192\n"
"s_add_u32 s34, s34, 0x2000\n"
"v_mfma_f32_16x16x32_bf16 v[160:163], a[112:115], a[64:67], v[160:163]\n"
"buffer_load_dwordx4 a[252:255], v1, s[4:7], s34 offen offset:0\n"
"v_mfma_f32_16x16x32_bf16 v[160:163], a[116:119], a[68:71], v[160:163]\n"
"ds_read_b64_tr_b16 a[166:167], v4 offset:9216\n"
"v_and_b32 v43, 0xffff0000, v43\n"
"v_mfma_f32_16x16x32_bf16 v[164:167], a[124:127], a[68:71], v[164:167]\n"
"ds_read_b64_tr_b16 a[168:169], v5 offset:0\n"
"v_and_b32 v47, 0xffff0000, v47\n"
"v_mfma_f32_16x16x32_bf16 v[164:167], a[120:123], a[64:67], v[164:167]\n"
"v_mul_f32 v43, v43, v56\n"
"v_fmac_f32 v43, v47, v57\n"
"v_mfma_f32_16x16x32_bf16 v[172:175], a[120:123], a[72:75], v[172:175]\n"
"ds_read_b64_tr_b16 a[170:171], v5 offset:1024\n"
"v_mul_f32 v47, v60, v56\n"
"v_mfma_f32_16x16x32_bf16 v[172:175], a[124:127], a[76:79], v[172:175]\n"
"ds_read_b64_tr_b16 a[172:173], v5 offset:8192\n"
"v_fmac_f32 v47, v61, v57\n"
"v_mfma_f32_16x16x32_bf16 v[168:171], a[116:119], a[76:79], v[168:171]\n"
"v_permlane32_swap_b32 v32, v40\n"
"v_permlane32_swap_b32 v33, v41\n"
"v_mfma_f32_16x16x32_bf16 v[168:171], a[112:115], a[72:75], v[168:171]\n"
"ds_read_b64_tr_b16 a[174:175], v5 offset:9216\n"
"v_permlane32_swap_b32 v34, v42\n"
"v_mfma_f32_16x16x32_bf16 v[140:143], a[104:107], a[72:75], v[140:143]\n"
"ds_read_b64_tr_b16 a[176:177], v6 offset:0\n"
"v_permlane32_swap_b32 v35, v43\n"
"v_mfma_f32_16x16x32_bf16 v[140:143], a[108:111], a[76:79], v[140:143]\n"
"ds_read_b64_tr_b16 a[178:179], v6 offset:1024\n"
"v_permlane32_swap_b32 v36, v44\n"
"v_mfma_f32_16x16x32_bf16 v[136:139], a[100:103], a[76:79], v[136:139]\n"
"v_permlane32_swap_b32 v37, v45\n"
"v_permlane32_swap_b32 v38, v46\n"
"v_mfma_f32_16x16x32_bf16 v[136:139], a[96:99], a[72:75], v[136:139]\n"
"ds_read_b64_tr_b16 a[180:181], v6 offset:8192\n"
"v_permlane32_swap_b32 v39, v47\n"
"v_mfma_f32_16x16x32_bf16 v[144:147], a[96:99], a[80:83], v[144:147]\n"
"ds_read_b64_tr_b16 a[182:183], v6 offset:9216\n"
"v_add_f32 v32, v32, v40\n"
"v_mfma_f32_16x16x32_bf16 v[144:147], a[100:103], a[84:87], v[144:147]\n"
"v_add_f32 v33, v33, v41\n"
"v_add_f32 v34, v34, v42\n"
"v_mfma_f32_16x16x32_bf16 v[148:151], a[108:111], a[84:87], v[148:151]\n"
"ds_read_b64_tr_b16 a[184:185], v7 offset:0\n"
"v_add_f32 v35, v35, v43\n"
"v_mfma_f32_16x16x32_bf16 v[148:151], a[104:107], a[80:83], v[148:151]\n"
"ds_read_b64_tr_b16 a[186:187], v7 offset:1024\n"
"v_add_f32 v36, v36, v44\n"
"v_mfma_f32_16x16x32_bf16 v[176:179], a[112:115], a[80:83], v[176:179]\n"
"v_add_f32 v37, v37, v45\n"
"v_add_f32 v38, v38, v46\n"
"v_mfma_f32_16x16x32_bf16 v[176:179], a[116:119], a[84:87], v[176:179]\n"
"ds_read_b64_tr_b16 a[188:189], v7 offset:8192\n"
"v_add_f32 v39, v39, v47\n"
"v_mfma_f32_16x16x32_bf16 v[180:183], a[124:127], a[84:87], v[180:183]\n"
"ds_read_b64_tr_b16 a[190:191], v7 offset:9216\n"
"v_permlane16_swap_b32 v32, v36\n"
"v_mfma_f32_16x16x32_bf16 v[180:183], a[120:123], a[80:83], v[180:183]\n"
"v_permlane16_swap_b32 v33, v37\n"
"v_permlane16_swap_b32 v34, v38\n"
"v_mfma_f32_16x16x32_bf16 v[188:191], a[120:123], a[88:91], v[188:191]\n"
"v_permlane16_swap_b32 v35, v39\n"
"v_add_f32 v32, v32, v36\n"
"v_mfma_f32_16x16x32_bf16 v[188:191], a[124:127], a[92:95], v[188:191]\n"
"v_add_f32 v33, v33, v37\n"
"v_add_f32 v34, v34, v38\n"
"v_mfma_f32_16x16x32_bf16 v[184:187], a[116:119], a[92:95], v[184:187]\n"
"v_add_f32 v35, v35, v39\n"
"v_add_f32_dpp v32, v32, v32 row_ror:8 row_mask:0xf bank_mask:0xf\n"
"v_mfma_f32_16x16x32_bf16 v[184:187], a[112:115], a[88:91], v[184:187]\n"
"v_add_f32_dpp v33, v33, v33 row_ror:8 row_mask:0xf bank_mask:0xf\n"
"v_add_f32_dpp v34, v34, v34 row_ror:8 row_mask:0xf bank_mask:0xf\n"
"v_mfma_f32_16x16x32_bf16 v[156:159], a[104:107], a[88:91], v[156:159]\n"
"s_mov_b32 s36, s12\n"
"s_and_b32 s37, s13, 0xffff\n"
"v_mfma_f32_16x16x32_bf16 v[156:159], a[108:111], a[92:95], v[156:159]\n"
"s_mov_b32 s38, 0x400000\n"
"v_add_f32_dpp v35, v35, v35 row_ror:8 row_mask:0xf bank_mask:0xf\n"
"v_mfma_f32_16x16x32_bf16 v[152:155], a[100:103], a[92:95], v[152:155]\n"
"v_add_f32_dpp v32, v32, v32 row_ror:4 row_mask:0xf bank_mask:0xf\n"
"v_add_f32_dpp v33, v33, v33 row_ror:4 row_mask:0xf bank_mask:0xf\n"
"v_mfma_f32_16x16x32_bf16 v[152:155], a[96:99], a[88:91], v[152:155]\n"
"s_waitcnt lgkmcnt(0)\n"
"v_add_f32_dpp v34, v34, v34 row_ror:4 row_mask:0xf bank_mask:0xf\n"
"s_add_u32 s34, s34, 0x2000\n"
"v_mfma_f32_16x16x32_bf16 v[64:67], a[160:163], a[128:131], v[64:67]\n"
"buffer_load_dwordx4 a[252:255], v1, s[4:7], s34 offen offset:0\n"
"v_mfma_f32_16x16x32_bf16 v[64:67], a[164:167], a[132:135], v[64:67]\n"
"ds_read_b128 a[64:67], v2 offset:16384\n"
"v_add_f32_dpp v35, v35, v35 row_ror:4 row_mask:0xf bank_mask:0xf\n"
"v_mfma_f32_16x16x32_bf16 v[68:71], a[172:175], a[132:135], v[68:71]\n"
"v_add_f32_dpp v32, v32, v32 row_ror:2 row_mask:0xf bank_mask:0xf\n"
"v_add_f32_dpp v33, v33, v33 row_ror:2 row_mask:0xf bank_mask:0xf\n"
"v_mfma_f32_16x16x32_bf16 v[68:71], a[168:171], a[128:131], v[68:71]\n"
"ds_read_b128 a[68:71], v3 offset:16384\n"
"v_add_f32_dpp v34, v34, v34 row_ror:2 row_mask:0xf bank_mask:0xf\n"
"v_mfma_f32_16x16x32_bf16 v[72:75], a[176:179], a[128:131], v[72:75]\n"
"v_add_f32_dpp v35, v35, v35 row_ror:2 row_mask:0xf bank_mask:0xf\n"
"v_add_f32_dpp v32, v32, v32 row_ror:1 row_mask:0xf bank_mask:0xf\n"
"v_mfma_f32_16x16x32_bf16 v[72:75], a[180:183], a[132:135], v[72:75]\n"
"v_add_f32_dpp v33, v33, v33 row_ror:1 row_mask:0xf bank_mask:0xf\n"
"v_add_f32_dpp v34, v34, v34 row_ror:1 row_mask:0xf bank_mask:0xf\n"
"v_mfma_f32_16x16x32_bf16 v[76:79], a[188:191], a[132:135], v[76:79]\n"
"ds_read_b128 a[72:75], v2 offset:18432\n"
"v_add_f32_dpp v35, v35, v35 row_ror:1 row_mask:0xf bank_mask:0xf\n"
"v_mfma_f32_16x16x32_bf16 v[76:79], a[184:187], a[128:131], v[76:79]\n"
"s_lshl_b32 s73, s64, 6\n"
"v_and_b32 v24, 48, v21\n"
"v_mfma_f32_16x16x32_bf16 v[92:95], a[184:187], a[136:139], v[92:95]\n"
"v_add_u32 v24, s73, v24\n"
"v_add_u32 v62, s70, v1\n"
"v_mfma_f32_16x16x32_bf16 v[92:95], a[188:191], a[140:143], v[92:95]\n"
"ds_read_b128 a[76:79], v3 offset:18432\n"
"s_waitcnt vmcnt(63)\n"
"v_mfma_f32_16x16x32_bf16 v[88:91], a[180:183], a[140:143], v[88:91]\n"
"ds_write_b128 v62, a[216:219] offset:49152\n"
"v_mfma_f32_16x16x32_bf16 v[88:91], a[176:179], a[136:139], v[88:91]\n"
"ds_write_b128 v62, a[220:223] offset:50176\n"
"v_mfma_f32_16x16x32_bf16 v[84:87], a[168:171], a[136:139], v[84:87]\n"
"ds_read_b128 a[80:83], v2 offset:20480\n"
"v_mfma_f32_16x16x32_bf16 v[84:87], a[172:175], a[140:143], v[84:87]\n"
"ds_write_b128 v62, a[224:227] offset:51200\n"
"v_mfma_f32_16x16x32_bf16 v[80:83], a[164:167], a[140:143], v[80:83]\n"
"ds_write_b128 v62, a[228:231] offset:52224\n"
"v_mfma_f32_16x16x32_bf16 v[80:83], a[160:163], a[136:139], v[80:83]\n"
"ds_read_b128 a[84:87], v3 offset:20480\n"
"v_mfma_f32_16x16x32_bf16 v[96:99], a[160:163], a[144:147], v[96:99]\n"
"ds_write_b128 v24, v[32:35] offset:32768\n"
"v_mfma_f32_16x16x32_bf16 v[96:99], a[164:167], a[148:151], v[96:99]\n"
"v_bfe_u32 v24, v21, 1, 2\n"
"v_and_b32 v27, 1, v21\n"
"v_mfma_f32_16x16x32_bf16 v[100:103], a[172:175], a[148:151], v[100:103]\n"
"ds_read_b128 a[88:91], v2 offset:22528\n"
"v_xor_b32 v27, 1, v27\n"
"v_mfma_f32_16x16x32_bf16 v[100:103], a[168:171], a[144:147], v[100:103]\n"
"buffer_load_dwordx4 a[192:195], v1, s[48:51], s78 offen offset:0 sc1\n"
"v_mfma_f32_16x16x32_bf16 v[104:107], a[176:179], a[144:147], v[104:107]\n"
"v_lshl_add_u32 v24, v27, 2, v24\n"
"v_lshlrev_b32 v24, 2, v24\n"
"v_mfma_f32_16x16x32_bf16 v[104:107], a[180:183], a[148:151], v[104:107]\n"
"ds_read_b128 a[92:95], v3 offset:22528\n"
"v_mfma_f32_16x16x32_bf16 v[108:111], a[188:191], a[148:151], v[108:111]\n"
"v_mfma_f32_16x16x32_bf16 v[108:111], a[184:187], a[144:147], v[108:111]\n"
"buffer_load_dwordx4 a[196:199], v1, s[48:51], s79 offen offset:0 sc1\n"
"v_mfma_f32_16x16x32_bf16 v[124:127], a[184:187], a[152:155], v[124:127]\n"
"v_mfma_f32_16x16x32_bf16 v[124:127], a[188:191], a[156:159], v[124:127]\n"
"v_mfma_f32_16x16x32_bf16 v[120:123], a[180:183], a[156:159], v[120:123]\n"
"v_mfma_f32_16x16x32_bf16 v[120:123], a[176:179], a[152:155], v[120:123]\n"
"v_mfma_f32_16x16x32_bf16 v[116:119], a[168:171], a[152:155], v[116:119]\n"
"buffer_load_dwordx4 a[200:203], v1, s[48:51], s80 offen offset:0 sc1\n"
"v_mfma_f32_16x16x32_bf16 v[116:119], a[172:175], a[156:159], v[116:119]\n"
"v_mfma_f32_16x16x32_bf16 v[112:115], a[164:167], a[156:159], v[112:115]\n"
"v_mfma_f32_16x16x32_bf16 v[112:115], a[160:163], a[152:155], v[112:115]\n"
"s_waitcnt lgkmcnt(0)\n"
"v_mfma_f32_16x16x32_bf16 v[192:195], a[160:163], a[64:67], v[192:195]\n"
"ds_read_b64_tr_b16 a[96:97], v4 offset:16384\n"
"v_mfma_f32_16x16x32_bf16 v[192:195], a[164:167], a[68:71], v[192:195]\n"
"ds_read_b64_tr_b16 a[98:99], v4 offset:17408\n"
"v_mfma_f32_16x16x32_bf16 v[196:199], a[172:175], a[68:71], v[196:199]\n"
"ds_read_b64_tr_b16 a[100:101], v4 offset:24576\n"
"v_mfma_f32_16x16x32_bf16 v[196:199], a[168:171], a[64:67], v[196:199]\n"
"buffer_load_dwordx4 a[204:207], v1, s[48:51], s81 offen offset:0 sc1\n"
"v_mfma_f32_16x16x32_bf16 v[200:203], a[176:179], a[64:67], v[200:203]\n"
"ds_read_b64_tr_b16 a[102:103], v4 offset:25600\n"
"v_mfma_f32_16x16x32_bf16 v[200:203], a[180:183], a[68:71], v[200:203]\n"
"ds_read_b64_tr_b16 a[104:105], v5 offset:16384\n"
"v_mfma_f32_16x16x32_bf16 v[204:207], a[188:191], a[68:71], v[204:207]\n"
"buffer_load_dwordx4 a[208:211], v1, s[48:51], s78 offen offset:1024 sc1\n"
"v_mfma_f32_16x16x32_bf16 v[204:207], a[184:187], a[64:67], v[204:207]\n"
"ds_read_b64_tr_b16 a[106:107], v5 offset:17408\n"
"v_mfma_f32_16x16x32_bf16 v[220:223], a[184:187], a[72:75], v[220:223]\n"
"ds_read_b64_tr_b16 a[108:109], v5 offset:24576\n"
"v_mfma_f32_16x16x32_bf16 v[220:223], a[188:191], a[76:79], v[220:223]\n"
"v_mfma_f32_16x16x32_bf16 v[216:219], a[180:183], a[76:79], v[216:219]\n"
"ds_read_b64_tr_b16 a[110:111], v5 offset:25600\n"
"v_mfma_f32_16x16x32_bf16 v[216:219], a[176:179], a[72:75], v[216:219]\n"
"ds_read_b64_tr_b16 a[112:113], v6 offset:16384\n"
"v_mfma_f32_16x16x32_bf16 v[212:215], a[168:171], a[72:75], v[212:215]\n"
"ds_read_b64_tr_b16 a[114:115], v6 offset:17408\n"
"v_mfma_f32_16x16x32_bf16 v[212:215], a[172:175], a[76:79], v[212:215]\n"
"buffer_load_dwordx4 a[212:215], v1, s[48:51], s79 offen offset:1024 sc1\n"
"v_mfma_f32_16x16x32_bf16 v[208:211], a[164:167], a[76:79], v[208:211]\n"
"ds_read_b64_tr_b16 a[116:117], v6 offset:24576\n"
"v_mfma_f32_16x16x32_bf16 v[208:211], a[160:163], a[72:75], v[208:211]\n"
"ds_read_b64_tr_b16 a[118:119], v6 offset:25600\n"
"v_mfma_f32_16x16x32_bf16 v[224:227], a[160:163], a[80:83], v[224:227]\n"
"buffer_load_dwordx4 a[216:219], v1, s[48:51], s80 offen offset:1024 sc1\n"
"v_mfma_f32_16x16x32_bf16 v[224:227], a[164:167], a[84:87], v[224:227]\n"
"ds_read_b64_tr_b16 a[120:121], v7 offset:16384\n"
"v_mfma_f32_16x16x32_bf16 v[228:231], a[172:175], a[84:87], v[228:231]\n"
"ds_read_b64_tr_b16 a[122:123], v7 offset:17408\n"
"v_mfma_f32_16x16x32_bf16 v[228:231], a[168:171], a[80:83], v[228:231]\n"
"v_mfma_f32_16x16x32_bf16 v[232:235], a[176:179], a[80:83], v[232:235]\n"
"ds_read_b64_tr_b16 a[124:125], v7 offset:24576\n"
"v_mfma_f32_16x16x32_bf16 v[232:235], a[180:183], a[84:87], v[232:235]\n"
"ds_read_b64_tr_b16 a[126:127], v7 offset:25600\n"
"v_mfma_f32_16x16x32_bf16 v[236:239], a[188:191], a[84:87], v[236:239]\n"
"buffer_load_dwordx4 a[220:223], v1, s[48:51], s81 offen offset:1024 sc1\n"
"v_mfma_f32_16x16x32_bf16 v[236:239], a[184:187], a[80:83], v[236:239]\n"
"v_mfma_f32_16x16x32_bf16 v[252:255], a[184:187], a[88:91], v[252:255]\n"
"v_add_u32 v2, 0x10000, v2\n"
"v_mfma_f32_16x16x32_bf16 v[252:255], a[188:191], a[92:95], v[252:255]\n"
"v_add_u32 v3, 0x10000, v3\n"
"v_mfma_f32_16x16x32_bf16 v[248:251], a[180:183], a[92:95], v[248:251]\n"
"v_add_u32 v4, 0xfffe8000, v4\n"
"v_mfma_f32_16x16x32_bf16 v[248:251], a[176:179], a[88:91], v[248:251]\n"
"v_add_u32 v5, 0xfffe8000, v5\n"
"v_mfma_f32_16x16x32_bf16 v[244:247], a[168:171], a[88:91], v[244:247]\n"
"v_add_u32 v6, 0xfffe8000, v6\n"
"v_mfma_f32_16x16x32_bf16 v[244:247], a[172:175], a[92:95], v[244:247]\n"
"v_add_u32 v7, 0xfffe8000, v7\n"
"v_mfma_f32_16x16x32_bf16 v[240:243], a[164:167], a[92:95], v[240:243]\n"
"v_mfma_f32_16x16x32_bf16 v[240:243], a[160:163], a[88:91], v[240:243]\n"
"s_waitcnt vmcnt(10) lgkmcnt(0)\n"
"s_barrier\n"
"v_mfma_f32_16x16x32_bf16 a[0:3], a[64:67], a[96:99], a[0:3]\n"
"v_lshrrev_b32 v62, 2, v21\n"
"v_and_b32 v63, 6, v62\n"
"v_mfma_f32_16x16x32_bf16 a[0:3], a[68:71], a[100:103], a[0:3]\n"
"ds_read_b128 a[160:163], v2 offset:0\n"
"s_add_u32 m0, s70, 0x10000\n"
"s_add_u32 s95, s72, 0x0\n"
"v_mfma_f32_16x16x32_bf16 a[4:7], a[76:79], a[100:103], a[4:7]\n"
"buffer_load_dwordx4 v10, s[36:39], s95 offen offset:0 lds\n"
"v_mfma_f32_16x16x32_bf16 a[4:7], a[72:75], a[96:99], a[4:7]\n"
"ds_read_b128 a[164:167], v3 offset:0\n"
"v_bfe_u32 v23, v21, 1, 1\n"
"v_mfma_f32_16x16x32_bf16 a[32:35], a[80:83], a[96:99], a[32:35]\n"
"s_add_u32 s95, s72, 0x7c00\n"
"v_xor_b32 v63, v63, v23\n"
"v_mfma_f32_16x16x32_bf16 a[32:35], a[84:87], a[100:103], a[32:35]\n"
"buffer_load_dwordx4 v10, s[36:39], s95 offen offset:1024 lds\n"
"v_mfma_f32_16x16x32_bf16 a[36:39], a[92:95], a[100:103], a[36:39]\n"
"ds_read_b128 a[168:171], v2 offset:2048\n"
"v_lshlrev_b32 v63, 4, v63\n"
"v_mfma_f32_16x16x32_bf16 a[36:39], a[88:91], a[96:99], a[36:39]\n"
"s_add_u32 s95, s72, 0xf800\n"
"v_lshl_add_u32 v62, v62, 7, v63\n"
"v_mfma_f32_16x16x32_bf16 a[44:47], a[88:91], a[104:107], a[44:47]\n"
"buffer_load_dwordx4 v11, s[36:39], s95 offen offset:2048 lds\n"
"v_mfma_f32_16x16x32_bf16 a[44:47], a[92:95], a[108:111], a[44:47]\n"
"ds_read_b128 a[172:175], v3 offset:2048\n"
"v_and_b32 v63, 1, v21\n"
"v_mfma_f32_16x16x32_bf16 a[40:43], a[84:87], a[108:111], a[40:43]\n"
"s_add_u32 s95, s72, 0x17400\n"
"v_lshl_add_u32 v62, v63, 3, v62\n"
"v_mfma_f32_16x16x32_bf16 a[40:43], a[80:83], a[104:107], a[40:43]\n"
"buffer_load_dwordx4 v11, s[36:39], s95 offen offset:3072 lds\n"
"v_mfma_f32_16x16x32_bf16 a[12:15], a[72:75], a[104:107], a[12:15]\n"
"ds_read_b128 a[176:179], v2 offset:4096\n"
"v_add_u32 v62, s70, v62\n"
"v_mfma_f32_16x16x32_bf16 a[12:15], a[76:79], a[108:111], a[12:15]\n"
"s_and_b32 s34, s96, 7\n"
"s_lshl_b32 s34, s34, 19\n"
"v_mfma_f32_16x16x32_bf16 a[8:11], a[68:71], a[108:111], a[8:11]\n"
"s_lshr_b32 s35, s96, 3\n"
"s_lshl_b32 s35, s35, 12\n"
"v_mfma_f32_16x16x32_bf16 a[8:11], a[64:67], a[104:107], a[8:11]\n"
"ds_read_b128 a[180:183], v3 offset:4096\n"
"s_add_u32 m0, s70, 0x14000\n"
"s_add_u32 s95, s72, 0x80000\n"
"v_mfma_f32_16x16x32_bf16 a[16:19], a[64:67], a[112:115], a[16:19]\n"
"buffer_load_dwordx4 v10, s[36:39], s95 offen offset:0 lds\n"
"v_mfma_f32_16x16x32_bf16 a[16:19], a[68:71], a[116:119], a[16:19]\n"
"s_add_u32 s34, s34, s35\n"
"s_add_u32 s34, s34, 0x3400000\n"
"v_mfma_f32_16x16x32_bf16 a[20:23], a[76:79], a[116:119], a[20:23]\n"
"ds_read_b128 a[184:187], v2 offset:6144\n"
"s_add_u32 s95, s72, 0x87c00\n"
"v_mfma_f32_16x16x32_bf16 a[20:23], a[72:75], a[112:115], a[20:23]\n"
"buffer_load_dwordx4 v10, s[36:39], s95 offen offset:1024 lds\n"
"v_mfma_f32_16x16x32_bf16 a[48:51], a[80:83], a[112:115], a[48:51]\n"
"s_waitcnt lgkmcnt(15)\n"
"v_xor_b32 v63, 0, v62\n"
"v_mfma_f32_16x16x32_bf16 a[48:51], a[84:87], a[116:119], a[48:51]\n"
"ds_read_b128 a[188:191], v3 offset:6144\n"
"s_add_u32 s95, s72, 0x8f800\n"
"v_mfma_f32_16x16x32_bf16 a[52:55], a[92:95], a[116:119], a[52:55]\n"
"buffer_load_dwordx4 v11, s[36:39], s95 offen offset:2048 lds\n"
"v_mfma_f32_16x16x32_bf16 a[52:55], a[88:91], a[112:115], a[52:55]\n"
"s_add_u32 s95, s72, 0x97400\n"
"v_mfma_f32_16x16x32_bf16 a[60:63], a[88:91], a[120:123], a[60:63]\n"
"buffer_load_dwordx4 v11, s[36:39], s95 offen offset:3072 lds\n"
"v_mfma_f32_16x16x32_bf16 a[60:63], a[92:95], a[124:127], a[60:63]\n"
"ds_read_b64_tr_b16 a[224:225], v63 offset:49152\n"
"v_mfma_f32_16x16x32_bf16 a[56:59], a[84:87], a[124:127], a[56:59]\n"
"ds_read_b64_tr_b16 a[226:227], v63 offset:51200\n"
"v_xor_b32 v63, 32, v62\n"
"v_mfma_f32_16x16x32_bf16 a[56:59], a[80:83], a[120:123], a[56:59]\n"
"s_add_u32 m0, s70, 0x18000\n"
"s_add_u32 s95, s72, 0x100000\n"
"v_mfma_f32_16x16x32_bf16 a[28:31], a[72:75], a[120:123], a[28:31]\n"
"buffer_load_dwordx4 v10, s[36:39], s95 offen offset:0 lds\n"
"v_mfma_f32_16x16x32_bf16 a[28:31], a[76:79], a[124:127], a[28:31]\n"
"ds_read_b64_tr_b16 a[228:229], v63 offset:49152\n"
"v_mfma_f32_16x16x32_bf16 a[24:27], a[68:71], a[124:127], a[24:27]\n"
"s_add_u32 s95, s72, 0x107c00\n"
"v_mfma_f32_16x16x32_bf16 a[24:27], a[64:67], a[120:123], a[24:27]\n"
"buffer_load_dwordx4 v10, s[36:39], s95 offen offset:1024 lds\n"
"s_waitcnt lgkmcnt(0)\n"
"v_mfma_f32_16x16x32_bf16 v[128:131], a[128:131], a[96:99], v[128:131]\n"
"ds_read_b64_tr_b16 a[230:231], v63 offset:51200\n"
"v_xor_b32 v63, 64, v62\n"
"v_mfma_f32_16x16x32_bf16 v[128:131], a[132:135], a[100:103], v[128:131]\n"
"ds_read_b64_tr_b16 a[64:65], v4 offset:0\n"
"v_mfma_f32_16x16x32_bf16 v[132:135], a[140:143], a[100:103], v[132:135]\n"
"ds_read_b64_tr_b16 a[66:67], v4 offset:1024\n"
"v_mfma_f32_16x16x32_bf16 v[132:135], a[136:139], a[96:99], v[132:135]\n"
"ds_read_b64_tr_b16 a[68:69], v4 offset:8192\n"
"s_add_u32 s95, s72, 0x10f800\n"
"v_mfma_f32_16x16x32_bf16 v[160:163], a[144:147], a[96:99], v[160:163]\n"
"buffer_load_dwordx4 v11, s[36:39], s95 offen offset:2048 lds\n"
"v_mfma_f32_16x16x32_bf16 v[160:163], a[148:151], a[100:103], v[160:163]\n"
"ds_read_b64_tr_b16 a[70:71], v4 offset:9216\n"
"v_mfma_f32_16x16x32_bf16 v[164:167], a[156:159], a[100:103], v[164:167]\n"
"ds_read_b64_tr_b16 a[72:73], v5 offset:0\n"
"s_add_u32 s95, s72, 0x117400\n"
"v_mfma_f32_16x16x32_bf16 v[164:167], a[152:155], a[96:99], v[164:167]\n"
"buffer_load_dwordx4 v11, s[36:39], s95 offen offset:3072 lds\n"
"v_mfma_f32_16x16x32_bf16 v[172:175], a[152:155], a[104:107], v[172:175]\n"
"ds_read_b64_tr_b16 a[74:75], v5 offset:1024\n"
"v_mfma_f32_16x16x32_bf16 v[172:175], a[156:159], a[108:111], v[172:175]\n"
"ds_read_b64_tr_b16 a[76:77], v5 offset:8192\n"
"s_add_u32 m0, s70, 0x1c000\n"
"s_add_u32 s95, s72, 0x180000\n"
"v_mfma_f32_16x16x32_bf16 v[168:171], a[148:151], a[108:111], v[168:171]\n"
"buffer_load_dwordx4 v10, s[36:39], s95 offen offset:0 lds\n"
"v_mfma_f32_16x16x32_bf16 v[168:171], a[144:147], a[104:107], v[168:171]\n"
"ds_read_b64_tr_b16 a[78:79], v5 offset:9216\n"
"v_mfma_f32_16x16x32_bf16 v[140:143], a[136:139], a[104:107], v[140:143]\n"
"ds_read_b64_tr_b16 a[80:81], v6 offset:0\n"
"v_mfma_f32_16x16x32_bf16 v[140:143], a[140:143], a[108:111], v[140:143]\n"
"ds_read_b64_tr_b16 a[82:83], v6 offset:1024\n"
"s_add_u32 s95, s72, 0x187c00\n"
"v_mfma_f32_16x16x32_bf16 v[136:139], a[132:135], a[108:111], v[136:139]\n"
"buffer_load_dwordx4 v10, s[36:39], s95 offen offset:1024 lds\n"
"v_mfma_f32_16x16x32_bf16 v[136:139], a[128:131], a[104:107], v[136:139]\n"
"ds_read_b64_tr_b16 a[84:85], v6 offset:8192\n"
"v_mfma_f32_16x16x32_bf16 v[144:147], a[128:131], a[112:115], v[144:147]\n"
"ds_read_b64_tr_b16 a[86:87], v6 offset:9216\n"
"s_add_u32 s95, s72, 0x18f800\n"
"v_mfma_f32_16x16x32_bf16 v[144:147], a[132:135], a[116:119], v[144:147]\n"
"buffer_load_dwordx4 v11, s[36:39], s95 offen offset:2048 lds\n"
"v_mfma_f32_16x16x32_bf16 v[148:151], a[140:143], a[116:119], v[148:151]\n"
"ds_read_b64_tr_b16 a[88:89], v7 offset:0\n"
"v_mfma_f32_16x16x32_bf16 v[148:151], a[136:139], a[112:115], v[148:151]\n"
"ds_read_b64_tr_b16 a[90:91], v7 offset:1024\n"
"s_add_u32 s95, s72, 0x197400\n"
"v_mfma_f32_16x16x32_bf16 v[176:179], a[144:147], a[112:115], v[176:179]\n"
"buffer_load_dwordx4 v11, s[36:39], s95 offen offset:3072 lds\n"
"v_mfma_f32_16x16x32_bf16 v[176:179], a[148:151], a[116:119], v[176:179]\n"
"ds_read_b64_tr_b16 a[92:93], v7 offset:8192\n"
"v_mfma_f32_16x16x32_bf16 v[180:183], a[156:159], a[116:119], v[180:183]\n"
"ds_read_b64_tr_b16 a[94:95], v7 offset:9216\n"
"v_mfma_f32_16x16x32_bf16 v[180:183], a[152:155], a[112:115], v[180:183]\n"
"ds_read_b64_tr_b16 a[232:233], v63 offset:49152\n"
"v_mfma_f32_16x16x32_bf16 v[188:191], a[152:155], a[120:123], v[188:191]\n"
"ds_read_b64_tr_b16 a[234:235], v63 offset:51200\n"
"v_xor_b32 v63, 96, v62\n"
"v_mfma_f32_16x16x32_bf16 v[188:191], a[156:159], a[124:127], v[188:191]\n"
"ds_read_b64_tr_b16 a[236:237], v63 offset:49152\n"
"v_mfma_f32_16x16x32_bf16 v[184:187], a[148:151], a[124:127], v[184:187]\n"
"ds_read_b64_tr_b16 a[238:239], v63 offset:51200\n"
"s_waitcnt lgkmcnt(0)\n"
"v_mfma_f32_16x16x32_bf16 v[184:187], a[144:147], a[120:123], v[184:187]\n"
"buffer_store_dwordx4 a[224:227], v1, s[48:51], s34 offen offset:0 sc1\n"
"v_mfma_f32_16x16x32_bf16 v[156:159], a[136:139], a[120:123], v[156:159]\n"
"buffer_store_dwordx4 a[228:231], v1, s[48:51], s34 offen offset:1024 sc1\n"
"v_mfma_f32_16x16x32_bf16 v[156:159], a[140:143], a[124:127], v[156:159]\n"
"buffer_store_dwordx4 a[232:235], v1, s[48:51], s34 offen offset:2048 sc1\n"
"v_mfma_f32_16x16x32_bf16 v[152:155], a[132:135], a[124:127], v[152:155]\n"
"buffer_store_dwordx4 a[236:239], v1, s[48:51], s34 offen offset:3072 sc1\n"
"v_mfma_f32_16x16x32_bf16 v[152:155], a[128:131], a[120:123], v[152:155]\n"
"s_waitcnt lgkmcnt(0)\n"
"v_mfma_f32_16x16x32_bf16 v[64:67], a[64:67], a[160:163], v[64:67]\n"
"ds_read_b32 v32, v24 offset:32768\n"
"v_mfma_f32_16x16x32_bf16 v[64:67], a[68:71], a[164:167], v[64:67]\n"
"ds_read_b128 a[96:99], v2 offset:16384\n"
"v_mfma_f32_16x16x32_bf16 v[68:71], a[76:79], a[164:167], v[68:71]\n"
"ds_read_b32 v33, v24 offset:32832\n"
"v_mfma_f32_16x16x32_bf16 v[68:71], a[72:75], a[160:163], v[68:71]\n"
"ds_read_b128 a[100:103], v3 offset:16384\n"
"v_mfma_f32_16x16x32_bf16 v[72:75], a[80:83], a[160:163], v[72:75]\n"
"ds_read_b32 v34, v24 offset:32896\n"
"v_mfma_f32_16x16x32_bf16 v[72:75], a[84:87], a[164:167], v[72:75]\n"
"ds_read_b32 v35, v24 offset:32960\n"
"v_mfma_f32_16x16x32_bf16 v[76:79], a[92:95], a[164:167], v[76:79]\n"
"ds_read_b128 a[104:107], v2 offset:18432\n"
"v_mfma_f32_16x16x32_bf16 v[76:79], a[88:91], a[160:163], v[76:79]\n"
"ds_read_b32 v36, v24 offset:32800\n"
"v_mfma_f32_16x16x32_bf16 v[92:95], a[88:91], a[168:171], v[92:95]\n"
"ds_read_b32 v37, v24 offset:32864\n"
"v_mfma_f32_16x16x32_bf16 v[92:95], a[92:95], a[172:175], v[92:95]\n"
"ds_read_b128 a[108:111], v3 offset:18432\n"
"v_mfma_f32_16x16x32_bf16 v[88:91], a[84:87], a[172:175], v[88:91]\n"
"ds_read_b32 v38, v24 offset:32928\n"
"v_mfma_f32_16x16x32_bf16 v[88:91], a[80:83], a[168:171], v[88:91]\n"
"ds_read_b32 v39, v24 offset:32992\n"
"s_waitcnt lgkmcnt(0)\n"
"v_mfma_f32_16x16x32_bf16 v[84:87], a[72:75], a[168:171], v[84:87]\n"
"ds_read_b128 a[112:115], v2 offset:20480\n"
"v_add_f32 v32, v32, v33\n"
"v_mfma_f32_16x16x32_bf16 v[84:87], a[76:79], a[172:175], v[84:87]\n"
"v_add_f32 v36, v36, v37\n"
"v_add_f32 v34, v34, v35\n"
"v_mfma_f32_16x16x32_bf16 v[80:83], a[68:71], a[172:175], v[80:83]\n"
"v_add_f32 v38, v38, v39\n"
"v_add_f32 v32, v32, v34\n"
"v_mfma_f32_16x16x32_bf16 v[80:83], a[64:67], a[168:171], v[80:83]\n"
"ds_read_b128 a[116:119], v3 offset:20480\n"
"v_add_f32 v36, v36, v38\n"
"v_mfma_f32_16x16x32_bf16 v[96:99], a[64:67], a[176:179], v[96:99]\n"
"v_cvt_pk_bf16_f32 v32, v32, v36\n"
"v_and_b32 v36, 0xffff0000, v32\n"
"v_mfma_f32_16x16x32_bf16 v[96:99], a[68:71], a[180:183], v[96:99]\n"
"v_lshlrev_b32 v32, 16, v32\n"
"v_mul_f32 v33, 0xbfb8aa3b, v32\n"
"v_mfma_f32_16x16x32_bf16 v[100:103], a[76:79], a[180:183], v[100:103]\n"
"ds_read_b128 a[120:123], v2 offset:22528\n"
"v_mfma_f32_16x16x32_bf16 v[100:103], a[72:75], a[176:179], v[100:103]\n"
"buffer_load_dwordx4 a[224:227], v1, s[48:51], s78 offen offset:2048 sc1\n"
"v_mfma_f32_16x16x32_bf16 v[104:107], a[80:83], a[176:179], v[104:107]\n"
"v_exp_f32 v33, v33\n"
"v_mfma_f32_16x16x32_bf16 v[104:107], a[84:87], a[180:183], v[104:107]\n"
"ds_read_b128 a[124:127], v3 offset:22528\n"
"v_add_f32 v33, 1.0, v33\n"
"v_mfma_f32_16x16x32_bf16 v[108:111], a[92:95], a[180:183], v[108:111]\n"
"v_rcp_f32 v33, v33\n"
"v_mfma_f32_16x16x32_bf16 v[108:111], a[88:91], a[176:179], v[108:111]\n"
"buffer_load_dwordx4 a[228:231], v1, s[48:51], s79 offen offset:2048 sc1\n"
"v_mfma_f32_16x16x32_bf16 v[124:127], a[88:91], a[184:187], v[124:127]\n"
"v_mul_f32 v32, v32, v33\n"
"v_cvt_pk_bf16_f32 v32, v32, v32\n"
"v_mfma_f32_16x16x32_bf16 v[124:127], a[92:95], a[188:191], v[124:127]\n"
"v_lshlrev_b32 v32, 16, v32\n"
"v_mul_f32 v32, v32, v36\n"
"v_mfma_f32_16x16x32_bf16 v[120:123], a[84:87], a[188:191], v[120:123]\n"
"v_cvt_pk_bf16_f32 v32, v32, v32\n"
"v_lshlrev_b32 v32, 16, v32\n"
"v_mfma_f32_16x16x32_bf16 v[120:123], a[80:83], a[184:187], v[120:123]\n"
"v_readlane_b32 s84, v32, 0\n"
"v_readlane_b32 s85, v32, 1\n"
"v_mfma_f32_16x16x32_bf16 v[116:119], a[72:75], a[184:187], v[116:119]\n"
"buffer_load_dwordx4 a[232:235], v1, s[48:51], s80 offen offset:2048 sc1\n"
"v_mfma_f32_16x16x32_bf16 v[116:119], a[76:79], a[188:191], v[116:119]\n"
"v_readlane_b32 s86, v32, 2\n"
"v_readlane_b32 s87, v32, 3\n"
"v_mfma_f32_16x16x32_bf16 v[112:115], a[68:71], a[188:191], v[112:115]\n"
"v_readlane_b32 s88, v32, 4\n"
"v_readlane_b32 s89, v32, 5\n"
"v_mfma_f32_16x16x32_bf16 v[112:115], a[64:67], a[184:187], v[112:115]\n"
"s_waitcnt lgkmcnt(0)\n"
"v_readlane_b32 s90, v32, 6\n"
"v_mfma_f32_16x16x32_bf16 v[192:195], a[64:67], a[96:99], v[192:195]\n"
"ds_read_b64_tr_b16 a[128:129], v4 offset:16384\n"
"v_readlane_b32 s91, v32, 7\n"
"v_mfma_f32_16x16x32_bf16 v[192:195], a[68:71], a[100:103], v[192:195]\n"
"ds_read_b64_tr_b16 a[130:131], v4 offset:17408\n"
"v_mov_b32 v34, 0\n"
"v_mfma_f32_16x16x32_bf16 v[196:199], a[76:79], a[100:103], v[196:199]\n"
"ds_read_b64_tr_b16 a[132:133], v4 offset:24576\n"
"v_mov_b32 v35, 0\n"
"v_mfma_f32_16x16x32_bf16 v[196:199], a[72:75], a[96:99], v[196:199]\n"
"buffer_load_dwordx4 a[236:239], v1, s[48:51], s81 offen offset:2048 sc1\n"
"v_mfma_f32_16x16x32_bf16 v[200:203], a[80:83], a[96:99], v[200:203]\n"
"ds_read_b64_tr_b16 a[134:135], v4 offset:25600\n"
"v_lshlrev_b32 v33, 16, v48\n"
"v_mfma_f32_16x16x32_bf16 v[200:203], a[84:87], a[100:103], v[200:203]\n"
"ds_read_b64_tr_b16 a[136:137], v5 offset:16384\n"
"v_and_b32 v48, 0xffff0000, v48\n"
"v_mfma_f32_16x16x32_bf16 v[204:207], a[92:95], a[100:103], v[204:207]\n"
"buffer_load_dwordx4 a[240:243], v1, s[48:51], s78 offen offset:3072 sc1\n"
"v_mfma_f32_16x16x32_bf16 v[204:207], a[88:91], a[96:99], v[204:207]\n"
"ds_read_b64_tr_b16 a[138:139], v5 offset:17408\n"
"v_fmac_f32 v34, s84, v33\n"
"v_mfma_f32_16x16x32_bf16 v[220:223], a[88:91], a[104:107], v[220:223]\n"
"ds_read_b64_tr_b16 a[140:141], v5 offset:24576\n"
"v_fmac_f32 v35, s84, v48\n"
"v_mfma_f32_16x16x32_bf16 v[220:223], a[92:95], a[108:111], v[220:223]\n"
"v_lshlrev_b32 v33, 16, v49\n"
"v_and_b32 v49, 0xffff0000, v49\n"
"v_mfma_f32_16x16x32_bf16 v[216:219], a[84:87], a[108:111], v[216:219]\n"
"ds_read_b64_tr_b16 a[142:143], v5 offset:25600\n"
"v_fmac_f32 v34, s85, v33\n"
"v_mfma_f32_16x16x32_bf16 v[216:219], a[80:83], a[104:107], v[216:219]\n"
"ds_read_b64_tr_b16 a[144:145], v6 offset:16384\n"
"v_fmac_f32 v35, s85, v49\n"
"v_mfma_f32_16x16x32_bf16 v[212:215], a[72:75], a[104:107], v[212:215]\n"
"ds_read_b64_tr_b16 a[146:147], v6 offset:17408\n"
"v_lshlrev_b32 v33, 16, v50\n"
"v_mfma_f32_16x16x32_bf16 v[212:215], a[76:79], a[108:111], v[212:215]\n"
"buffer_load_dwordx4 a[244:247], v1, s[48:51], s79 offen offset:3072 sc1\n"
"v_mfma_f32_16x16x32_bf16 v[208:211], a[68:71], a[108:111], v[208:211]\n"
"ds_read_b64_tr_b16 a[148:149], v6 offset:24576\n"
"v_and_b32 v50, 0xffff0000, v50\n"
"v_mfma_f32_16x16x32_bf16 v[208:211], a[64:67], a[104:107], v[208:211]\n"
"ds_read_b64_tr_b16 a[150:151], v6 offset:25600\n"
"v_fmac_f32 v34, s86, v33\n"
"v_mfma_f32_16x16x32_bf16 v[224:227], a[64:67], a[112:115], v[224:227]\n"
"buffer_load_dwordx4 a[248:251], v1, s[48:51], s80 offen offset:3072 sc1\n"
"v_mfma_f32_16x16x32_bf16 v[224:227], a[68:71], a[116:119], v[224:227]\n"
"ds_read_b64_tr_b16 a[152:153], v7 offset:16384\n"
"v_fmac_f32 v35, s86, v50\n"
"v_mfma_f32_16x16x32_bf16 v[228:231], a[76:79], a[116:119], v[228:231]\n"
"ds_read_b64_tr_b16 a[154:155], v7 offset:17408\n"
"v_lshlrev_b32 v33, 16, v51\n"
"v_mfma_f32_16x16x32_bf16 v[228:231], a[72:75], a[112:115], v[228:231]\n"
"v_and_b32 v51, 0xffff0000, v51\n"
"v_fmac_f32 v34, s87, v33\n"
"v_mfma_f32_16x16x32_bf16 v[232:235], a[80:83], a[112:115], v[232:235]\n"
"ds_read_b64_tr_b16 a[156:157], v7 offset:24576\n"
"v_fmac_f32 v35, s87, v51\n"
"v_mfma_f32_16x16x32_bf16 v[232:235], a[84:87], a[116:119], v[232:235]\n"
"ds_read_b64_tr_b16 a[158:159], v7 offset:25600\n"
"v_lshlrev_b32 v33, 16, v52\n"
"v_mfma_f32_16x16x32_bf16 v[236:239], a[92:95], a[116:119], v[236:239]\n"
"buffer_load_dwordx4 a[252:255], v1, s[48:51], s81 offen offset:3072 sc1\n"
"v_mfma_f32_16x16x32_bf16 v[236:239], a[88:91], a[112:115], v[236:239]\n"
"v_and_b32 v52, 0xffff0000, v52\n"
"v_fmac_f32 v34, s88, v33\n"
"v_mfma_f32_16x16x32_bf16 v[252:255], a[88:91], a[120:123], v[252:255]\n"
"v_fmac_f32 v35, s88, v52\n"
"v_lshlrev_b32 v33, 16, v53\n"
"v_mfma_f32_16x16x32_bf16 v[252:255], a[92:95], a[124:127], v[252:255]\n"
"v_and_b32 v53, 0xffff0000, v53\n"
"v_fmac_f32 v34, s89, v33\n"
"v_mfma_f32_16x16x32_bf16 v[248:251], a[84:87], a[124:127], v[248:251]\n"
"v_fmac_f32 v35, s89, v53\n"
"v_lshlrev_b32 v33, 16, v54\n"
"v_mfma_f32_16x16x32_bf16 v[248:251], a[80:83], a[120:123], v[248:251]\n"
"v_and_b32 v54, 0xffff0000, v54\n"
"v_fmac_f32 v34, s90, v33\n"
"v_mfma_f32_16x16x32_bf16 v[244:247], a[72:75], a[120:123], v[244:247]\n"
"v_fmac_f32 v35, s90, v54\n"
"v_lshlrev_b32 v33, 16, v55\n"
"v_mfma_f32_16x16x32_bf16 v[244:247], a[76:79], a[124:127], v[244:247]\n"
"v_and_b32 v55, 0xffff0000, v55\n"
"v_fmac_f32 v34, s91, v33\n"
"v_mfma_f32_16x16x32_bf16 v[240:243], a[68:71], a[124:127], v[240:243]\n"
"v_fmac_f32 v35, s91, v55\n"
"v_lshlrev_b32 v25, 1, v29\n"
"v_mfma_f32_16x16x32_bf16 v[240:243], a[64:67], a[120:123], v[240:243]\n"
"s_waitcnt vmcnt(12) lgkmcnt(0)\n"
"s_barrier\n"
"s_lshl_b32 s73, s92, 11\n"
"s_add_u32 s73, s73, 0x10000\n"
"buffer_store_dwordx2 v[34:35], v25, s[48:51], s73 offen sc1\n"
"v_lshl_or_b32 v30, s64, 6, v21\n"
"v_cmp_eq_u32 vcc, 0, v30\n"
"v_mov_b32 v30, -1\n"
"s_nop 0\n"
"v_cndmask_b32 v30, v30, 0, vcc\n"
"s_lshl_b32 s73, s32, 3\n"
"s_add_u32 s73, s73, 0x4000\n"
"s_mov_b32 s92, s73\n"
"v_mfma_f32_16x16x32_bf16 a[0:3], a[96:99], a[128:131], a[0:3]\n"
"v_cvt_pk_bf16_f32 v64, v64, v65\n"
"v_cvt_pk_bf16_f32 v65, v66, v67\n"
"v_mfma_f32_16x16x32_bf16 a[0:3], a[100:103], a[132:135], a[0:3]\n"
"ds_read_b64_tr_b16 a[64:65], v12 offset:0\n"
"s_add_u32 m0, s70, 0x20000\n"
"s_add_u32 s95, s72, 0x200000\n"
"v_mfma_f32_16x16x32_bf16 a[4:7], a[108:111], a[132:135], a[4:7]\n"
"buffer_load_dwordx4 v10, s[36:39], s95 offen offset:0 lds\n"
"v_mfma_f32_16x16x32_bf16 a[4:7], a[104:107], a[128:131], a[4:7]\n"
"v_cvt_pk_bf16_f32 v66, v68, v69\n"
"v_cvt_pk_bf16_f32 v67, v70, v71\n"
"v_mfma_f32_16x16x32_bf16 a[32:35], a[112:115], a[128:131], a[32:35]\n"
"ds_read_b64_tr_b16 a[66:67], v12 offset:1024\n"
"v_cvt_pk_bf16_f32 v72, v72, v73\n"
"v_mfma_f32_16x16x32_bf16 a[32:35], a[116:119], a[132:135], a[32:35]\n"
"s_add_u32 s95, s72, 0x207c00\n"
"v_cvt_pk_bf16_f32 v73, v74, v75\n"
"v_mfma_f32_16x16x32_bf16 a[36:39], a[124:127], a[132:135], a[36:39]\n"
"buffer_load_dwordx4 v10, s[36:39], s95 offen offset:1024 lds\n"
"v_mfma_f32_16x16x32_bf16 a[36:39], a[120:123], a[128:131], a[36:39]\n"
"ds_read_b64_tr_b16 a[68:69], v12 offset:8192\n"
"v_cvt_pk_bf16_f32 v74, v76, v77\n"
"v_mfma_f32_16x16x32_bf16 a[44:47], a[120:123], a[136:139], a[44:47]\n"
"v_cvt_pk_bf16_f32 v75, v78, v79\n"
"v_cvt_pk_bf16_f32 v80, v80, v81\n"
"v_mfma_f32_16x16x32_bf16 a[44:47], a[124:127], a[140:143], a[44:47]\n"
"v_cvt_pk_bf16_f32 v81, v82, v83\n"
"v_cvt_pk_bf16_f32 v82, v84, v85\n"
"v_mfma_f32_16x16x32_bf16 a[40:43], a[116:119], a[140:143], a[40:43]\n"
"ds_read_b64_tr_b16 a[70:71], v12 offset:9216\n"
"s_add_u32 s95, s72, 0x20f800\n"
"v_mfma_f32_16x16x32_bf16 a[40:43], a[112:115], a[136:139], a[40:43]\n"
"buffer_load_dwordx4 v11, s[36:39], s95 offen offset:2048 lds\n"
"v_mfma_f32_16x16x32_bf16 a[12:15], a[104:107], a[136:139], a[12:15]\n"
"v_cvt_pk_bf16_f32 v83, v86, v87\n"
"v_cvt_pk_bf16_f32 v88, v88, v89\n"
"v_mfma_f32_16x16x32_bf16 a[12:15], a[108:111], a[140:143], a[12:15]\n"
"ds_read_b64_tr_b16 a[72:73], v13 offset:0\n"
"s_add_u32 s95, s72, 0x217400\n"
"v_mfma_f32_16x16x32_bf16 a[8:11], a[100:103], a[140:143], a[8:11]\n"
"buffer_load_dwordx4 v11, s[36:39], s95 offen offset:3072 lds\n"
"v_mfma_f32_16x16x32_bf16 a[8:11], a[96:99], a[136:139], a[8:11]\n"
"v_cvt_pk_bf16_f32 v89, v90, v91\n"
"v_cvt_pk_bf16_f32 v90, v92, v93\n"
"v_mfma_f32_16x16x32_bf16 a[16:19], a[96:99], a[144:147], a[16:19]\n"
"ds_read_b64_tr_b16 a[74:75], v13 offset:1024\n"
"v_cvt_pk_bf16_f32 v91, v94, v95\n"
"v_mfma_f32_16x16x32_bf16 a[16:19], a[100:103], a[148:151], a[16:19]\n"
"v_cvt_pk_bf16_f32 v96, v96, v97\n"
"v_cvt_pk_bf16_f32 v97, v98, v99\n"
"v_mfma_f32_16x16x32_bf16 a[20:23], a[108:111], a[148:151], a[20:23]\n"
"v_cvt_pk_bf16_f32 v98, v100, v101\n"
"v_cvt_pk_bf16_f32 v99, v102, v103\n"
"v_mfma_f32_16x16x32_bf16 a[20:23], a[104:107], a[144:147], a[20:23]\n"
"ds_read_b64_tr_b16 a[76:77], v13 offset:8192\n"
"s_add_u32 m0, s70, 0x24000\n"
"s_add_u32 s95, s72, 0x280000\n"
"v_mfma_f32_16x16x32_bf16 a[48:51], a[112:115], a[144:147], a[48:51]\n"
"buffer_load_dwordx4 v10, s[36:39], s95 offen offset:0 lds\n"
"v_mfma_f32_16x16x32_bf16 a[48:51], a[116:119], a[148:151], a[48:51]\n"
"v_cvt_pk_bf16_f32 v104, v104, v105\n"
"v_cvt_pk_bf16_f32 v105, v106, v107\n"
"v_mfma_f32_16x16x32_bf16 a[52:55], a[124:127], a[148:151], a[52:55]\n"
"ds_read_b64_tr_b16 a[78:79], v13 offset:9216\n"
"s_add_u32 s95, s72, 0x287c00\n"
"v_mfma_f32_16x16x32_bf16 a[52:55], a[120:123], a[144:147], a[52:55]\n"
"buffer_load_dwordx4 v10, s[36:39], s95 offen offset:1024 lds\n"
"v_mfma_f32_16x16x32_bf16 a[60:63], a[120:123], a[152:155], a[60:63]\n"
"v_cvt_pk_bf16_f32 v106, v108, v109\n"
"v_cvt_pk_bf16_f32 v107, v110, v111\n"
"v_mfma_f32_16x16x32_bf16 a[60:63], a[124:127], a[156:159], a[60:63]\n"
"ds_read_b64_tr_b16 a[80:81], v14 offset:0\n"
"v_cvt_pk_bf16_f32 v112, v112, v113\n"
"v_mfma_f32_16x16x32_bf16 a[56:59], a[116:119], a[156:159], a[56:59]\n"
"s_add_u32 s95, s72, 0x28f800\n"
"v_cvt_pk_bf16_f32 v113, v114, v115\n"
"v_mfma_f32_16x16x32_bf16 a[56:59], a[112:115], a[152:155], a[56:59]\n"
"buffer_load_dwordx4 v11, s[36:39], s95 offen offset:2048 lds\n"
"v_mfma_f32_16x16x32_bf16 a[28:31], a[104:107], a[152:155], a[28:31]\n"
"ds_read_b64_tr_b16 a[82:83], v14 offset:1024\n"
"v_cvt_pk_bf16_f32 v114, v116, v117\n"
"v_mfma_f32_16x16x32_bf16 a[28:31], a[108:111], a[156:159], a[28:31]\n"
"v_cvt_pk_bf16_f32 v115, v118, v119\n"
"v_cvt_pk_bf16_f32 v120, v120, v121\n"
"v_mfma_f32_16x16x32_bf16 a[24:27], a[100:103], a[156:159], a[24:27]\n"
"v_cvt_pk_bf16_f32 v121, v122, v123\n"
"v_cvt_pk_bf16_f32 v122, v124, v125\n"
"v_mfma_f32_16x16x32_bf16 a[24:27], a[96:99], a[152:155], a[24:27]\n"
"ds_read_b64_tr_b16 a[84:85], v14 offset:8192\n"
"s_add_u32 s95, s72, 0x297400\n"
"v_mfma_f32_16x16x32_bf16 v[128:131], a[160:163], a[128:131], v[128:131]\n"
"buffer_load_dwordx4 v11, s[36:39], s95 offen offset:3072 lds\n"
"v_mfma_f32_16x16x32_bf16 v[128:131], a[164:167], a[132:135], v[128:131]\n"
"v_cvt_pk_bf16_f32 v123, v126, v127\n"
"v_cvt_pk_bf16_f32 v192, v192, v193\n"
"v_mfma_f32_16x16x32_bf16 v[132:135], a[172:175], a[132:135], v[132:135]\n"
"ds_read_b64_tr_b16 a[86:87], v14 offset:9216\n"
"v_cvt_pk_bf16_f32 v193, v194, v195\n"
"v_mfma_f32_16x16x32_bf16 v[132:135], a[168:171], a[128:131], v[132:135]\n"
"v_cvt_pk_bf16_f32 v194, v196, v197\n"
"v_cvt_pk_bf16_f32 v195, v198, v199\n"
"v_mfma_f32_16x16x32_bf16 v[160:163], a[176:179], a[128:131], v[160:163]\n"
"v_cvt_pk_bf16_f32 v200, v200, v201\n"
"v_cvt_pk_bf16_f32 v201, v202, v203\n"
"v_mfma_f32_16x16x32_bf16 v[160:163], a[180:183], a[132:135], v[160:163]\n"
"ds_read_b64_tr_b16 a[88:89], v15 offset:0\n"
"v_cvt_pk_bf16_f32 v202, v204, v205\n"
"v_mfma_f32_16x16x32_bf16 v[164:167], a[188:191], a[132:135], v[164:167]\n"
"v_cvt_pk_bf16_f32 v203, v206, v207\n"
"v_cvt_pk_bf16_f32 v208, v208, v209\n"
"v_mfma_f32_16x16x32_bf16 v[164:167], a[184:187], a[128:131], v[164:167]\n"
"v_cvt_pk_bf16_f32 v209, v210, v211\n"
"v_cvt_pk_bf16_f32 v210, v212, v213\n"
"v_mfma_f32_16x16x32_bf16 v[172:175], a[184:187], a[136:139], v[172:175]\n"
"ds_read_b64_tr_b16 a[90:91], v15 offset:1024\n"
"v_cvt_pk_bf16_f32 v211, v214, v215\n"
"v_mfma_f32_16x16x32_bf16 v[172:175], a[188:191], a[140:143], v[172:175]\n"
"v_cvt_pk_bf16_f32 v216, v216, v217\n"
"v_cvt_pk_bf16_f32 v217, v218, v219\n"
"v_mfma_f32_16x16x32_bf16 v[168:171], a[180:183], a[140:143], v[168:171]\n"
"v_cvt_pk_bf16_f32 v218, v220, v221\n"
"v_cvt_pk_bf16_f32 v219, v222, v223\n"
"v_mfma_f32_16x16x32_bf16 v[168:171], a[176:179], a[136:139], v[168:171]\n"
"ds_read_b64_tr_b16 a[92:93], v15 offset:8192\n"
"v_cvt_pk_bf16_f32 v224, v224, v225\n"
"v_mfma_f32_16x16x32_bf16 v[140:143], a[168:171], a[136:139], v[140:143]\n"
"v_cvt_pk_bf16_f32 v225, v226, v227\n"
"v_cvt_pk_bf16_f32 v226, v228, v229\n"
"v_mfma_f32_16x16x32_bf16 v[140:143], a[172:175], a[140:143], v[140:143]\n"
"v_cvt_pk_bf16_f32 v227, v230, v231\n"
"v_cvt_pk_bf16_f32 v232, v232, v233\n"
"v_mfma_f32_16x16x32_bf16 v[136:139], a[164:167], a[140:143], v[136:139]\n"
"ds_read_b64_tr_b16 a[94:95], v15 offset:9216\n"
"v_cvt_pk_bf16_f32 v233, v234, v235\n"
"v_mfma_f32_16x16x32_bf16 v[136:139], a[160:163], a[136:139], v[136:139]\n"
"v_cvt_pk_bf16_f32 v234, v236, v237\n"
"v_cvt_pk_bf16_f32 v235, v238, v239\n"
"v_mfma_f32_16x16x32_bf16 v[144:147], a[160:163], a[144:147], v[144:147]\n"
"v_cvt_pk_bf16_f32 v240, v240, v241\n"
"v_cvt_pk_bf16_f32 v241, v242, v243\n"
"v_mfma_f32_16x16x32_bf16 v[144:147], a[164:167], a[148:151], v[144:147]\n"
"v_cvt_pk_bf16_f32 v242, v244, v245\n"
"v_cvt_pk_bf16_f32 v243, v246, v247\n"
"v_mfma_f32_16x16x32_bf16 v[148:151], a[172:175], a[148:151], v[148:151]\n"
"v_cvt_pk_bf16_f32 v248, v248, v249\n"
"v_cvt_pk_bf16_f32 v249, v250, v251\n"
"v_mfma_f32_16x16x32_bf16 v[148:151], a[168:171], a[144:147], v[148:151]\n"
"v_cvt_pk_bf16_f32 v250, v252, v253\n"
"v_cvt_pk_bf16_f32 v251, v254, v255\n"
"v_mfma_f32_16x16x32_bf16 v[176:179], a[176:179], a[144:147], v[176:179]\n"
"v_cvt_pk_bf16_f32 v128, v128, v129\n"
"v_cvt_pk_bf16_f32 v129, v130, v131\n"
"v_mfma_f32_16x16x32_bf16 v[176:179], a[180:183], a[148:151], v[176:179]\n"
"v_cvt_pk_bf16_f32 v130, v132, v133\n"
"v_cvt_pk_bf16_f32 v131, v134, v135\n"
"v_mfma_f32_16x16x32_bf16 v[180:183], a[188:191], a[148:151], v[180:183]\n"
"v_cvt_pk_bf16_f32 v160, v160, v161\n"
"v_cvt_pk_bf16_f32 v161, v162, v163\n"
"v_mfma_f32_16x16x32_bf16 v[180:183], a[184:187], a[144:147], v[180:183]\n"
"v_cvt_pk_bf16_f32 v162, v164, v165\n"
"v_cvt_pk_bf16_f32 v163, v166, v167\n"
"v_mfma_f32_16x16x32_bf16 v[188:191], a[184:187], a[152:155], v[188:191]\n"
"v_cvt_pk_bf16_f32 v168, v168, v169\n"
"v_cvt_pk_bf16_f32 v169, v170, v171\n"
"v_mfma_f32_16x16x32_bf16 v[188:191], a[188:191], a[156:159], v[188:191]\n"
"v_cvt_pk_bf16_f32 v170, v172, v173\n"
"v_cvt_pk_bf16_f32 v171, v174, v175\n"
"v_mfma_f32_16x16x32_bf16 v[184:187], a[180:183], a[156:159], v[184:187]\n"
"v_cvt_pk_bf16_f32 v136, v136, v137\n"
"v_cvt_pk_bf16_f32 v137, v138, v139\n"
"v_mfma_f32_16x16x32_bf16 v[184:187], a[176:179], a[152:155], v[184:187]\n"
"v_cvt_pk_bf16_f32 v138, v140, v141\n"
"v_cvt_pk_bf16_f32 v139, v142, v143\n"
"v_mfma_f32_16x16x32_bf16 v[156:159], a[168:171], a[152:155], v[156:159]\n"
"v_cvt_pk_bf16_f32 v144, v144, v145\n"
"v_cvt_pk_bf16_f32 v145, v146, v147\n"
"v_mfma_f32_16x16x32_bf16 v[156:159], a[172:175], a[156:159], v[156:159]\n"
"v_cvt_pk_bf16_f32 v146, v148, v149\n"
"v_cvt_pk_bf16_f32 v147, v150, v151\n"
"v_mfma_f32_16x16x32_bf16 v[152:155], a[164:167], a[156:159], v[152:155]\n"
"v_cvt_pk_bf16_f32 v176, v176, v177\n"
"v_cvt_pk_bf16_f32 v177, v178, v179\n"
"v_mfma_f32_16x16x32_bf16 v[152:155], a[160:163], a[152:155], v[152:155]\n"
"s_waitcnt vmcnt(37) lgkmcnt(0)\n"
"v_cvt_pk_bf16_f32 v178, v180, v181\n"
"v_mfma_f32_16x16x32_bf16 v[32:35], a[64:67], a[192:195], 0\n"
"ds_read_b64_tr_b16 a[96:97], v16 offset:0\n"
"v_cvt_pk_bf16_f32 v179, v182, v183\n"
"v_mfma_f32_16x16x32_bf16 v[32:35], a[68:71], a[208:211], v[32:35]\n"
"ds_read_b64_tr_b16 a[98:99], v16 offset:1024\n"
"s_add_u32 s75, s78, 0x1000\n"
"v_mfma_f32_16x16x32_bf16 v[36:39], a[76:79], a[208:211], 0\n"
"buffer_load_dwordx4 a[128:131], v1, s[48:51], s75 offen offset:0 sc1\n"
"v_mfma_f32_16x16x32_bf16 v[36:39], a[72:75], a[192:195], v[36:39]\n"
"ds_read_b64_tr_b16 a[100:101], v16 offset:8192\n"
"v_cvt_pk_bf16_f32 v184, v184, v185\n"
"v_mfma_f32_16x16x32_bf16 v[44:47], a[72:75], a[196:199], 0\n"
"ds_read_b64_tr_b16 a[102:103], v16 offset:9216\n"
"s_add_u32 m0, s70, 0x0\n"
"s_add_u32 s95, s72, 0x300000\n"
"v_mfma_f32_16x16x32_bf16 v[44:47], a[76:79], a[212:215], v[44:47]\n"
"buffer_load_dwordx4 v10, s[36:39], s95 offen offset:0 lds\n"
"v_mfma_f32_16x16x32_bf16 v[40:43], a[68:71], a[212:215], 0\n"
"ds_read_b64_tr_b16 a[104:105], v17 offset:0\n"
"v_cvt_pk_bf16_f32 v185, v186, v187\n"
"v_mfma_f32_16x16x32_bf16 v[40:43], a[64:67], a[196:199], v[40:43]\n"
"ds_read_b64_tr_b16 a[106:107], v17 offset:1024\n"
"v_cvt_pk_bf16_f32 v186, v188, v189\n"
"v_mfma_f32_16x16x32_bf16 v[48:51], a[64:67], a[200:203], 0\n"
"v_cvt_pk_bf16_f32 v187, v190, v191\n"
"v_cvt_pk_bf16_f32 v152, v152, v153\n"
"v_mfma_f32_16x16x32_bf16 v[48:51], a[68:71], a[216:219], v[48:51]\n"
"ds_read_b64_tr_b16 a[108:109], v17 offset:8192\n"
"v_cvt_pk_bf16_f32 v153, v154, v155\n"
"v_mfma_f32_16x16x32_bf16 v[52:55], a[76:79], a[216:219], 0\n"
"ds_read_b64_tr_b16 a[110:111], v17 offset:9216\n"
"s_add_u32 s75, s79, 0x1000\n"
"v_mfma_f32_16x16x32_bf16 v[52:55], a[72:75], a[200:203], v[52:55]\n"
"buffer_load_dwordx4 a[132:135], v1, s[48:51], s75 offen offset:0 sc1\n"
"v_mfma_f32_16x16x32_bf16 v[60:63], a[72:75], a[204:207], 0\n"
"ds_read_b64_tr_b16 a[112:113], v18 offset:0\n"
"v_cvt_pk_bf16_f32 v154, v156, v157\n"
"v_mfma_f32_16x16x32_bf16 v[60:63], a[76:79], a[220:223], v[60:63]\n"
"ds_read_b64_tr_b16 a[114:115], v18 offset:1024\n"
"s_add_u32 s95, s72, 0x307c00\n"
"v_mfma_f32_16x16x32_bf16 v[56:59], a[68:71], a[220:223], 0\n"
"buffer_load_dwordx4 v10, s[36:39], s95 offen offset:1024 lds\n"
"v_mfma_f32_16x16x32_bf16 v[56:59], a[64:67], a[204:207], v[56:59]\n"
"ds_read_b64_tr_b16 a[116:117], v18 offset:8192\n"
"v_cvt_pk_bf16_f32 v155, v158, v159\n"
"v_mfma_f32_16x16x32_bf16 v[116:119], a[80:83], a[204:207], 0\n"
"ds_read_b64_tr_b16 a[118:119], v18 offset:9216\n"
"s_add_u32 s75, s80, 0x1000\n"
"v_mfma_f32_16x16x32_bf16 v[116:119], a[84:87], a[220:223], v[116:119]\n"
"buffer_load_dwordx4 a[136:139], v1, s[48:51], s75 offen offset:0 sc1\n"
"v_mfma_f32_16x16x32_bf16 v[124:127], a[92:95], a[220:223], 0\n"
"ds_read_b64_tr_b16 a[120:121], v19 offset:0\n"
"v_accvgpr_read_b32 v24, a0\n"
"v_mfma_f32_16x16x32_bf16 v[124:127], a[88:91], a[204:207], v[124:127]\n"
"ds_read_b64_tr_b16 a[122:123], v19 offset:1024\n"
"s_add_u32 s95, s72, 0x30f800\n"
"v_mfma_f32_16x16x32_bf16 v[108:111], a[88:91], a[200:203], 0\n"
"buffer_load_dwordx4 v11, s[36:39], s95 offen offset:2048 lds\n"
"v_mfma_f32_16x16x32_bf16 v[108:111], a[92:95], a[216:219], v[108:111]\n"
"ds_read_b64_tr_b16 a[124:125], v19 offset:8192\n"
"v_accvgpr_read_b32 v25, a1\n"
"v_mfma_f32_16x16x32_bf16 v[100:103], a[84:87], a[216:219], 0\n"
"ds_read_b64_tr_b16 a[126:127], v19 offset:9216\n"
"s_add_u32 s75, s81, 0x1000\n"
"v_mfma_f32_16x16x32_bf16 v[100:103], a[80:83], a[200:203], v[100:103]\n"
"buffer_load_dwordx4 a[140:143], v1, s[48:51], s75 offen offset:0 sc1\n"
"s_add_u32 s95, s72, 0x317400\n"
"v_mfma_f32_16x16x32_bf16 v[84:87], a[80:83], a[196:199], 0\n"
"buffer_load_dwordx4 v11, s[36:39], s95 offen offset:3072 lds\n"
"v_mfma_f32_16x16x32_bf16 v[84:87], a[84:87], a[212:215], v[84:87]\n"
"v_accvgpr_read_b32 v26, a2\n"
"v_accvgpr_read_b32 v27, a3\n"
"v_mfma_f32_16x16x32_bf16 v[92:95], a[92:95], a[212:215], 0\n"
"v_cvt_pk_bf16_f32 v28, v24, v25\n"
"v_cvt_pk_bf16_f32 v29, v26, v27\n"
"v_mfma_f32_16x16x32_bf16 v[92:95], a[88:91], a[196:199], v[92:95]\n"
"v_accvgpr_write_b32 a0, v28\n"
"v_accvgpr_write_b32 a1, v29\n"
"v_mfma_f32_16x16x32_bf16 v[76:79], a[88:91], a[192:195], 0\n"
"v_accvgpr_read_b32 v24, a4\n"
"v_accvgpr_read_b32 v25, a5\n"
"v_mfma_f32_16x16x32_bf16 v[76:79], a[92:95], a[208:211], v[76:79]\n"
"v_accvgpr_read_b32 v26, a6\n"
"v_accvgpr_read_b32 v27, a7\n"
"v_mfma_f32_16x16x32_bf16 v[68:71], a[84:87], a[208:211], 0\n"
"v_cvt_pk_bf16_f32 v28, v24, v25\n"
"v_cvt_pk_bf16_f32 v29, v26, v27\n"
"v_mfma_f32_16x16x32_bf16 v[68:71], a[80:83], a[192:195], v[68:71]\n"
"s_waitcnt vmcnt(45) lgkmcnt(0)\n"
"v_accvgpr_write_b32 a2, v28\n"
"v_mfma_f32_16x16x32_bf16 v[196:199], a[96:99], a[192:195], 0\n"
"ds_read_b64_tr_b16 a[64:65], v12 offset:16384\n"
"v_accvgpr_write_b32 a3, v29\n"
"v_mfma_f32_16x16x32_bf16 v[196:199], a[100:103], a[208:211], v[196:199]\n"
"ds_read_b64_tr_b16 a[66:67], v12 offset:17408\n"
"s_add_u32 s75, s78, 0x1000\n"
"v_mfma_f32_16x16x32_bf16 v[204:207], a[108:111], a[208:211], 0\n"
"buffer_load_dwordx4 a[144:147], v1, s[48:51], s75 offen offset:1024 sc1\n"
"v_mfma_f32_16x16x32_bf16 v[204:207], a[104:107], a[192:195], v[204:207]\n"
"ds_read_b64_tr_b16 a[68:69], v12 offset:24576\n"
"v_accvgpr_read_b32 v24, a8\n"
"v_mfma_f32_16x16x32_bf16 v[220:223], a[104:107], a[196:199], 0\n"
"ds_read_b64_tr_b16 a[70:71], v12 offset:25600\n"
"s_add_u32 m0, s70, 0x4000\n"
"s_add_u32 s95, s72, 0x380000\n"
"v_mfma_f32_16x16x32_bf16 v[220:223], a[108:111], a[212:215], v[220:223]\n"
"buffer_load_dwordx4 v10, s[36:39], s95 offen offset:0 lds\n"
"v_mfma_f32_16x16x32_bf16 v[212:215], a[100:103], a[212:215], 0\n"
"ds_read_b64_tr_b16 a[72:73], v13 offset:16384\n"
"v_accvgpr_read_b32 v25, a9\n"
"v_mfma_f32_16x16x32_bf16 v[212:215], a[96:99], a[196:199], v[212:215]\n"
"ds_read_b64_tr_b16 a[74:75], v13 offset:17408\n"
"v_accvgpr_read_b32 v26, a10\n"
"v_mfma_f32_16x16x32_bf16 v[228:231], a[96:99], a[200:203], 0\n"
"v_accvgpr_read_b32 v27, a11\n"
"v_cvt_pk_bf16_f32 v28, v24, v25\n"
"v_mfma_f32_16x16x32_bf16 v[228:231], a[100:103], a[216:219], v[228:231]\n"
"ds_read_b64_tr_b16 a[76:77], v13 offset:24576\n"
"v_cvt_pk_bf16_f32 v29, v26, v27\n"
"v_mfma_f32_16x16x32_bf16 v[236:239], a[108:111], a[216:219], 0\n"
"ds_read_b64_tr_b16 a[78:79], v13 offset:25600\n"
"s_add_u32 s75, s79, 0x1000\n"
"v_mfma_f32_16x16x32_bf16 v[236:239], a[104:107], a[200:203], v[236:239]\n"
"buffer_load_dwordx4 a[148:151], v1, s[48:51], s75 offen offset:1024 sc1\n"
"v_mfma_f32_16x16x32_bf16 v[252:255], a[104:107], a[204:207], 0\n"
"ds_read_b64_tr_b16 a[80:81], v14 offset:16384\n"
"v_accvgpr_write_b32 a8, v28\n"
"v_mfma_f32_16x16x32_bf16 v[252:255], a[108:111], a[220:223], v[252:255]\n"
"ds_read_b64_tr_b16 a[82:83], v14 offset:17408\n"
"s_add_u32 s95, s72, 0x387c00\n"
"v_mfma_f32_16x16x32_bf16 v[244:247], a[100:103], a[220:223], 0\n"
"buffer_load_dwordx4 v10, s[36:39], s95 offen offset:1024 lds\n"
"v_mfma_f32_16x16x32_bf16 v[244:247], a[96:99], a[204:207], v[244:247]\n"
"ds_read_b64_tr_b16 a[84:85], v14 offset:24576\n"
"v_accvgpr_write_b32 a9, v29\n"
"v_mfma_f32_16x16x32_bf16 v[180:183], a[112:115], a[204:207], 0\n"
"ds_read_b64_tr_b16 a[86:87], v14 offset:25600\n"
"s_add_u32 s75, s80, 0x1000\n"
"v_mfma_f32_16x16x32_bf16 v[180:183], a[116:119], a[220:223], v[180:183]\n"
"buffer_load_dwordx4 a[152:155], v1, s[48:51], s75 offen offset:1024 sc1\n"
"v_mfma_f32_16x16x32_bf16 v[188:191], a[124:127], a[220:223], 0\n"
"ds_read_b64_tr_b16 a[88:89], v15 offset:16384\n"
"v_accvgpr_read_b32 v24, a12\n"
"v_mfma_f32_16x16x32_bf16 v[188:191], a[120:123], a[204:207], v[188:191]\n"
"ds_read_b64_tr_b16 a[90:91], v15 offset:17408\n"
"s_add_u32 s95, s72, 0x38f800\n"
"v_mfma_f32_16x16x32_bf16 v[172:175], a[120:123], a[200:203], 0\n"
"buffer_load_dwordx4 v11, s[36:39], s95 offen offset:2048 lds\n"
"v_mfma_f32_16x16x32_bf16 v[172:175], a[124:127], a[216:219], v[172:175]\n"
"ds_read_b64_tr_b16 a[92:93], v15 offset:24576\n"
"v_accvgpr_read_b32 v25, a13\n"
"v_mfma_f32_16x16x32_bf16 v[164:167], a[116:119], a[216:219], 0\n"
"ds_read_b64_tr_b16 a[94:95], v15 offset:25600\n"
"s_add_u32 s75, s81, 0x1000\n"
"v_mfma_f32_16x16x32_bf16 v[164:167], a[112:115], a[200:203], v[164:167]\n"
"buffer_load_dwordx4 a[156:159], v1, s[48:51], s75 offen offset:1024 sc1\n"
"s_add_u32 s95, s72, 0x397400\n"
"v_mfma_f32_16x16x32_bf16 v[148:151], a[112:115], a[196:199], 0\n"
"buffer_load_dwordx4 v11, s[36:39], s95 offen offset:3072 lds\n"
"v_mfma_f32_16x16x32_bf16 v[148:151], a[116:119], a[212:215], v[148:151]\n"
"v_accvgpr_read_b32 v26, a14\n"
"v_accvgpr_read_b32 v27, a15\n"
"v_mfma_f32_16x16x32_bf16 v[156:159], a[124:127], a[212:215], 0\n"
"v_cvt_pk_bf16_f32 v28, v24, v25\n"
"v_cvt_pk_bf16_f32 v29, v26, v27\n"
"v_mfma_f32_16x16x32_bf16 v[156:159], a[120:123], a[196:199], v[156:159]\n"
"v_accvgpr_write_b32 a10, v28\n"
"v_accvgpr_write_b32 a11, v29\n"
"v_mfma_f32_16x16x32_bf16 v[140:143], a[120:123], a[192:195], 0\n"
"v_accvgpr_read_b32 v24, a16\n"
"v_accvgpr_read_b32 v25, a17\n"
"v_mfma_f32_16x16x32_bf16 v[140:143], a[124:127], a[208:211], v[140:143]\n"
"v_accvgpr_read_b32 v26, a18\n"
"v_accvgpr_read_b32 v27, a19\n"
"v_mfma_f32_16x16x32_bf16 v[132:135], a[116:119], a[208:211], 0\n"
"v_cvt_pk_bf16_f32 v28, v24, v25\n"
"v_cvt_pk_bf16_f32 v29, v26, v27\n"
"v_mfma_f32_16x16x32_bf16 v[132:135], a[112:115], a[192:195], v[132:135]\n"
"s_waitcnt vmcnt(25) lgkmcnt(0)\n"
"v_accvgpr_write_b32 a16, v28\n"
"v_mfma_f32_16x16x32_bf16 v[32:35], a[64:67], a[224:227], v[32:35]\n"
"ds_read_b64_tr_b16 a[96:97], v16 offset:16384\n"
"v_accvgpr_write_b32 a17, v29\n"
"v_mfma_f32_16x16x32_bf16 v[32:35], a[68:71], a[240:243], v[32:35]\n"
"ds_read_b64_tr_b16 a[98:99], v16 offset:17408\n"
"s_add_u32 s75, s78, 0x1000\n"
"v_mfma_f32_16x16x32_bf16 v[36:39], a[76:79], a[240:243], v[36:39]\n"
"buffer_load_dwordx4 a[160:163], v1, s[48:51], s75 offen offset:2048 sc1\n"
"v_mfma_f32_16x16x32_bf16 v[36:39], a[72:75], a[224:227], v[36:39]\n"
"ds_read_b64_tr_b16 a[100:101], v16 offset:24576\n"
"v_accvgpr_read_b32 v24, a20\n"
"v_mfma_f32_16x16x32_bf16 v[44:47], a[72:75], a[228:231], v[44:47]\n"
"ds_read_b64_tr_b16 a[102:103], v16 offset:25600\n"
"s_add_u32 s76, s77, 0x0\n"
"v_mfma_f32_16x16x32_bf16 v[44:47], a[76:79], a[244:247], v[44:47]\n"
"buffer_store_dwordx4 v[64:67], v1, s[48:51], s76 offen sc1\n"
"v_mfma_f32_16x16x32_bf16 v[40:43], a[68:71], a[244:247], v[40:43]\n"
"ds_read_b64_tr_b16 a[104:105], v17 offset:16384\n"
"v_accvgpr_read_b32 v25, a21\n"
"v_mfma_f32_16x16x32_bf16 v[40:43], a[64:67], a[228:231], v[40:43]\n"
"ds_read_b64_tr_b16 a[106:107], v17 offset:17408\n"
"v_accvgpr_read_b32 v26, a22\n"
"v_mfma_f32_16x16x32_bf16 v[48:51], a[64:67], a[232:235], v[48:51]\n"
"v_accvgpr_read_b32 v27, a23\n"
"v_cvt_pk_bf16_f32 v28, v24, v25\n"
"v_mfma_f32_16x16x32_bf16 v[48:51], a[68:71], a[248:251], v[48:51]\n"
"ds_read_b64_tr_b16 a[108:109], v17 offset:24576\n"
"v_cvt_pk_bf16_f32 v29, v26, v27\n"
"v_mfma_f32_16x16x32_bf16 v[52:55], a[76:79], a[248:251], v[52:55]\n"
"ds_read_b64_tr_b16 a[110:111], v17 offset:25600\n"
"s_add_u32 s75, s79, 0x1000\n"
"v_mfma_f32_16x16x32_bf16 v[52:55], a[72:75], a[232:235], v[52:55]\n"
"buffer_load_dwordx4 a[164:167], v1, s[48:51], s75 offen offset:2048 sc1\n"
"v_mfma_f32_16x16x32_bf16 v[60:63], a[72:75], a[236:239], v[60:63]\n"
"ds_read_b64_tr_b16 a[112:113], v18 offset:16384\n"
"v_accvgpr_write_b32 a18, v28\n"
"v_mfma_f32_16x16x32_bf16 v[60:63], a[76:79], a[252:255], v[60:63]\n"
"ds_read_b64_tr_b16 a[114:115], v18 offset:17408\n"
"v_accvgpr_write_b32 a19, v29\n"
"v_mfma_f32_16x16x32_bf16 v[56:59], a[68:71], a[252:255], v[56:59]\n"
"v_accvgpr_read_b32 v24, a24\n"
"v_accvgpr_read_b32 v25, a25\n"
"v_mfma_f32_16x16x32_bf16 v[56:59], a[64:67], a[236:239], v[56:59]\n"
"ds_read_b64_tr_b16 a[116:117], v18 offset:24576\n"
"v_accvgpr_read_b32 v26, a26\n"
"v_mfma_f32_16x16x32_bf16 v[116:119], a[80:83], a[236:239], v[116:119]\n"
"ds_read_b64_tr_b16 a[118:119], v18 offset:25600\n"
"s_add_u32 s75, s80, 0x1000\n"
"v_mfma_f32_16x16x32_bf16 v[116:119], a[84:87], a[252:255], v[116:119]\n"
"buffer_load_dwordx4 a[168:171], v1, s[48:51], s75 offen offset:2048 sc1\n"
"v_mfma_f32_16x16x32_bf16 v[124:127], a[92:95], a[252:255], v[124:127]\n"
"ds_read_b64_tr_b16 a[120:121], v19 offset:16384\n"
"v_accvgpr_read_b32 v27, a27\n"
"v_mfma_f32_16x16x32_bf16 v[124:127], a[88:91], a[236:239], v[124:127]\n"
"ds_read_b64_tr_b16 a[122:123], v19 offset:17408\n"
"v_cvt_pk_bf16_f32 v28, v24, v25\n"
"v_mfma_f32_16x16x32_bf16 v[108:111], a[88:91], a[232:235], v[108:111]\n"
"v_cvt_pk_bf16_f32 v29, v26, v27\n"
"v_accvgpr_write_b32 a24, v28\n"
"v_mfma_f32_16x16x32_bf16 v[108:111], a[92:95], a[248:251], v[108:111]\n"
"ds_read_b64_tr_b16 a[124:125], v19 offset:24576\n"
"v_accvgpr_write_b32 a25, v29\n"
"v_mfma_f32_16x16x32_bf16 v[100:103], a[84:87], a[248:251], v[100:103]\n"
"ds_read_b64_tr_b16 a[126:127], v19 offset:25600\n"
"s_add_u32 s75, s81, 0x1000\n"
"v_mfma_f32_16x16x32_bf16 v[100:103], a[80:83], a[232:235], v[100:103]\n"
"buffer_load_dwordx4 a[172:175], v1, s[48:51], s75 offen offset:2048 sc1\n"
"v_mfma_f32_16x16x32_bf16 v[84:87], a[80:83], a[228:231], v[84:87]\n"
"v_accvgpr_read_b32 v24, a28\n"
"v_accvgpr_read_b32 v25, a29\n"
"v_mfma_f32_16x16x32_bf16 v[84:87], a[84:87], a[244:247], v[84:87]\n"
"v_accvgpr_read_b32 v26, a30\n"
"v_accvgpr_read_b32 v27, a31\n"
"v_mfma_f32_16x16x32_bf16 v[92:95], a[92:95], a[244:247], v[92:95]\n"
"v_add_u32 v12, 0x8000, v12\n"
"v_add_u32 v13, 0x8000, v13\n"
"v_mfma_f32_16x16x32_bf16 v[92:95], a[88:91], a[228:231], v[92:95]\n"
"v_add_u32 v14, 0x8000, v14\n"
"v_add_u32 v15, 0x8000, v15\n"
"v_mfma_f32_16x16x32_bf16 v[76:79], a[88:91], a[224:227], v[76:79]\n"
"v_add_u32 v16, 0x8000, v16\n"
"v_add_u32 v17, 0x8000, v17\n"
"v_mfma_f32_16x16x32_bf16 v[76:79], a[92:95], a[240:243], v[76:79]\n"
"v_add_u32 v18, 0x8000, v18\n"
"v_add_u32 v19, 0x8000, v19\n"
"v_mfma_f32_16x16x32_bf16 v[68:71], a[84:87], a[240:243], v[68:71]\n"
"v_cvt_pk_bf16_f32 v28, v24, v25\n"
"v_cvt_pk_bf16_f32 v29, v26, v27\n"
"v_mfma_f32_16x16x32_bf16 v[68:71], a[80:83], a[224:227], v[68:71]\n"
"s_waitcnt vmcnt(30) lgkmcnt(0)\n"
"v_accvgpr_write_b32 a26, v28\n"
"v_mfma_f32_16x16x32_bf16 v[196:199], a[96:99], a[224:227], v[196:199]\n"
"ds_read_b64_tr_b16 a[64:65], v12 offset:0\n"
"v_accvgpr_write_b32 a27, v29\n"
"v_mfma_f32_16x16x32_bf16 v[196:199], a[100:103], a[240:243], v[196:199]\n"
"ds_read_b64_tr_b16 a[66:67], v12 offset:1024\n"
"s_add_u32 s75, s78, 0x1000\n"
"v_mfma_f32_16x16x32_bf16 v[204:207], a[108:111], a[240:243], v[204:207]\n"
"buffer_load_dwordx4 a[176:179], v1, s[48:51], s75 offen offset:3072 sc1\n"
"v_mfma_f32_16x16x32_bf16 v[204:207], a[104:107], a[224:227], v[204:207]\n"
"ds_read_b64_tr_b16 a[68:69], v12 offset:8192\n"
"v_accvgpr_read_b32 v24, a32\n"
"v_mfma_f32_16x16x32_bf16 v[220:223], a[104:107], a[228:231], v[220:223]\n"
"ds_read_b64_tr_b16 a[70:71], v12 offset:9216\n"
"s_add_u32 s76, s77, 0x400\n"
"v_mfma_f32_16x16x32_bf16 v[220:223], a[108:111], a[244:247], v[220:223]\n"
"buffer_store_dwordx4 v[72:75], v1, s[48:51], s76 offen sc1\n"
"v_mfma_f32_16x16x32_bf16 v[212:215], a[100:103], a[244:247], v[212:215]\n"
"ds_read_b64_tr_b16 a[72:73], v13 offset:0\n"
"v_accvgpr_read_b32 v25, a33\n"
"v_mfma_f32_16x16x32_bf16 v[212:215], a[96:99], a[228:231], v[212:215]\n"
"ds_read_b64_tr_b16 a[74:75], v13 offset:1024\n"
"v_accvgpr_read_b32 v26, a34\n"
"v_mfma_f32_16x16x32_bf16 v[228:231], a[96:99], a[232:235], v[228:231]\n"
"v_accvgpr_read_b32 v27, a35\n"
"v_cvt_pk_bf16_f32 v28, v24, v25\n"
"v_mfma_f32_16x16x32_bf16 v[228:231], a[100:103], a[248:251], v[228:231]\n"
"ds_read_b64_tr_b16 a[76:77], v13 offset:8192\n"
"v_cvt_pk_bf16_f32 v29, v26, v27\n"
"v_mfma_f32_16x16x32_bf16 v[236:239], a[108:111], a[248:251], v[236:239]\n"
"ds_read_b64_tr_b16 a[78:79], v13 offset:9216\n"
"s_add_u32 s75, s79, 0x1000\n"
"v_mfma_f32_16x16x32_bf16 v[236:239], a[104:107], a[232:235], v[236:239]\n"
"buffer_load_dwordx4 a[180:183], v1, s[48:51], s75 offen offset:3072 sc1\n"
"v_mfma_f32_16x16x32_bf16 v[252:255], a[104:107], a[236:239], v[252:255]\n"
"ds_read_b64_tr_b16 a[80:81], v14 offset:0\n"
"v_accvgpr_write_b32 a32, v28\n"
"v_mfma_f32_16x16x32_bf16 v[252:255], a[108:111], a[252:255], v[252:255]\n"
"ds_read_b64_tr_b16 a[82:83], v14 offset:1024\n"
"v_accvgpr_write_b32 a33, v29\n"
"v_mfma_f32_16x16x32_bf16 v[244:247], a[100:103], a[252:255], v[244:247]\n"
"v_accvgpr_read_b32 v24, a36\n"
"v_accvgpr_read_b32 v25, a37\n"
"v_mfma_f32_16x16x32_bf16 v[244:247], a[96:99], a[236:239], v[244:247]\n"
"ds_read_b64_tr_b16 a[84:85], v14 offset:8192\n"
"v_accvgpr_read_b32 v26, a38\n"
"v_mfma_f32_16x16x32_bf16 v[180:183], a[112:115], a[236:239], v[180:183]\n"
"ds_read_b64_tr_b16 a[86:87], v14 offset:9216\n"
"s_add_u32 s75, s80, 0x1000\n"
"v_mfma_f32_16x16x32_bf16 v[180:183], a[116:119], a[252:255], v[180:183]\n"
"buffer_load_dwordx4 a[184:187], v1, s[48:51], s75 offen offset:3072 sc1\n"
"v_mfma_f32_16x16x32_bf16 v[188:191], a[124:127], a[252:255], v[188:191]\n"
"ds_read_b64_tr_b16 a[88:89], v15 offset:0\n"
"v_accvgpr_read_b32 v27, a39\n"
"v_mfma_f32_16x16x32_bf16 v[188:191], a[120:123], a[236:239], v[188:191]\n"
"ds_read_b64_tr_b16 a[90:91], v15 offset:1024\n"
"v_cvt_pk_bf16_f32 v28, v24, v25\n"
"v_mfma_f32_16x16x32_bf16 v[172:175], a[120:123], a[232:235], v[172:175]\n"
"v_cvt_pk_bf16_f32 v29, v26, v27\n"
"v_accvgpr_write_b32 a34, v28\n"
"v_mfma_f32_16x16x32_bf16 v[172:175], a[124:127], a[248:251], v[172:175]\n"
"ds_read_b64_tr_b16 a[92:93], v15 offset:8192\n"
"v_accvgpr_write_b32 a35, v29\n"
"v_mfma_f32_16x16x32_bf16 v[164:167], a[116:119], a[248:251], v[164:167]\n"
"ds_read_b64_tr_b16 a[94:95], v15 offset:9216\n"
"s_add_u32 s75, s81, 0x1000\n"
"v_mfma_f32_16x16x32_bf16 v[164:167], a[112:115], a[232:235], v[164:167]\n"
"buffer_load_dwordx4 a[188:191], v1, s[48:51], s75 offen offset:3072 sc1\n"
"v_mfma_f32_16x16x32_bf16 v[148:151], a[112:115], a[228:231], v[148:151]\n"
"v_accvgpr_read_b32 v24, a40\n"
"v_accvgpr_read_b32 v25, a41\n"
"v_mfma_f32_16x16x32_bf16 v[148:151], a[116:119], a[244:247], v[148:151]\n"
"v_accvgpr_read_b32 v26, a42\n"
"v_accvgpr_read_b32 v27, a43\n"
"v_mfma_f32_16x16x32_bf16 v[156:159], a[124:127], a[244:247], v[156:159]\n"
"v_cvt_pk_bf16_f32 v28, v24, v25\n"
"v_cvt_pk_bf16_f32 v29, v26, v27\n"
"v_mfma_f32_16x16x32_bf16 v[156:159], a[120:123], a[228:231], v[156:159]\n"
"v_accvgpr_write_b32 a40, v28\n"
"v_accvgpr_write_b32 a41, v29\n"
"v_mfma_f32_16x16x32_bf16 v[140:143], a[120:123], a[224:227], v[140:143]\n"
"v_accvgpr_read_b32 v24, a44\n"
"v_accvgpr_read_b32 v25, a45\n"
"v_mfma_f32_16x16x32_bf16 v[140:143], a[124:127], a[240:243], v[140:143]\n"
"v_accvgpr_read_b32 v26, a46\n"
"v_accvgpr_read_b32 v27, a47\n"
"v_mfma_f32_16x16x32_bf16 v[132:135], a[116:119], a[240:243], v[132:135]\n"
"v_cvt_pk_bf16_f32 v28, v24, v25\n"
"v_cvt_pk_bf16_f32 v29, v26, v27\n"
"v_mfma_f32_16x16x32_bf16 v[132:135], a[112:115], a[224:227], v[132:135]\n"
"s_waitcnt vmcnt(11) lgkmcnt(0)\n"
"v_accvgpr_write_b32 a42, v28\n"
"v_mfma_f32_16x16x32_bf16 v[32:35], a[64:67], a[128:131], v[32:35]\n"
"ds_read_b64_tr_b16 a[96:97], v16 offset:0\n"
"v_accvgpr_write_b32 a43, v29\n"
"v_mfma_f32_16x16x32_bf16 v[32:35], a[68:71], a[144:147], v[32:35]\n"
"ds_read_b64_tr_b16 a[98:99], v16 offset:1024\n"
"s_add_u32 s75, s78, 0x2000\n"
"v_mfma_f32_16x16x32_bf16 v[36:39], a[76:79], a[144:147], v[36:39]\n"
"buffer_load_dwordx4 a[192:195], v1, s[48:51], s75 offen offset:0 sc1\n"
"v_mfma_f32_16x16x32_bf16 v[36:39], a[72:75], a[128:131], v[36:39]\n"
"ds_read_b64_tr_b16 a[100:101], v16 offset:8192\n"
"v_accvgpr_read_b32 v24, a48\n"
"v_mfma_f32_16x16x32_bf16 v[44:47], a[72:75], a[132:135], v[44:47]\n"
"ds_read_b64_tr_b16 a[102:103], v16 offset:9216\n"
"s_add_u32 s76, s77, 0x1000\n"
"v_mfma_f32_16x16x32_bf16 v[44:47], a[76:79], a[148:151], v[44:47]\n"
"buffer_store_dwordx4 v[80:83], v1, s[48:51], s76 offen sc1\n"
"v_mfma_f32_16x16x32_bf16 v[40:43], a[68:71], a[148:151], v[40:43]\n"
"ds_read_b64_tr_b16 a[104:105], v17 offset:0\n"
"v_accvgpr_read_b32 v25, a49\n"
"v_mfma_f32_16x16x32_bf16 v[40:43], a[64:67], a[132:135], v[40:43]\n"
"ds_read_b64_tr_b16 a[106:107], v17 offset:1024\n"
"v_accvgpr_read_b32 v26, a50\n"
"v_mfma_f32_16x16x32_bf16 v[48:51], a[64:67], a[136:139], v[48:51]\n"
"v_accvgpr_read_b32 v27, a51\n"
"v_cvt_pk_bf16_f32 v28, v24, v25\n"
"v_mfma_f32_16x16x32_bf16 v[48:51], a[68:71], a[152:155], v[48:51]\n"
"ds_read_b64_tr_b16 a[108:109], v17 offset:8192\n"
"v_cvt_pk_bf16_f32 v29, v26, v27\n"
"v_mfma_f32_16x16x32_bf16 v[52:55], a[76:79], a[152:155], v[52:55]\n"
"ds_read_b64_tr_b16 a[110:111], v17 offset:9216\n"
"s_add_u32 s75, s79, 0x2000\n"
"v_mfma_f32_16x16x32_bf16 v[52:55], a[72:75], a[136:139], v[52:55]\n"
"buffer_load_dwordx4 a[196:199], v1, s[48:51], s75 offen offset:0 sc1\n"
"v_mfma_f32_16x16x32_bf16 v[60:63], a[72:75], a[140:143], v[60:63]\n"
"ds_read_b64_tr_b16 a[112:113], v18 offset:0\n"
"v_accvgpr_write_b32 a48, v28\n"
"v_mfma_f32_16x16x32_bf16 v[60:63], a[76:79], a[156:159], v[60:63]\n"
"ds_read_b64_tr_b16 a[114:115], v18 offset:1024\n"
"v_accvgpr_write_b32 a49, v29\n"
"v_mfma_f32_16x16x32_bf16 v[56:59], a[68:71], a[156:159], v[56:59]\n"
"v_accvgpr_read_b32 v24, a52\n"
"v_accvgpr_read_b32 v25, a53\n"
"v_mfma_f32_16x16x32_bf16 v[56:59], a[64:67], a[140:143], v[56:59]\n"
"ds_read_b64_tr_b16 a[116:117], v18 offset:8192\n"
"v_accvgpr_read_b32 v26, a54\n"
"v_mfma_f32_16x16x32_bf16 v[116:119], a[80:83], a[140:143], v[116:119]\n"
"ds_read_b64_tr_b16 a[118:119], v18 offset:9216\n"
"s_add_u32 s75, s80, 0x2000\n"
"v_mfma_f32_16x16x32_bf16 v[116:119], a[84:87], a[156:159], v[116:119]\n"
"buffer_load_dwordx4 a[200:203], v1, s[48:51], s75 offen offset:0 sc1\n"
"v_mfma_f32_16x16x32_bf16 v[124:127], a[92:95], a[156:159], v[124:127]\n"
"ds_read_b64_tr_b16 a[120:121], v19 offset:0\n"
"v_accvgpr_read_b32 v27, a55\n"
"v_mfma_f32_16x16x32_bf16 v[124:127], a[88:91], a[140:143], v[124:127]\n"
"ds_read_b64_tr_b16 a[122:123], v19 offset:1024\n"
"v_cvt_pk_bf16_f32 v28, v24, v25\n"
"v_mfma_f32_16x16x32_bf16 v[108:111], a[88:91], a[136:139], v[108:111]\n"
"v_cvt_pk_bf16_f32 v29, v26, v27\n"
"v_accvgpr_write_b32 a50, v28\n"
"v_mfma_f32_16x16x32_bf16 v[108:111], a[92:95], a[152:155], v[108:111]\n"
"ds_read_b64_tr_b16 a[124:125], v19 offset:8192\n"
"v_accvgpr_write_b32 a51, v29\n"
"v_mfma_f32_16x16x32_bf16 v[100:103], a[84:87], a[152:155], v[100:103]\n"
"ds_read_b64_tr_b16 a[126:127], v19 offset:9216\n"
"s_add_u32 s75, s81, 0x2000\n"
"v_mfma_f32_16x16x32_bf16 v[100:103], a[80:83], a[136:139], v[100:103]\n"
"buffer_load_dwordx4 a[204:207], v1, s[48:51], s75 offen offset:0 sc1\n"
"v_mfma_f32_16x16x32_bf16 v[84:87], a[80:83], a[132:135], v[84:87]\n"
"v_accvgpr_read_b32 v24, a56\n"
"v_accvgpr_read_b32 v25, a57\n"
"v_mfma_f32_16x16x32_bf16 v[84:87], a[84:87], a[148:151], v[84:87]\n"
"v_accvgpr_read_b32 v26, a58\n"
"v_accvgpr_read_b32 v27, a59\n"
"v_mfma_f32_16x16x32_bf16 v[92:95], a[92:95], a[148:151], v[92:95]\n"
"v_cvt_pk_bf16_f32 v28, v24, v25\n"
"v_cvt_pk_bf16_f32 v29, v26, v27\n"
"v_mfma_f32_16x16x32_bf16 v[92:95], a[88:91], a[132:135], v[92:95]\n"
"v_accvgpr_write_b32 a56, v28\n"
"v_accvgpr_write_b32 a57, v29\n"
"v_mfma_f32_16x16x32_bf16 v[76:79], a[88:91], a[128:131], v[76:79]\n"
"v_accvgpr_read_b32 v24, a60\n"
"v_accvgpr_read_b32 v25, a61\n"
"v_mfma_f32_16x16x32_bf16 v[76:79], a[92:95], a[144:147], v[76:79]\n"
"v_accvgpr_read_b32 v26, a62\n"
"v_accvgpr_read_b32 v27, a63\n"
"v_mfma_f32_16x16x32_bf16 v[68:71], a[84:87], a[144:147], v[68:71]\n"
"v_cvt_pk_bf16_f32 v28, v24, v25\n"
"v_cvt_pk_bf16_f32 v29, v26, v27\n"
"v_mfma_f32_16x16x32_bf16 v[68:71], a[80:83], a[128:131], v[68:71]\n"
"s_waitcnt vmcnt(16) lgkmcnt(0)\n"
"v_accvgpr_write_b32 a58, v28\n"
"v_mfma_f32_16x16x32_bf16 v[196:199], a[96:99], a[128:131], v[196:199]\n"
"ds_read_b64_tr_b16 a[64:65], v12 offset:16384\n"
"v_accvgpr_write_b32 a59, v29\n"
"v_mfma_f32_16x16x32_bf16 v[196:199], a[100:103], a[144:147], v[196:199]\n"
"ds_read_b64_tr_b16 a[66:67], v12 offset:17408\n"
"s_add_u32 s75, s78, 0x2000\n"
"v_mfma_f32_16x16x32_bf16 v[204:207], a[108:111], a[144:147], v[204:207]\n"
"buffer_load_dwordx4 a[208:211], v1, s[48:51], s75 offen offset:1024 sc1\n"
"v_mfma_f32_16x16x32_bf16 v[204:207], a[104:107], a[128:131], v[204:207]\n"
"ds_read_b64_tr_b16 a[68:69], v12 offset:24576\n"
"v_mfma_f32_16x16x32_bf16 v[220:223], a[104:107], a[132:135], v[220:223]\n"
"ds_read_b64_tr_b16 a[70:71], v12 offset:25600\n"
"s_add_u32 s76, s77, 0x1400\n"
"v_mfma_f32_16x16x32_bf16 v[220:223], a[108:111], a[148:151], v[220:223]\n"
"buffer_store_dwordx4 v[88:91], v1, s[48:51], s76 offen sc1\n"
"v_mfma_f32_16x16x32_bf16 v[212:215], a[100:103], a[148:151], v[212:215]\n"
"ds_read_b64_tr_b16 a[72:73], v13 offset:16384\n"
"v_mfma_f32_16x16x32_bf16 v[212:215], a[96:99], a[132:135], v[212:215]\n"
"ds_read_b64_tr_b16 a[74:75], v13 offset:17408\n"
"v_mfma_f32_16x16x32_bf16 v[228:231], a[96:99], a[136:139], v[228:231]\n"
"v_mfma_f32_16x16x32_bf16 v[228:231], a[100:103], a[152:155], v[228:231]\n"
"ds_read_b64_tr_b16 a[76:77], v13 offset:24576\n"
"v_mfma_f32_16x16x32_bf16 v[236:239], a[108:111], a[152:155], v[236:239]\n"
"ds_read_b64_tr_b16 a[78:79], v13 offset:25600\n"
"s_add_u32 s75, s79, 0x2000\n"
"v_mfma_f32_16x16x32_bf16 v[236:239], a[104:107], a[136:139], v[236:239]\n"
"buffer_load_dwordx4 a[212:215], v1, s[48:51], s75 offen offset:1024 sc1\n"
"v_mfma_f32_16x16x32_bf16 v[252:255], a[104:107], a[140:143], v[252:255]\n"
"ds_read_b64_tr_b16 a[80:81], v14 offset:16384\n"
"v_mfma_f32_16x16x32_bf16 v[252:255], a[108:111], a[156:159], v[252:255]\n"
"ds_read_b64_tr_b16 a[82:83], v14 offset:17408\n"
"v_mfma_f32_16x16x32_bf16 v[244:247], a[100:103], a[156:159], v[244:247]\n"
"v_mfma_f32_16x16x32_bf16 v[244:247], a[96:99], a[140:143], v[244:247]\n"
"ds_read_b64_tr_b16 a[84:85], v14 offset:24576\n"
"v_mfma_f32_16x16x32_bf16 v[180:183], a[112:115], a[140:143], v[180:183]\n"
"ds_read_b64_tr_b16 a[86:87], v14 offset:25600\n"
"s_add_u32 s75, s80, 0x2000\n"
"v_mfma_f32_16x16x32_bf16 v[180:183], a[116:119], a[156:159], v[180:183]\n"
"buffer_load_dwordx4 a[216:219], v1, s[48:51], s75 offen offset:1024 sc1\n"
"v_mfma_f32_16x16x32_bf16 v[188:191], a[124:127], a[156:159], v[188:191]\n"
"ds_read_b64_tr_b16 a[88:89], v15 offset:16384\n"
"v_mfma_f32_16x16x32_bf16 v[188:191], a[120:123], a[140:143], v[188:191]\n"
"ds_read_b64_tr_b16 a[90:91], v15 offset:17408\n"
"v_mfma_f32_16x16x32_bf16 v[172:175], a[120:123], a[136:139], v[172:175]\n"
"v_mfma_f32_16x16x32_bf16 v[172:175], a[124:127], a[152:155], v[172:175]\n"
"ds_read_b64_tr_b16 a[92:93], v15 offset:24576\n"
"v_mfma_f32_16x16x32_bf16 v[164:167], a[116:119], a[152:155], v[164:167]\n"
"ds_read_b64_tr_b16 a[94:95], v15 offset:25600\n"
"s_add_u32 s75, s81, 0x2000\n"
"v_mfma_f32_16x16x32_bf16 v[164:167], a[112:115], a[136:139], v[164:167]\n"
"buffer_load_dwordx4 a[220:223], v1, s[48:51], s75 offen offset:1024 sc1\n"
"v_mfma_f32_16x16x32_bf16 v[148:151], a[112:115], a[132:135], v[148:151]\n"
"v_mfma_f32_16x16x32_bf16 v[148:151], a[116:119], a[148:151], v[148:151]\n"
"v_mfma_f32_16x16x32_bf16 v[156:159], a[124:127], a[148:151], v[156:159]\n"
"v_mfma_f32_16x16x32_bf16 v[156:159], a[120:123], a[132:135], v[156:159]\n"
"v_mfma_f32_16x16x32_bf16 v[140:143], a[120:123], a[128:131], v[140:143]\n"
"v_mfma_f32_16x16x32_bf16 v[140:143], a[124:127], a[144:147], v[140:143]\n"
"v_mfma_f32_16x16x32_bf16 v[132:135], a[116:119], a[144:147], v[132:135]\n"
"v_mfma_f32_16x16x32_bf16 v[132:135], a[112:115], a[128:131], v[132:135]\n"
"s_waitcnt vmcnt(10) lgkmcnt(0)\n"
"v_mfma_f32_16x16x32_bf16 v[32:35], a[64:67], a[160:163], v[32:35]\n"
"ds_read_b64_tr_b16 a[96:97], v16 offset:16384\n"
"v_mfma_f32_16x16x32_bf16 v[32:35], a[68:71], a[176:179], v[32:35]\n"
"ds_read_b64_tr_b16 a[98:99], v16 offset:17408\n"
"s_add_u32 s75, s78, 0x2000\n"
"v_mfma_f32_16x16x32_bf16 v[36:39], a[76:79], a[176:179], v[36:39]\n"
"buffer_load_dwordx4 a[224:227], v1, s[48:51], s75 offen offset:2048 sc1\n"
"v_mfma_f32_16x16x32_bf16 v[36:39], a[72:75], a[160:163], v[36:39]\n"
"ds_read_b64_tr_b16 a[100:101], v16 offset:24576\n"
"v_mfma_f32_16x16x32_bf16 v[44:47], a[72:75], a[164:167], v[44:47]\n"
"ds_read_b64_tr_b16 a[102:103], v16 offset:25600\n"
"s_add_u32 s76, s77, 0x4000\n"
"v_mfma_f32_16x16x32_bf16 v[44:47], a[76:79], a[180:183], v[44:47]\n"
"buffer_store_dwordx4 v[96:99], v1, s[48:51], s76 offen sc1\n"
"v_mfma_f32_16x16x32_bf16 v[40:43], a[68:71], a[180:183], v[40:43]\n"
"ds_read_b64_tr_b16 a[104:105], v17 offset:16384\n"
"v_mfma_f32_16x16x32_bf16 v[40:43], a[64:67], a[164:167], v[40:43]\n"
"ds_read_b64_tr_b16 a[106:107], v17 offset:17408\n"
"v_mfma_f32_16x16x32_bf16 v[48:51], a[64:67], a[168:171], v[48:51]\n"
"v_mfma_f32_16x16x32_bf16 v[48:51], a[68:71], a[184:187], v[48:51]\n"
"ds_read_b64_tr_b16 a[108:109], v17 offset:24576\n"
"v_mfma_f32_16x16x32_bf16 v[52:55], a[76:79], a[184:187], v[52:55]\n"
"ds_read_b64_tr_b16 a[110:111], v17 offset:25600\n"
"s_add_u32 s75, s79, 0x2000\n"
"v_mfma_f32_16x16x32_bf16 v[52:55], a[72:75], a[168:171], v[52:55]\n"
"buffer_load_dwordx4 a[228:231], v1, s[48:51], s75 offen offset:2048 sc1\n"
"v_mfma_f32_16x16x32_bf16 v[60:63], a[72:75], a[172:175], v[60:63]\n"
"ds_read_b64_tr_b16 a[112:113], v18 offset:16384\n"
"v_mfma_f32_16x16x32_bf16 v[60:63], a[76:79], a[188:191], v[60:63]\n"
"ds_read_b64_tr_b16 a[114:115], v18 offset:17408\n"
"v_mfma_f32_16x16x32_bf16 v[56:59], a[68:71], a[188:191], v[56:59]\n"
"v_mfma_f32_16x16x32_bf16 v[56:59], a[64:67], a[172:175], v[56:59]\n"
"ds_read_b64_tr_b16 a[116:117], v18 offset:24576\n"
"v_mfma_f32_16x16x32_bf16 v[116:119], a[80:83], a[172:175], v[116:119]\n"
"ds_read_b64_tr_b16 a[118:119], v18 offset:25600\n"
"s_add_u32 s75, s80, 0x2000\n"
"v_mfma_f32_16x16x32_bf16 v[116:119], a[84:87], a[188:191], v[116:119]\n"
"buffer_load_dwordx4 a[232:235], v1, s[48:51], s75 offen offset:2048 sc1\n"
"v_mfma_f32_16x16x32_bf16 v[124:127], a[92:95], a[188:191], v[124:127]\n"
"ds_read_b64_tr_b16 a[120:121], v19 offset:16384\n"
"v_mfma_f32_16x16x32_bf16 v[124:127], a[88:91], a[172:175], v[124:127]\n"
"ds_read_b64_tr_b16 a[122:123], v19 offset:17408\n"
"v_mfma_f32_16x16x32_bf16 v[108:111], a[88:91], a[168:171], v[108:111]\n"
"v_mfma_f32_16x16x32_bf16 v[108:111], a[92:95], a[184:187], v[108:111]\n"
"ds_read_b64_tr_b16 a[124:125], v19 offset:24576\n"
"v_mfma_f32_16x16x32_bf16 v[100:103], a[84:87], a[184:187], v[100:103]\n"
"ds_read_b64_tr_b16 a[126:127], v19 offset:25600\n"
"s_add_u32 s75, s81, 0x2000\n"
"v_mfma_f32_16x16x32_bf16 v[100:103], a[80:83], a[168:171], v[100:103]\n"
"buffer_load_dwordx4 a[236:239], v1, s[48:51], s75 offen offset:2048 sc1\n"
"v_mfma_f32_16x16x32_bf16 v[84:87], a[80:83], a[164:167], v[84:87]\n"
"v_mfma_f32_16x16x32_bf16 v[84:87], a[84:87], a[180:183], v[84:87]\n"
"v_mfma_f32_16x16x32_bf16 v[92:95], a[92:95], a[180:183], v[92:95]\n"
"v_add_u32 v12, 0x8000, v12\n"
"v_add_u32 v13, 0x8000, v13\n"
"v_mfma_f32_16x16x32_bf16 v[92:95], a[88:91], a[164:167], v[92:95]\n"
"v_add_u32 v14, 0x8000, v14\n"
"v_add_u32 v15, 0x8000, v15\n"
"v_mfma_f32_16x16x32_bf16 v[76:79], a[88:91], a[160:163], v[76:79]\n"
"v_add_u32 v16, 0x8000, v16\n"
"v_add_u32 v17, 0x8000, v17\n"
"v_mfma_f32_16x16x32_bf16 v[76:79], a[92:95], a[176:179], v[76:79]\n"
"v_add_u32 v18, 0x8000, v18\n"
"v_add_u32 v19, 0x8000, v19\n"
"v_mfma_f32_16x16x32_bf16 v[68:71], a[84:87], a[176:179], v[68:71]\n"
"v_mfma_f32_16x16x32_bf16 v[68:71], a[80:83], a[160:163], v[68:71]\n"
"s_waitcnt vmcnt(15) lgkmcnt(0)\n"
"s_barrier\n"
"v_mfma_f32_16x16x32_bf16 v[196:199], a[96:99], a[160:163], v[196:199]\n"
"ds_read_b64_tr_b16 a[64:65], v12 offset:0\n"
"v_mfma_f32_16x16x32_bf16 v[196:199], a[100:103], a[176:179], v[196:199]\n"
"ds_read_b64_tr_b16 a[66:67], v12 offset:1024\n"
"v_mfma_f32_16x16x32_bf16 v[204:207], a[108:111], a[176:179], v[204:207]\n"
"ds_read_b64_tr_b16 a[68:69], v12 offset:8192\n"
"v_mfma_f32_16x16x32_bf16 v[204:207], a[104:107], a[160:163], v[204:207]\n"
"ds_read_b64_tr_b16 a[70:71], v12 offset:9216\n"
"v_mfma_f32_16x16x32_bf16 v[220:223], a[104:107], a[164:167], v[220:223]\n"
"ds_read_b64_tr_b16 a[72:73], v13 offset:0\n"
"v_mfma_f32_16x16x32_bf16 v[220:223], a[108:111], a[180:183], v[220:223]\n"
"ds_read_b64_tr_b16 a[74:75], v13 offset:1024\n"
"v_mfma_f32_16x16x32_bf16 v[212:215], a[100:103], a[180:183], v[212:215]\n"
"ds_read_b64_tr_b16 a[76:77], v13 offset:8192\n"
"v_mfma_f32_16x16x32_bf16 v[212:215], a[96:99], a[164:167], v[212:215]\n"
"ds_read_b64_tr_b16 a[78:79], v13 offset:9216\n"
"v_mfma_f32_16x16x32_bf16 v[228:231], a[96:99], a[168:171], v[228:231]\n"
"ds_read_b64_tr_b16 a[80:81], v14 offset:0\n"
"v_mfma_f32_16x16x32_bf16 v[228:231], a[100:103], a[184:187], v[228:231]\n"
"ds_read_b64_tr_b16 a[82:83], v14 offset:1024\n"
"v_mfma_f32_16x16x32_bf16 v[236:239], a[108:111], a[184:187], v[236:239]\n"
"ds_read_b64_tr_b16 a[84:85], v14 offset:8192\n"
"v_mfma_f32_16x16x32_bf16 v[236:239], a[104:107], a[168:171], v[236:239]\n"
"ds_read_b64_tr_b16 a[86:87], v14 offset:9216\n"
"v_mfma_f32_16x16x32_bf16 v[252:255], a[104:107], a[172:175], v[252:255]\n"
"ds_read_b64_tr_b16 a[88:89], v15 offset:0\n"
"v_mfma_f32_16x16x32_bf16 v[252:255], a[108:111], a[188:191], v[252:255]\n"
"ds_read_b64_tr_b16 a[90:91], v15 offset:1024\n"
"v_mfma_f32_16x16x32_bf16 v[244:247], a[100:103], a[188:191], v[244:247]\n"
"ds_read_b64_tr_b16 a[92:93], v15 offset:8192\n"
"v_mfma_f32_16x16x32_bf16 v[244:247], a[96:99], a[172:175], v[244:247]\n"
"ds_read_b64_tr_b16 a[94:95], v15 offset:9216\n"
"v_mfma_f32_16x16x32_bf16 v[180:183], a[112:115], a[172:175], v[180:183]\n"
"v_mfma_f32_16x16x32_bf16 v[180:183], a[116:119], a[188:191], v[180:183]\n"
"ds_write_b128 v20, v[64:67] offset:0\n"
"s_add_u32 s75, s78, 0x2000\n"
"v_mfma_f32_16x16x32_bf16 v[188:191], a[124:127], a[188:191], v[188:191]\n"
"buffer_load_dwordx4 a[240:243], v1, s[48:51], s75 offen offset:3072 sc1\n"
"s_add_u32 s76, s77, 0x4400\n"
"v_mfma_f32_16x16x32_bf16 v[188:191], a[120:123], a[172:175], v[188:191]\n"
"buffer_store_dwordx4 v[104:107], v1, s[48:51], s76 offen sc1\n"
"v_mov_b32 v2, s68\n"
"v_mov_b32 v3, s69\n"
"v_mfma_f32_16x16x32_bf16 v[172:175], a[120:123], a[168:171], v[172:175]\n"
"buffer_store_dwordx2 v[2:3], v30, s[48:51], s92 offen sc1\n"
"v_mfma_f32_16x16x32_bf16 v[172:175], a[124:127], a[184:187], v[172:175]\n"
"ds_write_b128 v20, v[72:75] offset:1024\n"
"s_add_u32 s75, s79, 0x2000\n"
"v_mfma_f32_16x16x32_bf16 v[164:167], a[116:119], a[184:187], v[164:167]\n"
"buffer_load_dwordx4 a[244:247], v1, s[48:51], s75 offen offset:3072 sc1\n"
"v_mfma_f32_16x16x32_bf16 v[164:167], a[112:115], a[168:171], v[164:167]\n"
"s_add_u32 s75, s80, 0x2000\n"
"v_mfma_f32_16x16x32_bf16 v[148:151], a[112:115], a[164:167], v[148:151]\n"
"buffer_load_dwordx4 a[248:251], v1, s[48:51], s75 offen offset:3072 sc1\n"
"v_mfma_f32_16x16x32_bf16 v[148:151], a[116:119], a[180:183], v[148:151]\n"
"ds_write_b128 v20, v[80:83] offset:4096\n"
"v_mfma_f32_16x16x32_bf16 v[156:159], a[124:127], a[180:183], v[156:159]\n"
"s_add_u32 s75, s81, 0x2000\n"
"v_mfma_f32_16x16x32_bf16 v[156:159], a[120:123], a[164:167], v[156:159]\n"
"buffer_load_dwordx4 a[252:255], v1, s[48:51], s75 offen offset:3072 sc1\n"
"v_mfma_f32_16x16x32_bf16 v[140:143], a[120:123], a[160:163], v[140:143]\n"
"v_mfma_f32_16x16x32_bf16 v[140:143], a[124:127], a[176:179], v[140:143]\n"
"ds_write_b128 v20, v[88:91] offset:5120\n"
"v_mfma_f32_16x16x32_bf16 v[132:135], a[116:119], a[176:179], v[132:135]\n"
"v_mfma_f32_16x16x32_bf16 v[132:135], a[112:115], a[160:163], v[132:135]\n"
"s_waitcnt vmcnt(11) lgkmcnt(0)\n"
"v_mfma_f32_16x16x32_bf16 v[32:35], a[64:67], a[192:195], v[32:35]\n"
"ds_read_b64_tr_b16 a[96:97], v16 offset:0\n"
"v_mfma_f32_16x16x32_bf16 v[32:35], a[68:71], a[208:211], v[32:35]\n"
"ds_read_b64_tr_b16 a[98:99], v16 offset:1024\n"
"v_mfma_f32_16x16x32_bf16 v[36:39], a[76:79], a[208:211], v[36:39]\n"
"ds_read_b64_tr_b16 a[100:101], v16 offset:8192\n"
"v_mfma_f32_16x16x32_bf16 v[36:39], a[72:75], a[192:195], v[36:39]\n"
"ds_read_b64_tr_b16 a[102:103], v16 offset:9216\n"
"v_mfma_f32_16x16x32_bf16 v[44:47], a[72:75], a[196:199], v[44:47]\n"
"ds_read_b64_tr_b16 a[104:105], v17 offset:0\n"
"v_mfma_f32_16x16x32_bf16 v[44:47], a[76:79], a[212:215], v[44:47]\n"
"ds_read_b64_tr_b16 a[106:107], v17 offset:1024\n"
"v_mfma_f32_16x16x32_bf16 v[40:43], a[68:71], a[212:215], v[40:43]\n"
"ds_read_b64_tr_b16 a[108:109], v17 offset:8192\n"
"v_mfma_f32_16x16x32_bf16 v[40:43], a[64:67], a[196:199], v[40:43]\n"
"ds_read_b64_tr_b16 a[110:111], v17 offset:9216\n"
"v_mfma_f32_16x16x32_bf16 v[48:51], a[64:67], a[200:203], v[48:51]\n"
"ds_read_b64_tr_b16 a[112:113], v18 offset:0\n"
"v_mfma_f32_16x16x32_bf16 v[48:51], a[68:71], a[216:219], v[48:51]\n"
"ds_read_b64_tr_b16 a[114:115], v18 offset:1024\n"
"v_mfma_f32_16x16x32_bf16 v[52:55], a[76:79], a[216:219], v[52:55]\n"
"ds_read_b64_tr_b16 a[116:117], v18 offset:8192\n"
"v_mfma_f32_16x16x32_bf16 v[52:55], a[72:75], a[200:203], v[52:55]\n"
"ds_read_b64_tr_b16 a[118:119], v18 offset:9216\n"
"v_mfma_f32_16x16x32_bf16 v[60:63], a[72:75], a[204:207], v[60:63]\n"
"ds_read_b64_tr_b16 a[120:121], v19 offset:0\n"
"v_mfma_f32_16x16x32_bf16 v[60:63], a[76:79], a[220:223], v[60:63]\n"
"ds_read_b64_tr_b16 a[122:123], v19 offset:1024\n"
"v_mfma_f32_16x16x32_bf16 v[56:59], a[68:71], a[220:223], v[56:59]\n"
"ds_read_b64_tr_b16 a[124:125], v19 offset:8192\n"
"v_mfma_f32_16x16x32_bf16 v[56:59], a[64:67], a[204:207], v[56:59]\n"
"ds_read_b64_tr_b16 a[126:127], v19 offset:9216\n"
"v_mfma_f32_16x16x32_bf16 v[116:119], a[80:83], a[204:207], v[116:119]\n"
"v_mfma_f32_16x16x32_bf16 v[116:119], a[84:87], a[220:223], v[116:119]\n"
"ds_write_b128 v20, v[96:99] offset:16384\n"
"s_add_u32 s75, s78, 0x3000\n"
"v_mfma_f32_16x16x32_bf16 v[124:127], a[92:95], a[220:223], v[124:127]\n"
"buffer_load_dwordx4 a[128:131], v1, s[48:51], s75 offen offset:0 sc1\n"
"s_add_u32 s76, s77, 0x5000\n"
"v_mfma_f32_16x16x32_bf16 v[124:127], a[88:91], a[204:207], v[124:127]\n"
"buffer_store_dwordx4 v[112:115], v1, s[48:51], s76 offen sc1\n"
"v_mfma_f32_16x16x32_bf16 v[108:111], a[88:91], a[200:203], v[108:111]\n"
"v_mfma_f32_16x16x32_bf16 v[108:111], a[92:95], a[216:219], v[108:111]\n"
"ds_write_b128 v20, v[104:107] offset:17408\n"
"s_add_u32 s75, s79, 0x3000\n"
"v_mfma_f32_16x16x32_bf16 v[100:103], a[84:87], a[216:219], v[100:103]\n"
"buffer_load_dwordx4 a[132:135], v1, s[48:51], s75 offen offset:0 sc1\n"
"v_mfma_f32_16x16x32_bf16 v[100:103], a[80:83], a[200:203], v[100:103]\n"
"s_add_u32 s75, s80, 0x3000\n"
"v_mfma_f32_16x16x32_bf16 v[84:87], a[80:83], a[196:199], v[84:87]\n"
"buffer_load_dwordx4 a[136:139], v1, s[48:51], s75 offen offset:0 sc1\n"
"v_mfma_f32_16x16x32_bf16 v[84:87], a[84:87], a[212:215], v[84:87]\n"
"ds_write_b128 v20, v[112:115] offset:20480\n"
"v_mfma_f32_16x16x32_bf16 v[92:95], a[92:95], a[212:215], v[92:95]\n"
"s_add_u32 s75, s81, 0x3000\n"
"v_mfma_f32_16x16x32_bf16 v[92:95], a[88:91], a[196:199], v[92:95]\n"
"buffer_load_dwordx4 a[140:143], v1, s[48:51], s75 offen offset:0 sc1\n"
"v_mfma_f32_16x16x32_bf16 v[76:79], a[88:91], a[192:195], v[76:79]\n"
"v_mfma_f32_16x16x32_bf16 v[76:79], a[92:95], a[208:211], v[76:79]\n"
"ds_write_b128 v20, v[120:123] offset:21504\n"
"v_mfma_f32_16x16x32_bf16 v[68:71], a[84:87], a[208:211], v[68:71]\n"
"v_mfma_f32_16x16x32_bf16 v[68:71], a[80:83], a[192:195], v[68:71]\n"
"s_waitcnt vmcnt(16) lgkmcnt(0)\n"
"v_mfma_f32_16x16x32_bf16 v[196:199], a[96:99], a[192:195], v[196:199]\n"
"ds_read_b64_tr_b16 a[64:65], v12 offset:16384\n"
"v_mfma_f32_16x16x32_bf16 v[196:199], a[100:103], a[208:211], v[196:199]\n"
"ds_read_b64_tr_b16 a[66:67], v12 offset:17408\n"
"v_mfma_f32_16x16x32_bf16 v[204:207], a[108:111], a[208:211], v[204:207]\n"
"ds_read_b64_tr_b16 a[68:69], v12 offset:24576\n"
"v_mfma_f32_16x16x32_bf16 v[204:207], a[104:107], a[192:195], v[204:207]\n"
"ds_read_b64_tr_b16 a[70:71], v12 offset:25600\n"
"v_mfma_f32_16x16x32_bf16 v[220:223], a[104:107], a[196:199], v[220:223]\n"
"ds_read_b64_tr_b16 a[72:73], v13 offset:16384\n"
"v_mfma_f32_16x16x32_bf16 v[220:223], a[108:111], a[212:215], v[220:223]\n"
"ds_read_b64_tr_b16 a[74:75], v13 offset:17408\n"
"v_mfma_f32_16x16x32_bf16 v[212:215], a[100:103], a[212:215], v[212:215]\n"
"ds_read_b64_tr_b16 a[76:77], v13 offset:24576\n"
"v_mfma_f32_16x16x32_bf16 v[212:215], a[96:99], a[196:199], v[212:215]\n"
"ds_read_b64_tr_b16 a[78:79], v13 offset:25600\n"
"v_mfma_f32_16x16x32_bf16 v[228:231], a[96:99], a[200:203], v[228:231]\n"
"ds_read_b64_tr_b16 a[80:81], v14 offset:16384\n"
"v_mfma_f32_16x16x32_bf16 v[228:231], a[100:103], a[216:219], v[228:231]\n"
"ds_read_b64_tr_b16 a[82:83], v14 offset:17408\n"
"v_mfma_f32_16x16x32_bf16 v[236:239], a[108:111], a[216:219], v[236:239]\n"
"ds_read_b64_tr_b16 a[84:85], v14 offset:24576\n"
"v_mfma_f32_16x16x32_bf16 v[236:239], a[104:107], a[200:203], v[236:239]\n"
"ds_read_b64_tr_b16 a[86:87], v14 offset:25600\n"
"v_mfma_f32_16x16x32_bf16 v[252:255], a[104:107], a[204:207], v[252:255]\n"
"ds_read_b64_tr_b16 a[88:89], v15 offset:16384\n"
"v_mfma_f32_16x16x32_bf16 v[252:255], a[108:111], a[220:223], v[252:255]\n"
"ds_read_b64_tr_b16 a[90:91], v15 offset:17408\n"
"v_mfma_f32_16x16x32_bf16 v[244:247], a[100:103], a[220:223], v[244:247]\n"
"ds_read_b64_tr_b16 a[92:93], v15 offset:24576\n"
"v_mfma_f32_16x16x32_bf16 v[244:247], a[96:99], a[204:207], v[244:247]\n"
"ds_read_b64_tr_b16 a[94:95], v15 offset:25600\n"
"v_mfma_f32_16x16x32_bf16 v[180:183], a[112:115], a[204:207], v[180:183]\n"
"v_mfma_f32_16x16x32_bf16 v[180:183], a[116:119], a[220:223], v[180:183]\n"
"ds_write_b128 v22, v[128:131] offset:8192\n"
"s_add_u32 s75, s78, 0x3000\n"
"v_mfma_f32_16x16x32_bf16 v[188:191], a[124:127], a[220:223], v[188:191]\n"
"buffer_load_dwordx4 a[144:147], v1, s[48:51], s75 offen offset:1024 sc1\n"
"s_add_u32 s76, s77, 0x5400\n"
"v_mfma_f32_16x16x32_bf16 v[188:191], a[120:123], a[204:207], v[188:191]\n"
"buffer_store_dwordx4 v[120:123], v1, s[48:51], s76 offen sc1\n"
"v_mfma_f32_16x16x32_bf16 v[172:175], a[120:123], a[200:203], v[172:175]\n"
"v_mfma_f32_16x16x32_bf16 v[172:175], a[124:127], a[216:219], v[172:175]\n"
"ds_write_b128 v22, v[136:139] offset:9216\n"
"s_add_u32 s75, s79, 0x3000\n"
"v_mfma_f32_16x16x32_bf16 v[164:167], a[116:119], a[216:219], v[164:167]\n"
"buffer_load_dwordx4 a[148:151], v1, s[48:51], s75 offen offset:1024 sc1\n"
"v_mfma_f32_16x16x32_bf16 v[164:167], a[112:115], a[200:203], v[164:167]\n"
"s_add_u32 s75, s80, 0x3000\n"
"v_mfma_f32_16x16x32_bf16 v[148:151], a[112:115], a[196:199], v[148:151]\n"
"buffer_load_dwordx4 a[152:155], v1, s[48:51], s75 offen offset:1024 sc1\n"
"v_mfma_f32_16x16x32_bf16 v[148:151], a[116:119], a[212:215], v[148:151]\n"
"ds_write_b128 v22, v[144:147] offset:10240\n"
"v_mfma_f32_16x16x32_bf16 v[156:159], a[124:127], a[212:215], v[156:159]\n"
"s_add_u32 s75, s81, 0x3000\n"
"v_mfma_f32_16x16x32_bf16 v[156:159], a[120:123], a[196:199], v[156:159]\n"
"buffer_load_dwordx4 a[156:159], v1, s[48:51], s75 offen offset:1024 sc1\n"
"v_mfma_f32_16x16x32_bf16 v[140:143], a[120:123], a[192:195], v[140:143]\n"
"v_mfma_f32_16x16x32_bf16 v[140:143], a[124:127], a[208:211], v[140:143]\n"
"ds_write_b128 v22, v[152:155] offset:11264\n"
"v_mfma_f32_16x16x32_bf16 v[132:135], a[116:119], a[208:211], v[132:135]\n"
"v_mfma_f32_16x16x32_bf16 v[132:135], a[112:115], a[192:195], v[132:135]\n"
"s_waitcnt vmcnt(10) lgkmcnt(0)\n"
"v_mfma_f32_16x16x32_bf16 v[32:35], a[64:67], a[224:227], v[32:35]\n"
"ds_read_b64_tr_b16 a[96:97], v16 offset:16384\n"
"v_mfma_f32_16x16x32_bf16 v[32:35], a[68:71], a[240:243], v[32:35]\n"
"ds_read_b64_tr_b16 a[98:99], v16 offset:17408\n"
"v_mfma_f32_16x16x32_bf16 v[36:39], a[76:79], a[240:243], v[36:39]\n"
"ds_read_b64_tr_b16 a[100:101], v16 offset:24576\n"
"v_mfma_f32_16x16x32_bf16 v[36:39], a[72:75], a[224:227], v[36:39]\n"
"ds_read_b64_tr_b16 a[102:103], v16 offset:25600\n"
"v_mfma_f32_16x16x32_bf16 v[44:47], a[72:75], a[228:231], v[44:47]\n"
"ds_read_b64_tr_b16 a[104:105], v17 offset:16384\n"
"v_mfma_f32_16x16x32_bf16 v[44:47], a[76:79], a[244:247], v[44:47]\n"
"ds_read_b64_tr_b16 a[106:107], v17 offset:17408\n"
"v_mfma_f32_16x16x32_bf16 v[40:43], a[68:71], a[244:247], v[40:43]\n"
"ds_read_b64_tr_b16 a[108:109], v17 offset:24576\n"
"v_mfma_f32_16x16x32_bf16 v[40:43], a[64:67], a[228:231], v[40:43]\n"
"ds_read_b64_tr_b16 a[110:111], v17 offset:25600\n"
"v_mfma_f32_16x16x32_bf16 v[48:51], a[64:67], a[232:235], v[48:51]\n"
"ds_read_b64_tr_b16 a[112:113], v18 offset:16384\n"
"v_mfma_f32_16x16x32_bf16 v[48:51], a[68:71], a[248:251], v[48:51]\n"
"ds_read_b64_tr_b16 a[114:115], v18 offset:17408\n"
"v_mfma_f32_16x16x32_bf16 v[52:55], a[76:79], a[248:251], v[52:55]\n"
"ds_read_b64_tr_b16 a[116:117], v18 offset:24576\n"
"v_mfma_f32_16x16x32_bf16 v[52:55], a[72:75], a[232:235], v[52:55]\n"
"ds_read_b64_tr_b16 a[118:119], v18 offset:25600\n"
"v_mfma_f32_16x16x32_bf16 v[60:63], a[72:75], a[236:239], v[60:63]\n"
"ds_read_b64_tr_b16 a[120:121], v19 offset:16384\n"
"v_mfma_f32_16x16x32_bf16 v[60:63], a[76:79], a[252:255], v[60:63]\n"
"ds_read_b64_tr_b16 a[122:123], v19 offset:17408\n"
"v_mfma_f32_16x16x32_bf16 v[56:59], a[68:71], a[252:255], v[56:59]\n"
"ds_read_b64_tr_b16 a[124:125], v19 offset:24576\n"
"v_mfma_f32_16x16x32_bf16 v[56:59], a[64:67], a[236:239], v[56:59]\n"
"ds_read_b64_tr_b16 a[126:127], v19 offset:25600\n"
"v_mfma_f32_16x16x32_bf16 v[116:119], a[80:83], a[236:239], v[116:119]\n"
"v_mfma_f32_16x16x32_bf16 v[116:119], a[84:87], a[252:255], v[116:119]\n"
"ds_write_b128 v22, v[160:163] offset:24576\n"
"s_add_u32 s75, s78, 0x3000\n"
"v_mfma_f32_16x16x32_bf16 v[124:127], a[92:95], a[252:255], v[124:127]\n"
"buffer_load_dwordx4 a[160:163], v1, s[48:51], s75 offen offset:2048 sc1\n"
"s_add_u32 s76, s94, 0x2000\n"
"v_mfma_f32_16x16x32_bf16 v[124:127], a[88:91], a[236:239], v[124:127]\n"
"buffer_store_dwordx4 v[128:131], v1, s[48:51], s76 offen sc1\n"
"v_mfma_f32_16x16x32_bf16 v[108:111], a[88:91], a[232:235], v[108:111]\n"
"v_mfma_f32_16x16x32_bf16 v[108:111], a[92:95], a[248:251], v[108:111]\n"
"ds_write_b128 v22, v[168:171] offset:25600\n"
"s_add_u32 s75, s79, 0x3000\n"
"v_mfma_f32_16x16x32_bf16 v[100:103], a[84:87], a[248:251], v[100:103]\n"
"buffer_load_dwordx4 a[164:167], v1, s[48:51], s75 offen offset:2048 sc1\n"
"v_mfma_f32_16x16x32_bf16 v[100:103], a[80:83], a[232:235], v[100:103]\n"
"s_add_u32 s75, s80, 0x3000\n"
"v_mfma_f32_16x16x32_bf16 v[84:87], a[80:83], a[228:231], v[84:87]\n"
"buffer_load_dwordx4 a[168:171], v1, s[48:51], s75 offen offset:2048 sc1\n"
"v_mfma_f32_16x16x32_bf16 v[84:87], a[84:87], a[244:247], v[84:87]\n"
"ds_write_b128 v22, v[176:179] offset:26624\n"
"v_mfma_f32_16x16x32_bf16 v[92:95], a[92:95], a[244:247], v[92:95]\n"
"s_add_u32 s75, s81, 0x3000\n"
"v_add_u32 v12, 0xfffe0000, v12\n"
"v_add_u32 v13, 0xfffe0000, v13\n"
"v_mfma_f32_16x16x32_bf16 v[92:95], a[88:91], a[228:231], v[92:95]\n"
"buffer_load_dwordx4 a[172:175], v1, s[48:51], s75 offen offset:2048 sc1\n"
"v_add_u32 v14, 0xfffe0000, v14\n"
"v_add_u32 v15, 0xfffe0000, v15\n"
"v_mfma_f32_16x16x32_bf16 v[76:79], a[88:91], a[224:227], v[76:79]\n"
"v_add_u32 v16, 0xfffe0000, v16\n"
"v_add_u32 v17, 0xfffe0000, v17\n"
"v_mfma_f32_16x16x32_bf16 v[76:79], a[92:95], a[240:243], v[76:79]\n"
"ds_write_b128 v22, v[184:187] offset:27648\n"
"v_add_u32 v18, 0xfffe0000, v18\n"
"v_add_u32 v19, 0xfffe0000, v19\n"
"v_mfma_f32_16x16x32_bf16 v[68:71], a[84:87], a[240:243], v[68:71]\n"
"v_mfma_f32_16x16x32_bf16 v[68:71], a[80:83], a[224:227], v[68:71]\n"
"s_waitcnt vmcnt(15) lgkmcnt(0)\n"
"s_barrier\n"
"v_mfma_f32_16x16x32_bf16 v[196:199], a[96:99], a[224:227], v[196:199]\n"
"ds_read_b64_tr_b16 a[64:65], v12 offset:0\n"
"v_add_u32 v20, 0x10000, v20\n"
"v_add_u32 v22, 0x10000, v22\n"
"v_mfma_f32_16x16x32_bf16 v[196:199], a[100:103], a[240:243], v[196:199]\n"
"ds_read_b64_tr_b16 a[66:67], v12 offset:1024\n"
"v_mfma_f32_16x16x32_bf16 v[204:207], a[108:111], a[240:243], v[204:207]\n"
"ds_read_b64_tr_b16 a[68:69], v12 offset:8192\n"
"v_mfma_f32_16x16x32_bf16 v[204:207], a[104:107], a[224:227], v[204:207]\n"
"ds_read_b64_tr_b16 a[70:71], v12 offset:9216\n"
"v_mfma_f32_16x16x32_bf16 v[220:223], a[104:107], a[228:231], v[220:223]\n"
"ds_read_b64_tr_b16 a[72:73], v13 offset:0\n"
"v_mfma_f32_16x16x32_bf16 v[220:223], a[108:111], a[244:247], v[220:223]\n"
"ds_read_b64_tr_b16 a[74:75], v13 offset:1024\n"
"v_mfma_f32_16x16x32_bf16 v[212:215], a[100:103], a[244:247], v[212:215]\n"
"ds_read_b64_tr_b16 a[76:77], v13 offset:8192\n"
"v_mfma_f32_16x16x32_bf16 v[212:215], a[96:99], a[228:231], v[212:215]\n"
"ds_read_b64_tr_b16 a[78:79], v13 offset:9216\n"
"v_mfma_f32_16x16x32_bf16 v[228:231], a[96:99], a[232:235], v[228:231]\n"
"ds_read_b64_tr_b16 a[80:81], v14 offset:0\n"
"v_mfma_f32_16x16x32_bf16 v[228:231], a[100:103], a[248:251], v[228:231]\n"
"ds_read_b64_tr_b16 a[82:83], v14 offset:1024\n"
"v_mfma_f32_16x16x32_bf16 v[236:239], a[108:111], a[248:251], v[236:239]\n"
"ds_read_b64_tr_b16 a[84:85], v14 offset:8192\n"
"v_mfma_f32_16x16x32_bf16 v[236:239], a[104:107], a[232:235], v[236:239]\n"
"ds_read_b64_tr_b16 a[86:87], v14 offset:9216\n"
"v_mfma_f32_16x16x32_bf16 v[252:255], a[104:107], a[236:239], v[252:255]\n"
"ds_read_b64_tr_b16 a[88:89], v15 offset:0\n"
"v_mfma_f32_16x16x32_bf16 v[252:255], a[108:111], a[252:255], v[252:255]\n"
"ds_read_b64_tr_b16 a[90:91], v15 offset:1024\n"
"v_mfma_f32_16x16x32_bf16 v[244:247], a[100:103], a[252:255], v[244:247]\n"
"ds_read_b64_tr_b16 a[92:93], v15 offset:8192\n"
"v_mfma_f32_16x16x32_bf16 v[244:247], a[96:99], a[236:239], v[244:247]\n"
"ds_read_b64_tr_b16 a[94:95], v15 offset:9216\n"
"v_mfma_f32_16x16x32_bf16 v[180:183], a[112:115], a[236:239], v[180:183]\n"
"v_mfma_f32_16x16x32_bf16 v[180:183], a[116:119], a[252:255], v[180:183]\n"
"ds_write_b128 v20, v[192:195] offset:0\n"
"s_add_u32 s75, s78, 0x3000\n"
"v_mfma_f32_16x16x32_bf16 v[188:191], a[124:127], a[252:255], v[188:191]\n"
"buffer_load_dwordx4 a[176:179], v1, s[48:51], s75 offen offset:3072 sc1\n"
"s_add_u32 s76, s94, 0x2400\n"
"v_mfma_f32_16x16x32_bf16 v[188:191], a[120:123], a[236:239], v[188:191]\n"
"buffer_store_dwordx4 v[136:139], v1, s[48:51], s76 offen sc1\n"
"v_mfma_f32_16x16x32_bf16 v[172:175], a[120:123], a[232:235], v[172:175]\n"
"v_mfma_f32_16x16x32_bf16 v[172:175], a[124:127], a[248:251], v[172:175]\n"
"ds_write_b128 v20, v[200:203] offset:1024\n"
"s_add_u32 s75, s79, 0x3000\n"
"v_mfma_f32_16x16x32_bf16 v[164:167], a[116:119], a[248:251], v[164:167]\n"
"buffer_load_dwordx4 a[180:183], v1, s[48:51], s75 offen offset:3072 sc1\n"
"v_mfma_f32_16x16x32_bf16 v[164:167], a[112:115], a[232:235], v[164:167]\n"
"s_add_u32 s75, s80, 0x3000\n"
"v_mfma_f32_16x16x32_bf16 v[148:151], a[112:115], a[228:231], v[148:151]\n"
"buffer_load_dwordx4 a[184:187], v1, s[48:51], s75 offen offset:3072 sc1\n"
"v_mfma_f32_16x16x32_bf16 v[148:151], a[116:119], a[244:247], v[148:151]\n"
"ds_write_b128 v20, v[208:211] offset:4096\n"
"v_mfma_f32_16x16x32_bf16 v[156:159], a[124:127], a[244:247], v[156:159]\n"
"s_add_u32 s75, s81, 0x3000\n"
"v_mfma_f32_16x16x32_bf16 v[156:159], a[120:123], a[228:231], v[156:159]\n"
"buffer_load_dwordx4 a[188:191], v1, s[48:51], s75 offen offset:3072 sc1\n"
"v_mfma_f32_16x16x32_bf16 v[140:143], a[120:123], a[224:227], v[140:143]\n"
"v_mfma_f32_16x16x32_bf16 v[140:143], a[124:127], a[240:243], v[140:143]\n"
"ds_write_b128 v20, v[216:219] offset:5120\n"
"v_mfma_f32_16x16x32_bf16 v[132:135], a[116:119], a[240:243], v[132:135]\n"
"v_mfma_f32_16x16x32_bf16 v[132:135], a[112:115], a[224:227], v[132:135]\n"
"s_waitcnt vmcnt(10) lgkmcnt(0)\n"
"v_mfma_f32_16x16x32_bf16 v[32:35], a[64:67], a[128:131], v[32:35]\n"
"ds_read_b64_tr_b16 a[96:97], v16 offset:0\n"
"v_accvgpr_write_b32 a192, 0\n"
"v_mfma_f32_16x16x32_bf16 v[32:35], a[68:71], a[144:147], v[32:35]\n"
"ds_read_b64_tr_b16 a[98:99], v16 offset:1024\n"
"v_accvgpr_write_b32 a193, 0\n"
"v_mfma_f32_16x16x32_bf16 v[36:39], a[76:79], a[144:147], v[36:39]\n"
"ds_read_b64_tr_b16 a[100:101], v16 offset:8192\n"
"v_accvgpr_write_b32 a194, 0\n"
"v_mfma_f32_16x16x32_bf16 v[36:39], a[72:75], a[128:131], v[36:39]\n"
"ds_read_b64_tr_b16 a[102:103], v16 offset:9216\n"
"v_accvgpr_write_b32 a195, 0\n"
"v_mfma_f32_16x16x32_bf16 v[44:47], a[72:75], a[132:135], v[44:47]\n"
"ds_read_b64_tr_b16 a[104:105], v17 offset:0\n"
"v_accvgpr_write_b32 a196, 0\n"
"v_mfma_f32_16x16x32_bf16 v[44:47], a[76:79], a[148:151], v[44:47]\n"
"ds_read_b64_tr_b16 a[106:107], v17 offset:1024\n"
"v_accvgpr_write_b32 a197, 0\n"
"v_mfma_f32_16x16x32_bf16 v[40:43], a[68:71], a[148:151], v[40:43]\n"
"ds_read_b64_tr_b16 a[108:109], v17 offset:8192\n"
"v_accvgpr_write_b32 a198, 0\n"
"v_mfma_f32_16x16x32_bf16 v[40:43], a[64:67], a[132:135], v[40:43]\n"
"ds_read_b64_tr_b16 a[110:111], v17 offset:9216\n"
"v_accvgpr_write_b32 a199, 0\n"
"v_mfma_f32_16x16x32_bf16 v[48:51], a[64:67], a[136:139], v[48:51]\n"
"ds_read_b64_tr_b16 a[112:113], v18 offset:0\n"
"v_accvgpr_write_b32 a200, 0\n"
"v_mfma_f32_16x16x32_bf16 v[48:51], a[68:71], a[152:155], v[48:51]\n"
"ds_read_b64_tr_b16 a[114:115], v18 offset:1024\n"
"v_accvgpr_write_b32 a201, 0\n"
"v_mfma_f32_16x16x32_bf16 v[52:55], a[76:79], a[152:155], v[52:55]\n"
"ds_read_b64_tr_b16 a[116:117], v18 offset:8192\n"
"v_accvgpr_write_b32 a202, 0\n"
"v_mfma_f32_16x16x32_bf16 v[52:55], a[72:75], a[136:139], v[52:55]\n"
"ds_read_b64_tr_b16 a[118:119], v18 offset:9216\n"
"v_accvgpr_write_b32 a203, 0\n"
"v_mfma_f32_16x16x32_bf16 v[60:63], a[72:75], a[140:143], v[60:63]\n"
"ds_read_b64_tr_b16 a[120:121], v19 offset:0\n"
"v_accvgpr_write_b32 a204, 0\n"
"v_mfma_f32_16x16x32_bf16 v[60:63], a[76:79], a[156:159], v[60:63]\n"
"ds_read_b64_tr_b16 a[122:123], v19 offset:1024\n"
"v_accvgpr_write_b32 a205, 0\n"
"v_mfma_f32_16x16x32_bf16 v[56:59], a[68:71], a[156:159], v[56:59]\n"
"ds_read_b64_tr_b16 a[124:125], v19 offset:8192\n"
"v_accvgpr_write_b32 a206, 0\n"
"v_mfma_f32_16x16x32_bf16 v[56:59], a[64:67], a[140:143], v[56:59]\n"
"ds_read_b64_tr_b16 a[126:127], v19 offset:9216\n"
"v_accvgpr_write_b32 a207, 0\n"
"v_mfma_f32_16x16x32_bf16 v[116:119], a[80:83], a[140:143], v[116:119]\n"
"v_accvgpr_write_b32 a208, 0\n"
"v_accvgpr_write_b32 a209, 0\n"
"v_mfma_f32_16x16x32_bf16 v[116:119], a[84:87], a[156:159], v[116:119]\n"
"ds_write_b128 v20, v[224:227] offset:16384\n"
"v_mfma_f32_16x16x32_bf16 v[124:127], a[92:95], a[156:159], v[124:127]\n"
"s_add_u32 s76, s94, 0x2800\n"
"v_accvgpr_write_b32 a210, 0\n"
"v_mfma_f32_16x16x32_bf16 v[124:127], a[88:91], a[140:143], v[124:127]\n"
"buffer_store_dwordx4 v[144:147], v1, s[48:51], s76 offen sc1\n"
"v_mfma_f32_16x16x32_bf16 v[108:111], a[88:91], a[136:139], v[108:111]\n"
"v_accvgpr_write_b32 a211, 0\n"
"v_accvgpr_write_b32 a212, 0\n"
"v_mfma_f32_16x16x32_bf16 v[108:111], a[92:95], a[152:155], v[108:111]\n"
"ds_write_b128 v20, v[232:235] offset:17408\n"
"v_mfma_f32_16x16x32_bf16 v[100:103], a[84:87], a[152:155], v[100:103]\n"
"s_add_u32 s76, s94, 0x2c00\n"
"v_accvgpr_write_b32 a213, 0\n"
"v_mfma_f32_16x16x32_bf16 v[100:103], a[80:83], a[136:139], v[100:103]\n"
"buffer_store_dwordx4 v[152:155], v1, s[48:51], s76 offen sc1\n"
"v_mfma_f32_16x16x32_bf16 v[84:87], a[80:83], a[132:135], v[84:87]\n"
"v_accvgpr_write_b32 a214, 0\n"
"v_accvgpr_write_b32 a215, 0\n"
"v_mfma_f32_16x16x32_bf16 v[84:87], a[84:87], a[148:151], v[84:87]\n"
"ds_write_b128 v20, v[240:243] offset:20480\n"
"v_mfma_f32_16x16x32_bf16 v[92:95], a[92:95], a[148:151], v[92:95]\n"
"s_add_u32 s76, s94, 0x6000\n"
"v_accvgpr_write_b32 a216, 0\n"
"v_mfma_f32_16x16x32_bf16 v[92:95], a[88:91], a[132:135], v[92:95]\n"
"buffer_store_dwordx4 v[160:163], v1, s[48:51], s76 offen sc1\n"
"v_mfma_f32_16x16x32_bf16 v[76:79], a[88:91], a[128:131], v[76:79]\n"
"v_accvgpr_write_b32 a217, 0\n"
"v_accvgpr_write_b32 a218, 0\n"
"v_mfma_f32_16x16x32_bf16 v[76:79], a[92:95], a[144:147], v[76:79]\n"
"ds_write_b128 v20, v[248:251] offset:21504\n"
"v_mfma_f32_16x16x32_bf16 v[68:71], a[84:87], a[144:147], v[68:71]\n"
"v_accvgpr_write_b32 a219, 0\n"
"v_accvgpr_write_b32 a220, 0\n"
"v_mfma_f32_16x16x32_bf16 v[68:71], a[80:83], a[128:131], v[68:71]\n"
"s_waitcnt vmcnt(13) lgkmcnt(0)\n"
"v_accvgpr_write_b32 a221, 0\n"
"v_mfma_f32_16x16x32_bf16 v[196:199], a[96:99], a[128:131], v[196:199]\n"
"ds_read_b64_tr_b16 a[64:65], v12 offset:16384\n"
"v_accvgpr_write_b32 a222, 0\n"
"v_mfma_f32_16x16x32_bf16 v[196:199], a[100:103], a[144:147], v[196:199]\n"
"ds_read_b64_tr_b16 a[66:67], v12 offset:17408\n"
"v_accvgpr_write_b32 a223, 0\n"
"v_mfma_f32_16x16x32_bf16 v[204:207], a[108:111], a[144:147], v[204:207]\n"
"ds_read_b64_tr_b16 a[68:69], v12 offset:24576\n"
"v_accvgpr_write_b32 a224, 0\n"
"v_mfma_f32_16x16x32_bf16 v[204:207], a[104:107], a[128:131], v[204:207]\n"
"ds_read_b64_tr_b16 a[70:71], v12 offset:25600\n"
"v_accvgpr_write_b32 a225, 0\n"
"v_mfma_f32_16x16x32_bf16 v[220:223], a[104:107], a[132:135], v[220:223]\n"
"ds_read_b64_tr_b16 a[72:73], v13 offset:16384\n"
"v_accvgpr_write_b32 a226, 0\n"
"v_mfma_f32_16x16x32_bf16 v[220:223], a[108:111], a[148:151], v[220:223]\n"
"ds_read_b64_tr_b16 a[74:75], v13 offset:17408\n"
"v_accvgpr_write_b32 a227, 0\n"
"v_mfma_f32_16x16x32_bf16 v[212:215], a[100:103], a[148:151], v[212:215]\n"
"ds_read_b64_tr_b16 a[76:77], v13 offset:24576\n"
"v_accvgpr_write_b32 a228, 0\n"
"v_mfma_f32_16x16x32_bf16 v[212:215], a[96:99], a[132:135], v[212:215]\n"
"ds_read_b64_tr_b16 a[78:79], v13 offset:25600\n"
"v_accvgpr_write_b32 a229, 0\n"
"v_mfma_f32_16x16x32_bf16 v[228:231], a[96:99], a[136:139], v[228:231]\n"
"ds_read_b64_tr_b16 a[80:81], v14 offset:16384\n"
"v_accvgpr_write_b32 a230, 0\n"
"v_mfma_f32_16x16x32_bf16 v[228:231], a[100:103], a[152:155], v[228:231]\n"
"ds_read_b64_tr_b16 a[82:83], v14 offset:17408\n"
"v_accvgpr_write_b32 a231, 0\n"
"v_mfma_f32_16x16x32_bf16 v[236:239], a[108:111], a[152:155], v[236:239]\n"
"ds_read_b64_tr_b16 a[84:85], v14 offset:24576\n"
"v_accvgpr_write_b32 a232, 0\n"
"v_mfma_f32_16x16x32_bf16 v[236:239], a[104:107], a[136:139], v[236:239]\n"
"ds_read_b64_tr_b16 a[86:87], v14 offset:25600\n"
"v_accvgpr_write_b32 a233, 0\n"
"v_mfma_f32_16x16x32_bf16 v[252:255], a[104:107], a[140:143], v[252:255]\n"
"ds_read_b64_tr_b16 a[88:89], v15 offset:16384\n"
"v_accvgpr_write_b32 a234, 0\n"
"v_mfma_f32_16x16x32_bf16 v[252:255], a[108:111], a[156:159], v[252:255]\n"
"ds_read_b64_tr_b16 a[90:91], v15 offset:17408\n"
"v_accvgpr_write_b32 a235, 0\n"
"v_mfma_f32_16x16x32_bf16 v[244:247], a[100:103], a[156:159], v[244:247]\n"
"ds_read_b64_tr_b16 a[92:93], v15 offset:24576\n"
"v_accvgpr_write_b32 a236, 0\n"
"v_mfma_f32_16x16x32_bf16 v[244:247], a[96:99], a[140:143], v[244:247]\n"
"ds_read_b64_tr_b16 a[94:95], v15 offset:25600\n"
"v_accvgpr_write_b32 a237, 0\n"
"v_mfma_f32_16x16x32_bf16 v[180:183], a[112:115], a[140:143], v[180:183]\n"
"v_accvgpr_write_b32 a238, 0\n"
"v_accvgpr_write_b32 a239, 0\n"
"v_mfma_f32_16x16x32_bf16 v[180:183], a[116:119], a[156:159], v[180:183]\n"
"ds_write_b128 v22, a[0:3] offset:8192\n"
"v_mfma_f32_16x16x32_bf16 v[188:191], a[124:127], a[156:159], v[188:191]\n"
"s_add_u32 s76, s94, 0x6400\n"
"v_accvgpr_write_b32 a240, 0\n"
"v_mfma_f32_16x16x32_bf16 v[188:191], a[120:123], a[140:143], v[188:191]\n"
"buffer_store_dwordx4 v[168:171], v1, s[48:51], s76 offen sc1\n"
"v_mfma_f32_16x16x32_bf16 v[172:175], a[120:123], a[136:139], v[172:175]\n"
"v_accvgpr_write_b32 a241, 0\n"
"v_accvgpr_write_b32 a242, 0\n"
"v_mfma_f32_16x16x32_bf16 v[172:175], a[124:127], a[152:155], v[172:175]\n"
"ds_write_b128 v22, a[8:11] offset:9216\n"
"v_mfma_f32_16x16x32_bf16 v[164:167], a[116:119], a[152:155], v[164:167]\n"
"s_add_u32 s76, s94, 0x6800\n"
"v_accvgpr_write_b32 a243, 0\n"
"v_mfma_f32_16x16x32_bf16 v[164:167], a[112:115], a[136:139], v[164:167]\n"
"buffer_store_dwordx4 v[176:179], v1, s[48:51], s76 offen sc1\n"
"v_mfma_f32_16x16x32_bf16 v[148:151], a[112:115], a[132:135], v[148:151]\n"
"v_accvgpr_write_b32 a244, 0\n"
"v_accvgpr_write_b32 a245, 0\n"
"v_mfma_f32_16x16x32_bf16 v[148:151], a[116:119], a[148:151], v[148:151]\n"
"ds_write_b128 v22, a[16:19] offset:10240\n"
"v_mfma_f32_16x16x32_bf16 v[156:159], a[124:127], a[148:151], v[156:159]\n"
"s_add_u32 s76, s94, 0x6c00\n"
"v_accvgpr_write_b32 a246, 0\n"
"v_mfma_f32_16x16x32_bf16 v[156:159], a[120:123], a[132:135], v[156:159]\n"
"buffer_store_dwordx4 v[184:187], v1, s[48:51], s76 offen sc1\n"
"v_mfma_f32_16x16x32_bf16 v[140:143], a[120:123], a[128:131], v[140:143]\n"
"v_accvgpr_write_b32 a247, 0\n"
"v_accvgpr_write_b32 a248, 0\n"
"v_mfma_f32_16x16x32_bf16 v[140:143], a[124:127], a[144:147], v[140:143]\n"
"ds_write_b128 v22, a[24:27] offset:11264\n"
"v_mfma_f32_16x16x32_bf16 v[132:135], a[116:119], a[144:147], v[132:135]\n"
"v_accvgpr_write_b32 a249, 0\n"
"v_accvgpr_write_b32 a250, 0\n"
"v_mfma_f32_16x16x32_bf16 v[132:135], a[112:115], a[128:131], v[132:135]\n"
"s_waitcnt vmcnt(6) lgkmcnt(0)\n"
"v_accvgpr_write_b32 a251, 0\n"
"v_mfma_f32_16x16x32_bf16 v[32:35], a[64:67], a[160:163], v[32:35]\n"
"ds_read_b64_tr_b16 a[96:97], v16 offset:16384\n"
"s_lshl_b32 s76, s66, 17\n"
"s_sub_u32 s76, 0xf0000, s76\n"
"v_mfma_f32_16x16x32_bf16 v[32:35], a[68:71], a[176:179], v[32:35]\n"
"ds_read_b64_tr_b16 a[98:99], v16 offset:17408\n"
"v_accvgpr_write_b32 a252, 0\n"
"v_mfma_f32_16x16x32_bf16 v[36:39], a[76:79], a[176:179], v[36:39]\n"
"ds_read_b64_tr_b16 a[100:101], v16 offset:24576\n"
"s_add_u32 s77, s77, s76\n"
"s_add_u32 s33, s94, s76\n"
"v_mfma_f32_16x16x32_bf16 v[36:39], a[72:75], a[160:163], v[36:39]\n"
"ds_read_b64_tr_b16 a[102:103], v16 offset:25600\n"
"v_accvgpr_write_b32 a253, 0\n"
"v_mfma_f32_16x16x32_bf16 v[44:47], a[72:75], a[164:167], v[44:47]\n"
"ds_read_b64_tr_b16 a[104:105], v17 offset:16384\n"
"v_accvgpr_write_b32 a254, 0\n"
"v_mfma_f32_16x16x32_bf16 v[44:47], a[76:79], a[180:183], v[44:47]\n"
"ds_read_b64_tr_b16 a[106:107], v17 offset:17408\n"
"v_accvgpr_write_b32 a255, 0\n"
"v_mfma_f32_16x16x32_bf16 v[40:43], a[68:71], a[180:183], v[40:43]\n"
"ds_read_b64_tr_b16 a[108:109], v17 offset:24576\n"
"v_mfma_f32_16x16x32_bf16 v[40:43], a[64:67], a[164:167], v[40:43]\n"
"ds_read_b64_tr_b16 a[110:111], v17 offset:25600\n"
"v_mfma_f32_16x16x32_bf16 v[48:51], a[64:67], a[168:171], v[48:51]\n"
"ds_read_b64_tr_b16 a[112:113], v18 offset:16384\n"
"v_mfma_f32_16x16x32_bf16 v[48:51], a[68:71], a[184:187], v[48:51]\n"
"ds_read_b64_tr_b16 a[114:115], v18 offset:17408\n"
"v_mfma_f32_16x16x32_bf16 v[52:55], a[76:79], a[184:187], v[52:55]\n"
"ds_read_b64_tr_b16 a[116:117], v18 offset:24576\n"
"v_mfma_f32_16x16x32_bf16 v[52:55], a[72:75], a[168:171], v[52:55]\n"
"ds_read_b64_tr_b16 a[118:119], v18 offset:25600\n"
"v_mfma_f32_16x16x32_bf16 v[60:63], a[72:75], a[172:175], v[60:63]\n"
"ds_read_b64_tr_b16 a[120:121], v19 offset:16384\n"
"v_mfma_f32_16x16x32_bf16 v[60:63], a[76:79], a[188:191], v[60:63]\n"
"ds_read_b64_tr_b16 a[122:123], v19 offset:17408\n"
"v_mfma_f32_16x16x32_bf16 v[56:59], a[68:71], a[188:191], v[56:59]\n"
"ds_read_b64_tr_b16 a[124:125], v19 offset:24576\n"
"v_mfma_f32_16x16x32_bf16 v[56:59], a[64:67], a[172:175], v[56:59]\n"
"ds_read_b64_tr_b16 a[126:127], v19 offset:25600\n"
"v_mfma_f32_16x16x32_bf16 v[116:119], a[80:83], a[172:175], v[116:119]\n"
"v_mfma_f32_16x16x32_bf16 v[116:119], a[84:87], a[188:191], v[116:119]\n"
"ds_write_b128 v22, a[32:35] offset:24576\n"
"v_mfma_f32_16x16x32_bf16 v[124:127], a[92:95], a[188:191], v[124:127]\n"
"v_mfma_f32_16x16x32_bf16 v[124:127], a[88:91], a[172:175], v[124:127]\n"
"v_mfma_f32_16x16x32_bf16 v[108:111], a[88:91], a[168:171], v[108:111]\n"
"v_mfma_f32_16x16x32_bf16 v[108:111], a[92:95], a[184:187], v[108:111]\n"
"ds_write_b128 v22, a[40:43] offset:25600\n"
"v_mfma_f32_16x16x32_bf16 v[100:103], a[84:87], a[184:187], v[100:103]\n"
"v_mfma_f32_16x16x32_bf16 v[100:103], a[80:83], a[168:171], v[100:103]\n"
"v_mfma_f32_16x16x32_bf16 v[84:87], a[80:83], a[164:167], v[84:87]\n"
"v_mfma_f32_16x16x32_bf16 v[84:87], a[84:87], a[180:183], v[84:87]\n"
"ds_write_b128 v22, a[48:51] offset:26624\n"
"v_mfma_f32_16x16x32_bf16 v[92:95], a[92:95], a[180:183], v[92:95]\n"
"v_mfma_f32_16x16x32_bf16 v[92:95], a[88:91], a[164:167], v[92:95]\n"
"v_mfma_f32_16x16x32_bf16 v[76:79], a[88:91], a[160:163], v[76:79]\n"
"v_mfma_f32_16x16x32_bf16 v[76:79], a[92:95], a[176:179], v[76:79]\n"
"ds_write_b128 v22, a[56:59] offset:27648\n"
"v_mfma_f32_16x16x32_bf16 v[68:71], a[84:87], a[176:179], v[68:71]\n"
"v_mfma_f32_16x16x32_bf16 v[68:71], a[80:83], a[160:163], v[68:71]\n"
"s_waitcnt vmcnt(6) lgkmcnt(0)\n"
"s_barrier\n"
"v_mfma_f32_16x16x32_bf16 v[196:199], a[96:99], a[160:163], v[196:199]\n"
"s_and_b32 s74, s64, 1\n"
"s_lshl_b32 s74, s74, 14\n"
"v_mfma_f32_16x16x32_bf16 v[196:199], a[100:103], a[176:179], v[196:199]\n"
"ds_read_b128 a[128:131], v1 offset:32768\n"
"v_add_u32 v2, 0x10000, v1\n"
"v_mfma_f32_16x16x32_bf16 v[204:207], a[108:111], a[176:179], v[204:207]\n"
"ds_read_b128 a[132:135], v1 offset:33792\n"
"v_add_u32 v3, s74, v1\n"
"v_mfma_f32_16x16x32_bf16 v[204:207], a[104:107], a[160:163], v[204:207]\n"
"v_add_u32 v4, 0x10000, v3\n"
"v_add_u32 v7, 0x20000, v3\n"
"v_mfma_f32_16x16x32_bf16 v[220:223], a[104:107], a[164:167], v[220:223]\n"
"ds_read_b128 a[136:139], v1 offset:34816\n"
"v_add_u32 v8, s67, v1\n"
"v_mfma_f32_16x16x32_bf16 v[220:223], a[108:111], a[180:183], v[220:223]\n"
"v_mov_b32 v22, v8\n"
"v_mov_b32 v12, 0\n"
"v_mfma_f32_16x16x32_bf16 v[212:215], a[100:103], a[180:183], v[212:215]\n"
"ds_read_b128 a[140:143], v1 offset:35840\n"
"v_mov_b32 v13, 0\n"
"v_mfma_f32_16x16x32_bf16 v[212:215], a[96:99], a[164:167], v[212:215]\n"
"v_mov_b32 v14, 0\n"
"v_mov_b32 v15, 0\n"
"v_mfma_f32_16x16x32_bf16 v[228:231], a[96:99], a[168:171], v[228:231]\n"
"ds_read_b128 a[144:147], v1 offset:36864\n"
"v_mov_b32 v16, 0\n"
"v_mfma_f32_16x16x32_bf16 v[228:231], a[100:103], a[184:187], v[228:231]\n"
"v_mov_b32 v17, 0\n"
"v_mov_b32 v152, 0\n"
"v_mfma_f32_16x16x32_bf16 v[236:239], a[108:111], a[184:187], v[236:239]\n"
"ds_read_b128 a[148:151], v1 offset:37888\n"
"v_mov_b32 v153, 0\n"
"v_mfma_f32_16x16x32_bf16 v[236:239], a[104:107], a[168:171], v[236:239]\n"
"v_mov_b32 v154, 0\n"
"v_mov_b32 v155, 0\n"
"v_mfma_f32_16x16x32_bf16 v[252:255], a[104:107], a[172:175], v[252:255]\n"
"ds_read_b128 a[152:155], v1 offset:38912\n"
"v_mov_b32 v160, 0\n"
"v_mfma_f32_16x16x32_bf16 v[252:255], a[108:111], a[188:191], v[252:255]\n"
"v_mov_b32 v161, 0\n"
"v_mov_b32 v162, 0\n"
"v_mfma_f32_16x16x32_bf16 v[244:247], a[100:103], a[188:191], v[244:247]\n"
"ds_read_b128 a[156:159], v1 offset:39936\n"
"v_mov_b32 v163, 0\n"
"v_mfma_f32_16x16x32_bf16 v[244:247], a[96:99], a[172:175], v[244:247]\n"
"v_mov_b32 v128, 0x3f803f80\n"
"v_mov_b32 v129, 0x3f803f80\n"
"v_mfma_f32_16x16x32_bf16 v[180:183], a[112:115], a[172:175], v[180:183]\n"
"v_mov_b32 v130, 0x3f803f80\n"
"v_mov_b32 v131, 0x3f803f80\n"
"v_mfma_f32_16x16x32_bf16 v[180:183], a[116:119], a[188:191], v[180:183]\n"
"v_and_b32 v18, 15, v21\n"
"v_lshrrev_b32 v27, 4, v21\n"
"v_mfma_f32_16x16x32_bf16 v[188:191], a[124:127], a[188:191], v[188:191]\n"
"v_lshlrev_b32 v27, 2, v27\n"
"v_sub_u32 v18, v18, v27\n"
"v_mfma_f32_16x16x32_bf16 v[188:191], a[120:123], a[172:175], v[188:191]\n"
"v_mov_b32 v19, 0x7f000000\n"
"v_mov_b32 v144, 0xff800000\n"
"v_mfma_f32_16x16x32_bf16 v[172:175], a[120:123], a[168:171], v[172:175]\n"
"v_mov_b32 v145, 0xff800000\n"
"v_mov_b32 v146, 0xff800000\n"
"v_mfma_f32_16x16x32_bf16 v[172:175], a[124:127], a[184:187], v[172:175]\n"
"v_mov_b32 v147, 0xff800000\n"
"v_cmp_le_i32 vcc, 0, v18\n"
"v_mfma_f32_16x16x32_bf16 v[164:167], a[116:119], a[184:187], v[164:167]\n"
"v_mfma_f32_16x16x32_bf16 v[164:167], a[112:115], a[168:171], v[164:167]\n"
"v_cndmask_b32 v136, v144, v19, vcc\n"
"v_cmp_le_i32 vcc, 1, v18\n"
"v_mfma_f32_16x16x32_bf16 v[148:151], a[112:115], a[164:167], v[148:151]\n"
"v_mfma_f32_16x16x32_bf16 v[148:151], a[116:119], a[180:183], v[148:151]\n"
"v_cndmask_b32 v137, v144, v19, vcc\n"
"v_cmp_le_i32 vcc, 2, v18\n"
"v_mfma_f32_16x16x32_bf16 v[156:159], a[124:127], a[180:183], v[156:159]\n"
"v_mfma_f32_16x16x32_bf16 v[156:159], a[120:123], a[164:167], v[156:159]\n"
"v_cndmask_b32 v138, v144, v19, vcc\n"
"v_cmp_le_i32 vcc, 3, v18\n"
"v_mfma_f32_16x16x32_bf16 v[140:143], a[120:123], a[160:163], v[140:143]\n"
"v_mfma_f32_16x16x32_bf16 v[140:143], a[124:127], a[176:179], v[140:143]\n"
"v_cndmask_b32 v139, v144, v19, vcc\n"
"s_cmp_lg_u32 s64, 0\n"
"s_cselect_b64 s[86:87], -1, 0\n"
"v_mfma_f32_16x16x32_bf16 v[132:135], a[116:119], a[176:179], v[132:135]\n"
"s_lshl_b32 s89, s65, 20\n"
"s_add_u32 s89, s89, 0x3f8000\n"
"v_mfma_f32_16x16x32_bf16 v[132:135], a[112:115], a[160:163], v[132:135]\n"
"s_add_u32 s75, s66, 1\n"
"s_lshl_b32 s75, s75, 16\n"
"s_add_u32 s95, s89, s75\n"
"s_lshl_b32 s88, s66, 1\n"
"s_cmp_eq_u32 s66, 7\n"
"s_cselect_b32 s94, 3, 0\n"
"s_lshl_b32 s34, s65, 6\n"
"s_add_u32 s34, s34, 0x3000\n"
"s_lshl_b32 s71, s66, 3\n"
"s_add_u32 s71, s71, s34\n"
"v_lshl_or_b32 v20, s64, 6, v21\n"
"v_cmp_eq_u32 vcc, 0, v20\n"
"v_mov_b32 v20, -1\n"
"s_nop 0\n"
"v_cndmask_b32 v20, v20, 0, vcc\n"
"s_mov_b32 s91, 0x8000\n"
"s_waitcnt vmcnt(0) lgkmcnt(0)\n"
"s_barrier\n"
"v_mov_b32 v24, s68\n"
"v_mov_b32 v25, s69\n"
"buffer_store_dwordx2 v[24:25], v20, s[48:51], s71 offen sc1\n"
"s_mov_b32 s84, 0x3e0293ee\n"
"s_mov_b32 s85, 0x3e0293ee\n"
"v_pk_mul_f32 v[32:33], v[32:33], s[84:85]\n"
"v_pk_mul_f32 v[34:35], v[34:35], s[84:85]\n"
"v_pk_mul_f32 v[36:37], v[36:37], s[84:85]\n"
"v_pk_mul_f32 v[38:39], v[38:39], s[84:85]\n"
"v_pk_mul_f32 v[40:41], v[40:41], s[84:85]\n"
"v_pk_mul_f32 v[42:43], v[42:43], s[84:85]\n"
"v_pk_mul_f32 v[44:45], v[44:45], s[84:85]\n"
"v_pk_mul_f32 v[46:47], v[46:47], s[84:85]\n"
"v_pk_mul_f32 v[48:49], v[48:49], s[84:85]\n"
"v_pk_mul_f32 v[50:51], v[50:51], s[84:85]\n"
"v_pk_mul_f32 v[52:53], v[52:53], s[84:85]\n"
"v_pk_mul_f32 v[54:55], v[54:55], s[84:85]\n"
"v_pk_mul_f32 v[56:57], v[56:57], s[84:85]\n"
"v_pk_mul_f32 v[58:59], v[58:59], s[84:85]\n"
"v_pk_mul_f32 v[60:61], v[60:61], s[84:85]\n"
"v_pk_mul_f32 v[62:63], v[62:63], s[84:85]\n"
"v_pk_mul_f32 v[68:69], v[68:69], s[84:85]\n"
"v_pk_mul_f32 v[70:71], v[70:71], s[84:85]\n"
"v_pk_mul_f32 v[76:77], v[76:77], s[84:85]\n"
"v_pk_mul_f32 v[78:79], v[78:79], s[84:85]\n"
"v_pk_mul_f32 v[84:85], v[84:85], s[84:85]\n"
"v_pk_mul_f32 v[86:87], v[86:87], s[84:85]\n"
"v_pk_mul_f32 v[92:93], v[92:93], s[84:85]\n"
"v_pk_mul_f32 v[94:95], v[94:95], s[84:85]\n"
"v_pk_mul_f32 v[100:101], v[100:101], s[84:85]\n"
"v_pk_mul_f32 v[102:103], v[102:103], s[84:85]\n"
"v_pk_mul_f32 v[108:109], v[108:109], s[84:85]\n"
"v_pk_mul_f32 v[110:111], v[110:111], s[84:85]\n"
"v_pk_mul_f32 v[116:117], v[116:117], s[84:85]\n"
"v_pk_mul_f32 v[118:119], v[118:119], s[84:85]\n"
"v_pk_mul_f32 v[124:125], v[124:125], s[84:85]\n"
"v_pk_mul_f32 v[126:127], v[126:127], s[84:85]\n"
"v_pk_mul_f32 v[196:197], v[196:197], s[84:85]\n"
"v_pk_mul_f32 v[198:199], v[198:199], s[84:85]\n"
"v_pk_mul_f32 v[204:205], v[204:205], s[84:85]\n"
"v_pk_mul_f32 v[206:207], v[206:207], s[84:85]\n"
"v_pk_mul_f32 v[212:213], v[212:213], s[84:85]\n"
"v_pk_mul_f32 v[214:215], v[214:215], s[84:85]\n"
"v_pk_mul_f32 v[220:221], v[220:221], s[84:85]\n"
"v_pk_mul_f32 v[222:223], v[222:223], s[84:85]\n"
"v_pk_mul_f32 v[228:229], v[228:229], s[84:85]\n"
"v_pk_mul_f32 v[230:231], v[230:231], s[84:85]\n"
"v_pk_mul_f32 v[236:237], v[236:237], s[84:85]\n"
"v_pk_mul_f32 v[238:239], v[238:239], s[84:85]\n"
"v_pk_mul_f32 v[244:245], v[244:245], s[84:85]\n"
"v_pk_mul_f32 v[246:247], v[246:247], s[84:85]\n"
"v_pk_mul_f32 v[252:253], v[252:253], s[84:85]\n"
"v_pk_mul_f32 v[254:255], v[254:255], s[84:85]\n"
"v_pk_mul_f32 v[132:133], v[132:133], s[84:85]\n"
"v_pk_mul_f32 v[134:135], v[134:135], s[84:85]\n"
"v_pk_mul_f32 v[140:141], v[140:141], s[84:85]\n"
"v_pk_mul_f32 v[142:143], v[142:143], s[84:85]\n"
"v_pk_mul_f32 v[148:149], v[148:149], s[84:85]\n"
"v_pk_mul_f32 v[150:151], v[150:151], s[84:85]\n"
"v_pk_mul_f32 v[156:157], v[156:157], s[84:85]\n"
"v_pk_mul_f32 v[158:159], v[158:159], s[84:85]\n"
"v_pk_mul_f32 v[164:165], v[164:165], s[84:85]\n"
"v_pk_mul_f32 v[166:167], v[166:167], s[84:85]\n"
"v_pk_mul_f32 v[172:173], v[172:173], s[84:85]\n"
"v_pk_mul_f32 v[174:175], v[174:175], s[84:85]\n"
"v_pk_mul_f32 v[180:181], v[180:181], s[84:85]\n"
"v_pk_mul_f32 v[182:183], v[182:183], s[84:85]\n"
"v_pk_mul_f32 v[188:189], v[188:189], s[84:85]\n"
"v_pk_mul_f32 v[190:191], v[190:191], s[84:85]\n"
"v_cvt_pk_bf16_f32 v32, v32, v33\n"
"v_cvt_pk_bf16_f32 v33, v34, v35\n"
"v_cvt_pk_bf16_f32 v34, v36, v37\n"
"v_cvt_pk_bf16_f32 v35, v38, v39\n"
"v_cvt_pk_bf16_f32 v40, v40, v41\n"
"v_cvt_pk_bf16_f32 v41, v42, v43\n"
"v_cvt_pk_bf16_f32 v42, v44, v45\n"
"v_cvt_pk_bf16_f32 v43, v46, v47\n"
"v_cvt_pk_bf16_f32 v48, v48, v49\n"
"v_cvt_pk_bf16_f32 v49, v50, v51\n"
"v_cvt_pk_bf16_f32 v50, v52, v53\n"
"v_cvt_pk_bf16_f32 v51, v54, v55\n"
"v_cvt_pk_bf16_f32 v56, v56, v57\n"
"v_cvt_pk_bf16_f32 v57, v58, v59\n"
"v_cvt_pk_bf16_f32 v58, v60, v61\n"
"v_cvt_pk_bf16_f32 v59, v62, v63\n"
"s_add_u32 s35, s77, 0x0\n"
"buffer_store_dwordx4 v[192:195], v1, s[48:51], s35 offen sc1\n"
"v_cndmask_b32 v168, v144, 0, s[86:87]\n"
"v_cndmask_b32 v169, v145, 0, s[86:87]\n"
"v_cndmask_b32 v170, v146, 0, s[86:87]\n"
"v_cndmask_b32 v171, v147, 0, s[86:87]\n"
"v_cndmask_b32 v176, v144, 0, s[86:87]\n"
"v_med3_f32 v64, v136, v168, 0\n"
"v_med3_f32 v65, v137, v169, 0\n"
"v_med3_f32 v66, v138, v170, 0\n"
"v_med3_f32 v67, v139, v171, 0\n"
"v_med3_f32 v104, v136, v176, 0\n"
"v_med3_f32 v105, v137, v176, 0\n"
"v_med3_f32 v106, v138, v176, 0\n"
"v_med3_f32 v107, v139, v176, 0\n"
"s_bitcmp1_b32 s64, 0\n"
"s_cselect_b64 s[86:87], -1, 0\n"
"s_add_u32 s35, s77, 0x400\n"
"v_mfma_f32_16x16x32_bf16 v[64:67], a[128:131], v[32:35], v[64:67]\n"
"buffer_store_dwordx4 v[200:203], v1, s[48:51], s35 offen sc1\n"
"v_mfma_f32_16x16x32_bf16 v[72:75], a[128:131], v[40:43], 0\n"
"ds_read_b128 a[160:163], v1 offset:40960\n"
"v_cvt_pk_bf16_f32 v68, v68, v69\n"
"v_mfma_f32_16x16x32_bf16 v[80:83], a[128:131], v[48:51], 0\n"
"ds_read_b128 a[164:167], v1 offset:41984\n"
"v_cvt_pk_bf16_f32 v69, v70, v71\n"
"v_cvt_pk_bf16_f32 v70, v76, v77\n"
"v_cvt_pk_bf16_f32 v71, v78, v79\n"
"v_cvt_pk_bf16_f32 v84, v84, v85\n"
"v_cvt_pk_bf16_f32 v85, v86, v87\n"
"v_cvt_pk_bf16_f32 v86, v92, v93\n"
"v_cvt_pk_bf16_f32 v87, v94, v95\n"
"v_cvt_pk_bf16_f32 v100, v100, v101\n"
"v_cvt_pk_bf16_f32 v101, v102, v103\n"
"v_cvt_pk_bf16_f32 v102, v108, v109\n"
"v_cvt_pk_bf16_f32 v103, v110, v111\n"
"v_cvt_pk_bf16_f32 v116, v116, v117\n"
"v_cvt_pk_bf16_f32 v117, v118, v119\n"
"v_cvt_pk_bf16_f32 v118, v124, v125\n"
"v_cvt_pk_bf16_f32 v119, v126, v127\n"
"v_cvt_pk_bf16_f32 v196, v196, v197\n"
"v_mfma_f32_16x16x32_bf16 v[88:91], a[128:131], v[56:59], 0\n"
"ds_read_b128 a[168:171], v1 offset:43008\n"
"v_cvt_pk_bf16_f32 v197, v198, v199\n"
"v_mfma_f32_16x16x32_bf16 v[64:67], a[132:135], v[68:71], v[64:67]\n"
"ds_read_b128 a[128:131], v1 offset:49152\n"
"v_cvt_pk_bf16_f32 v198, v204, v205\n"
"v_mfma_f32_16x16x32_bf16 v[72:75], a[132:135], v[84:87], v[72:75]\n"
"ds_read_b128 a[172:175], v1 offset:44032\n"
"v_cvt_pk_bf16_f32 v199, v206, v207\n"
"v_mfma_f32_16x16x32_bf16 v[80:83], a[132:135], v[100:103], v[80:83]\n"
"ds_read_b128 a[176:179], v1 offset:45056\n"
"v_cvt_pk_bf16_f32 v212, v212, v213\n"
"v_cvt_pk_bf16_f32 v213, v214, v215\n"
"v_cvt_pk_bf16_f32 v214, v220, v221\n"
"v_cvt_pk_bf16_f32 v215, v222, v223\n"
"v_cvt_pk_bf16_f32 v228, v228, v229\n"
"v_cvt_pk_bf16_f32 v229, v230, v231\n"
"v_cvt_pk_bf16_f32 v230, v236, v237\n"
"v_cvt_pk_bf16_f32 v231, v238, v239\n"
"v_cvt_pk_bf16_f32 v244, v244, v245\n"
"v_cvt_pk_bf16_f32 v245, v246, v247\n"
"v_cvt_pk_bf16_f32 v246, v252, v253\n"
"v_cvt_pk_bf16_f32 v247, v254, v255\n"
"v_cvt_pk_bf16_f32 v132, v132, v133\n"
"v_mfma_f32_16x16x32_bf16 v[88:91], a[132:135], v[116:119], v[88:91]\n"
"ds_read_b128 a[180:183], v1 offset:46080\n"
"v_cvt_pk_bf16_f32 v133, v134, v135\n"
"v_mfma_f32_16x16x32_bf16 v[64:67], a[136:139], v[196:199], v[64:67]\n"
"ds_read_b128 a[132:135], v1 offset:50176\n"
"v_cvt_pk_bf16_f32 v134, v140, v141\n"
"v_mfma_f32_16x16x32_bf16 v[72:75], a[136:139], v[212:215], v[72:75]\n"
"ds_read_b128 a[184:187], v1 offset:47104\n"
"v_cvt_pk_bf16_f32 v135, v142, v143\n"
"v_mfma_f32_16x16x32_bf16 v[80:83], a[136:139], v[228:231], v[80:83]\n"
"ds_read_b128 a[188:191], v1 offset:48128\n"
"v_cvt_pk_bf16_f32 v148, v148, v149\n"
"v_cvt_pk_bf16_f32 v149, v150, v151\n"
"v_cvt_pk_bf16_f32 v150, v156, v157\n"
"v_cvt_pk_bf16_f32 v151, v158, v159\n"
"v_cvt_pk_bf16_f32 v164, v164, v165\n"
"v_cvt_pk_bf16_f32 v165, v166, v167\n"
"v_cvt_pk_bf16_f32 v166, v172, v173\n"
"v_cvt_pk_bf16_f32 v167, v174, v175\n"
"v_cvt_pk_bf16_f32 v180, v180, v181\n"
"v_cvt_pk_bf16_f32 v181, v182, v183\n"
"v_cvt_pk_bf16_f32 v182, v188, v189\n"
"v_cvt_pk_bf16_f32 v183, v190, v191\n"
"s_add_u32 s35, s77, 0x1000\n"
"v_mfma_f32_16x16x32_bf16 v[88:91], a[136:139], v[244:247], v[88:91]\n"
"buffer_store_dwordx4 v[208:211], v1, s[48:51], s35 offen sc1\n"
"v_mfma_f32_16x16x32_bf16 v[64:67], a[140:143], v[132:135], v[64:67]\n"
"ds_read_b128 a[136:139], v1 offset:51200\n"
"v_mfma_f32_16x16x32_bf16 v[72:75], a[140:143], v[148:151], v[72:75]\n"
"ds_write_b128 v22, v[48:51] offset:0\n"
"v_mfma_f32_16x16x32_bf16 v[80:83], a[140:143], v[164:167], v[80:83]\n"
"ds_write_b128 v22, v[56:59] offset:4096\n"
"v_mfma_f32_16x16x32_bf16 v[88:91], a[140:143], v[180:183], v[88:91]\n"
"v_mfma_f32_16x16x32_bf16 v[96:99], a[144:147], v[32:35], v[168:171]\n"
"ds_read_b128 a[140:143], v1 offset:52224\n"
"v_mfma_f32_16x16x32_bf16 v[104:107], a[144:147], v[40:43], v[104:107]\n"
"ds_write_b128 v22, v[100:103] offset:1024\n"
"v_mfma_f32_16x16x32_bf16 v[112:115], a[144:147], v[48:51], 0\n"
"ds_write_b128 v22, v[116:119] offset:5120\n"
"v_mfma_f32_16x16x32_bf16 v[120:123], a[144:147], v[56:59], 0\n"
"v_max3_f32 v24, v64, v65, v66\n"
"v_max3_f32 v25, v72, v73, v74\n"
"v_mfma_f32_16x16x32_bf16 v[96:99], a[148:151], v[68:71], v[96:99]\n"
"ds_read_b128 a[144:147], v1 offset:53248\n"
"v_max3_f32 v26, v80, v81, v82\n"
"v_mfma_f32_16x16x32_bf16 v[104:107], a[148:151], v[84:87], v[104:107]\n"
"ds_write_b128 v22, v[228:231] offset:2048\n"
"v_mfma_f32_16x16x32_bf16 v[112:115], a[148:151], v[100:103], v[112:115]\n"
"ds_write_b128 v22, v[244:247] offset:6144\n"
"v_mfma_f32_16x16x32_bf16 v[120:123], a[148:151], v[116:119], v[120:123]\n"
"v_max3_f32 v27, v88, v89, v90\n"
"v_max_f32 v24, v24, v67\n"
"v_mfma_f32_16x16x32_bf16 v[96:99], a[152:155], v[196:199], v[96:99]\n"
"ds_read_b128 a[148:151], v1 offset:54272\n"
"v_max_f32 v25, v25, v75\n"
"v_mfma_f32_16x16x32_bf16 v[104:107], a[152:155], v[212:215], v[104:107]\n"
"ds_write_b128 v22, v[164:167] offset:3072\n"
"v_mfma_f32_16x16x32_bf16 v[112:115], a[152:155], v[228:231], v[112:115]\n"
"v_max_f32 v26, v26, v83\n"
"v_max_f32 v27, v27, v91\n"
"v_mfma_f32_16x16x32_bf16 v[120:123], a[152:155], v[244:247], v[120:123]\n"
"v_mov_b32 v28, v24\n"
"v_mov_b32 v29, v25\n"
"v_mfma_f32_16x16x32_bf16 v[96:99], a[156:159], v[132:135], v[96:99]\n"
"ds_read_b128 a[152:155], v1 offset:55296\n"
"v_mov_b32 v30, v26\n"
"v_mfma_f32_16x16x32_bf16 v[104:107], a[156:159], v[148:151], v[104:107]\n"
"ds_write_b128 v22, v[180:183] offset:7168\n"
"v_mfma_f32_16x16x32_bf16 v[112:115], a[156:159], v[164:167], v[112:115]\n"
"s_add_u32 s35, s77, 0x1400\n"
"v_mov_b32 v31, v27\n"
"v_mfma_f32_16x16x32_bf16 v[120:123], a[156:159], v[180:183], v[120:123]\n"
"buffer_store_dwordx4 v[216:219], v1, s[48:51], s35 offen sc1\n"
"v_permlane16_swap_b32 v24, v28\n"
"v_permlane16_swap_b32 v25, v29\n"
"v_permlane16_swap_b32 v26, v30\n"
"v_permlane16_swap_b32 v27, v31\n"
"v_max_f32 v24, v24, v28\n"
"v_max_f32 v25, v25, v29\n"
"v_max_f32 v26, v26, v30\n"
"v_max_f32 v27, v27, v31\n"
"v_mov_b32 v28, v24\n"
"v_mov_b32 v29, v25\n"
"v_mov_b32 v30, v26\n"
"v_mov_b32 v31, v27\n"
"v_permlane32_swap_b32 v24, v28\n"
"v_permlane32_swap_b32 v25, v29\n"
"v_permlane32_swap_b32 v26, v30\n"
"v_permlane32_swap_b32 v27, v31\n"
"v_max_f32 v24, v24, v28\n"
"v_max_f32 v25, v25, v29\n"
"v_max_f32 v26, v26, v30\n"
"v_max_f32 v27, v27, v31\n"
"v_sub_f32 v204, 0, v24\n"
"v_sub_f32 v205, 0, v24\n"
"v_sub_f32 v206, 0, v24\n"
"v_sub_f32 v207, 0, v24\n"
"v_sub_f32 v220, 0, v25\n"
"v_sub_f32 v221, 0, v25\n"
"v_sub_f32 v222, 0, v25\n"
"v_sub_f32 v223, 0, v25\n"
"v_sub_f32 v236, 0, v26\n"
"v_sub_f32 v237, 0, v26\n"
"v_sub_f32 v238, 0, v26\n"
"v_sub_f32 v239, 0, v26\n"
"v_sub_f32 v252, 0, v27\n"
"v_sub_f32 v253, 0, v27\n"
"v_sub_f32 v254, 0, v27\n"
"v_sub_f32 v255, 0, v27\n"
"ds_read_b128 a[156:159], v1 offset:56320\n"
"s_add_u32 s35, s77, 0x4000\n"
"buffer_store_dwordx4 v[224:227], v1, s[48:51], s35 offen sc1\n"
"s_add_u32 s35, s77, 0x4400\n"
"buffer_store_dwordx4 v[232:235], v1, s[48:51], s35 offen sc1\n"
"v_mov_b32 v24, v236\n"
"v_mov_b32 v25, v252\n"
"s_lshl_b32 s75, s64, 9\n"
"v_lshlrev_b32 v26, 3, v21\n"
"v_add_u32 v26, s75, v26\n"
"ds_write_b64 v26, v[24:25] offset:32768\n"
"v_pk_add_f32 v[64:65], v[64:65], v[204:205]\n"
"v_pk_add_f32 v[66:67], v[66:67], v[204:205]\n"
"v_pk_add_f32 v[72:73], v[72:73], v[220:221]\n"
"v_pk_add_f32 v[74:75], v[74:75], v[220:221]\n"
"v_pk_add_f32 v[80:81], v[80:81], v[236:237]\n"
"v_pk_add_f32 v[82:83], v[82:83], v[236:237]\n"
"v_pk_add_f32 v[88:89], v[88:89], v[252:253]\n"
"v_pk_add_f32 v[90:91], v[90:91], v[252:253]\n"
"v_pk_add_f32 v[96:97], v[96:97], v[204:205]\n"
"v_pk_add_f32 v[98:99], v[98:99], v[204:205]\n"
"v_pk_add_f32 v[104:105], v[104:105], v[220:221]\n"
"v_pk_add_f32 v[106:107], v[106:107], v[220:221]\n"
"v_pk_add_f32 v[112:113], v[112:113], v[236:237]\n"
"v_pk_add_f32 v[114:115], v[114:115], v[236:237]\n"
"v_pk_add_f32 v[120:121], v[120:121], v[252:253]\n"
"v_pk_add_f32 v[122:123], v[122:123], v[252:253]\n"
"s_add_u32 s35, s77, 0x5000\n"
"buffer_store_dwordx4 v[240:243], v1, s[48:51], s35 offen sc1\n"
"v_exp_f32 v64, v64\n"
"v_exp_f32 v65, v65\n"
"v_add_f32 v12, v12, v64\n"
"v_exp_f32 v66, v66\n"
"v_add_f32 v12, v12, v65\n"
"v_cvt_pk_bf16_f32 v192, v64, v65\n"
"v_exp_f32 v67, v67\n"
"v_add_f32 v12, v12, v66\n"
"v_exp_f32 v72, v72\n"
"v_add_f32 v12, v12, v67\n"
"v_cvt_pk_bf16_f32 v193, v66, v67\n"
"v_exp_f32 v73, v73\n"
"v_add_f32 v13, v13, v72\n"
"v_exp_f32 v74, v74\n"
"v_add_f32 v13, v13, v73\n"
"v_cvt_pk_bf16_f32 v200, v72, v73\n"
"v_exp_f32 v75, v75\n"
"v_add_f32 v13, v13, v74\n"
"s_add_u32 s35, s33, 0x2000\n"
"buffer_store_dwordx4 a[0:3], v1, s[48:51], s35 offen sc1\n"
"v_exp_f32 v80, v80\n"
"v_add_f32 v13, v13, v75\n"
"v_cvt_pk_bf16_f32 v201, v74, v75\n"
"v_exp_f32 v81, v81\n"
"v_add_f32 v14, v14, v80\n"
"v_exp_f32 v82, v82\n"
"v_add_f32 v14, v14, v81\n"
"v_cvt_pk_bf16_f32 v208, v80, v81\n"
"v_exp_f32 v83, v83\n"
"v_add_f32 v14, v14, v82\n"
"v_exp_f32 v88, v88\n"
"v_add_f32 v14, v14, v83\n"
"v_cvt_pk_bf16_f32 v209, v82, v83\n"
"v_exp_f32 v89, v89\n"
"v_add_f32 v15, v15, v88\n"
"v_exp_f32 v90, v90\n"
"v_add_f32 v15, v15, v89\n"
"v_cvt_pk_bf16_f32 v216, v88, v89\n"
"v_exp_f32 v91, v91\n"
"v_add_f32 v15, v15, v90\n"
"s_add_u32 s35, s33, 0x2400\n"
"buffer_store_dwordx4 a[8:11], v1, s[48:51], s35 offen sc1\n"
"v_exp_f32 v96, v96\n"
"v_add_f32 v15, v15, v91\n"
"v_cvt_pk_bf16_f32 v217, v90, v91\n"
"v_exp_f32 v97, v97\n"
"v_add_f32 v12, v12, v96\n"
"v_exp_f32 v98, v98\n"
"v_add_f32 v12, v12, v97\n"
"v_cvt_pk_bf16_f32 v194, v96, v97\n"
"v_exp_f32 v99, v99\n"
"v_add_f32 v12, v12, v98\n"
"v_exp_f32 v104, v104\n"
"v_add_f32 v12, v12, v99\n"
"v_cvt_pk_bf16_f32 v195, v98, v99\n"
"v_exp_f32 v105, v105\n"
"v_add_f32 v13, v13, v104\n"
"v_exp_f32 v106, v106\n"
"v_add_f32 v13, v13, v105\n"
"v_cvt_pk_bf16_f32 v202, v104, v105\n"
"v_exp_f32 v107, v107\n"
"v_add_f32 v13, v13, v106\n"
"s_add_u32 s35, s33, 0x2800\n"
"buffer_store_dwordx4 a[16:19], v1, s[48:51], s35 offen sc1\n"
"v_add_f32 v13, v13, v107\n"
"v_cvt_pk_bf16_f32 v203, v106, v107\n"
"s_add_u32 s35, s77, 0x5400\n"
"buffer_store_dwordx4 v[248:251], v1, s[48:51], s35 offen sc1\n"
"v_xor_b32 v27, 0x2000, v22\n"
"v_xor_b32 v26, 0x200, v26\n"
"v_and_b32 v6, 7, v21\n"
"v_lshlrev_b32 v6, 3, v6\n"
"v_lshlrev_b32 v31, 5, v21\n"
"s_mov_b32 s75, 0x4000\n"
"s_cmp_ge_u32 s64, 2\n"
"s_cbranch_scc1 L_pair23_%=\n"
"v_cndmask_b32 v168, v144, v204, s[86:87]\n"
"v_cndmask_b32 v176, v144, v220, s[86:87]\n"
"v_cndmask_b32 v177, v145, v221, s[86:87]\n"
"v_cndmask_b32 v178, v146, v222, s[86:87]\n"
"v_cndmask_b32 v179, v147, v223, s[86:87]\n"
"v_med3_f32 v64, v136, v144, v168\n"
"v_med3_f32 v65, v137, v145, v168\n"
"v_med3_f32 v66, v138, v146, v168\n"
"v_med3_f32 v67, v139, v147, v168\n"
"s_waitcnt lgkmcnt(1)\n"
"s_add_u32 s35, s33, 0x2c00\n"
"v_mfma_f32_16x16x32_bf16 v[64:67], a[128:131], v[32:35], v[64:67]\n"
"buffer_store_dwordx4 a[24:27], v1, s[48:51], s35 offen sc1\n"
"v_exp_f32 v112, v112\n"
"v_mfma_f32_16x16x32_bf16 v[64:67], a[132:135], v[68:71], v[64:67]\n"
"v_mfma_f32_16x16x32_bf16 v[64:67], a[136:139], v[196:199], v[64:67]\n"
"v_exp_f32 v113, v113\n"
"v_mfma_f32_16x16x32_bf16 v[64:67], a[140:143], v[132:135], v[64:67]\n"
"v_add_f32 v14, v14, v112\n"
"v_mfma_f32_16x16x32_bf16 v[72:75], a[140:143], v[148:151], v[176:179]\n"
"v_exp_f32 v114, v114\n"
"v_mfma_f32_16x16x32_bf16 v[72:75], a[136:139], v[212:215], v[72:75]\n"
"v_add_f32 v14, v14, v113\n"
"v_med3_f32 v104, v136, v144, v176\n"
"v_med3_f32 v105, v137, v145, v176\n"
"v_med3_f32 v106, v138, v146, v176\n"
"v_med3_f32 v107, v139, v147, v176\n"
"v_mfma_f32_16x16x32_bf16 v[72:75], a[132:135], v[84:87], v[72:75]\n"
"v_exp_f32 v115, v115\n"
"v_mfma_f32_16x16x32_bf16 v[72:75], a[128:131], v[40:43], v[72:75]\n"
"v_cvt_pk_bf16_f32 v210, v112, v113\n"
"v_add_f32 v14, v14, v114\n"
"v_add_u32 v9, 0xc000, v1\n"
"v_mfma_f32_16x16x32_bf16 v[80:83], a[128:131], v[48:51], v[236:239]\n"
"v_exp_f32 v120, v120\n"
"v_mfma_f32_16x16x32_bf16 v[80:83], a[132:135], v[100:103], v[80:83]\n"
"ds_read_b128 v[172:175], v1 offset:64512\n"
"v_add_f32 v14, v14, v115\n"
"v_mfma_f32_16x16x32_bf16 v[80:83], a[136:139], v[228:231], v[80:83]\n"
"v_exp_f32 v121, v121\n"
"v_mfma_f32_16x16x32_bf16 v[80:83], a[140:143], v[164:167], v[80:83]\n"
"v_cvt_pk_bf16_f32 v211, v114, v115\n"
"v_add_f32 v15, v15, v120\n"
"s_waitcnt lgkmcnt(1)\n"
"s_barrier\n"
"v_mfma_f32_16x16x32_bf16 v[88:91], a[140:143], v[180:183], v[252:255]\n"
"v_exp_f32 v122, v122\n"
"v_mfma_f32_16x16x32_bf16 v[88:91], a[136:139], v[244:247], v[88:91]\n"
"ds_read_b128 a[140:143], v4 offset:3072\n"
"v_add_f32 v15, v15, v121\n"
"v_mfma_f32_16x16x32_bf16 v[88:91], a[132:135], v[116:119], v[88:91]\n"
"v_exp_f32 v123, v123\n"
"v_mfma_f32_16x16x32_bf16 v[88:91], a[128:131], v[56:59], v[88:91]\n"
"ds_read_b128 v[36:39], v27 offset:0\n"
"v_cvt_pk_bf16_f32 v218, v120, v121\n"
"v_add_f32 v15, v15, v122\n"
"v_mfma_f32_16x16x32_bf16 v[96:99], a[144:147], v[32:35], v[144:147]\n"
"v_exp_f32 v64, v64\n"
"v_mfma_f32_16x16x32_bf16 v[96:99], a[148:151], v[68:71], v[96:99]\n"
"ds_read_b128 a[136:139], v4 offset:2048\n"
"v_add_f32 v15, v15, v123\n"
"v_mfma_f32_16x16x32_bf16 v[96:99], a[152:155], v[196:199], v[96:99]\n"
"v_exp_f32 v65, v65\n"
"v_mfma_f32_16x16x32_bf16 v[96:99], a[156:159], v[132:135], v[96:99]\n"
"ds_read_b128 v[44:47], v27 offset:1024\n"
"v_cvt_pk_bf16_f32 v219, v122, v123\n"
"v_add_f32 v12, v12, v64\n"
"v_mfma_f32_16x16x32_bf16 v[104:107], a[156:159], v[148:151], v[104:107]\n"
"buffer_load_dwordx4 v[188:191], v31, s[48:51], s75 offen sc1\n"
"v_exp_f32 v66, v66\n"
"v_mfma_f32_16x16x32_bf16 v[104:107], a[152:155], v[212:215], v[104:107]\n"
"ds_read_b128 a[132:135], v4 offset:1024\n"
"v_add_f32 v12, v12, v65\n"
"v_mfma_f32_16x16x32_bf16 v[104:107], a[148:151], v[84:87], v[104:107]\n"
"buffer_load_dwordx4 v[184:187], v31, s[48:51], s75 offen offset:16 sc1\n"
"v_exp_f32 v67, v67\n"
"v_mfma_f32_16x16x32_bf16 v[104:107], a[144:147], v[40:43], v[104:107]\n"
"ds_read_b128 v[52:55], v27 offset:2048\n"
"v_cvt_pk_bf16_f32 v224, v64, v65\n"
"v_add_f32 v12, v12, v66\n"
"v_mfma_f32_16x16x32_bf16 v[112:115], a[144:147], v[48:51], v[236:239]\n"
"buffer_load_dwordx2 v[28:29], v6, s[48:51], s34 offen sc1\n"
"v_exp_f32 v72, v72\n"
"v_mfma_f32_16x16x32_bf16 v[112:115], a[148:151], v[100:103], v[112:115]\n"
"ds_read_b128 a[128:131], v4 offset:0\n"
"v_add_f32 v12, v12, v67\n"
"v_mfma_f32_16x16x32_bf16 v[112:115], a[152:155], v[228:231], v[112:115]\n"
"v_exp_f32 v73, v73\n"
"v_mfma_f32_16x16x32_bf16 v[112:115], a[156:159], v[164:167], v[112:115]\n"
"ds_read_b128 v[60:63], v27 offset:3072\n"
"v_cvt_pk_bf16_f32 v225, v66, v67\n"
"v_add_f32 v13, v13, v72\n"
"v_mfma_f32_16x16x32_bf16 v[120:123], a[156:159], v[180:183], v[252:255]\n"
"v_exp_f32 v74, v74\n"
"v_mfma_f32_16x16x32_bf16 v[120:123], a[152:155], v[244:247], v[120:123]\n"
"ds_read_b128 a[156:159], v4 offset:7168\n"
"v_add_f32 v13, v13, v73\n"
"s_add_u32 s35, s33, 0x6000\n"
"v_mfma_f32_16x16x32_bf16 v[120:123], a[148:151], v[116:119], v[120:123]\n"
"buffer_store_dwordx4 a[32:35], v1, s[48:51], s35 offen sc1\n"
"v_exp_f32 v75, v75\n"
"v_mfma_f32_16x16x32_bf16 v[120:123], a[144:147], v[56:59], v[120:123]\n"
"ds_read_b128 v[76:79], v27 offset:4096\n"
"v_cvt_pk_bf16_f32 v232, v72, v73\n"
"v_add_f32 v13, v13, v74\n"
"v_mfma_f32_16x16x32_bf16 a[0:3], a[160:163], v[192:195], 0\n"
"v_exp_f32 v80, v80\n"
"v_mfma_f32_16x16x32_bf16 a[32:35], a[160:163], v[200:203], 0\n"
"ds_read_b128 a[152:155], v4 offset:6144\n"
"v_add_f32 v13, v13, v75\n"
"v_mfma_f32_16x16x32_bf16 a[64:67], a[160:163], v[208:211], 0\n"
"v_exp_f32 v81, v81\n"
"v_mfma_f32_16x16x32_bf16 a[96:99], a[160:163], v[216:219], 0\n"
"ds_read_b128 v[92:95], v27 offset:5120\n"
"v_cvt_pk_bf16_f32 v233, v74, v75\n"
"v_add_f32 v14, v14, v80\n"
"v_mfma_f32_16x16x32_bf16 a[100:103], a[164:167], v[216:219], 0\n"
"v_exp_f32 v82, v82\n"
"v_mfma_f32_16x16x32_bf16 a[68:71], a[164:167], v[208:211], 0\n"
"ds_read_b128 a[148:151], v4 offset:5120\n"
"v_add_f32 v14, v14, v81\n"
"s_add_u32 s35, s33, 0x6400\n"
"v_mfma_f32_16x16x32_bf16 a[36:39], a[164:167], v[200:203], 0\n"
"buffer_store_dwordx4 a[40:43], v1, s[48:51], s35 offen sc1\n"
"v_exp_f32 v83, v83\n"
"v_mfma_f32_16x16x32_bf16 a[4:7], a[164:167], v[192:195], 0\n"
"ds_read_b128 v[108:111], v27 offset:6144\n"
"v_cvt_pk_bf16_f32 v240, v80, v81\n"
"v_add_f32 v14, v14, v82\n"
"v_mfma_f32_16x16x32_bf16 a[8:11], a[168:171], v[192:195], 0\n"
"v_exp_f32 v88, v88\n"
"v_mfma_f32_16x16x32_bf16 a[40:43], a[168:171], v[200:203], 0\n"
"ds_read_b128 a[144:147], v4 offset:4096\n"
"v_add_f32 v14, v14, v83\n"
"v_mfma_f32_16x16x32_bf16 a[72:75], a[168:171], v[208:211], 0\n"
"v_exp_f32 v89, v89\n"
"v_mfma_f32_16x16x32_bf16 a[104:107], a[168:171], v[216:219], 0\n"
"ds_read_b128 v[124:127], v27 offset:7168\n"
"v_cvt_pk_bf16_f32 v241, v82, v83\n"
"v_add_f32 v15, v15, v88\n"
"v_mfma_f32_16x16x32_bf16 a[108:111], a[172:175], v[216:219], 0\n"
"v_exp_f32 v90, v90\n"
"v_mfma_f32_16x16x32_bf16 a[76:79], a[172:175], v[208:211], 0\n"
"ds_read_b128 a[160:163], v1 offset:57344\n"
"v_add_f32 v15, v15, v89\n"
"s_add_u32 s35, s33, 0x6800\n"
"v_mfma_f32_16x16x32_bf16 a[44:47], a[172:175], v[200:203], 0\n"
"buffer_store_dwordx4 a[48:51], v1, s[48:51], s35 offen sc1\n"
"v_exp_f32 v91, v91\n"
"v_mfma_f32_16x16x32_bf16 a[12:15], a[172:175], v[192:195], 0\n"
"ds_read_b64 v[24:25], v26 offset:32768\n"
"v_cvt_pk_bf16_f32 v248, v88, v89\n"
"v_add_f32 v15, v15, v90\n"
"v_mfma_f32_16x16x32_bf16 a[16:19], a[176:179], v[192:195], 0\n"
"v_exp_f32 v96, v96\n"
"v_mfma_f32_16x16x32_bf16 a[48:51], a[176:179], v[200:203], 0\n"
"ds_read_b128 a[164:167], v1 offset:58368\n"
"v_add_f32 v15, v15, v91\n"
"v_mfma_f32_16x16x32_bf16 a[80:83], a[176:179], v[208:211], 0\n"
"v_exp_f32 v97, v97\n"
"v_mfma_f32_16x16x32_bf16 a[112:115], a[176:179], v[216:219], 0\n"
"v_cvt_pk_bf16_f32 v249, v90, v91\n"
"v_add_f32 v12, v12, v96\n"
"v_mfma_f32_16x16x32_bf16 a[116:119], a[180:183], v[216:219], 0\n"
"v_exp_f32 v98, v98\n"
"v_mfma_f32_16x16x32_bf16 a[84:87], a[180:183], v[208:211], 0\n"
"ds_read_b128 a[168:171], v1 offset:59392\n"
"v_add_f32 v12, v12, v97\n"
"s_add_u32 s35, s33, 0x6c00\n"
"v_mfma_f32_16x16x32_bf16 a[52:55], a[180:183], v[200:203], 0\n"
"buffer_store_dwordx4 a[56:59], v1, s[48:51], s35 offen sc1\n"
"v_exp_f32 v99, v99\n"
"v_mfma_f32_16x16x32_bf16 a[20:23], a[180:183], v[192:195], 0\n"
"v_cvt_pk_bf16_f32 v226, v96, v97\n"
"v_add_f32 v12, v12, v98\n"
"v_mfma_f32_16x16x32_bf16 a[24:27], a[184:187], v[192:195], 0\n"
"v_exp_f32 v104, v104\n"
"v_mfma_f32_16x16x32_bf16 a[56:59], a[184:187], v[200:203], 0\n"
"ds_read_b128 a[172:175], v1 offset:60416\n"
"v_add_f32 v12, v12, v99\n"
"v_mfma_f32_16x16x32_bf16 a[88:91], a[184:187], v[208:211], 0\n"
"v_exp_f32 v105, v105\n"
"v_mfma_f32_16x16x32_bf16 a[120:123], a[184:187], v[216:219], 0\n"
"v_cvt_pk_bf16_f32 v227, v98, v99\n"
"v_add_f32 v13, v13, v104\n"
"s_waitcnt lgkmcnt(3)\n"
"v_mov_b32 v140, v24\n"
"v_mov_b32 v141, v24\n"
"v_mov_b32 v142, v24\n"
"v_mov_b32 v143, v24\n"
"v_mov_b32 v156, v25\n"
"v_mov_b32 v157, v25\n"
"v_mov_b32 v158, v25\n"
"v_mov_b32 v159, v25\n"
"v_mfma_f32_16x16x32_bf16 a[124:127], a[188:191], v[216:219], 0\n"
"v_exp_f32 v106, v106\n"
"v_mfma_f32_16x16x32_bf16 a[92:95], a[188:191], v[208:211], 0\n"
"ds_read_b128 a[176:179], v1 offset:61440\n"
"v_add_f32 v13, v13, v105\n"
"v_mfma_f32_16x16x32_bf16 a[60:63], a[188:191], v[200:203], 0\n"
"v_exp_f32 v107, v107\n"
"v_mfma_f32_16x16x32_bf16 a[28:31], a[188:191], v[192:195], 0\n"
"v_cvt_pk_bf16_f32 v234, v104, v105\n"
"v_add_f32 v13, v13, v106\n"
"s_waitcnt vmcnt(4)\n"
"v_cmp_eq_u64 vcc, s[68:69], v[28:29]\n"
"s_nop 0\n"
"s_cmp_eq_u64 vcc, -1\n"
"s_cbranch_scc0 L_kv0_spin%=\n"
"LT_kv0_ok%=:\n"
"s_waitcnt vmcnt(5)\n"
"v_cmp_eq_u64 vcc, s[68:69], v[188:189]\n"
"v_cmp_eq_u64 s[84:85], s[68:69], v[190:191]\n"
"s_nop 0\n"
"s_and_b64 vcc, vcc, s[84:85]\n"
"v_cmp_eq_u64 s[84:85], s[68:69], v[184:185]\n"
"s_nop 0\n"
"s_and_b64 vcc, vcc, s[84:85]\n"
"v_cmp_eq_u64 s[84:85], s[68:69], v[186:187]\n"
"s_nop 0\n"
"s_and_b64 vcc, vcc, s[84:85]\n"
"s_cmp_eq_u64 vcc, -1\n"
"s_cbranch_scc0 L_ff0_spin%=\n"
"LT_ff0_ok%=:\n"
"s_add_u32 s71, s71, 0x1800\n"
"s_add_u32 s34, s34, 0x1800\n"
"v_mov_b32 v24, s68\n"
"v_mov_b32 v25, s69\n"
"s_waitcnt lgkmcnt(5)\n"
"v_mfma_f32_16x16x32_bf16 v[64:67], a[128:131], v[36:39], v[140:143]\n"
"v_exp_f32 v112, v112\n"
"v_mfma_f32_16x16x32_bf16 v[64:67], a[132:135], v[44:47], v[64:67]\n"
"ds_read_b128 a[180:183], v9 offset:13312\n"
"v_add_f32 v13, v13, v107\n"
"v_mfma_f32_16x16x32_bf16 v[64:67], a[136:139], v[52:55], v[64:67]\n"
"v_exp_f32 v113, v113\n"
"v_mfma_f32_16x16x32_bf16 v[64:67], a[140:143], v[60:63], v[64:67]\n"
"v_cvt_pk_bf16_f32 v235, v106, v107\n"
"v_add_f32 v14, v14, v112\n"
"v_mfma_f32_16x16x32_bf16 v[72:75], a[140:143], v[124:127], v[156:159]\n"
"v_exp_f32 v114, v114\n"
"v_mfma_f32_16x16x32_bf16 v[72:75], a[136:139], v[108:111], v[72:75]\n"
"ds_read_b128 a[184:187], v9 offset:14336\n"
"v_add_f32 v14, v14, v113\n"
"v_mfma_f32_16x16x32_bf16 v[72:75], a[132:135], v[92:95], v[72:75]\n"
"v_exp_f32 v115, v115\n"
"v_mfma_f32_16x16x32_bf16 v[72:75], a[128:131], v[76:79], v[72:75]\n"
"v_cvt_pk_bf16_f32 v242, v112, v113\n"
"v_add_f32 v14, v14, v114\n"
"v_mfma_f32_16x16x32_bf16 v[80:83], a[128:131], v[48:51], v[236:239]\n"
"v_exp_f32 v120, v120\n"
"v_mfma_f32_16x16x32_bf16 v[80:83], a[132:135], v[100:103], v[80:83]\n"
"ds_read_b128 a[188:191], v4 offset:15360\n"
"v_add_f32 v14, v14, v115\n"
"v_mfma_f32_16x16x32_bf16 v[80:83], a[136:139], v[228:231], v[80:83]\n"
"v_exp_f32 v121, v121\n"
"v_mfma_f32_16x16x32_bf16 v[80:83], a[140:143], v[164:167], v[80:83]\n"
"v_cvt_pk_bf16_f32 v243, v114, v115\n"
"v_add_f32 v15, v15, v120\n"
"s_waitcnt lgkmcnt(1)\n"
"s_barrier\n"
"v_mfma_f32_16x16x32_bf16 v[88:91], a[140:143], v[180:183], v[252:255]\n"
"v_exp_f32 v122, v122\n"
"v_mfma_f32_16x16x32_bf16 v[88:91], a[136:139], v[244:247], v[88:91]\n"
"ds_read_b128 a[140:143], v4 offset:35840\n"
"v_add_f32 v15, v15, v121\n"
"v_mfma_f32_16x16x32_bf16 v[88:91], a[132:135], v[116:119], v[88:91]\n"
"v_exp_f32 v123, v123\n"
"v_mfma_f32_16x16x32_bf16 v[88:91], a[128:131], v[56:59], v[88:91]\n"
"v_cvt_pk_bf16_f32 v250, v120, v121\n"
"v_add_f32 v15, v15, v122\n"
"v_mfma_f32_16x16x32_bf16 v[96:99], a[144:147], v[36:39], v[140:143]\n"
"v_exp_f32 v64, v64\n"
"v_mfma_f32_16x16x32_bf16 v[96:99], a[148:151], v[44:47], v[96:99]\n"
"ds_read_b128 a[136:139], v4 offset:34816\n"
"v_add_f32 v15, v15, v123\n"
"v_mfma_f32_16x16x32_bf16 v[96:99], a[152:155], v[52:55], v[96:99]\n"
"v_exp_f32 v65, v65\n"
"v_mfma_f32_16x16x32_bf16 v[96:99], a[156:159], v[60:63], v[96:99]\n"
"ds_read_b128 a[132:135], v4 offset:33792\n"
"v_cvt_pk_bf16_f32 v251, v122, v123\n"
"v_mfma_f32_16x16x32_bf16 v[104:107], a[156:159], v[124:127], v[156:159]\n"
"v_exp_f32 v66, v66\n"
"s_getpc_b64 s[6:7]\n"
"s_sub_u32 s88, s88, 1\n"
"s_cbranch_scc1 L_hand_%=\n"
"s_add_u32 s89, s89, s91\n"
"s_add_u32 m0, s67, 0x0\n"
"v_mfma_f32_16x16x32_bf16 v[104:107], a[152:155], v[108:111], v[104:107]\n"
"buffer_load_dwordx4 v8, s[48:51], s89 offen offset:0 sc1 lds\n"
"v_mfma_f32_16x16x32_bf16 v[104:107], a[148:151], v[92:95], v[104:107]\n"
"v_exp_f32 v67, v67\n"
"v_mfma_f32_16x16x32_bf16 v[104:107], a[144:147], v[76:79], v[104:107]\n"
"ds_read_b128 a[128:131], v4 offset:32768\n"
"v_cvt_pk_bf16_f32 v192, v64, v65\n"
"v_mfma_f32_16x16x32_bf16 v[112:115], a[144:147], v[48:51], v[236:239]\n"
"v_exp_f32 v72, v72\n"
"v_mfma_f32_16x16x32_bf16 v[112:115], a[148:151], v[100:103], v[112:115]\n"
"buffer_load_dwordx4 v8, s[48:51], s89 offen offset:1024 sc1 lds\n"
"v_mfma_f32_16x16x32_bf16 v[112:115], a[152:155], v[228:231], v[112:115]\n"
"v_exp_f32 v73, v73\n"
"v_mfma_f32_16x16x32_bf16 v[112:115], a[156:159], v[164:167], v[112:115]\n"
"v_cvt_pk_bf16_f32 v193, v66, v67\n"
"v_add_u32 v9, 0x0, v4\n"
"v_mfma_f32_16x16x32_bf16 v[120:123], a[156:159], v[180:183], v[252:255]\n"
"v_exp_f32 v74, v74\n"
"v_mfma_f32_16x16x32_bf16 v[120:123], a[152:155], v[244:247], v[120:123]\n"
"buffer_load_dwordx4 v8, s[48:51], s89 offen offset:2048 sc1 lds\n"
"v_mfma_f32_16x16x32_bf16 v[120:123], a[148:151], v[116:119], v[120:123]\n"
"v_exp_f32 v75, v75\n"
"v_mfma_f32_16x16x32_bf16 v[120:123], a[144:147], v[56:59], v[120:123]\n"
"ds_read_b128 a[156:159], v4 offset:39936\n"
"v_cvt_pk_bf16_f32 v200, v72, v73\n"
"v_mfma_f32_16x16x32_bf16 a[0:3], a[160:163], v[224:227], a[0:3]\n"
"v_exp_f32 v80, v80\n"
"v_mfma_f32_16x16x32_bf16 a[32:35], a[160:163], v[232:235], a[32:35]\n"
"buffer_load_dwordx4 v8, s[48:51], s89 offen offset:3072 sc1 lds\n"
"v_mfma_f32_16x16x32_bf16 a[64:67], a[160:163], v[240:243], a[64:67]\n"
"v_exp_f32 v81, v81\n"
"v_mfma_f32_16x16x32_bf16 a[96:99], a[160:163], v[248:251], a[96:99]\n"
"v_cvt_pk_bf16_f32 v201, v74, v75\n"
"v_add_f32 v14, v14, v80\n"
"v_mfma_f32_16x16x32_bf16 a[100:103], a[164:167], v[248:251], a[100:103]\n"
"v_exp_f32 v82, v82\n"
"v_mfma_f32_16x16x32_bf16 a[68:71], a[164:167], v[240:243], a[68:71]\n"
"ds_read_b128 a[152:155], v4 offset:38912\n"
"v_add_f32 v14, v14, v81\n"
"v_mfma_f32_16x16x32_bf16 a[36:39], a[164:167], v[232:235], a[36:39]\n"
"v_exp_f32 v83, v83\n"
"v_mfma_f32_16x16x32_bf16 a[4:7], a[164:167], v[224:227], a[4:7]\n"
"v_cvt_pk_bf16_f32 v208, v80, v81\n"
"v_add_f32 v14, v14, v82\n"
"v_mfma_f32_16x16x32_bf16 a[8:11], a[168:171], v[224:227], a[8:11]\n"
"v_exp_f32 v88, v88\n"
"v_mfma_f32_16x16x32_bf16 a[40:43], a[168:171], v[232:235], a[40:43]\n"
"ds_read_b128 a[148:151], v4 offset:37888\n"
"v_add_f32 v14, v14, v83\n"
"v_mfma_f32_16x16x32_bf16 a[72:75], a[168:171], v[240:243], a[72:75]\n"
"v_exp_f32 v89, v89\n"
"s_add_u32 s90, s89, 0x1000\n"
"s_add_u32 m0, s67, 0x1000\n"
"v_mfma_f32_16x16x32_bf16 a[104:107], a[168:171], v[248:251], a[104:107]\n"
"buffer_load_dwordx4 v8, s[48:51], s90 offen offset:0 sc1 lds\n"
"v_cvt_pk_bf16_f32 v209, v82, v83\n"
"v_add_f32 v15, v15, v88\n"
"v_mfma_f32_16x16x32_bf16 a[108:111], a[172:175], v[248:251], a[108:111]\n"
"v_exp_f32 v90, v90\n"
"v_mfma_f32_16x16x32_bf16 a[76:79], a[172:175], v[240:243], a[76:79]\n"
"ds_read_b128 a[144:147], v4 offset:36864\n"
"v_add_f32 v15, v15, v89\n"
"v_mfma_f32_16x16x32_bf16 a[44:47], a[172:175], v[232:235], a[44:47]\n"
"v_exp_f32 v91, v91\n"
"v_mfma_f32_16x16x32_bf16 a[12:15], a[172:175], v[224:227], a[12:15]\n"
"v_cvt_pk_bf16_f32 v216, v88, v89\n"
"v_add_f32 v15, v15, v90\n"
"v_mfma_f32_16x16x32_bf16 a[16:19], a[176:179], v[224:227], a[16:19]\n"
"v_exp_f32 v96, v96\n"
"v_mfma_f32_16x16x32_bf16 a[48:51], a[176:179], v[232:235], a[48:51]\n"
"ds_read_b128 a[160:163], v4 offset:8192\n"
"v_add_f32 v15, v15, v91\n"
"v_mfma_f32_16x16x32_bf16 a[80:83], a[176:179], v[240:243], a[80:83]\n"
"v_exp_f32 v97, v97\n"
"v_mfma_f32_16x16x32_bf16 a[112:115], a[176:179], v[248:251], a[112:115]\n"
"ds_read_b128 a[164:167], v4 offset:9216\n"
"v_cvt_pk_bf16_f32 v217, v90, v91\n"
"v_mfma_f32_16x16x32_bf16 a[116:119], a[180:183], v[248:251], a[116:119]\n"
"v_exp_f32 v98, v98\n"
"v_mfma_f32_16x16x32_bf16 a[84:87], a[180:183], v[240:243], a[84:87]\n"
"buffer_load_dwordx4 v8, s[48:51], s90 offen offset:1024 sc1 lds\n"
"v_mfma_f32_16x16x32_bf16 a[52:55], a[180:183], v[232:235], a[52:55]\n"
"v_exp_f32 v99, v99\n"
"v_mfma_f32_16x16x32_bf16 a[20:23], a[180:183], v[224:227], a[20:23]\n"
"ds_read_b128 a[168:171], v4 offset:10240\n"
"v_cvt_pk_bf16_f32 v194, v96, v97\n"
"v_mfma_f32_16x16x32_bf16 a[24:27], a[184:187], v[224:227], a[24:27]\n"
"v_exp_f32 v104, v104\n"
"v_mfma_f32_16x16x32_bf16 a[56:59], a[184:187], v[232:235], a[56:59]\n"
"buffer_load_dwordx4 v8, s[48:51], s90 offen offset:2048 sc1 lds\n"
"v_mfma_f32_16x16x32_bf16 a[88:91], a[184:187], v[240:243], a[88:91]\n"
"v_exp_f32 v105, v105\n"
"v_mfma_f32_16x16x32_bf16 a[120:123], a[184:187], v[248:251], a[120:123]\n"
"ds_read_b128 a[172:175], v4 offset:11264\n"
"v_cvt_pk_bf16_f32 v195, v98, v99\n"
"v_mfma_f32_16x16x32_bf16 a[124:127], v[172:175], v[248:251], a[124:127]\n"
"v_exp_f32 v106, v106\n"
"v_mfma_f32_16x16x32_bf16 a[92:95], v[172:175], v[240:243], a[92:95]\n"
"buffer_load_dwordx4 v8, s[48:51], s90 offen offset:3072 sc1 lds\n"
"v_mfma_f32_16x16x32_bf16 a[60:63], v[172:175], v[232:235], a[60:63]\n"
"v_exp_f32 v107, v107\n"
"v_mfma_f32_16x16x32_bf16 a[28:31], v[172:175], v[224:227], a[28:31]\n"
"ds_read_b128 a[176:179], v4 offset:12288\n"
"v_cvt_pk_bf16_f32 v202, v104, v105\n"
"s_waitcnt lgkmcnt(5)\n"
"v_mfma_f32_16x16x32_bf16 v[64:67], a[128:131], v[36:39], v[140:143]\n"
"v_exp_f32 v112, v112\n"
"v_mfma_f32_16x16x32_bf16 v[64:67], a[132:135], v[44:47], v[64:67]\n"
"ds_read_b128 a[180:183], v9 offset:13312\n"
"v_mfma_f32_16x16x32_bf16 v[64:67], a[136:139], v[52:55], v[64:67]\n"
"v_exp_f32 v113, v113\n"
"v_mfma_f32_16x16x32_bf16 v[64:67], a[140:143], v[60:63], v[64:67]\n"
"v_cvt_pk_bf16_f32 v203, v106, v107\n"
"v_add_f32 v14, v14, v112\n"
"v_mfma_f32_16x16x32_bf16 v[72:75], a[140:143], v[124:127], v[156:159]\n"
"v_exp_f32 v114, v114\n"
"v_mfma_f32_16x16x32_bf16 v[72:75], a[136:139], v[108:111], v[72:75]\n"
"ds_read_b128 a[184:187], v9 offset:14336\n"
"v_add_f32 v14, v14, v113\n"
"v_mfma_f32_16x16x32_bf16 v[72:75], a[132:135], v[92:95], v[72:75]\n"
"v_exp_f32 v115, v115\n"
"v_mfma_f32_16x16x32_bf16 v[72:75], a[128:131], v[76:79], v[72:75]\n"
"v_cvt_pk_bf16_f32 v210, v112, v113\n"
"v_add_f32 v14, v14, v114\n"
"v_mfma_f32_16x16x32_bf16 v[80:83], a[128:131], v[48:51], v[236:239]\n"
"v_exp_f32 v120, v120\n"
"v_mfma_f32_16x16x32_bf16 v[80:83], a[132:135], v[100:103], v[80:83]\n"
"ds_read_b128 v[172:175], v4 offset:48128\n"
"v_add_f32 v14, v14, v115\n"
"v_mfma_f32_16x16x32_bf16 v[80:83], a[136:139], v[228:231], v[80:83]\n"
"v_exp_f32 v121, v121\n"
"v_mfma_f32_16x16x32_bf16 v[80:83], a[140:143], v[164:167], v[80:83]\n"
"v_cvt_pk_bf16_f32 v211, v114, v115\n"
"v_add_f32 v15, v15, v120\n"
"v_mfma_f32_16x16x32_bf16 v[152:155], v[128:131], v[192:195], v[152:155]\n"
"s_waitcnt vmcnt(8) lgkmcnt(1)\n"
"s_barrier\n"
"v_mfma_f32_16x16x32_bf16 v[88:91], a[140:143], v[180:183], v[252:255]\n"
"buffer_store_dwordx2 v[24:25], v20, s[48:51], s71 offen sc1\n"
"v_exp_f32 v122, v122\n"
"v_mfma_f32_16x16x32_bf16 v[88:91], a[136:139], v[244:247], v[88:91]\n"
"ds_read_b128 a[140:143], v7 offset:3072\n"
"v_add_f32 v15, v15, v121\n"
"v_mfma_f32_16x16x32_bf16 v[88:91], a[132:135], v[116:119], v[88:91]\n"
"v_exp_f32 v123, v123\n"
"v_mfma_f32_16x16x32_bf16 v[88:91], a[128:131], v[56:59], v[88:91]\n"
"v_cvt_pk_bf16_f32 v218, v120, v121\n"
"v_add_f32 v15, v15, v122\n"
"v_mfma_f32_16x16x32_bf16 v[96:99], a[144:147], v[36:39], v[140:143]\n"
"v_exp_f32 v64, v64\n"
"v_mfma_f32_16x16x32_bf16 v[96:99], a[148:151], v[44:47], v[96:99]\n"
"ds_read_b128 a[136:139], v7 offset:2048\n"
"v_add_f32 v15, v15, v123\n"
"v_mfma_f32_16x16x32_bf16 v[96:99], a[152:155], v[52:55], v[96:99]\n"
"v_exp_f32 v65, v65\n"
"v_mfma_f32_16x16x32_bf16 v[96:99], a[156:159], v[60:63], v[96:99]\n"
"ds_read_b128 a[132:135], v7 offset:1024\n"
"v_cvt_pk_bf16_f32 v219, v122, v123\n"
"v_mfma_f32_16x16x32_bf16 v[160:163], v[128:131], v[200:203], v[160:163]\n"
"s_getpc_b64 s[6:7]\n"
"s_sub_u32 s88, s88, 1\n"
"s_cbranch_scc1 L_hand_%=\n"
"s_add_u32 s89, s89, s91\n"
"s_add_u32 m0, s67, 0x8000\n"
"v_mfma_f32_16x16x32_bf16 v[104:107], a[156:159], v[124:127], v[156:159]\n"
"v_exp_f32 v66, v66\n"
"v_mfma_f32_16x16x32_bf16 v[104:107], a[152:155], v[108:111], v[104:107]\n"
"buffer_load_dwordx4 v8, s[48:51], s89 offen offset:0 sc1 lds\n"
"v_mfma_f32_16x16x32_bf16 v[104:107], a[148:151], v[92:95], v[104:107]\n"
"v_exp_f32 v67, v67\n"
"v_mfma_f32_16x16x32_bf16 v[104:107], a[144:147], v[76:79], v[104:107]\n"
"ds_read_b128 a[128:131], v7 offset:0\n"
"v_cvt_pk_bf16_f32 v224, v64, v65\n"
"v_mfma_f32_16x16x32_bf16 v[112:115], a[144:147], v[48:51], v[236:239]\n"
"v_exp_f32 v72, v72\n"
"v_mfma_f32_16x16x32_bf16 v[112:115], a[148:151], v[100:103], v[112:115]\n"
"buffer_load_dwordx4 v8, s[48:51], s89 offen offset:1024 sc1 lds\n"
"v_mfma_f32_16x16x32_bf16 v[112:115], a[152:155], v[228:231], v[112:115]\n"
"v_exp_f32 v73, v73\n"
"v_mfma_f32_16x16x32_bf16 v[112:115], a[156:159], v[164:167], v[112:115]\n"
"v_cvt_pk_bf16_f32 v225, v66, v67\n"
"v_add_u32 v9, 0x8000, v4\n"
"v_mfma_f32_16x16x32_bf16 v[120:123], a[156:159], v[180:183], v[252:255]\n"
"v_exp_f32 v74, v74\n"
"v_mfma_f32_16x16x32_bf16 v[120:123], a[152:155], v[244:247], v[120:123]\n"
"buffer_load_dwordx4 v8, s[48:51], s89 offen offset:2048 sc1 lds\n"
"v_mfma_f32_16x16x32_bf16 v[120:123], a[148:151], v[116:119], v[120:123]\n"
"v_exp_f32 v75, v75\n"
"v_mfma_f32_16x16x32_bf16 v[120:123], a[144:147], v[56:59], v[120:123]\n"
"ds_read_b128 a[156:159], v7 offset:7168\n"
"v_cvt_pk_bf16_f32 v232, v72, v73\n"
"v_mfma_f32_16x16x32_bf16 a[192:195], a[160:163], v[192:195], a[192:195]\n"
"v_exp_f32 v80, v80\n"
"v_mfma_f32_16x16x32_bf16 a[224:227], a[160:163], v[200:203], a[224:227]\n"
"buffer_load_dwordx4 v8, s[48:51], s89 offen offset:3072 sc1 lds\n"
"v_mfma_f32_16x16x32_bf16 a[64:67], a[160:163], v[208:211], a[64:67]\n"
"v_exp_f32 v81, v81\n"
"v_mfma_f32_16x16x32_bf16 a[96:99], a[160:163], v[216:219], a[96:99]\n"
"v_cvt_pk_bf16_f32 v233, v74, v75\n"
"v_add_f32 v14, v14, v80\n"
"v_mfma_f32_16x16x32_bf16 a[100:103], a[164:167], v[216:219], a[100:103]\n"
"v_exp_f32 v82, v82\n"
"v_mfma_f32_16x16x32_bf16 a[68:71], a[164:167], v[208:211], a[68:71]\n"
"ds_read_b128 a[152:155], v7 offset:6144\n"
"v_add_f32 v14, v14, v81\n"
"v_mfma_f32_16x16x32_bf16 a[228:231], a[164:167], v[200:203], a[228:231]\n"
"v_exp_f32 v83, v83\n"
"v_mfma_f32_16x16x32_bf16 a[196:199], a[164:167], v[192:195], a[196:199]\n"
"v_cvt_pk_bf16_f32 v240, v80, v81\n"
"v_add_f32 v14, v14, v82\n"
"v_mfma_f32_16x16x32_bf16 a[200:203], a[168:171], v[192:195], a[200:203]\n"
"v_exp_f32 v88, v88\n"
"v_mfma_f32_16x16x32_bf16 a[232:235], a[168:171], v[200:203], a[232:235]\n"
"ds_read_b128 a[148:151], v7 offset:5120\n"
"v_add_f32 v14, v14, v83\n"
"v_mfma_f32_16x16x32_bf16 a[72:75], a[168:171], v[208:211], a[72:75]\n"
"v_exp_f32 v89, v89\n"
"s_add_u32 s90, s89, 0x1000\n"
"s_add_u32 m0, s67, 0x9000\n"
"v_mfma_f32_16x16x32_bf16 a[104:107], a[168:171], v[216:219], a[104:107]\n"
"buffer_load_dwordx4 v8, s[48:51], s90 offen offset:0 sc1 lds\n"
"v_cvt_pk_bf16_f32 v241, v82, v83\n"
"v_add_f32 v15, v15, v88\n"
"v_mfma_f32_16x16x32_bf16 a[108:111], a[172:175], v[216:219], a[108:111]\n"
"v_exp_f32 v90, v90\n"
"v_mfma_f32_16x16x32_bf16 a[76:79], a[172:175], v[208:211], a[76:79]\n"
"ds_read_b128 a[144:147], v7 offset:4096\n"
"v_add_f32 v15, v15, v89\n"
"v_mfma_f32_16x16x32_bf16 a[236:239], a[172:175], v[200:203], a[236:239]\n"
"v_exp_f32 v91, v91\n"
"v_mfma_f32_16x16x32_bf16 a[204:207], a[172:175], v[192:195], a[204:207]\n"
"v_cvt_pk_bf16_f32 v248, v88, v89\n"
"v_add_f32 v15, v15, v90\n"
"v_mfma_f32_16x16x32_bf16 a[208:211], a[176:179], v[192:195], a[208:211]\n"
"v_exp_f32 v96, v96\n"
"v_mfma_f32_16x16x32_bf16 a[240:243], a[176:179], v[200:203], a[240:243]\n"
"ds_read_b128 a[160:163], v4 offset:40960\n"
"v_add_f32 v15, v15, v91\n"
"v_mfma_f32_16x16x32_bf16 a[80:83], a[176:179], v[208:211], a[80:83]\n"
"v_exp_f32 v97, v97\n"
"v_mfma_f32_16x16x32_bf16 a[112:115], a[176:179], v[216:219], a[112:115]\n"
"ds_read_b128 a[164:167], v4 offset:41984\n"
"v_cvt_pk_bf16_f32 v249, v90, v91\n"
"v_mfma_f32_16x16x32_bf16 a[116:119], a[180:183], v[216:219], a[116:119]\n"
"v_exp_f32 v98, v98\n"
"v_mfma_f32_16x16x32_bf16 a[84:87], a[180:183], v[208:211], a[84:87]\n"
"buffer_load_dwordx4 v8, s[48:51], s90 offen offset:1024 sc1 lds\n"
"v_mfma_f32_16x16x32_bf16 a[244:247], a[180:183], v[200:203], a[244:247]\n"
"v_exp_f32 v99, v99\n"
"v_mfma_f32_16x16x32_bf16 a[212:215], a[180:183], v[192:195], a[212:215]\n"
"ds_read_b128 a[168:171], v4 offset:43008\n"
"v_cvt_pk_bf16_f32 v226, v96, v97\n"
"v_mfma_f32_16x16x32_bf16 a[216:219], a[184:187], v[192:195], a[216:219]\n"
"v_exp_f32 v104, v104\n"
"v_mfma_f32_16x16x32_bf16 a[248:251], a[184:187], v[200:203], a[248:251]\n"
"buffer_load_dwordx4 v8, s[48:51], s90 offen offset:2048 sc1 lds\n"
"v_mfma_f32_16x16x32_bf16 a[88:91], a[184:187], v[208:211], a[88:91]\n"
"v_exp_f32 v105, v105\n"
"v_mfma_f32_16x16x32_bf16 a[120:123], a[184:187], v[216:219], a[120:123]\n"
"ds_read_b128 a[172:175], v4 offset:44032\n"
"v_cvt_pk_bf16_f32 v227, v98, v99\n"
"v_mfma_f32_16x16x32_bf16 a[124:127], a[188:191], v[216:219], a[124:127]\n"
"v_exp_f32 v106, v106\n"
"v_mfma_f32_16x16x32_bf16 a[92:95], a[188:191], v[208:211], a[92:95]\n"
"buffer_load_dwordx4 v8, s[48:51], s90 offen offset:3072 sc1 lds\n"
"v_mfma_f32_16x16x32_bf16 a[252:255], a[188:191], v[200:203], a[252:255]\n"
"v_exp_f32 v107, v107\n"
"v_mfma_f32_16x16x32_bf16 a[220:223], a[188:191], v[192:195], a[220:223]\n"
"ds_read_b128 a[176:179], v4 offset:45056\n"
"v_cvt_pk_bf16_f32 v234, v104, v105\n"
"s_and_b32 s72, s64, 1\n"
"s_lshl_b32 s72, s72, 14\n"
"s_cmp_eq_u32 s66, 0\n"
"s_cselect_b32 s76, s72, 0\n"
"v_add_u32 v23, s76, v1\n"
"v_cndmask_b32 v168, v236, v144, s[86:87]\n"
"v_cndmask_b32 v169, v237, v145, s[86:87]\n"
"v_cndmask_b32 v170, v238, v146, s[86:87]\n"
"v_cndmask_b32 v171, v239, v147, s[86:87]\n"
"v_cndmask_b32 v176, v252, v144, s[86:87]\n"
"v_cndmask_b32 v177, v253, v145, s[86:87]\n"
"v_cndmask_b32 v178, v254, v146, s[86:87]\n"
"v_cndmask_b32 v179, v255, v147, s[86:87]\n"
"v_med3_f32 v64, v136, v144, v140\n"
"v_med3_f32 v65, v137, v145, v141\n"
"v_med3_f32 v66, v138, v146, v142\n"
"v_med3_f32 v67, v139, v147, v143\n"
"s_waitcnt lgkmcnt(5)\n"
"s_nop 0\n"
"v_mfma_f32_16x16x32_bf16 v[64:67], a[128:131], v[36:39], v[64:67]\n"
"v_exp_f32 v112, v112\n"
"v_mfma_f32_16x16x32_bf16 v[64:67], a[132:135], v[44:47], v[64:67]\n"
"ds_read_b128 a[180:183], v9 offset:13312\n"
"v_mfma_f32_16x16x32_bf16 v[64:67], a[136:139], v[52:55], v[64:67]\n"
"v_exp_f32 v113, v113\n"
"v_mfma_f32_16x16x32_bf16 v[64:67], a[140:143], v[60:63], v[64:67]\n"
"v_cvt_pk_bf16_f32 v235, v106, v107\n"
"v_add_f32 v14, v14, v112\n"
"v_mfma_f32_16x16x32_bf16 v[72:75], a[140:143], v[124:127], v[156:159]\n"
"v_exp_f32 v114, v114\n"
"v_mfma_f32_16x16x32_bf16 v[72:75], a[136:139], v[108:111], v[72:75]\n"
"ds_read_b128 a[184:187], v9 offset:14336\n"
"v_add_f32 v14, v14, v113\n"
"v_med3_f32 v104, v136, v144, v156\n"
"v_med3_f32 v105, v137, v145, v157\n"
"v_med3_f32 v106, v138, v146, v158\n"
"v_med3_f32 v107, v139, v147, v159\n"
"v_mfma_f32_16x16x32_bf16 v[72:75], a[132:135], v[92:95], v[72:75]\n"
"v_exp_f32 v115, v115\n"
"v_mfma_f32_16x16x32_bf16 v[72:75], a[128:131], v[76:79], v[72:75]\n"
"v_cvt_pk_bf16_f32 v242, v112, v113\n"
"v_add_f32 v14, v14, v114\n"
"v_mfma_f32_16x16x32_bf16 v[80:83], a[128:131], v[48:51], v[168:171]\n"
"v_exp_f32 v120, v120\n"
"v_mfma_f32_16x16x32_bf16 v[80:83], a[132:135], v[100:103], v[80:83]\n"
"ds_read_b128 a[188:191], v7 offset:15360\n"
"v_add_f32 v14, v14, v115\n"
"v_mfma_f32_16x16x32_bf16 v[80:83], a[136:139], v[228:231], v[80:83]\n"
"v_exp_f32 v121, v121\n"
"v_mfma_f32_16x16x32_bf16 v[80:83], a[140:143], v[164:167], v[80:83]\n"
"v_cvt_pk_bf16_f32 v243, v114, v115\n"
"v_add_f32 v15, v15, v120\n"
"v_mfma_f32_16x16x32_bf16 v[152:155], v[128:131], v[224:227], v[152:155]\n"
"s_waitcnt vmcnt(8) lgkmcnt(1)\n"
"s_barrier\n"
"v_mfma_f32_16x16x32_bf16 v[88:91], a[140:143], v[180:183], v[176:179]\n"
"v_exp_f32 v122, v122\n"
"v_mfma_f32_16x16x32_bf16 v[88:91], a[136:139], v[244:247], v[88:91]\n"
"ds_read_b128 a[140:143], v23 offset:3072\n"
"v_add_f32 v15, v15, v121\n"
"v_mfma_f32_16x16x32_bf16 v[88:91], a[132:135], v[116:119], v[88:91]\n"
"v_exp_f32 v123, v123\n"
"v_mfma_f32_16x16x32_bf16 v[88:91], a[128:131], v[56:59], v[88:91]\n"
"v_cvt_pk_bf16_f32 v250, v120, v121\n"
"v_add_f32 v15, v15, v122\n"
"v_mfma_f32_16x16x32_bf16 v[96:99], a[144:147], v[36:39], v[144:147]\n"
"v_exp_f32 v64, v64\n"
"v_mfma_f32_16x16x32_bf16 v[96:99], a[148:151], v[44:47], v[96:99]\n"
"ds_read_b128 a[136:139], v23 offset:2048\n"
"v_add_f32 v15, v15, v123\n"
"v_mfma_f32_16x16x32_bf16 v[96:99], a[152:155], v[52:55], v[96:99]\n"
"v_exp_f32 v65, v65\n"
"v_mfma_f32_16x16x32_bf16 v[96:99], a[156:159], v[60:63], v[96:99]\n"
"ds_read_b128 a[132:135], v23 offset:1024\n"
"v_cvt_pk_bf16_f32 v251, v122, v123\n"
"v_mfma_f32_16x16x32_bf16 v[160:163], v[128:131], v[232:235], v[160:163]\n"
"s_getpc_b64 s[6:7]\n"
"s_sub_u32 s88, s88, 1\n"
"s_cbranch_scc1 L_hand_%=\n"
"s_add_u32 s89, s89, s91\n"
"s_add_u32 m0, s67, 0x10000\n"
"v_mfma_f32_16x16x32_bf16 v[104:107], a[156:159], v[124:127], v[104:107]\n"
"v_exp_f32 v66, v66\n"
"v_mfma_f32_16x16x32_bf16 v[104:107], a[152:155], v[108:111], v[104:107]\n"
"buffer_load_dwordx4 v8, s[48:51], s89 offen offset:0 sc1 lds\n"
"v_mfma_f32_16x16x32_bf16 v[104:107], a[148:151], v[92:95], v[104:107]\n"
"v_exp_f32 v67, v67\n"
"v_mfma_f32_16x16x32_bf16 v[104:107], a[144:147], v[76:79], v[104:107]\n"
"ds_read_b128 a[128:131], v23 offset:0\n"
"v_cvt_pk_bf16_f32 v192, v64, v65\n"
"v_mfma_f32_16x16x32_bf16 v[112:115], a[144:147], v[48:51], v[168:171]\n"
"v_exp_f32 v72, v72\n"
"v_mfma_f32_16x16x32_bf16 v[112:115], a[148:151], v[100:103], v[112:115]\n"
"buffer_load_dwordx4 v8, s[48:51], s89 offen offset:1024 sc1 lds\n"
"v_mfma_f32_16x16x32_bf16 v[112:115], a[152:155], v[228:231], v[112:115]\n"
"v_exp_f32 v73, v73\n"
"v_mfma_f32_16x16x32_bf16 v[112:115], a[156:159], v[164:167], v[112:115]\n"
"v_cvt_pk_bf16_f32 v193, v66, v67\n"
"v_add_u32 v9, 0x0, v7\n"
"v_mfma_f32_16x16x32_bf16 v[120:123], a[156:159], v[180:183], v[176:179]\n"
"v_exp_f32 v74, v74\n"
"v_mfma_f32_16x16x32_bf16 v[120:123], a[152:155], v[244:247], v[120:123]\n"
"buffer_load_dwordx4 v8, s[48:51], s89 offen offset:2048 sc1 lds\n"
"v_mfma_f32_16x16x32_bf16 v[120:123], a[148:151], v[116:119], v[120:123]\n"
"v_exp_f32 v75, v75\n"
"v_mfma_f32_16x16x32_bf16 v[120:123], a[144:147], v[56:59], v[120:123]\n"
"ds_read_b128 a[156:159], v23 offset:7168\n"
"v_cvt_pk_bf16_f32 v200, v72, v73\n"
"v_mfma_f32_16x16x32_bf16 a[192:195], a[160:163], v[224:227], a[192:195]\n"
"v_exp_f32 v80, v80\n"
"v_mfma_f32_16x16x32_bf16 a[224:227], a[160:163], v[232:235], a[224:227]\n"
"buffer_load_dwordx4 v8, s[48:51], s89 offen offset:3072 sc1 lds\n"
"v_mfma_f32_16x16x32_bf16 a[64:67], a[160:163], v[240:243], a[64:67]\n"
"v_exp_f32 v81, v81\n"
"v_mfma_f32_16x16x32_bf16 a[96:99], a[160:163], v[248:251], a[96:99]\n"
"v_cvt_pk_bf16_f32 v201, v74, v75\n"
"v_add_f32 v14, v14, v80\n"
"v_mfma_f32_16x16x32_bf16 a[100:103], a[164:167], v[248:251], a[100:103]\n"
"v_exp_f32 v82, v82\n"
"v_mfma_f32_16x16x32_bf16 a[68:71], a[164:167], v[240:243], a[68:71]\n"
"ds_read_b128 a[152:155], v23 offset:6144\n"
"v_add_f32 v14, v14, v81\n"
"v_mfma_f32_16x16x32_bf16 a[228:231], a[164:167], v[232:235], a[228:231]\n"
"v_exp_f32 v83, v83\n"
"v_mfma_f32_16x16x32_bf16 a[196:199], a[164:167], v[224:227], a[196:199]\n"
"v_cvt_pk_bf16_f32 v208, v80, v81\n"
"v_add_f32 v14, v14, v82\n"
"v_mfma_f32_16x16x32_bf16 a[200:203], a[168:171], v[224:227], a[200:203]\n"
"v_exp_f32 v88, v88\n"
"v_mfma_f32_16x16x32_bf16 a[232:235], a[168:171], v[232:235], a[232:235]\n"
"ds_read_b128 a[148:151], v23 offset:5120\n"
"v_add_f32 v14, v14, v83\n"
"v_mfma_f32_16x16x32_bf16 a[72:75], a[168:171], v[240:243], a[72:75]\n"
"v_exp_f32 v89, v89\n"
"s_add_u32 s90, s89, 0x1000\n"
"s_add_u32 m0, s67, 0x11000\n"
"v_mfma_f32_16x16x32_bf16 a[104:107], a[168:171], v[248:251], a[104:107]\n"
"buffer_load_dwordx4 v8, s[48:51], s90 offen offset:0 sc1 lds\n"
"v_cvt_pk_bf16_f32 v209, v82, v83\n"
"v_add_f32 v15, v15, v88\n"
"v_mfma_f32_16x16x32_bf16 a[108:111], a[172:175], v[248:251], a[108:111]\n"
"v_exp_f32 v90, v90\n"
"v_mfma_f32_16x16x32_bf16 a[76:79], a[172:175], v[240:243], a[76:79]\n"
"ds_read_b128 a[144:147], v23 offset:4096\n"
"v_add_f32 v15, v15, v89\n"
"v_mfma_f32_16x16x32_bf16 a[236:239], a[172:175], v[232:235], a[236:239]\n"
"v_exp_f32 v91, v91\n"
"v_mfma_f32_16x16x32_bf16 a[204:207], a[172:175], v[224:227], a[204:207]\n"
"v_cvt_pk_bf16_f32 v216, v88, v89\n"
"v_add_f32 v15, v15, v90\n"
"v_mfma_f32_16x16x32_bf16 a[208:211], a[176:179], v[224:227], a[208:211]\n"
"v_exp_f32 v96, v96\n"
"v_mfma_f32_16x16x32_bf16 a[240:243], a[176:179], v[232:235], a[240:243]\n"
"ds_read_b128 a[160:163], v7 offset:8192\n"
"v_add_f32 v15, v15, v91\n"
"v_mfma_f32_16x16x32_bf16 a[80:83], a[176:179], v[240:243], a[80:83]\n"
"v_exp_f32 v97, v97\n"
"v_mfma_f32_16x16x32_bf16 a[112:115], a[176:179], v[248:251], a[112:115]\n"
"ds_read_b128 a[164:167], v7 offset:9216\n"
"v_cvt_pk_bf16_f32 v217, v90, v91\n"
"v_mfma_f32_16x16x32_bf16 a[116:119], a[180:183], v[248:251], a[116:119]\n"
"v_exp_f32 v98, v98\n"
"v_mfma_f32_16x16x32_bf16 a[84:87], a[180:183], v[240:243], a[84:87]\n"
"buffer_load_dwordx4 v8, s[48:51], s90 offen offset:1024 sc1 lds\n"
"v_mfma_f32_16x16x32_bf16 a[244:247], a[180:183], v[232:235], a[244:247]\n"
"v_exp_f32 v99, v99\n"
"v_mfma_f32_16x16x32_bf16 a[212:215], a[180:183], v[224:227], a[212:215]\n"
"ds_read_b128 a[168:171], v7 offset:10240\n"
"v_cvt_pk_bf16_f32 v194, v96, v97\n"
"v_mfma_f32_16x16x32_bf16 a[216:219], a[184:187], v[224:227], a[216:219]\n"
"v_exp_f32 v104, v104\n"
"v_mfma_f32_16x16x32_bf16 a[248:251], a[184:187], v[232:235], a[248:251]\n"
"buffer_load_dwordx4 v8, s[48:51], s90 offen offset:2048 sc1 lds\n"
"v_mfma_f32_16x16x32_bf16 a[88:91], a[184:187], v[240:243], a[88:91]\n"
"v_exp_f32 v105, v105\n"
"v_mfma_f32_16x16x32_bf16 a[120:123], a[184:187], v[248:251], a[120:123]\n"
"ds_read_b128 a[172:175], v7 offset:11264\n"
"v_cvt_pk_bf16_f32 v195, v98, v99\n"
"v_mfma_f32_16x16x32_bf16 a[124:127], v[172:175], v[248:251], a[124:127]\n"
"v_exp_f32 v106, v106\n"
"v_mfma_f32_16x16x32_bf16 a[92:95], v[172:175], v[240:243], a[92:95]\n"
"buffer_load_dwordx4 v8, s[48:51], s90 offen offset:3072 sc1 lds\n"
"v_mfma_f32_16x16x32_bf16 a[252:255], v[172:175], v[232:235], a[252:255]\n"
"v_exp_f32 v107, v107\n"
"v_mfma_f32_16x16x32_bf16 a[220:223], v[172:175], v[224:227], a[220:223]\n"
"ds_read_b128 a[176:179], v7 offset:12288\n"
"v_cvt_pk_bf16_f32 v202, v104, v105\n"
"s_branch L_remote_%=\n"
".p2align 3\n"
"L_pair23_%=:\n"
"s_waitcnt lgkmcnt(0)\n"
"s_nop 0\n"
"s_add_u32 s35, s33, 0x2c00\n"
"v_mfma_f32_16x16x32_bf16 v[64:67], a[128:131], v[32:35], v[204:207]\n"
"buffer_store_dwordx4 a[24:27], v1, s[48:51], s35 offen sc1\n"
"v_exp_f32 v112, v112\n"
"v_mfma_f32_16x16x32_bf16 v[64:67], a[132:135], v[68:71], v[64:67]\n"
"v_mfma_f32_16x16x32_bf16 v[64:67], a[136:139], v[196:199], v[64:67]\n"
"v_exp_f32 v113, v113\n"
"v_mfma_f32_16x16x32_bf16 v[64:67], a[140:143], v[132:135], v[64:67]\n"
"v_add_f32 v14, v14, v112\n"
"v_mfma_f32_16x16x32_bf16 v[72:75], a[140:143], v[148:151], v[220:223]\n"
"v_exp_f32 v114, v114\n"
"v_mfma_f32_16x16x32_bf16 v[72:75], a[136:139], v[212:215], v[72:75]\n"
"v_add_f32 v14, v14, v113\n"
"v_mfma_f32_16x16x32_bf16 v[72:75], a[132:135], v[84:87], v[72:75]\n"
"v_exp_f32 v115, v115\n"
"v_mfma_f32_16x16x32_bf16 v[72:75], a[128:131], v[40:43], v[72:75]\n"
"v_cvt_pk_bf16_f32 v210, v112, v113\n"
"v_add_f32 v14, v14, v114\n"
"v_add_u32 v9, 0xc000, v1\n"
"v_mfma_f32_16x16x32_bf16 v[80:83], a[128:131], v[48:51], v[236:239]\n"
"v_exp_f32 v120, v120\n"
"v_mfma_f32_16x16x32_bf16 v[80:83], a[132:135], v[100:103], v[80:83]\n"
"ds_read_b128 v[172:175], v1 offset:64512\n"
"v_add_f32 v14, v14, v115\n"
"v_mfma_f32_16x16x32_bf16 v[80:83], a[136:139], v[228:231], v[80:83]\n"
"v_exp_f32 v121, v121\n"
"v_mfma_f32_16x16x32_bf16 v[80:83], a[140:143], v[164:167], v[80:83]\n"
"v_cvt_pk_bf16_f32 v211, v114, v115\n"
"v_add_f32 v15, v15, v120\n"
"s_waitcnt lgkmcnt(0)\n"
"s_barrier\n"
"v_mfma_f32_16x16x32_bf16 v[88:91], a[140:143], v[180:183], v[252:255]\n"
"v_exp_f32 v122, v122\n"
"v_mfma_f32_16x16x32_bf16 v[88:91], a[136:139], v[244:247], v[88:91]\n"
"ds_read_b128 a[140:143], v2 offset:3072\n"
"v_add_f32 v15, v15, v121\n"
"v_mfma_f32_16x16x32_bf16 v[88:91], a[132:135], v[116:119], v[88:91]\n"
"v_exp_f32 v123, v123\n"
"v_mfma_f32_16x16x32_bf16 v[88:91], a[128:131], v[56:59], v[88:91]\n"
"ds_read_b128 v[36:39], v27 offset:0\n"
"v_cvt_pk_bf16_f32 v218, v120, v121\n"
"v_add_f32 v15, v15, v122\n"
"v_mfma_f32_16x16x32_bf16 v[96:99], a[144:147], v[32:35], v[204:207]\n"
"v_exp_f32 v64, v64\n"
"v_mfma_f32_16x16x32_bf16 v[96:99], a[148:151], v[68:71], v[96:99]\n"
"ds_read_b128 a[136:139], v2 offset:2048\n"
"v_add_f32 v15, v15, v123\n"
"v_mfma_f32_16x16x32_bf16 v[96:99], a[152:155], v[196:199], v[96:99]\n"
"v_exp_f32 v65, v65\n"
"v_mfma_f32_16x16x32_bf16 v[96:99], a[156:159], v[132:135], v[96:99]\n"
"ds_read_b128 v[44:47], v27 offset:1024\n"
"v_cvt_pk_bf16_f32 v219, v122, v123\n"
"v_add_f32 v12, v12, v64\n"
"v_mfma_f32_16x16x32_bf16 v[104:107], a[156:159], v[148:151], v[220:223]\n"
"buffer_load_dwordx4 v[188:191], v31, s[48:51], s75 offen sc1\n"
"v_exp_f32 v66, v66\n"
"v_mfma_f32_16x16x32_bf16 v[104:107], a[152:155], v[212:215], v[104:107]\n"
"ds_read_b128 a[132:135], v2 offset:1024\n"
"v_add_f32 v12, v12, v65\n"
"v_mfma_f32_16x16x32_bf16 v[104:107], a[148:151], v[84:87], v[104:107]\n"
"buffer_load_dwordx4 v[184:187], v31, s[48:51], s75 offen offset:16 sc1\n"
"v_exp_f32 v67, v67\n"
"v_mfma_f32_16x16x32_bf16 v[104:107], a[144:147], v[40:43], v[104:107]\n"
"ds_read_b128 v[52:55], v27 offset:2048\n"
"v_cvt_pk_bf16_f32 v224, v64, v65\n"
"v_add_f32 v12, v12, v66\n"
"v_mfma_f32_16x16x32_bf16 v[112:115], a[144:147], v[48:51], v[236:239]\n"
"buffer_load_dwordx2 v[28:29], v6, s[48:51], s34 offen sc1\n"
"v_exp_f32 v72, v72\n"
"v_mfma_f32_16x16x32_bf16 v[112:115], a[148:151], v[100:103], v[112:115]\n"
"ds_read_b128 a[128:131], v2 offset:0\n"
"v_add_f32 v12, v12, v67\n"
"v_mfma_f32_16x16x32_bf16 v[112:115], a[152:155], v[228:231], v[112:115]\n"
"v_exp_f32 v73, v73\n"
"v_mfma_f32_16x16x32_bf16 v[112:115], a[156:159], v[164:167], v[112:115]\n"
"ds_read_b128 v[60:63], v27 offset:3072\n"
"v_cvt_pk_bf16_f32 v225, v66, v67\n"
"v_add_f32 v13, v13, v72\n"
"v_mfma_f32_16x16x32_bf16 v[120:123], a[156:159], v[180:183], v[252:255]\n"
"v_exp_f32 v74, v74\n"
"v_mfma_f32_16x16x32_bf16 v[120:123], a[152:155], v[244:247], v[120:123]\n"
"ds_read_b128 a[156:159], v2 offset:7168\n"
"v_add_f32 v13, v13, v73\n"
"s_add_u32 s35, s33, 0x6000\n"
"v_mfma_f32_16x16x32_bf16 v[120:123], a[148:151], v[116:119], v[120:123]\n"
"buffer_store_dwordx4 a[32:35], v1, s[48:51], s35 offen sc1\n"
"v_exp_f32 v75, v75\n"
"v_mfma_f32_16x16x32_bf16 v[120:123], a[144:147], v[56:59], v[120:123]\n"
"ds_read_b128 v[76:79], v27 offset:4096\n"
"v_cvt_pk_bf16_f32 v232, v72, v73\n"
"v_add_f32 v13, v13, v74\n"
"v_mfma_f32_16x16x32_bf16 a[0:3], a[160:163], v[192:195], 0\n"
"v_exp_f32 v80, v80\n"
"v_mfma_f32_16x16x32_bf16 a[32:35], a[160:163], v[200:203], 0\n"
"ds_read_b128 a[152:155], v2 offset:6144\n"
"v_add_f32 v13, v13, v75\n"
"v_mfma_f32_16x16x32_bf16 a[64:67], a[160:163], v[208:211], 0\n"
"v_exp_f32 v81, v81\n"
"v_mfma_f32_16x16x32_bf16 a[96:99], a[160:163], v[216:219], 0\n"
"ds_read_b128 v[92:95], v27 offset:5120\n"
"v_cvt_pk_bf16_f32 v233, v74, v75\n"
"v_add_f32 v14, v14, v80\n"
"v_mfma_f32_16x16x32_bf16 a[100:103], a[164:167], v[216:219], 0\n"
"v_exp_f32 v82, v82\n"
"v_mfma_f32_16x16x32_bf16 a[68:71], a[164:167], v[208:211], 0\n"
"ds_read_b128 a[148:151], v2 offset:5120\n"
"v_add_f32 v14, v14, v81\n"
"s_add_u32 s35, s33, 0x6400\n"
"v_mfma_f32_16x16x32_bf16 a[36:39], a[164:167], v[200:203], 0\n"
"buffer_store_dwordx4 a[40:43], v1, s[48:51], s35 offen sc1\n"
"v_exp_f32 v83, v83\n"
"v_mfma_f32_16x16x32_bf16 a[4:7], a[164:167], v[192:195], 0\n"
"ds_read_b128 v[108:111], v27 offset:6144\n"
"v_cvt_pk_bf16_f32 v240, v80, v81\n"
"v_add_f32 v14, v14, v82\n"
"v_mfma_f32_16x16x32_bf16 a[8:11], a[168:171], v[192:195], 0\n"
"v_exp_f32 v88, v88\n"
"v_mfma_f32_16x16x32_bf16 a[40:43], a[168:171], v[200:203], 0\n"
"ds_read_b128 a[144:147], v2 offset:4096\n"
"v_add_f32 v14, v14, v83\n"
"v_mfma_f32_16x16x32_bf16 a[72:75], a[168:171], v[208:211], 0\n"
"v_exp_f32 v89, v89\n"
"v_mfma_f32_16x16x32_bf16 a[104:107], a[168:171], v[216:219], 0\n"
"ds_read_b128 v[124:127], v27 offset:7168\n"
"v_cvt_pk_bf16_f32 v241, v82, v83\n"
"v_add_f32 v15, v15, v88\n"
"v_mfma_f32_16x16x32_bf16 a[108:111], a[172:175], v[216:219], 0\n"
"v_exp_f32 v90, v90\n"
"v_mfma_f32_16x16x32_bf16 a[76:79], a[172:175], v[208:211], 0\n"
"ds_read_b128 a[160:163], v1 offset:57344\n"
"v_add_f32 v15, v15, v89\n"
"s_add_u32 s35, s33, 0x6800\n"
"v_mfma_f32_16x16x32_bf16 a[44:47], a[172:175], v[200:203], 0\n"
"buffer_store_dwordx4 a[48:51], v1, s[48:51], s35 offen sc1\n"
"v_exp_f32 v91, v91\n"
"v_mfma_f32_16x16x32_bf16 a[12:15], a[172:175], v[192:195], 0\n"
"ds_read_b64 v[24:25], v26 offset:32768\n"
"v_cvt_pk_bf16_f32 v248, v88, v89\n"
"v_add_f32 v15, v15, v90\n"
"v_mfma_f32_16x16x32_bf16 a[16:19], a[176:179], v[192:195], 0\n"
"v_exp_f32 v96, v96\n"
"v_mfma_f32_16x16x32_bf16 a[48:51], a[176:179], v[200:203], 0\n"
"ds_read_b128 a[164:167], v1 offset:58368\n"
"v_add_f32 v15, v15, v91\n"
"v_mfma_f32_16x16x32_bf16 a[80:83], a[176:179], v[208:211], 0\n"
"v_exp_f32 v97, v97\n"
"v_mfma_f32_16x16x32_bf16 a[112:115], a[176:179], v[216:219], 0\n"
"v_cvt_pk_bf16_f32 v249, v90, v91\n"
"v_add_f32 v12, v12, v96\n"
"v_mfma_f32_16x16x32_bf16 a[116:119], a[180:183], v[216:219], 0\n"
"v_exp_f32 v98, v98\n"
"v_mfma_f32_16x16x32_bf16 a[84:87], a[180:183], v[208:211], 0\n"
"ds_read_b128 a[168:171], v1 offset:59392\n"
"v_add_f32 v12, v12, v97\n"
"s_add_u32 s35, s33, 0x6c00\n"
"v_mfma_f32_16x16x32_bf16 a[52:55], a[180:183], v[200:203], 0\n"
"buffer_store_dwordx4 a[56:59], v1, s[48:51], s35 offen sc1\n"
"v_exp_f32 v99, v99\n"
"v_mfma_f32_16x16x32_bf16 a[20:23], a[180:183], v[192:195], 0\n"
"v_cvt_pk_bf16_f32 v226, v96, v97\n"
"v_add_f32 v12, v12, v98\n"
"v_mfma_f32_16x16x32_bf16 a[24:27], a[184:187], v[192:195], 0\n"
"v_exp_f32 v104, v104\n"
"v_mfma_f32_16x16x32_bf16 a[56:59], a[184:187], v[200:203], 0\n"
"ds_read_b128 a[172:175], v1 offset:60416\n"
"v_add_f32 v12, v12, v99\n"
"v_mfma_f32_16x16x32_bf16 a[88:91], a[184:187], v[208:211], 0\n"
"v_exp_f32 v105, v105\n"
"v_mfma_f32_16x16x32_bf16 a[120:123], a[184:187], v[216:219], 0\n"
"v_cvt_pk_bf16_f32 v227, v98, v99\n"
"v_add_f32 v13, v13, v104\n"
"s_waitcnt lgkmcnt(3)\n"
"v_mov_b32 v140, v24\n"
"v_mov_b32 v141, v24\n"
"v_mov_b32 v142, v24\n"
"v_mov_b32 v143, v24\n"
"v_mov_b32 v156, v25\n"
"v_mov_b32 v157, v25\n"
"v_mov_b32 v158, v25\n"
"v_mov_b32 v159, v25\n"
"v_mfma_f32_16x16x32_bf16 a[124:127], a[188:191], v[216:219], 0\n"
"v_exp_f32 v106, v106\n"
"v_mfma_f32_16x16x32_bf16 a[92:95], a[188:191], v[208:211], 0\n"
"ds_read_b128 a[176:179], v1 offset:61440\n"
"v_add_f32 v13, v13, v105\n"
"v_mfma_f32_16x16x32_bf16 a[60:63], a[188:191], v[200:203], 0\n"
"v_exp_f32 v107, v107\n"
"v_mfma_f32_16x16x32_bf16 a[28:31], a[188:191], v[192:195], 0\n"
"v_cvt_pk_bf16_f32 v234, v104, v105\n"
"v_add_f32 v13, v13, v106\n"
"s_waitcnt vmcnt(4)\n"
"v_cmp_eq_u64 vcc, s[68:69], v[28:29]\n"
"s_nop 0\n"
"s_cmp_eq_u64 vcc, -1\n"
"s_cbranch_scc0 L_kv1_spin%=\n"
"LT_kv1_ok%=:\n"
"s_waitcnt vmcnt(5)\n"
"v_cmp_eq_u64 vcc, s[68:69], v[188:189]\n"
"v_cmp_eq_u64 s[84:85], s[68:69], v[190:191]\n"
"s_nop 0\n"
"s_and_b64 vcc, vcc, s[84:85]\n"
"v_cmp_eq_u64 s[84:85], s[68:69], v[184:185]\n"
"s_nop 0\n"
"s_and_b64 vcc, vcc, s[84:85]\n"
"v_cmp_eq_u64 s[84:85], s[68:69], v[186:187]\n"
"s_nop 0\n"
"s_and_b64 vcc, vcc, s[84:85]\n"
"s_cmp_eq_u64 vcc, -1\n"
"s_cbranch_scc0 L_ff1_spin%=\n"
"LT_ff1_ok%=:\n"
"s_add_u32 s71, s71, 0x1800\n"
"s_add_u32 s34, s34, 0x1800\n"
"v_mov_b32 v24, s68\n"
"v_mov_b32 v25, s69\n"
"v_cndmask_b32 v168, v144, v204, s[86:87]\n"
"v_cndmask_b32 v169, v145, v205, s[86:87]\n"
"v_cndmask_b32 v170, v146, v206, s[86:87]\n"
"v_cndmask_b32 v171, v147, v207, s[86:87]\n"
"v_cndmask_b32 v176, v144, v220, s[86:87]\n"
"v_med3_f32 v64, v136, v168, v204\n"
"v_med3_f32 v65, v137, v169, v205\n"
"v_med3_f32 v66, v138, v170, v206\n"
"v_med3_f32 v67, v139, v171, v207\n"
"s_waitcnt lgkmcnt(5)\n"
"s_nop 0\n"
"v_mfma_f32_16x16x32_bf16 v[64:67], a[128:131], v[32:35], v[64:67]\n"
"v_exp_f32 v112, v112\n"
"v_mfma_f32_16x16x32_bf16 v[64:67], a[132:135], v[68:71], v[64:67]\n"
"ds_read_b128 a[180:183], v9 offset:13312\n"
"v_add_f32 v13, v13, v107\n"
"v_mfma_f32_16x16x32_bf16 v[64:67], a[136:139], v[196:199], v[64:67]\n"
"v_exp_f32 v113, v113\n"
"v_mfma_f32_16x16x32_bf16 v[64:67], a[140:143], v[132:135], v[64:67]\n"
"v_cvt_pk_bf16_f32 v235, v106, v107\n"
"v_add_f32 v14, v14, v112\n"
"v_mfma_f32_16x16x32_bf16 v[72:75], a[140:143], v[148:151], v[220:223]\n"
"v_exp_f32 v114, v114\n"
"v_mfma_f32_16x16x32_bf16 v[72:75], a[136:139], v[212:215], v[72:75]\n"
"ds_read_b128 a[184:187], v9 offset:14336\n"
"v_add_f32 v14, v14, v113\n"
"v_med3_f32 v104, v136, v176, v220\n"
"v_med3_f32 v105, v137, v176, v221\n"
"v_med3_f32 v106, v138, v176, v222\n"
"v_med3_f32 v107, v139, v176, v223\n"
"v_mfma_f32_16x16x32_bf16 v[72:75], a[132:135], v[84:87], v[72:75]\n"
"v_exp_f32 v115, v115\n"
"v_mfma_f32_16x16x32_bf16 v[72:75], a[128:131], v[40:43], v[72:75]\n"
"v_cvt_pk_bf16_f32 v242, v112, v113\n"
"v_add_f32 v14, v14, v114\n"
"v_add_u32 v9, 0x0, v2\n"
"v_mfma_f32_16x16x32_bf16 v[80:83], a[128:131], v[48:51], v[236:239]\n"
"v_exp_f32 v120, v120\n"
"v_mfma_f32_16x16x32_bf16 v[80:83], a[132:135], v[100:103], v[80:83]\n"
"ds_read_b128 a[188:191], v2 offset:15360\n"
"v_add_f32 v14, v14, v115\n"
"v_mfma_f32_16x16x32_bf16 v[80:83], a[136:139], v[228:231], v[80:83]\n"
"v_exp_f32 v121, v121\n"
"v_mfma_f32_16x16x32_bf16 v[80:83], a[140:143], v[164:167], v[80:83]\n"
"v_cvt_pk_bf16_f32 v243, v114, v115\n"
"v_add_f32 v15, v15, v120\n"
"s_waitcnt lgkmcnt(1)\n"
"s_barrier\n"
"v_mfma_f32_16x16x32_bf16 v[88:91], a[140:143], v[180:183], v[252:255]\n"
"v_exp_f32 v122, v122\n"
"v_mfma_f32_16x16x32_bf16 v[88:91], a[136:139], v[244:247], v[88:91]\n"
"ds_read_b128 a[140:143], v2 offset:19456\n"
"v_add_f32 v15, v15, v121\n"
"v_mfma_f32_16x16x32_bf16 v[88:91], a[132:135], v[116:119], v[88:91]\n"
"v_exp_f32 v123, v123\n"
"s_getpc_b64 s[6:7]\n"
"s_sub_u32 s88, s88, 1\n"
"s_cbranch_scc1 L_hand_%=\n"
"s_add_u32 s89, s89, s91\n"
"s_add_u32 m0, s67, 0x0\n"
"v_mfma_f32_16x16x32_bf16 v[88:91], a[128:131], v[56:59], v[88:91]\n"
"buffer_load_dwordx4 v8, s[48:51], s89 offen offset:0 sc1 lds\n"
"v_cvt_pk_bf16_f32 v250, v120, v121\n"
"v_add_f32 v15, v15, v122\n"
"v_mfma_f32_16x16x32_bf16 v[96:99], a[144:147], v[32:35], v[168:171]\n"
"v_exp_f32 v64, v64\n"
"v_mfma_f32_16x16x32_bf16 v[96:99], a[148:151], v[68:71], v[96:99]\n"
"ds_read_b128 a[136:139], v2 offset:18432\n"
"v_add_f32 v15, v15, v123\n"
"v_mfma_f32_16x16x32_bf16 v[96:99], a[152:155], v[196:199], v[96:99]\n"
"v_exp_f32 v65, v65\n"
"v_mfma_f32_16x16x32_bf16 v[96:99], a[156:159], v[132:135], v[96:99]\n"
"buffer_load_dwordx4 v8, s[48:51], s89 offen offset:1024 sc1 lds\n"
"v_cvt_pk_bf16_f32 v251, v122, v123\n"
"v_add_f32 v12, v12, v64\n"
"v_mfma_f32_16x16x32_bf16 v[104:107], a[156:159], v[148:151], v[104:107]\n"
"v_exp_f32 v66, v66\n"
"v_mfma_f32_16x16x32_bf16 v[104:107], a[152:155], v[212:215], v[104:107]\n"
"ds_read_b128 a[132:135], v2 offset:17408\n"
"v_add_f32 v12, v12, v65\n"
"v_mfma_f32_16x16x32_bf16 v[104:107], a[148:151], v[84:87], v[104:107]\n"
"v_exp_f32 v67, v67\n"
"v_mfma_f32_16x16x32_bf16 v[104:107], a[144:147], v[40:43], v[104:107]\n"
"buffer_load_dwordx4 v8, s[48:51], s89 offen offset:2048 sc1 lds\n"
"v_cvt_pk_bf16_f32 v192, v64, v65\n"
"v_add_f32 v12, v12, v66\n"
"v_mfma_f32_16x16x32_bf16 v[112:115], a[144:147], v[48:51], v[236:239]\n"
"v_exp_f32 v72, v72\n"
"v_mfma_f32_16x16x32_bf16 v[112:115], a[148:151], v[100:103], v[112:115]\n"
"ds_read_b128 a[128:131], v2 offset:16384\n"
"v_add_f32 v12, v12, v67\n"
"v_mfma_f32_16x16x32_bf16 v[112:115], a[152:155], v[228:231], v[112:115]\n"
"v_exp_f32 v73, v73\n"
"v_mfma_f32_16x16x32_bf16 v[112:115], a[156:159], v[164:167], v[112:115]\n"
"buffer_load_dwordx4 v8, s[48:51], s89 offen offset:3072 sc1 lds\n"
"v_cvt_pk_bf16_f32 v193, v66, v67\n"
"v_add_f32 v13, v13, v72\n"
"v_mfma_f32_16x16x32_bf16 v[120:123], a[156:159], v[180:183], v[252:255]\n"
"v_exp_f32 v74, v74\n"
"v_mfma_f32_16x16x32_bf16 v[120:123], a[152:155], v[244:247], v[120:123]\n"
"ds_read_b128 a[156:159], v2 offset:23552\n"
"v_add_f32 v13, v13, v73\n"
"v_mfma_f32_16x16x32_bf16 v[120:123], a[148:151], v[116:119], v[120:123]\n"
"v_exp_f32 v75, v75\n"
"v_mfma_f32_16x16x32_bf16 v[120:123], a[144:147], v[56:59], v[120:123]\n"
"v_cvt_pk_bf16_f32 v200, v72, v73\n"
"v_add_f32 v13, v13, v74\n"
"v_mfma_f32_16x16x32_bf16 a[0:3], a[160:163], v[224:227], a[0:3]\n"
"v_exp_f32 v80, v80\n"
"v_mfma_f32_16x16x32_bf16 a[32:35], a[160:163], v[232:235], a[32:35]\n"
"ds_read_b128 a[152:155], v2 offset:22528\n"
"v_add_f32 v13, v13, v75\n"
"v_mfma_f32_16x16x32_bf16 a[64:67], a[160:163], v[240:243], a[64:67]\n"
"v_exp_f32 v81, v81\n"
"s_add_u32 s90, s89, 0x1000\n"
"s_add_u32 m0, s67, 0x1000\n"
"v_mfma_f32_16x16x32_bf16 a[96:99], a[160:163], v[248:251], a[96:99]\n"
"buffer_load_dwordx4 v8, s[48:51], s90 offen offset:0 sc1 lds\n"
"v_cvt_pk_bf16_f32 v201, v74, v75\n"
"v_add_f32 v14, v14, v80\n"
"v_mfma_f32_16x16x32_bf16 a[100:103], a[164:167], v[248:251], a[100:103]\n"
"v_exp_f32 v82, v82\n"
"v_mfma_f32_16x16x32_bf16 a[68:71], a[164:167], v[240:243], a[68:71]\n"
"ds_read_b128 a[148:151], v2 offset:21504\n"
"v_add_f32 v14, v14, v81\n"
"v_mfma_f32_16x16x32_bf16 a[36:39], a[164:167], v[232:235], a[36:39]\n"
"v_exp_f32 v83, v83\n"
"v_mfma_f32_16x16x32_bf16 a[4:7], a[164:167], v[224:227], a[4:7]\n"
"v_cvt_pk_bf16_f32 v208, v80, v81\n"
"v_add_f32 v14, v14, v82\n"
"v_mfma_f32_16x16x32_bf16 a[8:11], a[168:171], v[224:227], a[8:11]\n"
"v_exp_f32 v88, v88\n"
"v_mfma_f32_16x16x32_bf16 a[40:43], a[168:171], v[232:235], a[40:43]\n"
"ds_read_b128 a[144:147], v2 offset:20480\n"
"v_add_f32 v14, v14, v83\n"
"v_mfma_f32_16x16x32_bf16 a[72:75], a[168:171], v[240:243], a[72:75]\n"
"v_exp_f32 v89, v89\n"
"v_mfma_f32_16x16x32_bf16 a[104:107], a[168:171], v[248:251], a[104:107]\n"
"v_cvt_pk_bf16_f32 v209, v82, v83\n"
"v_add_f32 v15, v15, v88\n"
"v_mfma_f32_16x16x32_bf16 a[108:111], a[172:175], v[248:251], a[108:111]\n"
"v_exp_f32 v90, v90\n"
"v_mfma_f32_16x16x32_bf16 a[76:79], a[172:175], v[240:243], a[76:79]\n"
"ds_read_b128 a[160:163], v2 offset:8192\n"
"v_add_f32 v15, v15, v89\n"
"v_mfma_f32_16x16x32_bf16 a[44:47], a[172:175], v[232:235], a[44:47]\n"
"v_exp_f32 v91, v91\n"
"v_mfma_f32_16x16x32_bf16 a[12:15], a[172:175], v[224:227], a[12:15]\n"
"buffer_load_dwordx4 v8, s[48:51], s90 offen offset:1024 sc1 lds\n"
"v_cvt_pk_bf16_f32 v216, v88, v89\n"
"v_add_f32 v15, v15, v90\n"
"v_mfma_f32_16x16x32_bf16 a[16:19], a[176:179], v[224:227], a[16:19]\n"
"v_exp_f32 v96, v96\n"
"v_mfma_f32_16x16x32_bf16 a[48:51], a[176:179], v[232:235], a[48:51]\n"
"ds_read_b128 a[164:167], v2 offset:9216\n"
"v_add_f32 v15, v15, v91\n"
"v_mfma_f32_16x16x32_bf16 a[80:83], a[176:179], v[240:243], a[80:83]\n"
"v_exp_f32 v97, v97\n"
"v_mfma_f32_16x16x32_bf16 a[112:115], a[176:179], v[248:251], a[112:115]\n"
"v_cvt_pk_bf16_f32 v217, v90, v91\n"
"v_add_f32 v12, v12, v96\n"
"v_mfma_f32_16x16x32_bf16 a[116:119], a[180:183], v[248:251], a[116:119]\n"
"v_exp_f32 v98, v98\n"
"v_mfma_f32_16x16x32_bf16 a[84:87], a[180:183], v[240:243], a[84:87]\n"
"ds_read_b128 a[168:171], v2 offset:10240\n"
"v_add_f32 v12, v12, v97\n"
"v_mfma_f32_16x16x32_bf16 a[52:55], a[180:183], v[232:235], a[52:55]\n"
"v_exp_f32 v99, v99\n"
"v_mfma_f32_16x16x32_bf16 a[20:23], a[180:183], v[224:227], a[20:23]\n"
"buffer_load_dwordx4 v8, s[48:51], s90 offen offset:2048 sc1 lds\n"
"v_cvt_pk_bf16_f32 v194, v96, v97\n"
"v_add_f32 v12, v12, v98\n"
"v_mfma_f32_16x16x32_bf16 a[24:27], a[184:187], v[224:227], a[24:27]\n"
"v_exp_f32 v104, v104\n"
"v_mfma_f32_16x16x32_bf16 a[56:59], a[184:187], v[232:235], a[56:59]\n"
"ds_read_b128 a[172:175], v2 offset:11264\n"
"v_add_f32 v12, v12, v99\n"
"v_mfma_f32_16x16x32_bf16 a[88:91], a[184:187], v[240:243], a[88:91]\n"
"v_exp_f32 v105, v105\n"
"v_mfma_f32_16x16x32_bf16 a[120:123], a[184:187], v[248:251], a[120:123]\n"
"v_cvt_pk_bf16_f32 v195, v98, v99\n"
"v_add_f32 v13, v13, v104\n"
"v_mfma_f32_16x16x32_bf16 a[124:127], v[172:175], v[248:251], a[124:127]\n"
"v_exp_f32 v106, v106\n"
"v_mfma_f32_16x16x32_bf16 a[92:95], v[172:175], v[240:243], a[92:95]\n"
"ds_read_b128 a[176:179], v2 offset:12288\n"
"v_add_f32 v13, v13, v105\n"
"v_mfma_f32_16x16x32_bf16 a[60:63], v[172:175], v[232:235], a[60:63]\n"
"v_exp_f32 v107, v107\n"
"v_mfma_f32_16x16x32_bf16 a[28:31], v[172:175], v[224:227], a[28:31]\n"
"buffer_load_dwordx4 v8, s[48:51], s90 offen offset:3072 sc1 lds\n"
"v_cvt_pk_bf16_f32 v202, v104, v105\n"
"v_add_f32 v13, v13, v106\n"
"v_cndmask_b32 v168, v144, v204, s[86:87]\n"
"v_cndmask_b32 v176, v144, v220, s[86:87]\n"
"v_cndmask_b32 v177, v145, v221, s[86:87]\n"
"v_cndmask_b32 v178, v146, v222, s[86:87]\n"
"v_cndmask_b32 v179, v147, v223, s[86:87]\n"
"v_med3_f32 v64, v136, v144, v168\n"
"v_med3_f32 v65, v137, v145, v168\n"
"v_med3_f32 v66, v138, v146, v168\n"
"v_med3_f32 v67, v139, v147, v168\n"
"s_waitcnt lgkmcnt(5)\n"
"s_nop 0\n"
"v_mfma_f32_16x16x32_bf16 v[64:67], a[128:131], v[32:35], v[64:67]\n"
"v_exp_f32 v112, v112\n"
"v_mfma_f32_16x16x32_bf16 v[64:67], a[132:135], v[68:71], v[64:67]\n"
"ds_read_b128 a[180:183], v9 offset:13312\n"
"v_add_f32 v13, v13, v107\n"
"v_mfma_f32_16x16x32_bf16 v[64:67], a[136:139], v[196:199], v[64:67]\n"
"v_exp_f32 v113, v113\n"
"v_mfma_f32_16x16x32_bf16 v[64:67], a[140:143], v[132:135], v[64:67]\n"
"v_cvt_pk_bf16_f32 v203, v106, v107\n"
"v_add_f32 v14, v14, v112\n"
"v_mfma_f32_16x16x32_bf16 v[72:75], a[140:143], v[148:151], v[176:179]\n"
"v_exp_f32 v114, v114\n"
"v_mfma_f32_16x16x32_bf16 v[72:75], a[136:139], v[212:215], v[72:75]\n"
"ds_read_b128 a[184:187], v9 offset:14336\n"
"v_add_f32 v14, v14, v113\n"
"v_med3_f32 v104, v136, v144, v176\n"
"v_med3_f32 v105, v137, v145, v176\n"
"v_med3_f32 v106, v138, v146, v176\n"
"v_med3_f32 v107, v139, v147, v176\n"
"v_mfma_f32_16x16x32_bf16 v[72:75], a[132:135], v[84:87], v[72:75]\n"
"v_exp_f32 v115, v115\n"
"v_mfma_f32_16x16x32_bf16 v[72:75], a[128:131], v[40:43], v[72:75]\n"
"v_cvt_pk_bf16_f32 v210, v112, v113\n"
"v_add_f32 v14, v14, v114\n"
"v_add_u32 v9, 0x4000, v2\n"
"v_mfma_f32_16x16x32_bf16 v[80:83], a[128:131], v[48:51], v[236:239]\n"
"v_exp_f32 v120, v120\n"
"v_mfma_f32_16x16x32_bf16 v[80:83], a[132:135], v[100:103], v[80:83]\n"
"ds_read_b128 v[172:175], v2 offset:31744\n"
"v_add_f32 v14, v14, v115\n"
"v_mfma_f32_16x16x32_bf16 v[80:83], a[136:139], v[228:231], v[80:83]\n"
"v_exp_f32 v121, v121\n"
"v_mfma_f32_16x16x32_bf16 v[80:83], a[140:143], v[164:167], v[80:83]\n"
"v_cvt_pk_bf16_f32 v211, v114, v115\n"
"v_add_f32 v15, v15, v120\n"
"s_waitcnt vmcnt(8) lgkmcnt(1)\n"
"s_barrier\n"
"v_mfma_f32_16x16x32_bf16 v[88:91], a[140:143], v[180:183], v[252:255]\n"
"buffer_store_dwordx2 v[24:25], v20, s[48:51], s71 offen sc1\n"
"v_exp_f32 v122, v122\n"
"v_mfma_f32_16x16x32_bf16 v[88:91], a[136:139], v[244:247], v[88:91]\n"
"ds_read_b128 a[140:143], v4 offset:35840\n"
"v_add_f32 v15, v15, v121\n"
"v_mfma_f32_16x16x32_bf16 v[88:91], a[132:135], v[116:119], v[88:91]\n"
"v_exp_f32 v123, v123\n"
"s_getpc_b64 s[6:7]\n"
"s_sub_u32 s88, s88, 1\n"
"s_cbranch_scc1 L_hand_%=\n"
"s_add_u32 s89, s89, s91\n"
"s_add_u32 m0, s67, 0x8000\n"
"v_mfma_f32_16x16x32_bf16 v[88:91], a[128:131], v[56:59], v[88:91]\n"
"buffer_load_dwordx4 v8, s[48:51], s89 offen offset:0 sc1 lds\n"
"v_cvt_pk_bf16_f32 v218, v120, v121\n"
"v_add_f32 v15, v15, v122\n"
"v_mfma_f32_16x16x32_bf16 v[96:99], a[144:147], v[32:35], v[144:147]\n"
"v_exp_f32 v64, v64\n"
"v_mfma_f32_16x16x32_bf16 v[96:99], a[148:151], v[68:71], v[96:99]\n"
"ds_read_b128 a[136:139], v4 offset:34816\n"
"v_add_f32 v15, v15, v123\n"
"v_mfma_f32_16x16x32_bf16 v[96:99], a[152:155], v[196:199], v[96:99]\n"
"v_exp_f32 v65, v65\n"
"v_mfma_f32_16x16x32_bf16 v[96:99], a[156:159], v[132:135], v[96:99]\n"
"buffer_load_dwordx4 v8, s[48:51], s89 offen offset:1024 sc1 lds\n"
"v_cvt_pk_bf16_f32 v219, v122, v123\n"
"v_add_f32 v12, v12, v64\n"
"v_mfma_f32_16x16x32_bf16 v[104:107], a[156:159], v[148:151], v[104:107]\n"
"v_exp_f32 v66, v66\n"
"v_mfma_f32_16x16x32_bf16 v[104:107], a[152:155], v[212:215], v[104:107]\n"
"ds_read_b128 a[132:135], v4 offset:33792\n"
"v_add_f32 v12, v12, v65\n"
"v_mfma_f32_16x16x32_bf16 v[104:107], a[148:151], v[84:87], v[104:107]\n"
"v_exp_f32 v67, v67\n"
"v_mfma_f32_16x16x32_bf16 v[104:107], a[144:147], v[40:43], v[104:107]\n"
"buffer_load_dwordx4 v8, s[48:51], s89 offen offset:2048 sc1 lds\n"
"v_cvt_pk_bf16_f32 v224, v64, v65\n"
"v_add_f32 v12, v12, v66\n"
"v_mfma_f32_16x16x32_bf16 v[112:115], a[144:147], v[48:51], v[236:239]\n"
"v_exp_f32 v72, v72\n"
"v_mfma_f32_16x16x32_bf16 v[112:115], a[148:151], v[100:103], v[112:115]\n"
"ds_read_b128 a[128:131], v4 offset:32768\n"
"v_add_f32 v12, v12, v67\n"
"v_mfma_f32_16x16x32_bf16 v[112:115], a[152:155], v[228:231], v[112:115]\n"
"v_exp_f32 v73, v73\n"
"v_mfma_f32_16x16x32_bf16 v[112:115], a[156:159], v[164:167], v[112:115]\n"
"buffer_load_dwordx4 v8, s[48:51], s89 offen offset:3072 sc1 lds\n"
"v_cvt_pk_bf16_f32 v225, v66, v67\n"
"v_add_f32 v13, v13, v72\n"
"v_mfma_f32_16x16x32_bf16 v[120:123], a[156:159], v[180:183], v[252:255]\n"
"v_exp_f32 v74, v74\n"
"v_mfma_f32_16x16x32_bf16 v[120:123], a[152:155], v[244:247], v[120:123]\n"
"ds_read_b128 a[156:159], v4 offset:39936\n"
"v_add_f32 v13, v13, v73\n"
"v_mfma_f32_16x16x32_bf16 v[120:123], a[148:151], v[116:119], v[120:123]\n"
"v_exp_f32 v75, v75\n"
"v_mfma_f32_16x16x32_bf16 v[120:123], a[144:147], v[56:59], v[120:123]\n"
"v_cvt_pk_bf16_f32 v232, v72, v73\n"
"v_add_f32 v13, v13, v74\n"
"v_mfma_f32_16x16x32_bf16 a[0:3], a[160:163], v[192:195], a[0:3]\n"
"v_exp_f32 v80, v80\n"
"v_mfma_f32_16x16x32_bf16 a[32:35], a[160:163], v[200:203], a[32:35]\n"
"ds_read_b128 a[152:155], v4 offset:38912\n"
"v_add_f32 v13, v13, v75\n"
"v_mfma_f32_16x16x32_bf16 a[64:67], a[160:163], v[208:211], a[64:67]\n"
"v_exp_f32 v81, v81\n"
"s_add_u32 s90, s89, 0x1000\n"
"s_add_u32 m0, s67, 0x9000\n"
"v_mfma_f32_16x16x32_bf16 a[96:99], a[160:163], v[216:219], a[96:99]\n"
"buffer_load_dwordx4 v8, s[48:51], s90 offen offset:0 sc1 lds\n"
"v_cvt_pk_bf16_f32 v233, v74, v75\n"
"v_add_f32 v14, v14, v80\n"
"v_mfma_f32_16x16x32_bf16 a[100:103], a[164:167], v[216:219], a[100:103]\n"
"v_exp_f32 v82, v82\n"
"v_mfma_f32_16x16x32_bf16 a[68:71], a[164:167], v[208:211], a[68:71]\n"
"ds_read_b128 a[148:151], v4 offset:37888\n"
"v_add_f32 v14, v14, v81\n"
"v_mfma_f32_16x16x32_bf16 a[36:39], a[164:167], v[200:203], a[36:39]\n"
"v_exp_f32 v83, v83\n"
"v_mfma_f32_16x16x32_bf16 a[4:7], a[164:167], v[192:195], a[4:7]\n"
"v_cvt_pk_bf16_f32 v240, v80, v81\n"
"v_add_f32 v14, v14, v82\n"
"v_mfma_f32_16x16x32_bf16 a[8:11], a[168:171], v[192:195], a[8:11]\n"
"v_exp_f32 v88, v88\n"
"v_mfma_f32_16x16x32_bf16 a[40:43], a[168:171], v[200:203], a[40:43]\n"
"ds_read_b128 a[144:147], v4 offset:36864\n"
"v_add_f32 v14, v14, v83\n"
"v_mfma_f32_16x16x32_bf16 a[72:75], a[168:171], v[208:211], a[72:75]\n"
"v_exp_f32 v89, v89\n"
"v_mfma_f32_16x16x32_bf16 a[104:107], a[168:171], v[216:219], a[104:107]\n"
"v_cvt_pk_bf16_f32 v241, v82, v83\n"
"v_add_f32 v15, v15, v88\n"
"v_mfma_f32_16x16x32_bf16 a[108:111], a[172:175], v[216:219], a[108:111]\n"
"v_exp_f32 v90, v90\n"
"v_mfma_f32_16x16x32_bf16 a[76:79], a[172:175], v[208:211], a[76:79]\n"
"ds_read_b128 a[160:163], v2 offset:24576\n"
"v_add_f32 v15, v15, v89\n"
"v_mfma_f32_16x16x32_bf16 a[44:47], a[172:175], v[200:203], a[44:47]\n"
"v_exp_f32 v91, v91\n"
"v_mfma_f32_16x16x32_bf16 a[12:15], a[172:175], v[192:195], a[12:15]\n"
"buffer_load_dwordx4 v8, s[48:51], s90 offen offset:1024 sc1 lds\n"
"v_cvt_pk_bf16_f32 v248, v88, v89\n"
"v_add_f32 v15, v15, v90\n"
"v_mfma_f32_16x16x32_bf16 a[16:19], a[176:179], v[192:195], a[16:19]\n"
"v_exp_f32 v96, v96\n"
"v_mfma_f32_16x16x32_bf16 a[48:51], a[176:179], v[200:203], a[48:51]\n"
"ds_read_b128 a[164:167], v2 offset:25600\n"
"v_add_f32 v15, v15, v91\n"
"v_mfma_f32_16x16x32_bf16 a[80:83], a[176:179], v[208:211], a[80:83]\n"
"v_exp_f32 v97, v97\n"
"v_mfma_f32_16x16x32_bf16 a[112:115], a[176:179], v[216:219], a[112:115]\n"
"v_cvt_pk_bf16_f32 v249, v90, v91\n"
"v_add_f32 v12, v12, v96\n"
"v_mfma_f32_16x16x32_bf16 a[116:119], a[180:183], v[216:219], a[116:119]\n"
"v_exp_f32 v98, v98\n"
"v_mfma_f32_16x16x32_bf16 a[84:87], a[180:183], v[208:211], a[84:87]\n"
"ds_read_b128 a[168:171], v2 offset:26624\n"
"v_add_f32 v12, v12, v97\n"
"v_mfma_f32_16x16x32_bf16 a[52:55], a[180:183], v[200:203], a[52:55]\n"
"v_exp_f32 v99, v99\n"
"v_mfma_f32_16x16x32_bf16 a[20:23], a[180:183], v[192:195], a[20:23]\n"
"buffer_load_dwordx4 v8, s[48:51], s90 offen offset:2048 sc1 lds\n"
"v_cvt_pk_bf16_f32 v226, v96, v97\n"
"v_add_f32 v12, v12, v98\n"
"v_mfma_f32_16x16x32_bf16 a[24:27], a[184:187], v[192:195], a[24:27]\n"
"v_exp_f32 v104, v104\n"
"v_mfma_f32_16x16x32_bf16 a[56:59], a[184:187], v[200:203], a[56:59]\n"
"ds_read_b128 a[172:175], v2 offset:27648\n"
"v_add_f32 v12, v12, v99\n"
"v_mfma_f32_16x16x32_bf16 a[88:91], a[184:187], v[208:211], a[88:91]\n"
"v_exp_f32 v105, v105\n"
"v_mfma_f32_16x16x32_bf16 a[120:123], a[184:187], v[216:219], a[120:123]\n"
"v_cvt_pk_bf16_f32 v227, v98, v99\n"
"v_add_f32 v13, v13, v104\n"
"v_mfma_f32_16x16x32_bf16 a[124:127], a[188:191], v[216:219], a[124:127]\n"
"v_exp_f32 v106, v106\n"
"v_mfma_f32_16x16x32_bf16 a[92:95], a[188:191], v[208:211], a[92:95]\n"
"ds_read_b128 a[176:179], v2 offset:28672\n"
"v_add_f32 v13, v13, v105\n"
"v_mfma_f32_16x16x32_bf16 a[60:63], a[188:191], v[200:203], a[60:63]\n"
"v_exp_f32 v107, v107\n"
"v_mfma_f32_16x16x32_bf16 a[28:31], a[188:191], v[192:195], a[28:31]\n"
"buffer_load_dwordx4 v8, s[48:51], s90 offen offset:3072 sc1 lds\n"
"v_cvt_pk_bf16_f32 v234, v104, v105\n"
"v_add_f32 v13, v13, v106\n"
"s_and_b32 s72, s64, 1\n"
"s_lshl_b32 s72, s72, 14\n"
"s_cmp_eq_u32 s66, 0\n"
"s_cselect_b32 s76, s72, 0\n"
"v_add_u32 v23, s76, v1\n"
"v_cndmask_b32 v168, v236, v144, s[86:87]\n"
"v_cndmask_b32 v169, v237, v145, s[86:87]\n"
"v_cndmask_b32 v170, v238, v146, s[86:87]\n"
"v_cndmask_b32 v171, v239, v147, s[86:87]\n"
"v_cndmask_b32 v176, v252, v144, s[86:87]\n"
"v_cndmask_b32 v177, v253, v145, s[86:87]\n"
"v_cndmask_b32 v178, v254, v146, s[86:87]\n"
"v_cndmask_b32 v179, v255, v147, s[86:87]\n"
"v_med3_f32 v64, v136, v144, v140\n"
"v_med3_f32 v65, v137, v145, v141\n"
"v_med3_f32 v66, v138, v146, v142\n"
"v_med3_f32 v67, v139, v147, v143\n"
"s_waitcnt lgkmcnt(5)\n"
"s_nop 0\n"
"v_mfma_f32_16x16x32_bf16 v[64:67], a[128:131], v[36:39], v[64:67]\n"
"v_exp_f32 v112, v112\n"
"v_mfma_f32_16x16x32_bf16 v[64:67], a[132:135], v[44:47], v[64:67]\n"
"ds_read_b128 a[180:183], v9 offset:13312\n"
"v_add_f32 v13, v13, v107\n"
"v_mfma_f32_16x16x32_bf16 v[64:67], a[136:139], v[52:55], v[64:67]\n"
"v_exp_f32 v113, v113\n"
"v_mfma_f32_16x16x32_bf16 v[64:67], a[140:143], v[60:63], v[64:67]\n"
"v_cvt_pk_bf16_f32 v235, v106, v107\n"
"v_add_f32 v14, v14, v112\n"
"v_mfma_f32_16x16x32_bf16 v[72:75], a[140:143], v[124:127], v[156:159]\n"
"v_exp_f32 v114, v114\n"
"v_mfma_f32_16x16x32_bf16 v[72:75], a[136:139], v[108:111], v[72:75]\n"
"ds_read_b128 a[184:187], v9 offset:14336\n"
"v_add_f32 v14, v14, v113\n"
"v_med3_f32 v104, v136, v144, v156\n"
"v_med3_f32 v105, v137, v145, v157\n"
"v_med3_f32 v106, v138, v146, v158\n"
"v_med3_f32 v107, v139, v147, v159\n"
"v_mfma_f32_16x16x32_bf16 v[72:75], a[132:135], v[92:95], v[72:75]\n"
"v_exp_f32 v115, v115\n"
"v_mfma_f32_16x16x32_bf16 v[72:75], a[128:131], v[76:79], v[72:75]\n"
"v_cvt_pk_bf16_f32 v242, v112, v113\n"
"v_add_f32 v14, v14, v114\n"
"v_mfma_f32_16x16x32_bf16 v[80:83], a[128:131], v[48:51], v[168:171]\n"
"v_exp_f32 v120, v120\n"
"v_mfma_f32_16x16x32_bf16 v[80:83], a[132:135], v[100:103], v[80:83]\n"
"ds_read_b128 a[188:191], v4 offset:48128\n"
"v_add_f32 v14, v14, v115\n"
"v_mfma_f32_16x16x32_bf16 v[80:83], a[136:139], v[228:231], v[80:83]\n"
"v_exp_f32 v121, v121\n"
"v_mfma_f32_16x16x32_bf16 v[80:83], a[140:143], v[164:167], v[80:83]\n"
"v_cvt_pk_bf16_f32 v243, v114, v115\n"
"v_add_f32 v15, v15, v120\n"
"s_waitcnt vmcnt(8) lgkmcnt(1)\n"
"s_barrier\n"
"v_mfma_f32_16x16x32_bf16 v[88:91], a[140:143], v[180:183], v[176:179]\n"
"v_exp_f32 v122, v122\n"
"v_mfma_f32_16x16x32_bf16 v[88:91], a[136:139], v[244:247], v[88:91]\n"
"ds_read_b128 a[140:143], v23 offset:3072\n"
"v_add_f32 v15, v15, v121\n"
"v_mfma_f32_16x16x32_bf16 v[88:91], a[132:135], v[116:119], v[88:91]\n"
"v_exp_f32 v123, v123\n"
"v_mfma_f32_16x16x32_bf16 v[88:91], a[128:131], v[56:59], v[88:91]\n"
"v_cvt_pk_bf16_f32 v250, v120, v121\n"
"v_add_f32 v15, v15, v122\n"
"v_mfma_f32_16x16x32_bf16 v[96:99], a[144:147], v[36:39], v[144:147]\n"
"v_exp_f32 v64, v64\n"
"v_mfma_f32_16x16x32_bf16 v[96:99], a[148:151], v[44:47], v[96:99]\n"
"ds_read_b128 a[136:139], v23 offset:2048\n"
"v_add_f32 v15, v15, v123\n"
"v_mfma_f32_16x16x32_bf16 v[96:99], a[152:155], v[52:55], v[96:99]\n"
"v_exp_f32 v65, v65\n"
"v_mfma_f32_16x16x32_bf16 v[96:99], a[156:159], v[60:63], v[96:99]\n"
"ds_read_b128 a[132:135], v23 offset:1024\n"
"v_cvt_pk_bf16_f32 v251, v122, v123\n"
"v_mfma_f32_16x16x32_bf16 v[104:107], a[156:159], v[124:127], v[104:107]\n"
"v_exp_f32 v66, v66\n"
"s_getpc_b64 s[6:7]\n"
"s_sub_u32 s88, s88, 1\n"
"s_cbranch_scc1 L_hand_%=\n"
"s_add_u32 s89, s89, s91\n"
"s_add_u32 m0, s67, 0x10000\n"
"v_mfma_f32_16x16x32_bf16 v[104:107], a[152:155], v[108:111], v[104:107]\n"
"buffer_load_dwordx4 v8, s[48:51], s89 offen offset:0 sc1 lds\n"
"v_mfma_f32_16x16x32_bf16 v[104:107], a[148:151], v[92:95], v[104:107]\n"
"v_exp_f32 v67, v67\n"
"v_mfma_f32_16x16x32_bf16 v[104:107], a[144:147], v[76:79], v[104:107]\n"
"ds_read_b128 a[128:131], v23 offset:0\n"
"v_cvt_pk_bf16_f32 v192, v64, v65\n"
"v_mfma_f32_16x16x32_bf16 v[112:115], a[144:147], v[48:51], v[168:171]\n"
"v_exp_f32 v72, v72\n"
"v_mfma_f32_16x16x32_bf16 v[112:115], a[148:151], v[100:103], v[112:115]\n"
"buffer_load_dwordx4 v8, s[48:51], s89 offen offset:1024 sc1 lds\n"
"v_mfma_f32_16x16x32_bf16 v[112:115], a[152:155], v[228:231], v[112:115]\n"
"v_exp_f32 v73, v73\n"
"v_mfma_f32_16x16x32_bf16 v[112:115], a[156:159], v[164:167], v[112:115]\n"
"v_cvt_pk_bf16_f32 v193, v66, v67\n"
"v_add_u32 v9, 0x8000, v4\n"
"v_mfma_f32_16x16x32_bf16 v[120:123], a[156:159], v[180:183], v[176:179]\n"
"v_exp_f32 v74, v74\n"
"v_mfma_f32_16x16x32_bf16 v[120:123], a[152:155], v[244:247], v[120:123]\n"
"buffer_load_dwordx4 v8, s[48:51], s89 offen offset:2048 sc1 lds\n"
"v_mfma_f32_16x16x32_bf16 v[120:123], a[148:151], v[116:119], v[120:123]\n"
"v_exp_f32 v75, v75\n"
"v_mfma_f32_16x16x32_bf16 v[120:123], a[144:147], v[56:59], v[120:123]\n"
"ds_read_b128 a[156:159], v23 offset:7168\n"
"v_cvt_pk_bf16_f32 v200, v72, v73\n"
"v_mfma_f32_16x16x32_bf16 a[0:3], a[160:163], v[224:227], a[0:3]\n"
"v_exp_f32 v80, v80\n"
"v_mfma_f32_16x16x32_bf16 a[32:35], a[160:163], v[232:235], a[32:35]\n"
"buffer_load_dwordx4 v8, s[48:51], s89 offen offset:3072 sc1 lds\n"
"v_mfma_f32_16x16x32_bf16 a[64:67], a[160:163], v[240:243], a[64:67]\n"
"v_exp_f32 v81, v81\n"
"v_mfma_f32_16x16x32_bf16 a[96:99], a[160:163], v[248:251], a[96:99]\n"
"v_cvt_pk_bf16_f32 v201, v74, v75\n"
"v_add_f32 v14, v14, v80\n"
"v_mfma_f32_16x16x32_bf16 a[100:103], a[164:167], v[248:251], a[100:103]\n"
"v_exp_f32 v82, v82\n"
"v_mfma_f32_16x16x32_bf16 a[68:71], a[164:167], v[240:243], a[68:71]\n"
"ds_read_b128 a[152:155], v23 offset:6144\n"
"v_add_f32 v14, v14, v81\n"
"v_mfma_f32_16x16x32_bf16 a[36:39], a[164:167], v[232:235], a[36:39]\n"
"v_exp_f32 v83, v83\n"
"v_mfma_f32_16x16x32_bf16 a[4:7], a[164:167], v[224:227], a[4:7]\n"
"v_cvt_pk_bf16_f32 v208, v80, v81\n"
"v_add_f32 v14, v14, v82\n"
"v_mfma_f32_16x16x32_bf16 a[8:11], a[168:171], v[224:227], a[8:11]\n"
"v_exp_f32 v88, v88\n"
"v_mfma_f32_16x16x32_bf16 a[40:43], a[168:171], v[232:235], a[40:43]\n"
"ds_read_b128 a[148:151], v23 offset:5120\n"
"v_add_f32 v14, v14, v83\n"
"v_mfma_f32_16x16x32_bf16 a[72:75], a[168:171], v[240:243], a[72:75]\n"
"v_exp_f32 v89, v89\n"
"s_add_u32 s90, s89, 0x1000\n"
"s_add_u32 m0, s67, 0x11000\n"
"v_mfma_f32_16x16x32_bf16 a[104:107], a[168:171], v[248:251], a[104:107]\n"
"buffer_load_dwordx4 v8, s[48:51], s90 offen offset:0 sc1 lds\n"
"v_cvt_pk_bf16_f32 v209, v82, v83\n"
"v_add_f32 v15, v15, v88\n"
"v_mfma_f32_16x16x32_bf16 a[108:111], a[172:175], v[248:251], a[108:111]\n"
"v_exp_f32 v90, v90\n"
"v_mfma_f32_16x16x32_bf16 a[76:79], a[172:175], v[240:243], a[76:79]\n"
"ds_read_b128 a[144:147], v23 offset:4096\n"
"v_add_f32 v15, v15, v89\n"
"v_mfma_f32_16x16x32_bf16 a[44:47], a[172:175], v[232:235], a[44:47]\n"
"v_exp_f32 v91, v91\n"
"v_mfma_f32_16x16x32_bf16 a[12:15], a[172:175], v[224:227], a[12:15]\n"
"v_cvt_pk_bf16_f32 v216, v88, v89\n"
"v_add_f32 v15, v15, v90\n"
"v_mfma_f32_16x16x32_bf16 a[16:19], a[176:179], v[224:227], a[16:19]\n"
"v_exp_f32 v96, v96\n"
"v_mfma_f32_16x16x32_bf16 a[48:51], a[176:179], v[232:235], a[48:51]\n"
"ds_read_b128 a[160:163], v4 offset:40960\n"
"v_add_f32 v15, v15, v91\n"
"v_mfma_f32_16x16x32_bf16 a[80:83], a[176:179], v[240:243], a[80:83]\n"
"v_exp_f32 v97, v97\n"
"v_mfma_f32_16x16x32_bf16 a[112:115], a[176:179], v[248:251], a[112:115]\n"
"ds_read_b128 a[164:167], v4 offset:41984\n"
"v_cvt_pk_bf16_f32 v217, v90, v91\n"
"v_mfma_f32_16x16x32_bf16 a[116:119], a[180:183], v[248:251], a[116:119]\n"
"v_exp_f32 v98, v98\n"
"v_mfma_f32_16x16x32_bf16 a[84:87], a[180:183], v[240:243], a[84:87]\n"
"buffer_load_dwordx4 v8, s[48:51], s90 offen offset:1024 sc1 lds\n"
"v_mfma_f32_16x16x32_bf16 a[52:55], a[180:183], v[232:235], a[52:55]\n"
"v_exp_f32 v99, v99\n"
"v_mfma_f32_16x16x32_bf16 a[20:23], a[180:183], v[224:227], a[20:23]\n"
"ds_read_b128 a[168:171], v4 offset:43008\n"
"v_cvt_pk_bf16_f32 v194, v96, v97\n"
"v_mfma_f32_16x16x32_bf16 a[24:27], a[184:187], v[224:227], a[24:27]\n"
"v_exp_f32 v104, v104\n"
"v_mfma_f32_16x16x32_bf16 a[56:59], a[184:187], v[232:235], a[56:59]\n"
"buffer_load_dwordx4 v8, s[48:51], s90 offen offset:2048 sc1 lds\n"
"v_mfma_f32_16x16x32_bf16 a[88:91], a[184:187], v[240:243], a[88:91]\n"
"v_exp_f32 v105, v105\n"
"v_mfma_f32_16x16x32_bf16 a[120:123], a[184:187], v[248:251], a[120:123]\n"
"ds_read_b128 a[172:175], v4 offset:44032\n"
"v_cvt_pk_bf16_f32 v195, v98, v99\n"
"v_mfma_f32_16x16x32_bf16 a[124:127], v[172:175], v[248:251], a[124:127]\n"
"v_exp_f32 v106, v106\n"
"v_mfma_f32_16x16x32_bf16 a[92:95], v[172:175], v[240:243], a[92:95]\n"
"buffer_load_dwordx4 v8, s[48:51], s90 offen offset:3072 sc1 lds\n"
"v_mfma_f32_16x16x32_bf16 a[60:63], v[172:175], v[232:235], a[60:63]\n"
"v_exp_f32 v107, v107\n"
"v_mfma_f32_16x16x32_bf16 a[28:31], v[172:175], v[224:227], a[28:31]\n"
"ds_read_b128 a[176:179], v4 offset:45056\n"
"v_cvt_pk_bf16_f32 v202, v104, v105\n"
"L_remote_%=:\n"
"s_add_u32 s70, s67, 0x1000\n"
"s_lshl_b32 s96, s66, 1\n"
"s_cmp_eq_u32 s66, 0\n"
"s_cselect_b32 s96, 13, s96\n"
"s_cbranch_scc0 L_AB_%=\n"
"v_lshrrev_b32 v24, 2, v21\n"
"v_and_b32 v25, 3, v21\n"
"v_lshlrev_b32 v25, 4, v25\n"
"v_cmp_gt_u32 vcc, 8, v24\n"
"v_lshl_add_u32 v24, v24, 11, v25\n"
"s_nop 0\n"
"v_cndmask_b32 v25, -1, v24, vcc\n"
"s_mul_i32 s73, s64, 0x1c000\n"
"s_lshl_b32 s74, s32, 6\n"
"s_add_u32 s73, s73, s74\n"
"s_add_u32 s73, s73, 0x10000\n"
"buffer_load_dwordx4 v[136:139], v24, s[48:51], s73 offen sc1\n"
"s_add_u32 s73, s73, 0x8000\n"
"buffer_load_dwordx4 v[144:147], v24, s[48:51], s73 offen sc1\n"
"s_add_u32 s73, s73, 0x8000\n"
"buffer_load_dwordx4 v[168:171], v24, s[48:51], s73 offen sc1\n"
"s_add_u32 s73, s73, 0x8000\n"
"buffer_load_dwordx4 v[176:179], v25, s[48:51], s73 offen sc1\n"
"s_add_u32 s73, s73, 0x8000\n"
"s_waitcnt lgkmcnt(5)\n"
"v_mfma_f32_16x16x32_bf16 v[64:67], a[128:131], v[36:39], v[140:143]\n"
"v_exp_f32 v112, v112\n"
"v_mfma_f32_16x16x32_bf16 v[64:67], a[132:135], v[44:47], v[64:67]\n"
"ds_read_b128 a[180:183], v9 offset:13312\n"
"v_mfma_f32_16x16x32_bf16 v[64:67], a[136:139], v[52:55], v[64:67]\n"
"v_exp_f32 v113, v113\n"
"v_mfma_f32_16x16x32_bf16 v[64:67], a[140:143], v[60:63], v[64:67]\n"
"v_cvt_pk_bf16_f32 v203, v106, v107\n"
"v_add_f32 v14, v14, v112\n"
"v_mfma_f32_16x16x32_bf16 v[72:75], a[140:143], v[124:127], v[156:159]\n"
"v_exp_f32 v114, v114\n"
"v_mfma_f32_16x16x32_bf16 v[72:75], a[136:139], v[108:111], v[72:75]\n"
"ds_read_b128 a[184:187], v9 offset:14336\n"
"v_add_f32 v14, v14, v113\n"
"v_mfma_f32_16x16x32_bf16 v[72:75], a[132:135], v[92:95], v[72:75]\n"
"v_exp_f32 v115, v115\n"
"v_mfma_f32_16x16x32_bf16 v[72:75], a[128:131], v[76:79], v[72:75]\n"
"v_cvt_pk_bf16_f32 v210, v112, v113\n"
"v_add_f32 v14, v14, v114\n"
"v_mfma_f32_16x16x32_bf16 v[80:83], a[128:131], v[48:51], v[236:239]\n"
"v_exp_f32 v120, v120\n"
"v_mfma_f32_16x16x32_bf16 v[80:83], a[132:135], v[100:103], v[80:83]\n"
"ds_read_b128 v[172:175], v3 offset:15360\n"
"v_add_f32 v14, v14, v115\n"
"v_mfma_f32_16x16x32_bf16 v[80:83], a[136:139], v[228:231], v[80:83]\n"
"v_exp_f32 v121, v121\n"
"v_mfma_f32_16x16x32_bf16 v[80:83], a[140:143], v[164:167], v[80:83]\n"
"v_cvt_pk_bf16_f32 v211, v114, v115\n"
"v_add_f32 v15, v15, v120\n"
"v_mfma_f32_16x16x32_bf16 v[152:155], v[128:131], v[192:195], v[152:155]\n"
"s_waitcnt vmcnt(8) lgkmcnt(1)\n"
"s_barrier\n"
"v_mfma_f32_16x16x32_bf16 v[88:91], a[140:143], v[180:183], v[252:255]\n"
"v_exp_f32 v122, v122\n"
"v_mfma_f32_16x16x32_bf16 v[88:91], a[136:139], v[244:247], v[88:91]\n"
"ds_read_b128 a[140:143], v3 offset:35840\n"
"v_add_f32 v15, v15, v121\n"
"v_mfma_f32_16x16x32_bf16 v[88:91], a[132:135], v[116:119], v[88:91]\n"
"v_exp_f32 v123, v123\n"
"v_mfma_f32_16x16x32_bf16 v[88:91], a[128:131], v[56:59], v[88:91]\n"
"v_cvt_pk_bf16_f32 v218, v120, v121\n"
"v_add_f32 v15, v15, v122\n"
"v_mfma_f32_16x16x32_bf16 v[96:99], a[144:147], v[36:39], v[140:143]\n"
"v_exp_f32 v64, v64\n"
"v_mfma_f32_16x16x32_bf16 v[96:99], a[148:151], v[44:47], v[96:99]\n"
"ds_read_b128 a[136:139], v3 offset:34816\n"
"v_add_f32 v15, v15, v123\n"
"v_mfma_f32_16x16x32_bf16 v[96:99], a[152:155], v[52:55], v[96:99]\n"
"v_exp_f32 v65, v65\n"
"v_mfma_f32_16x16x32_bf16 v[96:99], a[156:159], v[60:63], v[96:99]\n"
"ds_read_b128 a[132:135], v3 offset:33792\n"
"v_cvt_pk_bf16_f32 v219, v122, v123\n"
"v_mfma_f32_16x16x32_bf16 v[160:163], v[128:131], v[200:203], v[160:163]\n"
"s_getpc_b64 s[6:7]\n"
"s_sub_u32 s88, s88, 1\n"
"s_cbranch_scc1 L_hand_%=\n"
"s_add_u32 s89, s89, s91\n"
"s_add_u32 m0, s67, 0x18000\n"
"v_mfma_f32_16x16x32_bf16 v[104:107], a[156:159], v[124:127], v[156:159]\n"
"v_exp_f32 v66, v66\n"
"v_mfma_f32_16x16x32_bf16 v[104:107], a[152:155], v[108:111], v[104:107]\n"
"buffer_load_dwordx4 v8, s[48:51], s89 offen offset:0 sc1 lds\n"
"v_mfma_f32_16x16x32_bf16 v[104:107], a[148:151], v[92:95], v[104:107]\n"
"v_exp_f32 v67, v67\n"
"v_mfma_f32_16x16x32_bf16 v[104:107], a[144:147], v[76:79], v[104:107]\n"
"ds_read_b128 a[128:131], v3 offset:32768\n"
"v_cvt_pk_bf16_f32 v224, v64, v65\n"
"v_mfma_f32_16x16x32_bf16 v[112:115], a[144:147], v[48:51], v[236:239]\n"
"v_exp_f32 v72, v72\n"
"v_mfma_f32_16x16x32_bf16 v[112:115], a[148:151], v[100:103], v[112:115]\n"
"buffer_load_dwordx4 v8, s[48:51], s89 offen offset:1024 sc1 lds\n"
"v_mfma_f32_16x16x32_bf16 v[112:115], a[152:155], v[228:231], v[112:115]\n"
"v_exp_f32 v73, v73\n"
"v_mfma_f32_16x16x32_bf16 v[112:115], a[156:159], v[164:167], v[112:115]\n"
"v_cvt_pk_bf16_f32 v225, v66, v67\n"
"v_add_u32 v9, 0x0, v3\n"
"v_mfma_f32_16x16x32_bf16 v[120:123], a[156:159], v[180:183], v[252:255]\n"
"v_exp_f32 v74, v74\n"
"v_mfma_f32_16x16x32_bf16 v[120:123], a[152:155], v[244:247], v[120:123]\n"
"buffer_load_dwordx4 v8, s[48:51], s89 offen offset:2048 sc1 lds\n"
"v_mfma_f32_16x16x32_bf16 v[120:123], a[148:151], v[116:119], v[120:123]\n"
"v_exp_f32 v75, v75\n"
"v_mfma_f32_16x16x32_bf16 v[120:123], a[144:147], v[56:59], v[120:123]\n"
"ds_read_b128 a[156:159], v3 offset:39936\n"
"v_cvt_pk_bf16_f32 v232, v72, v73\n"
"v_mfma_f32_16x16x32_bf16 a[192:195], a[160:163], v[192:195], a[192:195]\n"
"v_exp_f32 v80, v80\n"
"v_mfma_f32_16x16x32_bf16 a[224:227], a[160:163], v[200:203], a[224:227]\n"
"buffer_load_dwordx4 v8, s[48:51], s89 offen offset:3072 sc1 lds\n"
"v_mfma_f32_16x16x32_bf16 a[64:67], a[160:163], v[208:211], a[64:67]\n"
"v_exp_f32 v81, v81\n"
"v_mfma_f32_16x16x32_bf16 a[96:99], a[160:163], v[216:219], a[96:99]\n"
"v_cvt_pk_bf16_f32 v233, v74, v75\n"
"v_add_f32 v14, v14, v80\n"
"v_mfma_f32_16x16x32_bf16 a[100:103], a[164:167], v[216:219], a[100:103]\n"
"v_exp_f32 v82, v82\n"
"v_mfma_f32_16x16x32_bf16 a[68:71], a[164:167], v[208:211], a[68:71]\n"
"ds_read_b128 a[152:155], v3 offset:38912\n"
"v_add_f32 v14, v14, v81\n"
"v_mfma_f32_16x16x32_bf16 a[228:231], a[164:167], v[200:203], a[228:231]\n"
"v_exp_f32 v83, v83\n"
"v_mfma_f32_16x16x32_bf16 a[196:199], a[164:167], v[192:195], a[196:199]\n"
"v_cvt_pk_bf16_f32 v240, v80, v81\n"
"v_add_f32 v14, v14, v82\n"
"v_mfma_f32_16x16x32_bf16 a[200:203], a[168:171], v[192:195], a[200:203]\n"
"v_exp_f32 v88, v88\n"
"v_mfma_f32_16x16x32_bf16 a[232:235], a[168:171], v[200:203], a[232:235]\n"
"ds_read_b128 a[148:151], v3 offset:37888\n"
"v_add_f32 v14, v14, v83\n"
"v_mfma_f32_16x16x32_bf16 a[72:75], a[168:171], v[208:211], a[72:75]\n"
"v_exp_f32 v89, v89\n"
"s_add_u32 s90, s89, 0x1000\n"
"s_add_u32 m0, s67, 0x19000\n"
"v_mfma_f32_16x16x32_bf16 a[104:107], a[168:171], v[216:219], a[104:107]\n"
"buffer_load_dwordx4 v8, s[48:51], s90 offen offset:0 sc1 lds\n"
"v_cvt_pk_bf16_f32 v241, v82, v83\n"
"v_add_f32 v15, v15, v88\n"
"v_mfma_f32_16x16x32_bf16 a[108:111], a[172:175], v[216:219], a[108:111]\n"
"v_exp_f32 v90, v90\n"
"v_mfma_f32_16x16x32_bf16 a[76:79], a[172:175], v[208:211], a[76:79]\n"
"ds_read_b128 a[144:147], v3 offset:36864\n"
"v_add_f32 v15, v15, v89\n"
"v_mfma_f32_16x16x32_bf16 a[236:239], a[172:175], v[200:203], a[236:239]\n"
"v_exp_f32 v91, v91\n"
"v_mfma_f32_16x16x32_bf16 a[204:207], a[172:175], v[192:195], a[204:207]\n"
"v_cvt_pk_bf16_f32 v248, v88, v89\n"
"v_add_f32 v15, v15, v90\n"
"v_mfma_f32_16x16x32_bf16 a[208:211], a[176:179], v[192:195], a[208:211]\n"
"v_exp_f32 v96, v96\n"
"v_mfma_f32_16x16x32_bf16 a[240:243], a[176:179], v[200:203], a[240:243]\n"
"ds_read_b128 a[160:163], v3 offset:8192\n"
"v_add_f32 v15, v15, v91\n"
"v_mfma_f32_16x16x32_bf16 a[80:83], a[176:179], v[208:211], a[80:83]\n"
"v_exp_f32 v97, v97\n"
"v_mfma_f32_16x16x32_bf16 a[112:115], a[176:179], v[216:219], a[112:115]\n"
"ds_read_b128 a[164:167], v3 offset:9216\n"
"v_cvt_pk_bf16_f32 v249, v90, v91\n"
"v_mfma_f32_16x16x32_bf16 a[116:119], a[180:183], v[216:219], a[116:119]\n"
"v_exp_f32 v98, v98\n"
"v_mfma_f32_16x16x32_bf16 a[84:87], a[180:183], v[208:211], a[84:87]\n"
"buffer_load_dwordx4 v8, s[48:51], s90 offen offset:1024 sc1 lds\n"
"v_mfma_f32_16x16x32_bf16 a[244:247], a[180:183], v[200:203], a[244:247]\n"
"v_exp_f32 v99, v99\n"
"v_mfma_f32_16x16x32_bf16 a[212:215], a[180:183], v[192:195], a[212:215]\n"
"ds_read_b128 a[168:171], v3 offset:10240\n"
"v_cvt_pk_bf16_f32 v226, v96, v97\n"
"v_mfma_f32_16x16x32_bf16 a[216:219], a[184:187], v[192:195], a[216:219]\n"
"v_exp_f32 v104, v104\n"
"v_mfma_f32_16x16x32_bf16 a[248:251], a[184:187], v[200:203], a[248:251]\n"
"buffer_load_dwordx4 v8, s[48:51], s90 offen offset:2048 sc1 lds\n"
"v_mfma_f32_16x16x32_bf16 a[88:91], a[184:187], v[208:211], a[88:91]\n"
"v_exp_f32 v105, v105\n"
"v_mfma_f32_16x16x32_bf16 a[120:123], a[184:187], v[216:219], a[120:123]\n"
"ds_read_b128 a[172:175], v3 offset:11264\n"
"v_cvt_pk_bf16_f32 v227, v98, v99\n"
"v_mfma_f32_16x16x32_bf16 a[124:127], a[188:191], v[216:219], a[124:127]\n"
"v_exp_f32 v106, v106\n"
"v_mfma_f32_16x16x32_bf16 a[92:95], a[188:191], v[208:211], a[92:95]\n"
"buffer_load_dwordx4 v8, s[48:51], s90 offen offset:3072 sc1 lds\n"
"v_mfma_f32_16x16x32_bf16 a[252:255], a[188:191], v[200:203], a[252:255]\n"
"v_exp_f32 v107, v107\n"
"v_mfma_f32_16x16x32_bf16 a[220:223], a[188:191], v[192:195], a[220:223]\n"
"ds_read_b128 a[176:179], v3 offset:12288\n"
"v_cvt_pk_bf16_f32 v234, v104, v105\n"
"s_waitcnt vmcnt(8)\n"
"v_pk_add_f32 v[136:137], v[136:137], v[144:145]\n"
"v_pk_add_f32 v[138:139], v[138:139], v[146:147]\n"
"v_pk_add_f32 v[136:137], v[136:137], v[168:169]\n"
"v_pk_add_f32 v[138:139], v[138:139], v[170:171]\n"
"v_pk_add_f32 v[136:137], v[136:137], v[176:177]\n"
"v_pk_add_f32 v[138:139], v[138:139], v[178:179]\n"
"s_nop 0\n"
"v_add_f32_dpp v136, v136, v136 row_ror:4 row_mask:0xf bank_mask:0xf\n"
"v_add_f32_dpp v137, v137, v137 row_ror:4 row_mask:0xf bank_mask:0xf\n"
"v_add_f32_dpp v138, v138, v138 row_ror:4 row_mask:0xf bank_mask:0xf\n"
"v_add_f32_dpp v139, v139, v139 row_ror:4 row_mask:0xf bank_mask:0xf\n"
"v_add_f32_dpp v136, v136, v136 row_ror:8 row_mask:0xf bank_mask:0xf\n"
"v_add_f32_dpp v137, v137, v137 row_ror:8 row_mask:0xf bank_mask:0xf\n"
"v_add_f32_dpp v138, v138, v138 row_ror:8 row_mask:0xf bank_mask:0xf\n"
"v_add_f32_dpp v139, v139, v139 row_ror:8 row_mask:0xf bank_mask:0xf\n"
"v_mov_b32 v144, v136\n"
"v_mov_b32 v145, v137\n"
"v_mov_b32 v146, v138\n"
"v_mov_b32 v147, v139\n"
"v_permlane16_swap_b32 v136, v144\n"
"v_permlane16_swap_b32 v137, v145\n"
"v_permlane16_swap_b32 v138, v146\n"
"v_permlane16_swap_b32 v139, v147\n"
"v_add_f32 v136, v136, v144\n"
"v_add_f32 v137, v137, v145\n"
"v_add_f32 v138, v138, v146\n"
"v_add_f32 v139, v139, v147\n"
"v_mov_b32 v144, v136\n"
"v_mov_b32 v145, v137\n"
"v_mov_b32 v146, v138\n"
"v_mov_b32 v147, v139\n"
"v_permlane32_swap_b32 v136, v144\n"
"v_permlane32_swap_b32 v137, v145\n"
"v_permlane32_swap_b32 v138, v146\n"
"v_permlane32_swap_b32 v139, v147\n"
"v_add_f32 v136, v136, v144\n"
"v_add_f32 v137, v137, v145\n"
"v_add_f32 v138, v138, v146\n"
"v_add_f32 v139, v139, v147\n"
"v_and_b32 v24, 48, v1\n"
"s_lshl_b32 s73, s64, 6\n"
"v_add_u32 v24, 0x20000, v24\n"
"v_add_u32 v25, s73, v24\n"
"ds_write_b128 v25, v[136:139]\n"
"s_waitcnt lgkmcnt(0)\n"
"s_barrier\n"
"ds_read_b128 v[136:139], v24 offset:0\n"
"ds_read_b128 v[144:147], v24 offset:64\n"
"ds_read_b128 v[168:171], v24 offset:128\n"
"ds_read_b128 v[176:179], v24 offset:192\n"
"v_lshl_or_b32 v25, s64, 6, v21\n"
"v_cmp_gt_u32 vcc, 4, v25\n"
"s_lshl_b32 s73, s32, 6\n"
"s_nop 0\n"
"v_cndmask_b32 v25, -1, v1, vcc\n"
"s_add_u32 s73, s73, 0x90000\n"
"s_waitcnt lgkmcnt(0)\n"
"v_pk_add_f32 v[136:137], v[136:137], v[144:145]\n"
"v_pk_add_f32 v[138:139], v[138:139], v[146:147]\n"
"v_pk_add_f32 v[136:137], v[136:137], v[168:169]\n"
"v_pk_add_f32 v[138:139], v[138:139], v[170:171]\n"
"v_pk_add_f32 v[136:137], v[136:137], v[176:177]\n"
"v_pk_add_f32 v[138:139], v[138:139], v[178:179]\n"
"buffer_store_dwordx4 v[136:139], v25, s[48:51], s73 offen sc1\n"
"L_fin1_%=:\n"
"s_nop 5\n"
"v_mov_b32 v188, v12\n"
"v_mov_b32 v189, v13\n"
"s_nop 0\n"
"v_permlane16_swap_b32 v12, v188\n"
"v_permlane16_swap_b32 v13, v189\n"
"v_add_f32 v12, v12, v188\n"
"v_add_f32 v13, v13, v189\n"
"v_mov_b32 v188, v12\n"
"v_mov_b32 v189, v13\n"
"s_nop 0\n"
"v_permlane32_swap_b32 v12, v188\n"
"v_permlane32_swap_b32 v13, v189\n"
"v_add_f32 v12, v12, v188\n"
"v_add_f32 v13, v13, v189\n"
"v_add_f32 v188, v12, v13\n"
"v_mov_b32 v189, 0x5f800000\n"
"v_cmp_lt_f32 vcc, v188, v189\n"
"s_nop 0\n"
"s_cmp_eq_u64 vcc, -1\n"
"s_cselect_b32 s35, 0, 1\n"
"v_rcp_f32 v12, v12\n"
"v_rcp_f32 v7, v13\n"
"v_mov_b32 v13, v12\n"
"s_sub_u32 s73, s78, 0x100000\n"
"s_lshl_b32 s73, s73, 3\n"
"s_lshl_b32 s74, s65, 12\n"
"s_add_u32 s73, s73, s74\n"
"s_add_u32 s73, s73, 0x2400000\n"
"v_accvgpr_read_b32 v188, a0\n"
"v_accvgpr_read_b32 v189, a1\n"
"v_accvgpr_read_b32 v190, a2\n"
"v_accvgpr_read_b32 v191, a3\n"
"v_pk_mul_f32 v[188:189], v[188:189], v[12:13]\n"
"v_pk_mul_f32 v[190:191], v[190:191], v[12:13]\n"
"v_cvt_pk_bf16_f32 v24, v188, v189\n"
"v_cvt_pk_bf16_f32 v25, v190, v191\n"
"v_accvgpr_read_b32 v188, a4\n"
"v_accvgpr_read_b32 v189, a5\n"
"v_accvgpr_read_b32 v190, a6\n"
"v_accvgpr_read_b32 v191, a7\n"
"v_pk_mul_f32 v[188:189], v[188:189], v[12:13]\n"
"v_pk_mul_f32 v[190:191], v[190:191], v[12:13]\n"
"v_cvt_pk_bf16_f32 v26, v188, v189\n"
"v_cvt_pk_bf16_f32 v27, v190, v191\n"
"buffer_store_dwordx4 v[24:27], v1, s[48:51], s73 offen offset:0 sc1\n"
"v_accvgpr_read_b32 v188, a8\n"
"v_accvgpr_read_b32 v189, a9\n"
"v_accvgpr_read_b32 v190, a10\n"
"v_accvgpr_read_b32 v191, a11\n"
"v_pk_mul_f32 v[188:189], v[188:189], v[12:13]\n"
"v_pk_mul_f32 v[190:191], v[190:191], v[12:13]\n"
"v_cvt_pk_bf16_f32 v28, v188, v189\n"
"v_cvt_pk_bf16_f32 v29, v190, v191\n"
"v_accvgpr_read_b32 v188, a12\n"
"v_accvgpr_read_b32 v189, a13\n"
"v_accvgpr_read_b32 v190, a14\n"
"v_accvgpr_read_b32 v191, a15\n"
"v_pk_mul_f32 v[188:189], v[188:189], v[12:13]\n"
"v_pk_mul_f32 v[190:191], v[190:191], v[12:13]\n"
"v_cvt_pk_bf16_f32 v30, v188, v189\n"
"v_cvt_pk_bf16_f32 v31, v190, v191\n"
"buffer_store_dwordx4 v[28:31], v1, s[48:51], s73 offen offset:1024 sc1\n"
"v_accvgpr_read_b32 v188, a16\n"
"v_accvgpr_read_b32 v189, a17\n"
"v_accvgpr_read_b32 v190, a18\n"
"v_accvgpr_read_b32 v191, a19\n"
"v_pk_mul_f32 v[188:189], v[188:189], v[12:13]\n"
"v_pk_mul_f32 v[190:191], v[190:191], v[12:13]\n"
"v_cvt_pk_bf16_f32 v24, v188, v189\n"
"v_cvt_pk_bf16_f32 v25, v190, v191\n"
"v_accvgpr_read_b32 v188, a20\n"
"v_accvgpr_read_b32 v189, a21\n"
"v_accvgpr_read_b32 v190, a22\n"
"v_accvgpr_read_b32 v191, a23\n"
"v_pk_mul_f32 v[188:189], v[188:189], v[12:13]\n"
"v_pk_mul_f32 v[190:191], v[190:191], v[12:13]\n"
"v_cvt_pk_bf16_f32 v26, v188, v189\n"
"v_cvt_pk_bf16_f32 v27, v190, v191\n"
"buffer_store_dwordx4 v[24:27], v1, s[48:51], s73 offen offset:2048 sc1\n"
"v_accvgpr_read_b32 v188, a24\n"
"v_accvgpr_read_b32 v189, a25\n"
"v_accvgpr_read_b32 v190, a26\n"
"v_accvgpr_read_b32 v191, a27\n"
"v_pk_mul_f32 v[188:189], v[188:189], v[12:13]\n"
"v_pk_mul_f32 v[190:191], v[190:191], v[12:13]\n"
"v_cvt_pk_bf16_f32 v28, v188, v189\n"
"v_cvt_pk_bf16_f32 v29, v190, v191\n"
"v_accvgpr_read_b32 v188, a28\n"
"v_accvgpr_read_b32 v189, a29\n"
"v_accvgpr_read_b32 v190, a30\n"
"v_accvgpr_read_b32 v191, a31\n"
"v_pk_mul_f32 v[188:189], v[188:189], v[12:13]\n"
"v_pk_mul_f32 v[190:191], v[190:191], v[12:13]\n"
"v_cvt_pk_bf16_f32 v30, v188, v189\n"
"v_cvt_pk_bf16_f32 v31, v190, v191\n"
"buffer_store_dwordx4 v[28:31], v1, s[48:51], s73 offen offset:3072 sc1\n"
"v_mov_b32 v12, v7\n"
"v_mov_b32 v13, v7\n"
"s_sub_u32 s73, s79, 0x100000\n"
"s_lshl_b32 s73, s73, 3\n"
"s_lshl_b32 s74, s65, 12\n"
"s_add_u32 s73, s73, s74\n"
"s_add_u32 s73, s73, 0x2400000\n"
"v_accvgpr_read_b32 v188, a32\n"
"v_accvgpr_read_b32 v189, a33\n"
"v_accvgpr_read_b32 v190, a34\n"
"v_accvgpr_read_b32 v191, a35\n"
"v_pk_mul_f32 v[188:189], v[188:189], v[12:13]\n"
"v_pk_mul_f32 v[190:191], v[190:191], v[12:13]\n"
"v_cvt_pk_bf16_f32 v24, v188, v189\n"
"v_cvt_pk_bf16_f32 v25, v190, v191\n"
"v_accvgpr_read_b32 v188, a36\n"
"v_accvgpr_read_b32 v189, a37\n"
"v_accvgpr_read_b32 v190, a38\n"
"v_accvgpr_read_b32 v191, a39\n"
"v_pk_mul_f32 v[188:189], v[188:189], v[12:13]\n"
"v_pk_mul_f32 v[190:191], v[190:191], v[12:13]\n"
"v_cvt_pk_bf16_f32 v26, v188, v189\n"
"v_cvt_pk_bf16_f32 v27, v190, v191\n"
"buffer_store_dwordx4 v[24:27], v1, s[48:51], s73 offen offset:0 sc1\n"
"v_accvgpr_read_b32 v188, a40\n"
"v_accvgpr_read_b32 v189, a41\n"
"v_accvgpr_read_b32 v190, a42\n"
"v_accvgpr_read_b32 v191, a43\n"
"v_pk_mul_f32 v[188:189], v[188:189], v[12:13]\n"
"v_pk_mul_f32 v[190:191], v[190:191], v[12:13]\n"
"v_cvt_pk_bf16_f32 v28, v188, v189\n"
"v_cvt_pk_bf16_f32 v29, v190, v191\n"
"v_accvgpr_read_b32 v188, a44\n"
"v_accvgpr_read_b32 v189, a45\n"
"v_accvgpr_read_b32 v190, a46\n"
"v_accvgpr_read_b32 v191, a47\n"
"v_pk_mul_f32 v[188:189], v[188:189], v[12:13]\n"
"v_pk_mul_f32 v[190:191], v[190:191], v[12:13]\n"
"v_cvt_pk_bf16_f32 v30, v188, v189\n"
"v_cvt_pk_bf16_f32 v31, v190, v191\n"
"buffer_store_dwordx4 v[28:31], v1, s[48:51], s73 offen offset:1024 sc1\n"
"v_accvgpr_read_b32 v188, a48\n"
"v_accvgpr_read_b32 v189, a49\n"
"v_accvgpr_read_b32 v190, a50\n"
"v_accvgpr_read_b32 v191, a51\n"
"v_pk_mul_f32 v[188:189], v[188:189], v[12:13]\n"
"v_pk_mul_f32 v[190:191], v[190:191], v[12:13]\n"
"v_cvt_pk_bf16_f32 v24, v188, v189\n"
"v_cvt_pk_bf16_f32 v25, v190, v191\n"
"v_accvgpr_read_b32 v188, a52\n"
"v_accvgpr_read_b32 v189, a53\n"
"v_accvgpr_read_b32 v190, a54\n"
"v_accvgpr_read_b32 v191, a55\n"
"v_pk_mul_f32 v[188:189], v[188:189], v[12:13]\n"
"v_pk_mul_f32 v[190:191], v[190:191], v[12:13]\n"
"v_cvt_pk_bf16_f32 v26, v188, v189\n"
"v_cvt_pk_bf16_f32 v27, v190, v191\n"
"buffer_store_dwordx4 v[24:27], v1, s[48:51], s73 offen offset:2048 sc1\n"
"v_accvgpr_read_b32 v188, a56\n"
"v_accvgpr_read_b32 v189, a57\n"
"v_accvgpr_read_b32 v190, a58\n"
"v_accvgpr_read_b32 v191, a59\n"
"v_pk_mul_f32 v[188:189], v[188:189], v[12:13]\n"
"v_pk_mul_f32 v[190:191], v[190:191], v[12:13]\n"
"v_cvt_pk_bf16_f32 v28, v188, v189\n"
"v_cvt_pk_bf16_f32 v29, v190, v191\n"
"v_accvgpr_read_b32 v188, a60\n"
"v_accvgpr_read_b32 v189, a61\n"
"v_accvgpr_read_b32 v190, a62\n"
"v_accvgpr_read_b32 v191, a63\n"
"v_pk_mul_f32 v[188:189], v[188:189], v[12:13]\n"
"v_pk_mul_f32 v[190:191], v[190:191], v[12:13]\n"
"v_cvt_pk_bf16_f32 v30, v188, v189\n"
"v_cvt_pk_bf16_f32 v31, v190, v191\n"
"buffer_store_dwordx4 v[28:31], v1, s[48:51], s73 offen offset:3072 sc1\n"
"s_and_b32 s73, s65, 1\n"
"s_lshl_b32 s73, s73, 2\n"
"s_add_u32 s73, s73, s64\n"
"s_lshl_b32 s73, s73, 16\n"
"s_lshl_b32 s92, s3, 19\n"
"s_add_u32 s92, s92, s73\n"
"s_add_u32 s92, s92, 0x3400000\n"
"s_cmp_eq_u32 s35, 0\n"
"s_cbranch_scc0 L_bad1_%=\n"
"s_waitcnt lgkmcnt(5)\n"
"v_mfma_f32_16x16x32_bf16 v[64:67], a[128:131], v[36:39], v[140:143]\n"
"v_exp_f32 v112, v112\n"
"v_mfma_f32_16x16x32_bf16 v[64:67], a[132:135], v[44:47], v[64:67]\n"
"ds_read_b128 a[180:183], v9 offset:13312\n"
"s_add_u32 s73, s92, 0x6000\n"
"v_mfma_f32_16x16x32_bf16 v[64:67], a[136:139], v[52:55], v[64:67]\n"
"buffer_load_dwordx4 a[0:3], v1, s[48:51], s73 offen offset:0 sc1\n"
"v_exp_f32 v113, v113\n"
"v_mfma_f32_16x16x32_bf16 v[64:67], a[140:143], v[60:63], v[64:67]\n"
"v_cvt_pk_bf16_f32 v235, v106, v107\n"
"v_add_f32 v14, v14, v112\n"
"v_mfma_f32_16x16x32_bf16 v[72:75], a[140:143], v[124:127], v[156:159]\n"
"v_exp_f32 v114, v114\n"
"v_mfma_f32_16x16x32_bf16 v[72:75], a[136:139], v[108:111], v[72:75]\n"
"ds_read_b128 a[184:187], v9 offset:14336\n"
"v_add_f32 v14, v14, v113\n"
"v_mfma_f32_16x16x32_bf16 v[72:75], a[132:135], v[92:95], v[72:75]\n"
"buffer_load_dwordx4 a[4:7], v1, s[48:51], s73 offen offset:1024 sc1\n"
"v_exp_f32 v115, v115\n"
"v_mfma_f32_16x16x32_bf16 v[72:75], a[128:131], v[76:79], v[72:75]\n"
"v_cvt_pk_bf16_f32 v242, v112, v113\n"
"v_add_f32 v14, v14, v114\n"
"v_mfma_f32_16x16x32_bf16 v[80:83], a[128:131], v[48:51], v[236:239]\n"
"v_exp_f32 v120, v120\n"
"v_mfma_f32_16x16x32_bf16 v[80:83], a[132:135], v[100:103], v[80:83]\n"
"ds_read_b128 a[188:191], v3 offset:48128\n"
"v_add_f32 v14, v14, v115\n"
"v_mfma_f32_16x16x32_bf16 v[80:83], a[136:139], v[228:231], v[80:83]\n"
"v_exp_f32 v121, v121\n"
"v_mfma_f32_16x16x32_bf16 v[80:83], a[140:143], v[164:167], v[80:83]\n"
"v_cvt_pk_bf16_f32 v243, v114, v115\n"
"v_add_f32 v15, v15, v120\n"
"v_mfma_f32_16x16x32_bf16 v[152:155], v[128:131], v[224:227], v[152:155]\n"
"s_waitcnt vmcnt(8) lgkmcnt(1)\n"
"s_barrier\n"
"v_mfma_f32_16x16x32_bf16 v[88:91], a[140:143], v[180:183], v[252:255]\n"
"v_exp_f32 v122, v122\n"
"v_mfma_f32_16x16x32_bf16 v[88:91], a[136:139], v[244:247], v[88:91]\n"
"ds_read_b128 a[140:143], v4 offset:3072\n"
"v_add_f32 v15, v15, v121\n"
"v_mfma_f32_16x16x32_bf16 v[88:91], a[132:135], v[116:119], v[88:91]\n"
"buffer_load_dwordx4 a[8:11], v1, s[48:51], s73 offen offset:2048 sc1\n"
"v_exp_f32 v123, v123\n"
"v_mfma_f32_16x16x32_bf16 v[88:91], a[128:131], v[56:59], v[88:91]\n"
"v_cvt_pk_bf16_f32 v250, v120, v121\n"
"v_add_f32 v15, v15, v122\n"
"v_mfma_f32_16x16x32_bf16 v[96:99], a[144:147], v[36:39], v[140:143]\n"
"v_exp_f32 v64, v64\n"
"v_mfma_f32_16x16x32_bf16 v[96:99], a[148:151], v[44:47], v[96:99]\n"
"ds_read_b128 a[136:139], v4 offset:2048\n"
"v_add_f32 v15, v15, v123\n"
"v_mfma_f32_16x16x32_bf16 v[96:99], a[152:155], v[52:55], v[96:99]\n"
"buffer_load_dwordx4 a[12:15], v1, s[48:51], s73 offen offset:3072 sc1\n"
"v_exp_f32 v65, v65\n"
"v_mfma_f32_16x16x32_bf16 v[96:99], a[156:159], v[60:63], v[96:99]\n"
"ds_read_b128 a[132:135], v4 offset:1024\n"
"v_cvt_pk_bf16_f32 v251, v122, v123\n"
"v_mfma_f32_16x16x32_bf16 v[160:163], v[128:131], v[232:235], v[160:163]\n"
"s_getpc_b64 s[6:7]\n"
"s_sub_u32 s88, s88, 1\n"
"s_cbranch_scc1 L_hand_%=\n"
"s_add_u32 s89, s89, s91\n"
"s_add_u32 m0, s67, 0x0\n"
"v_mfma_f32_16x16x32_bf16 v[104:107], a[156:159], v[124:127], v[156:159]\n"
"v_exp_f32 v66, v66\n"
"v_mfma_f32_16x16x32_bf16 v[104:107], a[152:155], v[108:111], v[104:107]\n"
"buffer_load_dwordx4 v8, s[48:51], s89 offen offset:0 sc1 lds\n"
"v_mfma_f32_16x16x32_bf16 v[104:107], a[148:151], v[92:95], v[104:107]\n"
"v_exp_f32 v67, v67\n"
"v_mfma_f32_16x16x32_bf16 v[104:107], a[144:147], v[76:79], v[104:107]\n"
"ds_read_b128 a[128:131], v4 offset:0\n"
"v_cvt_pk_bf16_f32 v192, v64, v65\n"
"v_mfma_f32_16x16x32_bf16 v[112:115], a[144:147], v[48:51], v[236:239]\n"
"v_exp_f32 v72, v72\n"
"v_mfma_f32_16x16x32_bf16 v[112:115], a[148:151], v[100:103], v[112:115]\n"
"buffer_load_dwordx4 v8, s[48:51], s89 offen offset:1024 sc1 lds\n"
"s_add_u32 s73, s92, 0x7000\n"
"v_mfma_f32_16x16x32_bf16 v[112:115], a[152:155], v[228:231], v[112:115]\n"
"buffer_load_dwordx4 a[16:19], v1, s[48:51], s73 offen offset:0 sc1\n"
"v_exp_f32 v73, v73\n"
"v_mfma_f32_16x16x32_bf16 v[112:115], a[156:159], v[164:167], v[112:115]\n"
"v_cvt_pk_bf16_f32 v193, v66, v67\n"
"v_add_u32 v9, 0x8000, v3\n"
"v_mfma_f32_16x16x32_bf16 v[120:123], a[156:159], v[180:183], v[252:255]\n"
"v_exp_f32 v74, v74\n"
"v_mfma_f32_16x16x32_bf16 v[120:123], a[152:155], v[244:247], v[120:123]\n"
"buffer_load_dwordx4 v8, s[48:51], s89 offen offset:2048 sc1 lds\n"
"v_mfma_f32_16x16x32_bf16 v[120:123], a[148:151], v[116:119], v[120:123]\n"
"v_exp_f32 v75, v75\n"
"v_mfma_f32_16x16x32_bf16 v[120:123], a[144:147], v[56:59], v[120:123]\n"
"ds_read_b128 a[156:159], v4 offset:7168\n"
"v_cvt_pk_bf16_f32 v200, v72, v73\n"
"v_mfma_f32_16x16x32_bf16 a[192:195], a[160:163], v[224:227], a[192:195]\n"
"v_exp_f32 v80, v80\n"
"v_mfma_f32_16x16x32_bf16 a[224:227], a[160:163], v[232:235], a[224:227]\n"
"buffer_load_dwordx4 v8, s[48:51], s89 offen offset:3072 sc1 lds\n"
"v_mfma_f32_16x16x32_bf16 a[64:67], a[160:163], v[240:243], a[64:67]\n"
"buffer_load_dwordx4 a[20:23], v1, s[48:51], s73 offen offset:1024 sc1\n"
"v_exp_f32 v81, v81\n"
"v_mfma_f32_16x16x32_bf16 a[96:99], a[160:163], v[248:251], a[96:99]\n"
"v_cvt_pk_bf16_f32 v201, v74, v75\n"
"v_add_f32 v14, v14, v80\n"
"v_mfma_f32_16x16x32_bf16 a[100:103], a[164:167], v[248:251], a[100:103]\n"
"v_exp_f32 v82, v82\n"
"v_mfma_f32_16x16x32_bf16 a[68:71], a[164:167], v[240:243], a[68:71]\n"
"ds_read_b128 a[152:155], v4 offset:6144\n"
"v_add_f32 v14, v14, v81\n"
"v_mfma_f32_16x16x32_bf16 a[228:231], a[164:167], v[232:235], a[228:231]\n"
"buffer_load_dwordx4 a[24:27], v1, s[48:51], s73 offen offset:2048 sc1\n"
"v_exp_f32 v83, v83\n"
"v_mfma_f32_16x16x32_bf16 a[196:199], a[164:167], v[224:227], a[196:199]\n"
"v_cvt_pk_bf16_f32 v208, v80, v81\n"
"v_add_f32 v14, v14, v82\n"
"v_mfma_f32_16x16x32_bf16 a[200:203], a[168:171], v[224:227], a[200:203]\n"
"v_exp_f32 v88, v88\n"
"v_mfma_f32_16x16x32_bf16 a[232:235], a[168:171], v[232:235], a[232:235]\n"
"ds_read_b128 a[148:151], v4 offset:5120\n"
"v_add_f32 v14, v14, v83\n"
"s_add_u32 s90, s89, 0x1000\n"
"s_add_u32 m0, s67, 0x1000\n"
"v_mfma_f32_16x16x32_bf16 a[72:75], a[168:171], v[240:243], a[72:75]\n"
"buffer_load_dwordx4 a[28:31], v1, s[48:51], s73 offen offset:3072 sc1\n"
"v_exp_f32 v89, v89\n"
"v_mfma_f32_16x16x32_bf16 a[104:107], a[168:171], v[248:251], a[104:107]\n"
"buffer_load_dwordx4 v8, s[48:51], s90 offen offset:0 sc1 lds\n"
"v_cvt_pk_bf16_f32 v209, v82, v83\n"
"v_add_f32 v15, v15, v88\n"
"v_mfma_f32_16x16x32_bf16 a[108:111], a[172:175], v[248:251], a[108:111]\n"
"v_exp_f32 v90, v90\n"
"v_mfma_f32_16x16x32_bf16 a[76:79], a[172:175], v[240:243], a[76:79]\n"
"ds_read_b128 a[144:147], v4 offset:4096\n"
"v_add_f32 v15, v15, v89\n"
"s_add_u32 s73, s92, 0x8000\n"
"v_mfma_f32_16x16x32_bf16 a[236:239], a[172:175], v[232:235], a[236:239]\n"
"buffer_load_dwordx4 a[32:35], v1, s[48:51], s73 offen offset:0 sc1\n"
"v_exp_f32 v91, v91\n"
"v_mfma_f32_16x16x32_bf16 a[204:207], a[172:175], v[224:227], a[204:207]\n"
"v_cvt_pk_bf16_f32 v216, v88, v89\n"
"v_add_f32 v15, v15, v90\n"
"v_mfma_f32_16x16x32_bf16 a[208:211], a[176:179], v[224:227], a[208:211]\n"
"v_exp_f32 v96, v96\n"
"v_mfma_f32_16x16x32_bf16 a[240:243], a[176:179], v[232:235], a[240:243]\n"
"ds_read_b128 a[160:163], v3 offset:40960\n"
"v_add_f32 v15, v15, v91\n"
"v_mfma_f32_16x16x32_bf16 a[80:83], a[176:179], v[240:243], a[80:83]\n"
"buffer_load_dwordx4 a[36:39], v1, s[48:51], s73 offen offset:1024 sc1\n"
"v_exp_f32 v97, v97\n"
"v_mfma_f32_16x16x32_bf16 a[112:115], a[176:179], v[248:251], a[112:115]\n"
"ds_read_b128 a[164:167], v3 offset:41984\n"
"v_cvt_pk_bf16_f32 v217, v90, v91\n"
"v_mfma_f32_16x16x32_bf16 a[116:119], a[180:183], v[248:251], a[116:119]\n"
"v_exp_f32 v98, v98\n"
"v_mfma_f32_16x16x32_bf16 a[84:87], a[180:183], v[240:243], a[84:87]\n"
"buffer_load_dwordx4 v8, s[48:51], s90 offen offset:1024 sc1 lds\n"
"v_mfma_f32_16x16x32_bf16 a[244:247], a[180:183], v[232:235], a[244:247]\n"
"v_exp_f32 v99, v99\n"
"v_mfma_f32_16x16x32_bf16 a[212:215], a[180:183], v[224:227], a[212:215]\n"
"ds_read_b128 a[168:171], v3 offset:43008\n"
"v_cvt_pk_bf16_f32 v194, v96, v97\n"
"v_mfma_f32_16x16x32_bf16 a[216:219], a[184:187], v[224:227], a[216:219]\n"
"v_exp_f32 v104, v104\n"
"v_mfma_f32_16x16x32_bf16 a[248:251], a[184:187], v[232:235], a[248:251]\n"
"buffer_load_dwordx4 v8, s[48:51], s90 offen offset:2048 sc1 lds\n"
"v_mfma_f32_16x16x32_bf16 a[88:91], a[184:187], v[240:243], a[88:91]\n"
"v_exp_f32 v105, v105\n"
"v_mfma_f32_16x16x32_bf16 a[120:123], a[184:187], v[248:251], a[120:123]\n"
"ds_read_b128 a[172:175], v3 offset:44032\n"
"v_cvt_pk_bf16_f32 v195, v98, v99\n"
"v_mfma_f32_16x16x32_bf16 a[124:127], v[172:175], v[248:251], a[124:127]\n"
"v_exp_f32 v106, v106\n"
"v_mfma_f32_16x16x32_bf16 a[92:95], v[172:175], v[240:243], a[92:95]\n"
"buffer_load_dwordx4 v8, s[48:51], s90 offen offset:3072 sc1 lds\n"
"v_mfma_f32_16x16x32_bf16 a[252:255], v[172:175], v[232:235], a[252:255]\n"
"buffer_load_dwordx4 a[40:43], v1, s[48:51], s73 offen offset:2048 sc1\n"
"v_exp_f32 v107, v107\n"
"v_mfma_f32_16x16x32_bf16 a[220:223], v[172:175], v[224:227], a[220:223]\n"
"ds_read_b128 a[176:179], v3 offset:45056\n"
"v_cvt_pk_bf16_f32 v202, v104, v105\n"
"s_sub_u32 s96, s96, 1\n"
"s_cbranch_scc1 L_drainB_%=\n"
"s_waitcnt lgkmcnt(5)\n"
"v_mfma_f32_16x16x32_bf16 v[64:67], a[128:131], v[36:39], v[140:143]\n"
"v_exp_f32 v112, v112\n"
"v_mfma_f32_16x16x32_bf16 v[64:67], a[132:135], v[44:47], v[64:67]\n"
"ds_read_b128 a[180:183], v9 offset:13312\n"
"v_mfma_f32_16x16x32_bf16 v[64:67], a[136:139], v[52:55], v[64:67]\n"
"buffer_load_dwordx4 a[44:47], v1, s[48:51], s73 offen offset:3072 sc1\n"
"v_exp_f32 v113, v113\n"
"v_mfma_f32_16x16x32_bf16 v[64:67], a[140:143], v[60:63], v[64:67]\n"
"v_cvt_pk_bf16_f32 v203, v106, v107\n"
"v_add_f32 v14, v14, v112\n"
"v_mfma_f32_16x16x32_bf16 v[72:75], a[140:143], v[124:127], v[156:159]\n"
"v_exp_f32 v114, v114\n"
"v_mfma_f32_16x16x32_bf16 v[72:75], a[136:139], v[108:111], v[72:75]\n"
"ds_read_b128 a[184:187], v9 offset:14336\n"
"v_add_f32 v14, v14, v113\n"
"s_add_u32 s73, s92, 0x9000\n"
"v_mfma_f32_16x16x32_bf16 v[72:75], a[132:135], v[92:95], v[72:75]\n"
"buffer_load_dwordx4 a[48:51], v1, s[48:51], s73 offen offset:0 sc1\n"
"v_exp_f32 v115, v115\n"
"v_mfma_f32_16x16x32_bf16 v[72:75], a[128:131], v[76:79], v[72:75]\n"
"v_cvt_pk_bf16_f32 v210, v112, v113\n"
"v_add_f32 v14, v14, v114\n"
"v_mfma_f32_16x16x32_bf16 v[80:83], a[128:131], v[48:51], v[236:239]\n"
"v_exp_f32 v120, v120\n"
"v_mfma_f32_16x16x32_bf16 v[80:83], a[132:135], v[100:103], v[80:83]\n"
"ds_read_b128 v[172:175], v4 offset:15360\n"
"v_add_f32 v14, v14, v115\n"
"v_mfma_f32_16x16x32_bf16 v[80:83], a[136:139], v[228:231], v[80:83]\n"
"v_exp_f32 v121, v121\n"
"v_mfma_f32_16x16x32_bf16 v[80:83], a[140:143], v[164:167], v[80:83]\n"
"v_cvt_pk_bf16_f32 v211, v114, v115\n"
"v_add_f32 v15, v15, v120\n"
"v_mfma_f32_16x16x32_bf16 v[152:155], v[128:131], v[192:195], v[152:155]\n"
"s_waitcnt vmcnt(8) lgkmcnt(1)\n"
"s_barrier\n"
"v_mfma_f32_16x16x32_bf16 v[88:91], a[140:143], v[180:183], v[252:255]\n"
"v_exp_f32 v122, v122\n"
"v_mfma_f32_16x16x32_bf16 v[88:91], a[136:139], v[244:247], v[88:91]\n"
"ds_read_b128 a[140:143], v4 offset:35840\n"
"v_add_f32 v15, v15, v121\n"
"v_mfma_f32_16x16x32_bf16 v[88:91], a[132:135], v[116:119], v[88:91]\n"
"buffer_load_dwordx4 a[52:55], v1, s[48:51], s73 offen offset:1024 sc1\n"
"v_exp_f32 v123, v123\n"
"v_mfma_f32_16x16x32_bf16 v[88:91], a[128:131], v[56:59], v[88:91]\n"
"v_cvt_pk_bf16_f32 v218, v120, v121\n"
"v_add_f32 v15, v15, v122\n"
"v_mfma_f32_16x16x32_bf16 v[96:99], a[144:147], v[36:39], v[140:143]\n"
"v_exp_f32 v64, v64\n"
"v_mfma_f32_16x16x32_bf16 v[96:99], a[148:151], v[44:47], v[96:99]\n"
"ds_read_b128 a[136:139], v4 offset:34816\n"
"v_add_f32 v15, v15, v123\n"
"v_mfma_f32_16x16x32_bf16 v[96:99], a[152:155], v[52:55], v[96:99]\n"
"buffer_load_dwordx4 a[56:59], v1, s[48:51], s73 offen offset:2048 sc1\n"
"v_exp_f32 v65, v65\n"
"v_mfma_f32_16x16x32_bf16 v[96:99], a[156:159], v[60:63], v[96:99]\n"
"ds_read_b128 a[132:135], v4 offset:33792\n"
"v_cvt_pk_bf16_f32 v219, v122, v123\n"
"v_mfma_f32_16x16x32_bf16 v[160:163], v[128:131], v[200:203], v[160:163]\n"
"s_getpc_b64 s[6:7]\n"
"s_sub_u32 s88, s88, 1\n"
"s_cbranch_scc1 L_hand_%=\n"
"s_add_u32 s89, s89, s91\n"
"s_add_u32 m0, s67, 0x8000\n"
"v_mfma_f32_16x16x32_bf16 v[104:107], a[156:159], v[124:127], v[156:159]\n"
"v_exp_f32 v66, v66\n"
"v_mfma_f32_16x16x32_bf16 v[104:107], a[152:155], v[108:111], v[104:107]\n"
"buffer_load_dwordx4 v8, s[48:51], s89 offen offset:0 sc1 lds\n"
"v_mfma_f32_16x16x32_bf16 v[104:107], a[148:151], v[92:95], v[104:107]\n"
"v_exp_f32 v67, v67\n"
"v_mfma_f32_16x16x32_bf16 v[104:107], a[144:147], v[76:79], v[104:107]\n"
"ds_read_b128 a[128:131], v4 offset:32768\n"
"v_cvt_pk_bf16_f32 v224, v64, v65\n"
"v_mfma_f32_16x16x32_bf16 v[112:115], a[144:147], v[48:51], v[236:239]\n"
"v_exp_f32 v72, v72\n"
"v_mfma_f32_16x16x32_bf16 v[112:115], a[148:151], v[100:103], v[112:115]\n"
"buffer_load_dwordx4 v8, s[48:51], s89 offen offset:1024 sc1 lds\n"
"v_mfma_f32_16x16x32_bf16 v[112:115], a[152:155], v[228:231], v[112:115]\n"
"v_exp_f32 v73, v73\n"
"v_mfma_f32_16x16x32_bf16 v[112:115], a[156:159], v[164:167], v[112:115]\n"
"v_cvt_pk_bf16_f32 v225, v66, v67\n"
"v_add_u32 v9, 0x0, v4\n"
"v_mfma_f32_16x16x32_bf16 v[120:123], a[156:159], v[180:183], v[252:255]\n"
"v_exp_f32 v74, v74\n"
"v_mfma_f32_16x16x32_bf16 v[120:123], a[152:155], v[244:247], v[120:123]\n"
"buffer_load_dwordx4 v8, s[48:51], s89 offen offset:2048 sc1 lds\n"
"v_mfma_f32_16x16x32_bf16 v[120:123], a[148:151], v[116:119], v[120:123]\n"
"v_exp_f32 v75, v75\n"
"v_mfma_f32_16x16x32_bf16 v[120:123], a[144:147], v[56:59], v[120:123]\n"
"ds_read_b128 a[156:159], v4 offset:39936\n"
"v_cvt_pk_bf16_f32 v232, v72, v73\n"
"v_mfma_f32_16x16x32_bf16 a[192:195], a[160:163], v[192:195], a[192:195]\n"
"v_exp_f32 v80, v80\n"
"v_mfma_f32_16x16x32_bf16 a[224:227], a[160:163], v[200:203], a[224:227]\n"
"buffer_load_dwordx4 v8, s[48:51], s89 offen offset:3072 sc1 lds\n"
"v_mfma_f32_16x16x32_bf16 a[64:67], a[160:163], v[208:211], a[64:67]\n"
"buffer_load_dwordx4 a[60:63], v1, s[48:51], s73 offen offset:3072 sc1\n"
"v_exp_f32 v81, v81\n"
"v_mfma_f32_16x16x32_bf16 a[96:99], a[160:163], v[216:219], a[96:99]\n"
"v_cvt_pk_bf16_f32 v233, v74, v75\n"
"v_add_f32 v14, v14, v80\n"
"v_mfma_f32_16x16x32_bf16 a[100:103], a[164:167], v[216:219], a[100:103]\n"
"v_exp_f32 v82, v82\n"
"v_mfma_f32_16x16x32_bf16 a[68:71], a[164:167], v[208:211], a[68:71]\n"
"ds_read_b128 a[152:155], v4 offset:38912\n"
"v_add_f32 v14, v14, v81\n"
"s_add_u32 s73, s92, 0xa000\n"
"v_mfma_f32_16x16x32_bf16 a[228:231], a[164:167], v[200:203], a[228:231]\n"
"buffer_load_dwordx4 v[136:139], v1, s[48:51], s73 offen offset:0 sc1\n"
"v_exp_f32 v83, v83\n"
"v_mfma_f32_16x16x32_bf16 a[196:199], a[164:167], v[192:195], a[196:199]\n"
"v_cvt_pk_bf16_f32 v240, v80, v81\n"
"v_add_f32 v14, v14, v82\n"
"v_mfma_f32_16x16x32_bf16 a[200:203], a[168:171], v[192:195], a[200:203]\n"
"v_exp_f32 v88, v88\n"
"v_mfma_f32_16x16x32_bf16 a[232:235], a[168:171], v[200:203], a[232:235]\n"
"ds_read_b128 a[148:151], v4 offset:37888\n"
"v_add_f32 v14, v14, v83\n"
"s_add_u32 s90, s89, 0x1000\n"
"s_add_u32 m0, s67, 0x9000\n"
"v_mfma_f32_16x16x32_bf16 a[72:75], a[168:171], v[208:211], a[72:75]\n"
"buffer_load_dwordx4 v[144:147], v1, s[48:51], s73 offen offset:1024 sc1\n"
"v_exp_f32 v89, v89\n"
"v_mfma_f32_16x16x32_bf16 a[104:107], a[168:171], v[216:219], a[104:107]\n"
"buffer_load_dwordx4 v8, s[48:51], s90 offen offset:0 sc1 lds\n"
"v_cvt_pk_bf16_f32 v241, v82, v83\n"
"v_add_f32 v15, v15, v88\n"
"v_mfma_f32_16x16x32_bf16 a[108:111], a[172:175], v[216:219], a[108:111]\n"
"v_exp_f32 v90, v90\n"
"v_mfma_f32_16x16x32_bf16 a[76:79], a[172:175], v[208:211], a[76:79]\n"
"ds_read_b128 a[144:147], v4 offset:36864\n"
"v_add_f32 v15, v15, v89\n"
"v_mfma_f32_16x16x32_bf16 a[236:239], a[172:175], v[200:203], a[236:239]\n"
"buffer_load_dwordx4 v[168:171], v1, s[48:51], s73 offen offset:2048 sc1\n"
"v_exp_f32 v91, v91\n"
"v_mfma_f32_16x16x32_bf16 a[204:207], a[172:175], v[192:195], a[204:207]\n"
"v_cvt_pk_bf16_f32 v248, v88, v89\n"
"v_add_f32 v15, v15, v90\n"
"v_mfma_f32_16x16x32_bf16 a[208:211], a[176:179], v[192:195], a[208:211]\n"
"v_exp_f32 v96, v96\n"
"v_mfma_f32_16x16x32_bf16 a[240:243], a[176:179], v[200:203], a[240:243]\n"
"ds_read_b128 a[160:163], v4 offset:8192\n"
"v_add_f32 v15, v15, v91\n"
"v_mfma_f32_16x16x32_bf16 a[80:83], a[176:179], v[208:211], a[80:83]\n"
"buffer_load_dwordx4 v[176:179], v1, s[48:51], s73 offen offset:3072 sc1\n"
"v_exp_f32 v97, v97\n"
"v_mfma_f32_16x16x32_bf16 a[112:115], a[176:179], v[216:219], a[112:115]\n"
"ds_read_b128 a[164:167], v4 offset:9216\n"
"v_cvt_pk_bf16_f32 v249, v90, v91\n"
"v_mfma_f32_16x16x32_bf16 a[116:119], a[180:183], v[216:219], a[116:119]\n"
"v_exp_f32 v98, v98\n"
"v_mfma_f32_16x16x32_bf16 a[84:87], a[180:183], v[208:211], a[84:87]\n"
"buffer_load_dwordx4 v8, s[48:51], s90 offen offset:1024 sc1 lds\n"
"v_mfma_f32_16x16x32_bf16 a[244:247], a[180:183], v[200:203], a[244:247]\n"
"v_exp_f32 v99, v99\n"
"v_mfma_f32_16x16x32_bf16 a[212:215], a[180:183], v[192:195], a[212:215]\n"
"ds_read_b128 a[168:171], v4 offset:10240\n"
"v_cvt_pk_bf16_f32 v226, v96, v97\n"
"v_mfma_f32_16x16x32_bf16 a[216:219], a[184:187], v[192:195], a[216:219]\n"
"v_exp_f32 v104, v104\n"
"v_mfma_f32_16x16x32_bf16 a[248:251], a[184:187], v[200:203], a[248:251]\n"
"buffer_load_dwordx4 v8, s[48:51], s90 offen offset:2048 sc1 lds\n"
"v_mfma_f32_16x16x32_bf16 a[88:91], a[184:187], v[208:211], a[88:91]\n"
"v_exp_f32 v105, v105\n"
"v_mfma_f32_16x16x32_bf16 a[120:123], a[184:187], v[216:219], a[120:123]\n"
"ds_read_b128 a[172:175], v4 offset:11264\n"
"v_cvt_pk_bf16_f32 v227, v98, v99\n"
"v_mfma_f32_16x16x32_bf16 a[124:127], a[188:191], v[216:219], a[124:127]\n"
"v_exp_f32 v106, v106\n"
"v_mfma_f32_16x16x32_bf16 a[92:95], a[188:191], v[208:211], a[92:95]\n"
"buffer_load_dwordx4 v8, s[48:51], s90 offen offset:3072 sc1 lds\n"
"s_add_u32 s73, s92, 0xb000\n"
"v_mfma_f32_16x16x32_bf16 a[252:255], a[188:191], v[200:203], a[252:255]\n"
"buffer_load_dwordx4 v[184:187], v1, s[48:51], s73 offen offset:0 sc1\n"
"v_exp_f32 v107, v107\n"
"v_mfma_f32_16x16x32_bf16 a[220:223], a[188:191], v[192:195], a[220:223]\n"
"ds_read_b128 a[176:179], v4 offset:12288\n"
"v_cvt_pk_bf16_f32 v234, v104, v105\n"
"s_waitcnt lgkmcnt(5)\n"
"v_mfma_f32_16x16x32_bf16 v[64:67], a[128:131], v[36:39], v[140:143]\n"
"v_exp_f32 v112, v112\n"
"v_mfma_f32_16x16x32_bf16 v[64:67], a[132:135], v[44:47], v[64:67]\n"
"ds_read_b128 a[180:183], v9 offset:13312\n"
"v_mfma_f32_16x16x32_bf16 v[64:67], a[136:139], v[52:55], v[64:67]\n"
"buffer_load_dwordx4 v[204:207], v1, s[48:51], s73 offen offset:1024 sc1\n"
"v_exp_f32 v113, v113\n"
"v_mfma_f32_16x16x32_bf16 v[64:67], a[140:143], v[60:63], v[64:67]\n"
"v_cvt_pk_bf16_f32 v235, v106, v107\n"
"v_add_f32 v14, v14, v112\n"
"v_mfma_f32_16x16x32_bf16 v[72:75], a[140:143], v[124:127], v[156:159]\n"
"v_exp_f32 v114, v114\n"
"v_mfma_f32_16x16x32_bf16 v[72:75], a[136:139], v[108:111], v[72:75]\n"
"ds_read_b128 a[184:187], v9 offset:14336\n"
"v_add_f32 v14, v14, v113\n"
"v_mfma_f32_16x16x32_bf16 v[72:75], a[132:135], v[92:95], v[72:75]\n"
"buffer_load_dwordx4 v[220:223], v1, s[48:51], s73 offen offset:2048 sc1\n"
"v_exp_f32 v115, v115\n"
"v_mfma_f32_16x16x32_bf16 v[72:75], a[128:131], v[76:79], v[72:75]\n"
"v_cvt_pk_bf16_f32 v242, v112, v113\n"
"v_add_f32 v14, v14, v114\n"
"v_mfma_f32_16x16x32_bf16 v[80:83], a[128:131], v[48:51], v[236:239]\n"
"v_exp_f32 v120, v120\n"
"v_mfma_f32_16x16x32_bf16 v[80:83], a[132:135], v[100:103], v[80:83]\n"
"ds_read_b128 a[188:191], v4 offset:48128\n"
"v_add_f32 v14, v14, v115\n"
"v_mfma_f32_16x16x32_bf16 v[80:83], a[136:139], v[228:231], v[80:83]\n"
"v_exp_f32 v121, v121\n"
"v_mfma_f32_16x16x32_bf16 v[80:83], a[140:143], v[164:167], v[80:83]\n"
"v_cvt_pk_bf16_f32 v243, v114, v115\n"
"v_add_f32 v15, v15, v120\n"
"v_mfma_f32_16x16x32_bf16 v[152:155], v[128:131], v[224:227], v[152:155]\n"
"s_waitcnt vmcnt(8) lgkmcnt(1)\n"
"s_barrier\n"
"v_mfma_f32_16x16x32_bf16 v[88:91], a[140:143], v[180:183], v[252:255]\n"
"v_exp_f32 v122, v122\n"
"v_mfma_f32_16x16x32_bf16 v[88:91], a[136:139], v[244:247], v[88:91]\n"
"ds_read_b128 a[140:143], v3 offset:3072\n"
"v_add_f32 v15, v15, v121\n"
"v_mfma_f32_16x16x32_bf16 v[88:91], a[132:135], v[116:119], v[88:91]\n"
"buffer_load_dwordx4 v[188:191], v1, s[48:51], s73 offen offset:3072 sc1\n"
"v_exp_f32 v123, v123\n"
"v_mfma_f32_16x16x32_bf16 v[88:91], a[128:131], v[56:59], v[88:91]\n"
"v_cvt_pk_bf16_f32 v250, v120, v121\n"
"v_add_f32 v15, v15, v122\n"
"v_mfma_f32_16x16x32_bf16 v[96:99], a[144:147], v[36:39], v[140:143]\n"
"v_exp_f32 v64, v64\n"
"v_mfma_f32_16x16x32_bf16 v[96:99], a[148:151], v[44:47], v[96:99]\n"
"ds_read_b128 a[136:139], v3 offset:2048\n"
"v_add_f32 v15, v15, v123\n"
"s_add_u32 s73, s92, 0xc000\n"
"v_mfma_f32_16x16x32_bf16 v[96:99], a[152:155], v[52:55], v[96:99]\n"
"buffer_load_dwordx4 v[32:35], v1, s[48:51], s73 offen offset:0 sc1\n"
"v_exp_f32 v65, v65\n"
"v_mfma_f32_16x16x32_bf16 v[96:99], a[156:159], v[60:63], v[96:99]\n"
"ds_read_b128 a[132:135], v3 offset:1024\n"
"v_cvt_pk_bf16_f32 v251, v122, v123\n"
"v_mfma_f32_16x16x32_bf16 v[160:163], v[128:131], v[232:235], v[160:163]\n"
"s_getpc_b64 s[6:7]\n"
"s_sub_u32 s88, s88, 1\n"
"s_cbranch_scc1 L_hand_%=\n"
"s_add_u32 s89, s89, s91\n"
"s_add_u32 m0, s67, 0x10000\n"
"v_mfma_f32_16x16x32_bf16 v[104:107], a[156:159], v[124:127], v[156:159]\n"
"v_exp_f32 v66, v66\n"
"v_mfma_f32_16x16x32_bf16 v[104:107], a[152:155], v[108:111], v[104:107]\n"
"buffer_load_dwordx4 v8, s[48:51], s89 offen offset:0 sc1 lds\n"
"v_mfma_f32_16x16x32_bf16 v[104:107], a[148:151], v[92:95], v[104:107]\n"
"v_exp_f32 v67, v67\n"
"v_mfma_f32_16x16x32_bf16 v[104:107], a[144:147], v[76:79], v[104:107]\n"
"ds_read_b128 a[128:131], v3 offset:0\n"
"v_cvt_pk_bf16_f32 v192, v64, v65\n"
"v_mfma_f32_16x16x32_bf16 v[112:115], a[144:147], v[48:51], v[236:239]\n"
"v_exp_f32 v72, v72\n"
"v_mfma_f32_16x16x32_bf16 v[112:115], a[148:151], v[100:103], v[112:115]\n"
"buffer_load_dwordx4 v8, s[48:51], s89 offen offset:1024 sc1 lds\n"
"v_mfma_f32_16x16x32_bf16 v[112:115], a[152:155], v[228:231], v[112:115]\n"
"buffer_load_dwordx4 v[68:71], v1, s[48:51], s73 offen offset:1024 sc1\n"
"v_exp_f32 v73, v73\n"
"v_mfma_f32_16x16x32_bf16 v[112:115], a[156:159], v[164:167], v[112:115]\n"
"v_cvt_pk_bf16_f32 v193, v66, v67\n"
"v_add_u32 v9, 0x8000, v4\n"
"v_mfma_f32_16x16x32_bf16 v[120:123], a[156:159], v[180:183], v[252:255]\n"
"v_exp_f32 v74, v74\n"
"v_mfma_f32_16x16x32_bf16 v[120:123], a[152:155], v[244:247], v[120:123]\n"
"buffer_load_dwordx4 v8, s[48:51], s89 offen offset:2048 sc1 lds\n"
"v_mfma_f32_16x16x32_bf16 v[120:123], a[148:151], v[116:119], v[120:123]\n"
"v_exp_f32 v75, v75\n"
"v_mfma_f32_16x16x32_bf16 v[120:123], a[144:147], v[56:59], v[120:123]\n"
"ds_read_b128 a[156:159], v3 offset:7168\n"
"v_cvt_pk_bf16_f32 v200, v72, v73\n"
"v_mfma_f32_16x16x32_bf16 a[192:195], a[160:163], v[224:227], a[192:195]\n"
"v_exp_f32 v80, v80\n"
"v_mfma_f32_16x16x32_bf16 a[224:227], a[160:163], v[232:235], a[224:227]\n"
"buffer_load_dwordx4 v8, s[48:51], s89 offen offset:3072 sc1 lds\n"
"v_mfma_f32_16x16x32_bf16 a[64:67], a[160:163], v[240:243], a[64:67]\n"
"buffer_load_dwordx4 v[196:199], v1, s[48:51], s73 offen offset:2048 sc1\n"
"v_exp_f32 v81, v81\n"
"v_mfma_f32_16x16x32_bf16 a[96:99], a[160:163], v[248:251], a[96:99]\n"
"v_cvt_pk_bf16_f32 v201, v74, v75\n"
"v_add_f32 v14, v14, v80\n"
"v_mfma_f32_16x16x32_bf16 a[100:103], a[164:167], v[248:251], a[100:103]\n"
"v_exp_f32 v82, v82\n"
"v_mfma_f32_16x16x32_bf16 a[68:71], a[164:167], v[240:243], a[68:71]\n"
"ds_read_b128 a[152:155], v3 offset:6144\n"
"v_add_f32 v14, v14, v81\n"
"v_mfma_f32_16x16x32_bf16 a[228:231], a[164:167], v[232:235], a[228:231]\n"
"buffer_load_dwordx4 v[132:135], v1, s[48:51], s73 offen offset:3072 sc1\n"
"v_exp_f32 v83, v83\n"
"v_mfma_f32_16x16x32_bf16 a[196:199], a[164:167], v[224:227], a[196:199]\n"
"v_cvt_pk_bf16_f32 v208, v80, v81\n"
"v_add_f32 v14, v14, v82\n"
"v_mfma_f32_16x16x32_bf16 a[200:203], a[168:171], v[224:227], a[200:203]\n"
"v_exp_f32 v88, v88\n"
"v_mfma_f32_16x16x32_bf16 a[232:235], a[168:171], v[232:235], a[232:235]\n"
"ds_read_b128 a[148:151], v3 offset:5120\n"
"v_add_f32 v14, v14, v83\n"
"s_add_u32 s90, s89, 0x1000\n"
"s_add_u32 m0, s67, 0x11000\n"
"s_add_u32 s73, s92, 0xd000\n"
"v_mfma_f32_16x16x32_bf16 a[72:75], a[168:171], v[240:243], a[72:75]\n"
"buffer_load_dwordx4 v[40:43], v1, s[48:51], s73 offen offset:0 sc1\n"
"v_exp_f32 v89, v89\n"
"v_mfma_f32_16x16x32_bf16 a[104:107], a[168:171], v[248:251], a[104:107]\n"
"buffer_load_dwordx4 v8, s[48:51], s90 offen offset:0 sc1 lds\n"
"v_cvt_pk_bf16_f32 v209, v82, v83\n"
"v_add_f32 v15, v15, v88\n"
"v_mfma_f32_16x16x32_bf16 a[108:111], a[172:175], v[248:251], a[108:111]\n"
"v_exp_f32 v90, v90\n"
"v_mfma_f32_16x16x32_bf16 a[76:79], a[172:175], v[240:243], a[76:79]\n"
"ds_read_b128 a[144:147], v3 offset:4096\n"
"v_add_f32 v15, v15, v89\n"
"v_mfma_f32_16x16x32_bf16 a[236:239], a[172:175], v[232:235], a[236:239]\n"
"buffer_load_dwordx4 v[84:87], v1, s[48:51], s73 offen offset:1024 sc1\n"
"v_exp_f32 v91, v91\n"
"v_mfma_f32_16x16x32_bf16 a[204:207], a[172:175], v[224:227], a[204:207]\n"
"v_cvt_pk_bf16_f32 v216, v88, v89\n"
"v_add_f32 v15, v15, v90\n"
"v_mfma_f32_16x16x32_bf16 a[208:211], a[176:179], v[224:227], a[208:211]\n"
"v_exp_f32 v96, v96\n"
"v_mfma_f32_16x16x32_bf16 a[240:243], a[176:179], v[232:235], a[240:243]\n"
"ds_read_b128 a[160:163], v4 offset:40960\n"
"v_add_f32 v15, v15, v91\n"
"v_mfma_f32_16x16x32_bf16 a[80:83], a[176:179], v[240:243], a[80:83]\n"
"buffer_load_dwordx4 v[212:215], v1, s[48:51], s73 offen offset:2048 sc1\n"
"v_exp_f32 v97, v97\n"
"v_mfma_f32_16x16x32_bf16 a[112:115], a[176:179], v[248:251], a[112:115]\n"
"ds_read_b128 a[164:167], v4 offset:41984\n"
"v_cvt_pk_bf16_f32 v217, v90, v91\n"
"v_mfma_f32_16x16x32_bf16 a[116:119], a[180:183], v[248:251], a[116:119]\n"
"v_exp_f32 v98, v98\n"
"v_mfma_f32_16x16x32_bf16 a[84:87], a[180:183], v[240:243], a[84:87]\n"
"buffer_load_dwordx4 v8, s[48:51], s90 offen offset:1024 sc1 lds\n"
"v_mfma_f32_16x16x32_bf16 a[244:247], a[180:183], v[232:235], a[244:247]\n"
"v_exp_f32 v99, v99\n"
"v_mfma_f32_16x16x32_bf16 a[212:215], a[180:183], v[224:227], a[212:215]\n"
"ds_read_b128 a[168:171], v4 offset:43008\n"
"v_cvt_pk_bf16_f32 v194, v96, v97\n"
"v_mfma_f32_16x16x32_bf16 a[216:219], a[184:187], v[224:227], a[216:219]\n"
"v_exp_f32 v104, v104\n"
"v_mfma_f32_16x16x32_bf16 a[248:251], a[184:187], v[232:235], a[248:251]\n"
"buffer_load_dwordx4 v8, s[48:51], s90 offen offset:2048 sc1 lds\n"
"v_mfma_f32_16x16x32_bf16 a[88:91], a[184:187], v[240:243], a[88:91]\n"
"v_exp_f32 v105, v105\n"
"v_mfma_f32_16x16x32_bf16 a[120:123], a[184:187], v[248:251], a[120:123]\n"
"ds_read_b128 a[172:175], v4 offset:44032\n"
"v_cvt_pk_bf16_f32 v195, v98, v99\n"
"v_mfma_f32_16x16x32_bf16 a[124:127], v[172:175], v[248:251], a[124:127]\n"
"v_exp_f32 v106, v106\n"
"v_mfma_f32_16x16x32_bf16 a[92:95], v[172:175], v[240:243], a[92:95]\n"
"buffer_load_dwordx4 v8, s[48:51], s90 offen offset:3072 sc1 lds\n"
"v_mfma_f32_16x16x32_bf16 a[252:255], v[172:175], v[232:235], a[252:255]\n"
"buffer_load_dwordx4 v[148:151], v1, s[48:51], s73 offen offset:3072 sc1\n"
"v_exp_f32 v107, v107\n"
"v_mfma_f32_16x16x32_bf16 a[220:223], v[172:175], v[224:227], a[220:223]\n"
"ds_read_b128 a[176:179], v4 offset:45056\n"
"v_cvt_pk_bf16_f32 v202, v104, v105\n"
"s_sub_u32 s96, s96, 1\n"
"s_cbranch_scc1 L_drainB_%=\n"
".p2align 6\n"
"L_B0_%=:\n"
"s_waitcnt lgkmcnt(5)\n"
"v_mfma_f32_16x16x32_bf16 v[64:67], a[128:131], v[36:39], v[140:143]\n"
"v_exp_f32 v112, v112\n"
"v_mfma_f32_16x16x32_bf16 v[64:67], a[132:135], v[44:47], v[64:67]\n"
"ds_read_b128 a[180:183], v9 offset:13312\n"
"v_mfma_f32_16x16x32_bf16 v[64:67], a[136:139], v[52:55], v[64:67]\n"
"v_exp_f32 v113, v113\n"
"v_mfma_f32_16x16x32_bf16 v[64:67], a[140:143], v[60:63], v[64:67]\n"
"v_cvt_pk_bf16_f32 v203, v106, v107\n"
"v_add_f32 v14, v14, v112\n"
"v_mfma_f32_16x16x32_bf16 v[72:75], a[140:143], v[124:127], v[156:159]\n"
"v_exp_f32 v114, v114\n"
"v_mfma_f32_16x16x32_bf16 v[72:75], a[136:139], v[108:111], v[72:75]\n"
"ds_read_b128 a[184:187], v9 offset:14336\n"
"v_add_f32 v14, v14, v113\n"
"v_mfma_f32_16x16x32_bf16 v[72:75], a[132:135], v[92:95], v[72:75]\n"
"v_exp_f32 v115, v115\n"
"v_mfma_f32_16x16x32_bf16 v[72:75], a[128:131], v[76:79], v[72:75]\n"
"v_cvt_pk_bf16_f32 v210, v112, v113\n"
"v_add_f32 v14, v14, v114\n"
"v_mfma_f32_16x16x32_bf16 v[80:83], a[128:131], v[48:51], v[236:239]\n"
"v_exp_f32 v120, v120\n"
"v_mfma_f32_16x16x32_bf16 v[80:83], a[132:135], v[100:103], v[80:83]\n"
"ds_read_b128 v[172:175], v3 offset:15360\n"
"v_add_f32 v14, v14, v115\n"
"v_mfma_f32_16x16x32_bf16 v[80:83], a[136:139], v[228:231], v[80:83]\n"
"v_exp_f32 v121, v121\n"
"v_mfma_f32_16x16x32_bf16 v[80:83], a[140:143], v[164:167], v[80:83]\n"
"v_cvt_pk_bf16_f32 v211, v114, v115\n"
"v_add_f32 v15, v15, v120\n"
"v_mfma_f32_16x16x32_bf16 v[152:155], v[128:131], v[192:195], v[152:155]\n"
"s_waitcnt vmcnt(8) lgkmcnt(1)\n"
"s_barrier\n"
"v_mfma_f32_16x16x32_bf16 v[88:91], a[140:143], v[180:183], v[252:255]\n"
"v_exp_f32 v122, v122\n"
"v_mfma_f32_16x16x32_bf16 v[88:91], a[136:139], v[244:247], v[88:91]\n"
"ds_read_b128 a[140:143], v3 offset:35840\n"
"v_add_f32 v15, v15, v121\n"
"v_mfma_f32_16x16x32_bf16 v[88:91], a[132:135], v[116:119], v[88:91]\n"
"v_exp_f32 v123, v123\n"
"v_mfma_f32_16x16x32_bf16 v[88:91], a[128:131], v[56:59], v[88:91]\n"
"v_cvt_pk_bf16_f32 v218, v120, v121\n"
"v_add_f32 v15, v15, v122\n"
"v_mfma_f32_16x16x32_bf16 v[96:99], a[144:147], v[36:39], v[140:143]\n"
"v_exp_f32 v64, v64\n"
"v_mfma_f32_16x16x32_bf16 v[96:99], a[148:151], v[44:47], v[96:99]\n"
"ds_read_b128 a[136:139], v3 offset:34816\n"
"v_add_f32 v15, v15, v123\n"
"v_mfma_f32_16x16x32_bf16 v[96:99], a[152:155], v[52:55], v[96:99]\n"
"v_exp_f32 v65, v65\n"
"v_mfma_f32_16x16x32_bf16 v[96:99], a[156:159], v[60:63], v[96:99]\n"
"ds_read_b128 a[132:135], v3 offset:33792\n"
"v_cvt_pk_bf16_f32 v219, v122, v123\n"
"v_mfma_f32_16x16x32_bf16 v[160:163], v[128:131], v[200:203], v[160:163]\n"
"s_getpc_b64 s[6:7]\n"
"s_sub_u32 s88, s88, 1\n"
"s_cbranch_scc1 L_hand_%=\n"
"s_add_u32 s89, s89, s91\n"
"s_add_u32 m0, s67, 0x18000\n"
"v_mfma_f32_16x16x32_bf16 v[104:107], a[156:159], v[124:127], v[156:159]\n"
"v_exp_f32 v66, v66\n"
"v_mfma_f32_16x16x32_bf16 v[104:107], a[152:155], v[108:111], v[104:107]\n"
"buffer_load_dwordx4 v8, s[48:51], s89 offen offset:0 sc1 lds\n"
"v_mfma_f32_16x16x32_bf16 v[104:107], a[148:151], v[92:95], v[104:107]\n"
"v_exp_f32 v67, v67\n"
"v_mfma_f32_16x16x32_bf16 v[104:107], a[144:147], v[76:79], v[104:107]\n"
"ds_read_b128 a[128:131], v3 offset:32768\n"
"v_cvt_pk_bf16_f32 v224, v64, v65\n"
"v_mfma_f32_16x16x32_bf16 v[112:115], a[144:147], v[48:51], v[236:239]\n"
"v_exp_f32 v72, v72\n"
"v_mfma_f32_16x16x32_bf16 v[112:115], a[148:151], v[100:103], v[112:115]\n"
"buffer_load_dwordx4 v8, s[48:51], s89 offen offset:1024 sc1 lds\n"
"v_mfma_f32_16x16x32_bf16 v[112:115], a[152:155], v[228:231], v[112:115]\n"
"v_exp_f32 v73, v73\n"
"v_mfma_f32_16x16x32_bf16 v[112:115], a[156:159], v[164:167], v[112:115]\n"
"v_cvt_pk_bf16_f32 v225, v66, v67\n"
"v_add_u32 v9, 0x0, v3\n"
"v_mfma_f32_16x16x32_bf16 v[120:123], a[156:159], v[180:183], v[252:255]\n"
"v_exp_f32 v74, v74\n"
"v_mfma_f32_16x16x32_bf16 v[120:123], a[152:155], v[244:247], v[120:123]\n"
"buffer_load_dwordx4 v8, s[48:51], s89 offen offset:2048 sc1 lds\n"
"v_mfma_f32_16x16x32_bf16 v[120:123], a[148:151], v[116:119], v[120:123]\n"
"v_exp_f32 v75, v75\n"
"v_mfma_f32_16x16x32_bf16 v[120:123], a[144:147], v[56:59], v[120:123]\n"
"ds_read_b128 a[156:159], v3 offset:39936\n"
"v_cvt_pk_bf16_f32 v232, v72, v73\n"
"v_mfma_f32_16x16x32_bf16 a[192:195], a[160:163], v[192:195], a[192:195]\n"
"v_exp_f32 v80, v80\n"
"v_mfma_f32_16x16x32_bf16 a[224:227], a[160:163], v[200:203], a[224:227]\n"
"buffer_load_dwordx4 v8, s[48:51], s89 offen offset:3072 sc1 lds\n"
"v_mfma_f32_16x16x32_bf16 a[64:67], a[160:163], v[208:211], a[64:67]\n"
"v_exp_f32 v81, v81\n"
"v_mfma_f32_16x16x32_bf16 a[96:99], a[160:163], v[216:219], a[96:99]\n"
"v_cvt_pk_bf16_f32 v233, v74, v75\n"
"v_add_f32 v14, v14, v80\n"
"v_mfma_f32_16x16x32_bf16 a[100:103], a[164:167], v[216:219], a[100:103]\n"
"v_exp_f32 v82, v82\n"
"v_mfma_f32_16x16x32_bf16 a[68:71], a[164:167], v[208:211], a[68:71]\n"
"ds_read_b128 a[152:155], v3 offset:38912\n"
"v_add_f32 v14, v14, v81\n"
"v_mfma_f32_16x16x32_bf16 a[228:231], a[164:167], v[200:203], a[228:231]\n"
"v_exp_f32 v83, v83\n"
"v_mfma_f32_16x16x32_bf16 a[196:199], a[164:167], v[192:195], a[196:199]\n"
"v_cvt_pk_bf16_f32 v240, v80, v81\n"
"v_add_f32 v14, v14, v82\n"
"v_mfma_f32_16x16x32_bf16 a[200:203], a[168:171], v[192:195], a[200:203]\n"
"v_exp_f32 v88, v88\n"
"v_mfma_f32_16x16x32_bf16 a[232:235], a[168:171], v[200:203], a[232:235]\n"
"ds_read_b128 a[148:151], v3 offset:37888\n"
"v_add_f32 v14, v14, v83\n"
"v_mfma_f32_16x16x32_bf16 a[72:75], a[168:171], v[208:211], a[72:75]\n"
"v_exp_f32 v89, v89\n"
"s_add_u32 s90, s89, 0x1000\n"
"s_add_u32 m0, s67, 0x19000\n"
"v_mfma_f32_16x16x32_bf16 a[104:107], a[168:171], v[216:219], a[104:107]\n"
"buffer_load_dwordx4 v8, s[48:51], s90 offen offset:0 sc1 lds\n"
"v_cvt_pk_bf16_f32 v241, v82, v83\n"
"v_add_f32 v15, v15, v88\n"
"v_mfma_f32_16x16x32_bf16 a[108:111], a[172:175], v[216:219], a[108:111]\n"
"v_exp_f32 v90, v90\n"
"v_mfma_f32_16x16x32_bf16 a[76:79], a[172:175], v[208:211], a[76:79]\n"
"ds_read_b128 a[144:147], v3 offset:36864\n"
"v_add_f32 v15, v15, v89\n"
"v_mfma_f32_16x16x32_bf16 a[236:239], a[172:175], v[200:203], a[236:239]\n"
"v_exp_f32 v91, v91\n"
"v_mfma_f32_16x16x32_bf16 a[204:207], a[172:175], v[192:195], a[204:207]\n"
"v_cvt_pk_bf16_f32 v248, v88, v89\n"
"v_add_f32 v15, v15, v90\n"
"v_mfma_f32_16x16x32_bf16 a[208:211], a[176:179], v[192:195], a[208:211]\n"
"v_exp_f32 v96, v96\n"
"v_mfma_f32_16x16x32_bf16 a[240:243], a[176:179], v[200:203], a[240:243]\n"
"ds_read_b128 a[160:163], v3 offset:8192\n"
"v_add_f32 v15, v15, v91\n"
"v_mfma_f32_16x16x32_bf16 a[80:83], a[176:179], v[208:211], a[80:83]\n"
"v_exp_f32 v97, v97\n"
"v_mfma_f32_16x16x32_bf16 a[112:115], a[176:179], v[216:219], a[112:115]\n"
"ds_read_b128 a[164:167], v3 offset:9216\n"
"v_cvt_pk_bf16_f32 v249, v90, v91\n"
"v_mfma_f32_16x16x32_bf16 a[116:119], a[180:183], v[216:219], a[116:119]\n"
"v_exp_f32 v98, v98\n"
"v_mfma_f32_16x16x32_bf16 a[84:87], a[180:183], v[208:211], a[84:87]\n"
"buffer_load_dwordx4 v8, s[48:51], s90 offen offset:1024 sc1 lds\n"
"v_mfma_f32_16x16x32_bf16 a[244:247], a[180:183], v[200:203], a[244:247]\n"
"v_exp_f32 v99, v99\n"
"v_mfma_f32_16x16x32_bf16 a[212:215], a[180:183], v[192:195], a[212:215]\n"
"ds_read_b128 a[168:171], v3 offset:10240\n"
"v_cvt_pk_bf16_f32 v226, v96, v97\n"
"v_mfma_f32_16x16x32_bf16 a[216:219], a[184:187], v[192:195], a[216:219]\n"
"v_exp_f32 v104, v104\n"
"v_mfma_f32_16x16x32_bf16 a[248:251], a[184:187], v[200:203], a[248:251]\n"
"buffer_load_dwordx4 v8, s[48:51], s90 offen offset:2048 sc1 lds\n"
"v_mfma_f32_16x16x32_bf16 a[88:91], a[184:187], v[208:211], a[88:91]\n"
"v_exp_f32 v105, v105\n"
"v_mfma_f32_16x16x32_bf16 a[120:123], a[184:187], v[216:219], a[120:123]\n"
"ds_read_b128 a[172:175], v3 offset:11264\n"
"v_cvt_pk_bf16_f32 v227, v98, v99\n"
"v_mfma_f32_16x16x32_bf16 a[124:127], a[188:191], v[216:219], a[124:127]\n"
"v_exp_f32 v106, v106\n"
"v_mfma_f32_16x16x32_bf16 a[92:95], a[188:191], v[208:211], a[92:95]\n"
"buffer_load_dwordx4 v8, s[48:51], s90 offen offset:3072 sc1 lds\n"
"v_mfma_f32_16x16x32_bf16 a[252:255], a[188:191], v[200:203], a[252:255]\n"
"v_exp_f32 v107, v107\n"
"v_mfma_f32_16x16x32_bf16 a[220:223], a[188:191], v[192:195], a[220:223]\n"
"ds_read_b128 a[176:179], v3 offset:12288\n"
"v_cvt_pk_bf16_f32 v234, v104, v105\n"
"L_B1_%=:\n"
"s_waitcnt lgkmcnt(5)\n"
"v_mfma_f32_16x16x32_bf16 v[64:67], a[128:131], v[36:39], v[140:143]\n"
"v_exp_f32 v112, v112\n"
"v_mfma_f32_16x16x32_bf16 v[64:67], a[132:135], v[44:47], v[64:67]\n"
"ds_read_b128 a[180:183], v9 offset:13312\n"
"v_mfma_f32_16x16x32_bf16 v[64:67], a[136:139], v[52:55], v[64:67]\n"
"v_exp_f32 v113, v113\n"
"v_mfma_f32_16x16x32_bf16 v[64:67], a[140:143], v[60:63], v[64:67]\n"
"v_cvt_pk_bf16_f32 v235, v106, v107\n"
"v_add_f32 v14, v14, v112\n"
"v_mfma_f32_16x16x32_bf16 v[72:75], a[140:143], v[124:127], v[156:159]\n"
"v_exp_f32 v114, v114\n"
"v_mfma_f32_16x16x32_bf16 v[72:75], a[136:139], v[108:111], v[72:75]\n"
"ds_read_b128 a[184:187], v9 offset:14336\n"
"v_add_f32 v14, v14, v113\n"
"v_mfma_f32_16x16x32_bf16 v[72:75], a[132:135], v[92:95], v[72:75]\n"
"v_exp_f32 v115, v115\n"
"v_mfma_f32_16x16x32_bf16 v[72:75], a[128:131], v[76:79], v[72:75]\n"
"v_cvt_pk_bf16_f32 v242, v112, v113\n"
"v_add_f32 v14, v14, v114\n"
"v_mfma_f32_16x16x32_bf16 v[80:83], a[128:131], v[48:51], v[236:239]\n"
"v_exp_f32 v120, v120\n"
"v_mfma_f32_16x16x32_bf16 v[80:83], a[132:135], v[100:103], v[80:83]\n"
"ds_read_b128 a[188:191], v3 offset:48128\n"
"v_add_f32 v14, v14, v115\n"
"v_mfma_f32_16x16x32_bf16 v[80:83], a[136:139], v[228:231], v[80:83]\n"
"v_exp_f32 v121, v121\n"
"v_mfma_f32_16x16x32_bf16 v[80:83], a[140:143], v[164:167], v[80:83]\n"
"v_cvt_pk_bf16_f32 v243, v114, v115\n"
"v_add_f32 v15, v15, v120\n"
"v_mfma_f32_16x16x32_bf16 v[152:155], v[128:131], v[224:227], v[152:155]\n"
"s_waitcnt vmcnt(8) lgkmcnt(1)\n"
"s_barrier\n"
"v_mfma_f32_16x16x32_bf16 v[88:91], a[140:143], v[180:183], v[252:255]\n"
"v_exp_f32 v122, v122\n"
"v_mfma_f32_16x16x32_bf16 v[88:91], a[136:139], v[244:247], v[88:91]\n"
"ds_read_b128 a[140:143], v4 offset:3072\n"
"v_add_f32 v15, v15, v121\n"
"v_mfma_f32_16x16x32_bf16 v[88:91], a[132:135], v[116:119], v[88:91]\n"
"v_exp_f32 v123, v123\n"
"v_mfma_f32_16x16x32_bf16 v[88:91], a[128:131], v[56:59], v[88:91]\n"
"v_cvt_pk_bf16_f32 v250, v120, v121\n"
"v_add_f32 v15, v15, v122\n"
"v_mfma_f32_16x16x32_bf16 v[96:99], a[144:147], v[36:39], v[140:143]\n"
"v_exp_f32 v64, v64\n"
"v_mfma_f32_16x16x32_bf16 v[96:99], a[148:151], v[44:47], v[96:99]\n"
"ds_read_b128 a[136:139], v4 offset:2048\n"
"v_add_f32 v15, v15, v123\n"
"v_mfma_f32_16x16x32_bf16 v[96:99], a[152:155], v[52:55], v[96:99]\n"
"v_exp_f32 v65, v65\n"
"v_mfma_f32_16x16x32_bf16 v[96:99], a[156:159], v[60:63], v[96:99]\n"
"ds_read_b128 a[132:135], v4 offset:1024\n"
"v_cvt_pk_bf16_f32 v251, v122, v123\n"
"v_mfma_f32_16x16x32_bf16 v[160:163], v[128:131], v[232:235], v[160:163]\n"
"s_getpc_b64 s[6:7]\n"
"s_sub_u32 s88, s88, 1\n"
"s_cbranch_scc1 L_hand_%=\n"
"s_add_u32 s89, s89, s91\n"
"s_add_u32 m0, s67, 0x0\n"
"v_mfma_f32_16x16x32_bf16 v[104:107], a[156:159], v[124:127], v[156:159]\n"
"v_exp_f32 v66, v66\n"
"v_mfma_f32_16x16x32_bf16 v[104:107], a[152:155], v[108:111], v[104:107]\n"
"buffer_load_dwordx4 v8, s[48:51], s89 offen offset:0 sc1 lds\n"
"v_mfma_f32_16x16x32_bf16 v[104:107], a[148:151], v[92:95], v[104:107]\n"
"v_exp_f32 v67, v67\n"
"v_mfma_f32_16x16x32_bf16 v[104:107], a[144:147], v[76:79], v[104:107]\n"
"ds_read_b128 a[128:131], v4 offset:0\n"
"v_cvt_pk_bf16_f32 v192, v64, v65\n"
"v_mfma_f32_16x16x32_bf16 v[112:115], a[144:147], v[48:51], v[236:239]\n"
"v_exp_f32 v72, v72\n"
"v_mfma_f32_16x16x32_bf16 v[112:115], a[148:151], v[100:103], v[112:115]\n"
"buffer_load_dwordx4 v8, s[48:51], s89 offen offset:1024 sc1 lds\n"
"v_mfma_f32_16x16x32_bf16 v[112:115], a[152:155], v[228:231], v[112:115]\n"
"v_exp_f32 v73, v73\n"
"v_mfma_f32_16x16x32_bf16 v[112:115], a[156:159], v[164:167], v[112:115]\n"
"v_cvt_pk_bf16_f32 v193, v66, v67\n"
"v_add_u32 v9, 0x8000, v3\n"
"v_mfma_f32_16x16x32_bf16 v[120:123], a[156:159], v[180:183], v[252:255]\n"
"v_exp_f32 v74, v74\n"
"v_mfma_f32_16x16x32_bf16 v[120:123], a[152:155], v[244:247], v[120:123]\n"
"buffer_load_dwordx4 v8, s[48:51], s89 offen offset:2048 sc1 lds\n"
"v_mfma_f32_16x16x32_bf16 v[120:123], a[148:151], v[116:119], v[120:123]\n"
"v_exp_f32 v75, v75\n"
"v_mfma_f32_16x16x32_bf16 v[120:123], a[144:147], v[56:59], v[120:123]\n"
"ds_read_b128 a[156:159], v4 offset:7168\n"
"v_cvt_pk_bf16_f32 v200, v72, v73\n"
"v_mfma_f32_16x16x32_bf16 a[192:195], a[160:163], v[224:227], a[192:195]\n"
"v_exp_f32 v80, v80\n"
"v_mfma_f32_16x16x32_bf16 a[224:227], a[160:163], v[232:235], a[224:227]\n"
"buffer_load_dwordx4 v8, s[48:51], s89 offen offset:3072 sc1 lds\n"
"v_mfma_f32_16x16x32_bf16 a[64:67], a[160:163], v[240:243], a[64:67]\n"
"v_exp_f32 v81, v81\n"
"v_mfma_f32_16x16x32_bf16 a[96:99], a[160:163], v[248:251], a[96:99]\n"
"v_cvt_pk_bf16_f32 v201, v74, v75\n"
"v_add_f32 v14, v14, v80\n"
"v_mfma_f32_16x16x32_bf16 a[100:103], a[164:167], v[248:251], a[100:103]\n"
"v_exp_f32 v82, v82\n"
"v_mfma_f32_16x16x32_bf16 a[68:71], a[164:167], v[240:243], a[68:71]\n"
"ds_read_b128 a[152:155], v4 offset:6144\n"
"v_add_f32 v14, v14, v81\n"
"v_mfma_f32_16x16x32_bf16 a[228:231], a[164:167], v[232:235], a[228:231]\n"
"v_exp_f32 v83, v83\n"
"v_mfma_f32_16x16x32_bf16 a[196:199], a[164:167], v[224:227], a[196:199]\n"
"v_cvt_pk_bf16_f32 v208, v80, v81\n"
"v_add_f32 v14, v14, v82\n"
"v_mfma_f32_16x16x32_bf16 a[200:203], a[168:171], v[224:227], a[200:203]\n"
"v_exp_f32 v88, v88\n"
"v_mfma_f32_16x16x32_bf16 a[232:235], a[168:171], v[232:235], a[232:235]\n"
"ds_read_b128 a[148:151], v4 offset:5120\n"
"v_add_f32 v14, v14, v83\n"
"v_mfma_f32_16x16x32_bf16 a[72:75], a[168:171], v[240:243], a[72:75]\n"
"v_exp_f32 v89, v89\n"
"s_add_u32 s90, s89, 0x1000\n"
"s_add_u32 m0, s67, 0x1000\n"
"v_mfma_f32_16x16x32_bf16 a[104:107], a[168:171], v[248:251], a[104:107]\n"
"buffer_load_dwordx4 v8, s[48:51], s90 offen offset:0 sc1 lds\n"
"v_cvt_pk_bf16_f32 v209, v82, v83\n"
"v_add_f32 v15, v15, v88\n"
"v_mfma_f32_16x16x32_bf16 a[108:111], a[172:175], v[248:251], a[108:111]\n"
"v_exp_f32 v90, v90\n"
"v_mfma_f32_16x16x32_bf16 a[76:79], a[172:175], v[240:243], a[76:79]\n"
"ds_read_b128 a[144:147], v4 offset:4096\n"
"v_add_f32 v15, v15, v89\n"
"v_mfma_f32_16x16x32_bf16 a[236:239], a[172:175], v[232:235], a[236:239]\n"
"v_exp_f32 v91, v91\n"
"v_mfma_f32_16x16x32_bf16 a[204:207], a[172:175], v[224:227], a[204:207]\n"
"v_cvt_pk_bf16_f32 v216, v88, v89\n"
"v_add_f32 v15, v15, v90\n"
"v_mfma_f32_16x16x32_bf16 a[208:211], a[176:179], v[224:227], a[208:211]\n"
"v_exp_f32 v96, v96\n"
"v_mfma_f32_16x16x32_bf16 a[240:243], a[176:179], v[232:235], a[240:243]\n"
"ds_read_b128 a[160:163], v3 offset:40960\n"
"v_add_f32 v15, v15, v91\n"
"v_mfma_f32_16x16x32_bf16 a[80:83], a[176:179], v[240:243], a[80:83]\n"
"v_exp_f32 v97, v97\n"
"v_mfma_f32_16x16x32_bf16 a[112:115], a[176:179], v[248:251], a[112:115]\n"
"ds_read_b128 a[164:167], v3 offset:41984\n"
"v_cvt_pk_bf16_f32 v217, v90, v91\n"
"v_mfma_f32_16x16x32_bf16 a[116:119], a[180:183], v[248:251], a[116:119]\n"
"v_exp_f32 v98, v98\n"
"v_mfma_f32_16x16x32_bf16 a[84:87], a[180:183], v[240:243], a[84:87]\n"
"buffer_load_dwordx4 v8, s[48:51], s90 offen offset:1024 sc1 lds\n"
"v_mfma_f32_16x16x32_bf16 a[244:247], a[180:183], v[232:235], a[244:247]\n"
"v_exp_f32 v99, v99\n"
"v_mfma_f32_16x16x32_bf16 a[212:215], a[180:183], v[224:227], a[212:215]\n"
"ds_read_b128 a[168:171], v3 offset:43008\n"
"v_cvt_pk_bf16_f32 v194, v96, v97\n"
"v_mfma_f32_16x16x32_bf16 a[216:219], a[184:187], v[224:227], a[216:219]\n"
"v_exp_f32 v104, v104\n"
"v_mfma_f32_16x16x32_bf16 a[248:251], a[184:187], v[232:235], a[248:251]\n"
"buffer_load_dwordx4 v8, s[48:51], s90 offen offset:2048 sc1 lds\n"
"v_mfma_f32_16x16x32_bf16 a[88:91], a[184:187], v[240:243], a[88:91]\n"
"v_exp_f32 v105, v105\n"
"v_mfma_f32_16x16x32_bf16 a[120:123], a[184:187], v[248:251], a[120:123]\n"
"ds_read_b128 a[172:175], v3 offset:44032\n"
"v_cvt_pk_bf16_f32 v195, v98, v99\n"
"v_mfma_f32_16x16x32_bf16 a[124:127], v[172:175], v[248:251], a[124:127]\n"
"v_exp_f32 v106, v106\n"
"v_mfma_f32_16x16x32_bf16 a[92:95], v[172:175], v[240:243], a[92:95]\n"
"buffer_load_dwordx4 v8, s[48:51], s90 offen offset:3072 sc1 lds\n"
"v_mfma_f32_16x16x32_bf16 a[252:255], v[172:175], v[232:235], a[252:255]\n"
"v_exp_f32 v107, v107\n"
"v_mfma_f32_16x16x32_bf16 a[220:223], v[172:175], v[224:227], a[220:223]\n"
"ds_read_b128 a[176:179], v3 offset:45056\n"
"v_cvt_pk_bf16_f32 v202, v104, v105\n"
"s_sub_u32 s96, s96, 1\n"
"s_cbranch_scc1 L_drainB_%=\n"
"L_B2_%=:\n"
"s_waitcnt lgkmcnt(5)\n"
"v_mfma_f32_16x16x32_bf16 v[64:67], a[128:131], v[36:39], v[140:143]\n"
"v_exp_f32 v112, v112\n"
"v_mfma_f32_16x16x32_bf16 v[64:67], a[132:135], v[44:47], v[64:67]\n"
"ds_read_b128 a[180:183], v9 offset:13312\n"
"v_mfma_f32_16x16x32_bf16 v[64:67], a[136:139], v[52:55], v[64:67]\n"
"v_exp_f32 v113, v113\n"
"v_mfma_f32_16x16x32_bf16 v[64:67], a[140:143], v[60:63], v[64:67]\n"
"v_cvt_pk_bf16_f32 v203, v106, v107\n"
"v_add_f32 v14, v14, v112\n"
"v_mfma_f32_16x16x32_bf16 v[72:75], a[140:143], v[124:127], v[156:159]\n"
"v_exp_f32 v114, v114\n"
"v_mfma_f32_16x16x32_bf16 v[72:75], a[136:139], v[108:111], v[72:75]\n"
"ds_read_b128 a[184:187], v9 offset:14336\n"
"v_add_f32 v14, v14, v113\n"
"v_mfma_f32_16x16x32_bf16 v[72:75], a[132:135], v[92:95], v[72:75]\n"
"v_exp_f32 v115, v115\n"
"v_mfma_f32_16x16x32_bf16 v[72:75], a[128:131], v[76:79], v[72:75]\n"
"v_cvt_pk_bf16_f32 v210, v112, v113\n"
"v_add_f32 v14, v14, v114\n"
"v_mfma_f32_16x16x32_bf16 v[80:83], a[128:131], v[48:51], v[236:239]\n"
"v_exp_f32 v120, v120\n"
"v_mfma_f32_16x16x32_bf16 v[80:83], a[132:135], v[100:103], v[80:83]\n"
"ds_read_b128 v[172:175], v4 offset:15360\n"
"v_add_f32 v14, v14, v115\n"
"v_mfma_f32_16x16x32_bf16 v[80:83], a[136:139], v[228:231], v[80:83]\n"
"v_exp_f32 v121, v121\n"
"v_mfma_f32_16x16x32_bf16 v[80:83], a[140:143], v[164:167], v[80:83]\n"
"v_cvt_pk_bf16_f32 v211, v114, v115\n"
"v_add_f32 v15, v15, v120\n"
"v_mfma_f32_16x16x32_bf16 v[152:155], v[128:131], v[192:195], v[152:155]\n"
"s_waitcnt vmcnt(8) lgkmcnt(1)\n"
"s_barrier\n"
"v_mfma_f32_16x16x32_bf16 v[88:91], a[140:143], v[180:183], v[252:255]\n"
"v_exp_f32 v122, v122\n"
"v_mfma_f32_16x16x32_bf16 v[88:91], a[136:139], v[244:247], v[88:91]\n"
"ds_read_b128 a[140:143], v4 offset:35840\n"
"v_add_f32 v15, v15, v121\n"
"v_mfma_f32_16x16x32_bf16 v[88:91], a[132:135], v[116:119], v[88:91]\n"
"v_exp_f32 v123, v123\n"
"v_mfma_f32_16x16x32_bf16 v[88:91], a[128:131], v[56:59], v[88:91]\n"
"v_cvt_pk_bf16_f32 v218, v120, v121\n"
"v_add_f32 v15, v15, v122\n"
"v_mfma_f32_16x16x32_bf16 v[96:99], a[144:147], v[36:39], v[140:143]\n"
"v_exp_f32 v64, v64\n"
"v_mfma_f32_16x16x32_bf16 v[96:99], a[148:151], v[44:47], v[96:99]\n"
"ds_read_b128 a[136:139], v4 offset:34816\n"
"v_add_f32 v15, v15, v123\n"
"v_mfma_f32_16x16x32_bf16 v[96:99], a[152:155], v[52:55], v[96:99]\n"
"v_exp_f32 v65, v65\n"
"v_mfma_f32_16x16x32_bf16 v[96:99], a[156:159], v[60:63], v[96:99]\n"
"ds_read_b128 a[132:135], v4 offset:33792\n"
"v_cvt_pk_bf16_f32 v219, v122, v123\n"
"v_mfma_f32_16x16x32_bf16 v[160:163], v[128:131], v[200:203], v[160:163]\n"
"s_getpc_b64 s[6:7]\n"
"s_sub_u32 s88, s88, 1\n"
"s_cbranch_scc1 L_hand_%=\n"
"s_add_u32 s89, s89, s91\n"
"s_add_u32 m0, s67, 0x8000\n"
"v_mfma_f32_16x16x32_bf16 v[104:107], a[156:159], v[124:127], v[156:159]\n"
"v_exp_f32 v66, v66\n"
"v_mfma_f32_16x16x32_bf16 v[104:107], a[152:155], v[108:111], v[104:107]\n"
"buffer_load_dwordx4 v8, s[48:51], s89 offen offset:0 sc1 lds\n"
"v_mfma_f32_16x16x32_bf16 v[104:107], a[148:151], v[92:95], v[104:107]\n"
"v_exp_f32 v67, v67\n"
"v_mfma_f32_16x16x32_bf16 v[104:107], a[144:147], v[76:79], v[104:107]\n"
"ds_read_b128 a[128:131], v4 offset:32768\n"
"v_cvt_pk_bf16_f32 v224, v64, v65\n"
"v_mfma_f32_16x16x32_bf16 v[112:115], a[144:147], v[48:51], v[236:239]\n"
"v_exp_f32 v72, v72\n"
"v_mfma_f32_16x16x32_bf16 v[112:115], a[148:151], v[100:103], v[112:115]\n"
"buffer_load_dwordx4 v8, s[48:51], s89 offen offset:1024 sc1 lds\n"
"v_mfma_f32_16x16x32_bf16 v[112:115], a[152:155], v[228:231], v[112:115]\n"
"v_exp_f32 v73, v73\n"
"v_mfma_f32_16x16x32_bf16 v[112:115], a[156:159], v[164:167], v[112:115]\n"
"v_cvt_pk_bf16_f32 v225, v66, v67\n"
"v_add_u32 v9, 0x0, v4\n"
"v_mfma_f32_16x16x32_bf16 v[120:123], a[156:159], v[180:183], v[252:255]\n"
"v_exp_f32 v74, v74\n"
"v_mfma_f32_16x16x32_bf16 v[120:123], a[152:155], v[244:247], v[120:123]\n"
"buffer_load_dwordx4 v8, s[48:51], s89 offen offset:2048 sc1 lds\n"
"v_mfma_f32_16x16x32_bf16 v[120:123], a[148:151], v[116:119], v[120:123]\n"
"v_exp_f32 v75, v75\n"
"v_mfma_f32_16x16x32_bf16 v[120:123], a[144:147], v[56:59], v[120:123]\n"
"ds_read_b128 a[156:159], v4 offset:39936\n"
"v_cvt_pk_bf16_f32 v232, v72, v73\n"
"v_mfma_f32_16x16x32_bf16 a[192:195], a[160:163], v[192:195], a[192:195]\n"
"v_exp_f32 v80, v80\n"
"v_mfma_f32_16x16x32_bf16 a[224:227], a[160:163], v[200:203], a[224:227]\n"
"buffer_load_dwordx4 v8, s[48:51], s89 offen offset:3072 sc1 lds\n"
"v_mfma_f32_16x16x32_bf16 a[64:67], a[160:163], v[208:211], a[64:67]\n"
"v_exp_f32 v81, v81\n"
"v_mfma_f32_16x16x32_bf16 a[96:99], a[160:163], v[216:219], a[96:99]\n"
"v_cvt_pk_bf16_f32 v233, v74, v75\n"
"v_add_f32 v14, v14, v80\n"
"v_mfma_f32_16x16x32_bf16 a[100:103], a[164:167], v[216:219], a[100:103]\n"
"v_exp_f32 v82, v82\n"
"v_mfma_f32_16x16x32_bf16 a[68:71], a[164:167], v[208:211], a[68:71]\n"
"ds_read_b128 a[152:155], v4 offset:38912\n"
"v_add_f32 v14, v14, v81\n"
"v_mfma_f32_16x16x32_bf16 a[228:231], a[164:167], v[200:203], a[228:231]\n"
"v_exp_f32 v83, v83\n"
"v_mfma_f32_16x16x32_bf16 a[196:199], a[164:167], v[192:195], a[196:199]\n"
"v_cvt_pk_bf16_f32 v240, v80, v81\n"
"v_add_f32 v14, v14, v82\n"
"v_mfma_f32_16x16x32_bf16 a[200:203], a[168:171], v[192:195], a[200:203]\n"
"v_exp_f32 v88, v88\n"
"v_mfma_f32_16x16x32_bf16 a[232:235], a[168:171], v[200:203], a[232:235]\n"
"ds_read_b128 a[148:151], v4 offset:37888\n"
"v_add_f32 v14, v14, v83\n"
"v_mfma_f32_16x16x32_bf16 a[72:75], a[168:171], v[208:211], a[72:75]\n"
"v_exp_f32 v89, v89\n"
"s_add_u32 s90, s89, 0x1000\n"
"s_add_u32 m0, s67, 0x9000\n"
"v_mfma_f32_16x16x32_bf16 a[104:107], a[168:171], v[216:219], a[104:107]\n"
"buffer_load_dwordx4 v8, s[48:51], s90 offen offset:0 sc1 lds\n"
"v_cvt_pk_bf16_f32 v241, v82, v83\n"
"v_add_f32 v15, v15, v88\n"
"v_mfma_f32_16x16x32_bf16 a[108:111], a[172:175], v[216:219], a[108:111]\n"
"v_exp_f32 v90, v90\n"
"v_mfma_f32_16x16x32_bf16 a[76:79], a[172:175], v[208:211], a[76:79]\n"
"ds_read_b128 a[144:147], v4 offset:36864\n"
"v_add_f32 v15, v15, v89\n"
"v_mfma_f32_16x16x32_bf16 a[236:239], a[172:175], v[200:203], a[236:239]\n"
"v_exp_f32 v91, v91\n"
"v_mfma_f32_16x16x32_bf16 a[204:207], a[172:175], v[192:195], a[204:207]\n"
"v_cvt_pk_bf16_f32 v248, v88, v89\n"
"v_add_f32 v15, v15, v90\n"
"v_mfma_f32_16x16x32_bf16 a[208:211], a[176:179], v[192:195], a[208:211]\n"
"v_exp_f32 v96, v96\n"
"v_mfma_f32_16x16x32_bf16 a[240:243], a[176:179], v[200:203], a[240:243]\n"
"ds_read_b128 a[160:163], v4 offset:8192\n"
"v_add_f32 v15, v15, v91\n"
"v_mfma_f32_16x16x32_bf16 a[80:83], a[176:179], v[208:211], a[80:83]\n"
"v_exp_f32 v97, v97\n"
"v_mfma_f32_16x16x32_bf16 a[112:115], a[176:179], v[216:219], a[112:115]\n"
"ds_read_b128 a[164:167], v4 offset:9216\n"
"v_cvt_pk_bf16_f32 v249, v90, v91\n"
"v_mfma_f32_16x16x32_bf16 a[116:119], a[180:183], v[216:219], a[116:119]\n"
"v_exp_f32 v98, v98\n"
"v_mfma_f32_16x16x32_bf16 a[84:87], a[180:183], v[208:211], a[84:87]\n"
"buffer_load_dwordx4 v8, s[48:51], s90 offen offset:1024 sc1 lds\n"
"v_mfma_f32_16x16x32_bf16 a[244:247], a[180:183], v[200:203], a[244:247]\n"
"v_exp_f32 v99, v99\n"
"v_mfma_f32_16x16x32_bf16 a[212:215], a[180:183], v[192:195], a[212:215]\n"
"ds_read_b128 a[168:171], v4 offset:10240\n"
"v_cvt_pk_bf16_f32 v226, v96, v97\n"
"v_mfma_f32_16x16x32_bf16 a[216:219], a[184:187], v[192:195], a[216:219]\n"
"v_exp_f32 v104, v104\n"
"v_mfma_f32_16x16x32_bf16 a[248:251], a[184:187], v[200:203], a[248:251]\n"
"buffer_load_dwordx4 v8, s[48:51], s90 offen offset:2048 sc1 lds\n"
"v_mfma_f32_16x16x32_bf16 a[88:91], a[184:187], v[208:211], a[88:91]\n"
"v_exp_f32 v105, v105\n"
"v_mfma_f32_16x16x32_bf16 a[120:123], a[184:187], v[216:219], a[120:123]\n"
"ds_read_b128 a[172:175], v4 offset:11264\n"
"v_cvt_pk_bf16_f32 v227, v98, v99\n"
"v_mfma_f32_16x16x32_bf16 a[124:127], a[188:191], v[216:219], a[124:127]\n"
"v_exp_f32 v106, v106\n"
"v_mfma_f32_16x16x32_bf16 a[92:95], a[188:191], v[208:211], a[92:95]\n"
"buffer_load_dwordx4 v8, s[48:51], s90 offen offset:3072 sc1 lds\n"
"v_mfma_f32_16x16x32_bf16 a[252:255], a[188:191], v[200:203], a[252:255]\n"
"v_exp_f32 v107, v107\n"
"v_mfma_f32_16x16x32_bf16 a[220:223], a[188:191], v[192:195], a[220:223]\n"
"ds_read_b128 a[176:179], v4 offset:12288\n"
"v_cvt_pk_bf16_f32 v234, v104, v105\n"
"L_B3_%=:\n"
"s_waitcnt lgkmcnt(5)\n"
"v_mfma_f32_16x16x32_bf16 v[64:67], a[128:131], v[36:39], v[140:143]\n"
"v_exp_f32 v112, v112\n"
"v_mfma_f32_16x16x32_bf16 v[64:67], a[132:135], v[44:47], v[64:67]\n"
"ds_read_b128 a[180:183], v9 offset:13312\n"
"v_mfma_f32_16x16x32_bf16 v[64:67], a[136:139], v[52:55], v[64:67]\n"
"v_exp_f32 v113, v113\n"
"v_mfma_f32_16x16x32_bf16 v[64:67], a[140:143], v[60:63], v[64:67]\n"
"v_cvt_pk_bf16_f32 v235, v106, v107\n"
"v_add_f32 v14, v14, v112\n"
"v_mfma_f32_16x16x32_bf16 v[72:75], a[140:143], v[124:127], v[156:159]\n"
"v_exp_f32 v114, v114\n"
"v_mfma_f32_16x16x32_bf16 v[72:75], a[136:139], v[108:111], v[72:75]\n"
"ds_read_b128 a[184:187], v9 offset:14336\n"
"v_add_f32 v14, v14, v113\n"
"v_mfma_f32_16x16x32_bf16 v[72:75], a[132:135], v[92:95], v[72:75]\n"
"v_exp_f32 v115, v115\n"
"v_mfma_f32_16x16x32_bf16 v[72:75], a[128:131], v[76:79], v[72:75]\n"
"v_cvt_pk_bf16_f32 v242, v112, v113\n"
"v_add_f32 v14, v14, v114\n"
"v_mfma_f32_16x16x32_bf16 v[80:83], a[128:131], v[48:51], v[236:239]\n"
"v_exp_f32 v120, v120\n"
"v_mfma_f32_16x16x32_bf16 v[80:83], a[132:135], v[100:103], v[80:83]\n"
"ds_read_b128 a[188:191], v4 offset:48128\n"
"v_add_f32 v14, v14, v115\n"
"v_mfma_f32_16x16x32_bf16 v[80:83], a[136:139], v[228:231], v[80:83]\n"
"v_exp_f32 v121, v121\n"
"v_mfma_f32_16x16x32_bf16 v[80:83], a[140:143], v[164:167], v[80:83]\n"
"v_cvt_pk_bf16_f32 v243, v114, v115\n"
"v_add_f32 v15, v15, v120\n"
"v_mfma_f32_16x16x32_bf16 v[152:155], v[128:131], v[224:227], v[152:155]\n"
"s_waitcnt vmcnt(8) lgkmcnt(1)\n"
"s_barrier\n"
"v_mfma_f32_16x16x32_bf16 v[88:91], a[140:143], v[180:183], v[252:255]\n"
"v_exp_f32 v122, v122\n"
"v_mfma_f32_16x16x32_bf16 v[88:91], a[136:139], v[244:247], v[88:91]\n"
"ds_read_b128 a[140:143], v3 offset:3072\n"
"v_add_f32 v15, v15, v121\n"
"v_mfma_f32_16x16x32_bf16 v[88:91], a[132:135], v[116:119], v[88:91]\n"
"v_exp_f32 v123, v123\n"
"v_mfma_f32_16x16x32_bf16 v[88:91], a[128:131], v[56:59], v[88:91]\n"
"v_cvt_pk_bf16_f32 v250, v120, v121\n"
"v_add_f32 v15, v15, v122\n"
"v_mfma_f32_16x16x32_bf16 v[96:99], a[144:147], v[36:39], v[140:143]\n"
"v_exp_f32 v64, v64\n"
"v_mfma_f32_16x16x32_bf16 v[96:99], a[148:151], v[44:47], v[96:99]\n"
"ds_read_b128 a[136:139], v3 offset:2048\n"
"v_add_f32 v15, v15, v123\n"
"v_mfma_f32_16x16x32_bf16 v[96:99], a[152:155], v[52:55], v[96:99]\n"
"v_exp_f32 v65, v65\n"
"v_mfma_f32_16x16x32_bf16 v[96:99], a[156:159], v[60:63], v[96:99]\n"
"ds_read_b128 a[132:135], v3 offset:1024\n"
"v_cvt_pk_bf16_f32 v251, v122, v123\n"
"v_mfma_f32_16x16x32_bf16 v[160:163], v[128:131], v[232:235], v[160:163]\n"
"s_getpc_b64 s[6:7]\n"
"s_sub_u32 s88, s88, 1\n"
"s_cbranch_scc1 L_hand_%=\n"
"s_add_u32 s89, s89, s91\n"
"s_add_u32 m0, s67, 0x10000\n"
"v_mfma_f32_16x16x32_bf16 v[104:107], a[156:159], v[124:127], v[156:159]\n"
"v_exp_f32 v66, v66\n"
"v_mfma_f32_16x16x32_bf16 v[104:107], a[152:155], v[108:111], v[104:107]\n"
"buffer_load_dwordx4 v8, s[48:51], s89 offen offset:0 sc1 lds\n"
"v_mfma_f32_16x16x32_bf16 v[104:107], a[148:151], v[92:95], v[104:107]\n"
"v_exp_f32 v67, v67\n"
"v_mfma_f32_16x16x32_bf16 v[104:107], a[144:147], v[76:79], v[104:107]\n"
"ds_read_b128 a[128:131], v3 offset:0\n"
"v_cvt_pk_bf16_f32 v192, v64, v65\n"
"v_mfma_f32_16x16x32_bf16 v[112:115], a[144:147], v[48:51], v[236:239]\n"
"v_exp_f32 v72, v72\n"
"v_mfma_f32_16x16x32_bf16 v[112:115], a[148:151], v[100:103], v[112:115]\n"
"buffer_load_dwordx4 v8, s[48:51], s89 offen offset:1024 sc1 lds\n"
"v_mfma_f32_16x16x32_bf16 v[112:115], a[152:155], v[228:231], v[112:115]\n"
"v_exp_f32 v73, v73\n"
"v_mfma_f32_16x16x32_bf16 v[112:115], a[156:159], v[164:167], v[112:115]\n"
"v_cvt_pk_bf16_f32 v193, v66, v67\n"
"v_add_u32 v9, 0x8000, v4\n"
"v_mfma_f32_16x16x32_bf16 v[120:123], a[156:159], v[180:183], v[252:255]\n"
"v_exp_f32 v74, v74\n"
"v_mfma_f32_16x16x32_bf16 v[120:123], a[152:155], v[244:247], v[120:123]\n"
"buffer_load_dwordx4 v8, s[48:51], s89 offen offset:2048 sc1 lds\n"
"v_mfma_f32_16x16x32_bf16 v[120:123], a[148:151], v[116:119], v[120:123]\n"
"v_exp_f32 v75, v75\n"
"v_mfma_f32_16x16x32_bf16 v[120:123], a[144:147], v[56:59], v[120:123]\n"
"ds_read_b128 a[156:159], v3 offset:7168\n"
"v_cvt_pk_bf16_f32 v200, v72, v73\n"
"v_mfma_f32_16x16x32_bf16 a[192:195], a[160:163], v[224:227], a[192:195]\n"
"v_exp_f32 v80, v80\n"
"v_mfma_f32_16x16x32_bf16 a[224:227], a[160:163], v[232:235], a[224:227]\n"
"buffer_load_dwordx4 v8, s[48:51], s89 offen offset:3072 sc1 lds\n"
"v_mfma_f32_16x16x32_bf16 a[64:67], a[160:163], v[240:243], a[64:67]\n"
"v_exp_f32 v81, v81\n"
"v_mfma_f32_16x16x32_bf16 a[96:99], a[160:163], v[248:251], a[96:99]\n"
"v_cvt_pk_bf16_f32 v201, v74, v75\n"
"v_add_f32 v14, v14, v80\n"
"v_mfma_f32_16x16x32_bf16 a[100:103], a[164:167], v[248:251], a[100:103]\n"
"v_exp_f32 v82, v82\n"
"v_mfma_f32_16x16x32_bf16 a[68:71], a[164:167], v[240:243], a[68:71]\n"
"ds_read_b128 a[152:155], v3 offset:6144\n"
"v_add_f32 v14, v14, v81\n"
"v_mfma_f32_16x16x32_bf16 a[228:231], a[164:167], v[232:235], a[228:231]\n"
"v_exp_f32 v83, v83\n"
"v_mfma_f32_16x16x32_bf16 a[196:199], a[164:167], v[224:227], a[196:199]\n"
"v_cvt_pk_bf16_f32 v208, v80, v81\n"
"v_add_f32 v14, v14, v82\n"
"v_mfma_f32_16x16x32_bf16 a[200:203], a[168:171], v[224:227], a[200:203]\n"
"v_exp_f32 v88, v88\n"
"v_mfma_f32_16x16x32_bf16 a[232:235], a[168:171], v[232:235], a[232:235]\n"
"ds_read_b128 a[148:151], v3 offset:5120\n"
"v_add_f32 v14, v14, v83\n"
"v_mfma_f32_16x16x32_bf16 a[72:75], a[168:171], v[240:243], a[72:75]\n"
"v_exp_f32 v89, v89\n"
"s_add_u32 s90, s89, 0x1000\n"
"s_add_u32 m0, s67, 0x11000\n"
"v_mfma_f32_16x16x32_bf16 a[104:107], a[168:171], v[248:251], a[104:107]\n"
"buffer_load_dwordx4 v8, s[48:51], s90 offen offset:0 sc1 lds\n"
"v_cvt_pk_bf16_f32 v209, v82, v83\n"
"v_add_f32 v15, v15, v88\n"
"v_mfma_f32_16x16x32_bf16 a[108:111], a[172:175], v[248:251], a[108:111]\n"
"v_exp_f32 v90, v90\n"
"v_mfma_f32_16x16x32_bf16 a[76:79], a[172:175], v[240:243], a[76:79]\n"
"ds_read_b128 a[144:147], v3 offset:4096\n"
"v_add_f32 v15, v15, v89\n"
"v_mfma_f32_16x16x32_bf16 a[236:239], a[172:175], v[232:235], a[236:239]\n"
"v_exp_f32 v91, v91\n"
"v_mfma_f32_16x16x32_bf16 a[204:207], a[172:175], v[224:227], a[204:207]\n"
"v_cvt_pk_bf16_f32 v216, v88, v89\n"
"v_add_f32 v15, v15, v90\n"
"v_mfma_f32_16x16x32_bf16 a[208:211], a[176:179], v[224:227], a[208:211]\n"
"v_exp_f32 v96, v96\n"
"v_mfma_f32_16x16x32_bf16 a[240:243], a[176:179], v[232:235], a[240:243]\n"
"ds_read_b128 a[160:163], v4 offset:40960\n"
"v_add_f32 v15, v15, v91\n"
"v_mfma_f32_16x16x32_bf16 a[80:83], a[176:179], v[240:243], a[80:83]\n"
"v_exp_f32 v97, v97\n"
"v_mfma_f32_16x16x32_bf16 a[112:115], a[176:179], v[248:251], a[112:115]\n"
"ds_read_b128 a[164:167], v4 offset:41984\n"
"v_cvt_pk_bf16_f32 v217, v90, v91\n"
"v_mfma_f32_16x16x32_bf16 a[116:119], a[180:183], v[248:251], a[116:119]\n"
"v_exp_f32 v98, v98\n"
"v_mfma_f32_16x16x32_bf16 a[84:87], a[180:183], v[240:243], a[84:87]\n"
"buffer_load_dwordx4 v8, s[48:51], s90 offen offset:1024 sc1 lds\n"
"v_mfma_f32_16x16x32_bf16 a[244:247], a[180:183], v[232:235], a[244:247]\n"
"v_exp_f32 v99, v99\n"
"v_mfma_f32_16x16x32_bf16 a[212:215], a[180:183], v[224:227], a[212:215]\n"
"ds_read_b128 a[168:171], v4 offset:43008\n"
"v_cvt_pk_bf16_f32 v194, v96, v97\n"
"v_mfma_f32_16x16x32_bf16 a[216:219], a[184:187], v[224:227], a[216:219]\n"
"v_exp_f32 v104, v104\n"
"v_mfma_f32_16x16x32_bf16 a[248:251], a[184:187], v[232:235], a[248:251]\n"
"buffer_load_dwordx4 v8, s[48:51], s90 offen offset:2048 sc1 lds\n"
"v_mfma_f32_16x16x32_bf16 a[88:91], a[184:187], v[240:243], a[88:91]\n"
"v_exp_f32 v105, v105\n"
"v_mfma_f32_16x16x32_bf16 a[120:123], a[184:187], v[248:251], a[120:123]\n"
"ds_read_b128 a[172:175], v4 offset:44032\n"
"v_cvt_pk_bf16_f32 v195, v98, v99\n"
"v_mfma_f32_16x16x32_bf16 a[124:127], v[172:175], v[248:251], a[124:127]\n"
"v_exp_f32 v106, v106\n"
"v_mfma_f32_16x16x32_bf16 a[92:95], v[172:175], v[240:243], a[92:95]\n"
"buffer_load_dwordx4 v8, s[48:51], s90 offen offset:3072 sc1 lds\n"
"v_mfma_f32_16x16x32_bf16 a[252:255], v[172:175], v[232:235], a[252:255]\n"
"v_exp_f32 v107, v107\n"
"v_mfma_f32_16x16x32_bf16 a[220:223], v[172:175], v[224:227], a[220:223]\n"
"ds_read_b128 a[176:179], v4 offset:45056\n"
"v_cvt_pk_bf16_f32 v202, v104, v105\n"
"s_sub_u32 s96, s96, 1\n"
"s_cbranch_scc0 L_B0_%=\n"
"L_drainB_%=:\n"
"s_waitcnt vmcnt(16)\n"
"v_cmp_eq_u64 vcc, s[68:69], v[18:19]\n"
"s_nop 0\n"
"s_cmp_eq_u64 vcc, -1\n"
"s_cbranch_scc0 L_yb_spin%=\n"
"LT_yb_ok%=:\n"
"ds_read_b128 a[180:183], v9 offset:13312\n"
"ds_read_b128 a[184:187], v9 offset:14336\n"
"v_cmp_eq_u32 vcc, 0, v21\n"
"v_mov_b32 v27, -1\n"
"s_lshl_b32 s73, s66, 2\n"
"s_add_u32 s73, s73, s64\n"
"v_cndmask_b32 v27, v27, 0, vcc\n"
"s_lshl_b32 s73, s73, 8\n"
"s_lshl_b32 s74, s65, 3\n"
"s_add_u32 s73, s73, s74\n"
"s_add_u32 s73, s73, 0x92000\n"
"v_mov_b32 v30, s68\n"
"v_mov_b32 v31, s69\n"
"s_cmp_eq_u32 s35, 0\n"
"s_cselect_b32 s73, s73, 0x40000000\n"
"buffer_store_dwordx2 v[30:31], v27, s[48:51], s73 offen sc1\n"
"s_waitcnt lgkmcnt(0)\n"
"s_barrier\n"
"s_and_b32 s73, s66, 1\n"
"s_lshl_b32 s73, s73, 16\n"
"s_add_u32 s76, s73, s67\n"
"s_xor_b32 s95, s76, 0x10000\n"
"s_add_u32 s97, s67, 0x20000\n"
"v_add_u32 v4, s97, v1\n"
"v_add_u32 v3, s95, v1\n"
"v_add_u32 v2, s76, v1\n"
"ds_read_b128 v[36:39], v4 offset:0\n"
"ds_read_b128 v[44:47], v4 offset:1024\n"
"ds_read_b128 v[52:55], v4 offset:2048\n"
"ds_read_b128 v[60:63], v4 offset:3072\n"
"ds_read_b128 v[76:79], v4 offset:4096\n"
"ds_read_b128 v[92:95], v4 offset:5120\n"
"ds_read_b128 v[108:111], v4 offset:6144\n"
"ds_read_b128 v[124:127], v4 offset:7168\n"
"ds_read_b128 a[128:131], v2 offset:0\n"
"ds_read_b128 a[132:135], v2 offset:1024\n"
"ds_read_b128 a[136:139], v2 offset:2048\n"
"ds_read_b128 a[140:143], v2 offset:3072\n"
"ds_read_b128 a[144:147], v2 offset:4096\n"
"ds_read_b128 a[148:151], v2 offset:5120\n"
"ds_read_b128 a[152:155], v2 offset:6144\n"
"ds_read_b128 a[156:159], v2 offset:7168\n"
"v_cvt_pk_bf16_f32 v203, v106, v107\n"
"v_exp_f32 v112, v112\n"
"s_nop 0\n"
"v_add_f32 v14, v14, v112\n"
"v_exp_f32 v113, v113\n"
"s_nop 0\n"
"v_add_f32 v14, v14, v113\n"
"v_cvt_pk_bf16_f32 v210, v112, v113\n"
"v_exp_f32 v114, v114\n"
"s_nop 0\n"
"v_add_f32 v14, v14, v114\n"
"v_exp_f32 v115, v115\n"
"s_nop 0\n"
"v_add_f32 v14, v14, v115\n"
"v_cvt_pk_bf16_f32 v211, v114, v115\n"
"v_exp_f32 v120, v120\n"
"s_nop 0\n"
"v_add_f32 v15, v15, v120\n"
"v_exp_f32 v121, v121\n"
"s_nop 0\n"
"v_add_f32 v15, v15, v121\n"
"v_cvt_pk_bf16_f32 v218, v120, v121\n"
"v_exp_f32 v122, v122\n"
"s_nop 0\n"
"v_add_f32 v15, v15, v122\n"
"v_exp_f32 v123, v123\n"
"s_nop 0\n"
"v_add_f32 v15, v15, v123\n"
"v_cvt_pk_bf16_f32 v219, v122, v123\n"
"s_waitcnt lgkmcnt(0)\n"
"v_mfma_f32_16x16x32_bf16 a[192:195], a[160:163], v[192:195], a[192:195]\n"
"v_mfma_f32_16x16x32_bf16 a[224:227], a[160:163], v[200:203], a[224:227]\n"
"v_mfma_f32_16x16x32_bf16 a[196:199], a[164:167], v[192:195], a[196:199]\n"
"v_mfma_f32_16x16x32_bf16 a[228:231], a[164:167], v[200:203], a[228:231]\n"
"v_mfma_f32_16x16x32_bf16 a[200:203], a[168:171], v[192:195], a[200:203]\n"
"v_mfma_f32_16x16x32_bf16 a[232:235], a[168:171], v[200:203], a[232:235]\n"
"v_mfma_f32_16x16x32_bf16 a[204:207], a[172:175], v[192:195], a[204:207]\n"
"v_mfma_f32_16x16x32_bf16 a[236:239], a[172:175], v[200:203], a[236:239]\n"
"v_mfma_f32_16x16x32_bf16 a[208:211], a[176:179], v[192:195], a[208:211]\n"
"v_mfma_f32_16x16x32_bf16 a[240:243], a[176:179], v[200:203], a[240:243]\n"
"v_mfma_f32_16x16x32_bf16 a[212:215], a[180:183], v[192:195], a[212:215]\n"
"v_mfma_f32_16x16x32_bf16 a[244:247], a[180:183], v[200:203], a[244:247]\n"
"v_mfma_f32_16x16x32_bf16 a[216:219], a[184:187], v[192:195], a[216:219]\n"
"v_mfma_f32_16x16x32_bf16 a[248:251], a[184:187], v[200:203], a[248:251]\n"
"v_mfma_f32_16x16x32_bf16 a[220:223], a[188:191], v[192:195], a[220:223]\n"
"v_mfma_f32_16x16x32_bf16 a[252:255], a[188:191], v[200:203], a[252:255]\n"
"v_mfma_f32_16x16x32_bf16 a[64:67], a[160:163], v[208:211], a[64:67]\n"
"v_mfma_f32_16x16x32_bf16 a[96:99], a[160:163], v[216:219], a[96:99]\n"
"v_mfma_f32_16x16x32_bf16 a[68:71], a[164:167], v[208:211], a[68:71]\n"
"v_mfma_f32_16x16x32_bf16 a[100:103], a[164:167], v[216:219], a[100:103]\n"
"v_mfma_f32_16x16x32_bf16 a[72:75], a[168:171], v[208:211], a[72:75]\n"
"ds_write_b128 v4, a[192:195] offset:0\n"
"v_mfma_f32_16x16x32_bf16 a[104:107], a[168:171], v[216:219], a[104:107]\n"
"v_mfma_f32_16x16x32_bf16 a[76:79], a[172:175], v[208:211], a[76:79]\n"
"ds_write_b128 v3, a[224:227] offset:32768\n"
"v_mfma_f32_16x16x32_bf16 a[108:111], a[172:175], v[216:219], a[108:111]\n"
"v_mfma_f32_16x16x32_bf16 a[80:83], a[176:179], v[208:211], a[80:83]\n"
"ds_write_b128 v4, a[196:199] offset:1024\n"
"v_mfma_f32_16x16x32_bf16 a[112:115], a[176:179], v[216:219], a[112:115]\n"
"v_mfma_f32_16x16x32_bf16 a[84:87], a[180:183], v[208:211], a[84:87]\n"
"ds_write_b128 v3, a[228:231] offset:33792\n"
"v_mfma_f32_16x16x32_bf16 a[116:119], a[180:183], v[216:219], a[116:119]\n"
"v_mfma_f32_16x16x32_bf16 a[88:91], a[184:187], v[208:211], a[88:91]\n"
"ds_write_b128 v4, a[200:203] offset:2048\n"
"v_mfma_f32_16x16x32_bf16 a[120:123], a[184:187], v[216:219], a[120:123]\n"
"v_mfma_f32_16x16x32_bf16 a[92:95], a[188:191], v[208:211], a[92:95]\n"
"ds_write_b128 v3, a[232:235] offset:34816\n"
"v_mfma_f32_16x16x32_bf16 a[124:127], a[188:191], v[216:219], a[124:127]\n"
"v_mfma_f32_16x16x32_bf16 v[152:155], v[128:131], v[192:195], v[152:155]\n"
"v_mfma_f32_16x16x32_bf16 v[160:163], v[128:131], v[200:203], v[160:163]\n"
"ds_write_b128 v4, a[204:207] offset:3072\n"
"ds_write_b128 v3, a[236:239] offset:35840\n"
"ds_write_b128 v4, a[208:211] offset:4096\n"
"ds_write_b128 v3, a[240:243] offset:36864\n"
"ds_write_b128 v4, a[212:215] offset:5120\n"
"ds_write_b128 v3, a[244:247] offset:37888\n"
"ds_write_b128 v4, a[216:219] offset:6144\n"
"ds_write_b128 v3, a[248:251] offset:38912\n"
"ds_write_b128 v4, a[220:223] offset:7168\n"
"ds_write_b128 v3, a[252:255] offset:39936\n"
"L_fin_%=:\n"
"v_mov_b32 v16, v152\n"
"v_mov_b32 v17, v160\n"
"v_mov_b32 v24, v14\n"
"v_mov_b32 v25, v15\n"
"s_nop 0\n"
"v_permlane16_swap_b32 v14, v24\n"
"v_permlane16_swap_b32 v15, v25\n"
"v_add_f32 v14, v14, v24\n"
"v_add_f32 v15, v15, v25\n"
"v_mov_b32 v24, v14\n"
"v_mov_b32 v25, v15\n"
"s_nop 0\n"
"v_permlane32_swap_b32 v14, v24\n"
"v_permlane32_swap_b32 v15, v25\n"
"v_add_f32 v14, v14, v24\n"
"v_add_f32 v15, v15, v25\n"
"s_waitcnt lgkmcnt(15)\n"
"ds_write_b64 v2, v[16:17]\n"
"s_lshl_b32 s75, s65, 1\n"
"s_sub_u32 s74, 124, s75\n"
"s_cmp_lt_u32 s64, 2\n"
"s_cselect_b32 s75, s75, s74\n"
"s_add_u32 s75, s75, s64\n"
"s_lshl_b32 s75, s75, 4\n"
"s_lshl_b32 s71, s75, 10\n"
"s_lshl_b32 s74, s3, 7\n"
"s_add_u32 s71, s71, s74\n"
"v_and_b32 v7, 15, v21\n"
"v_lshrrev_b32 v25, 4, v21\n"
"v_lshlrev_b32 v6, 3, v25\n"
"v_lshl_add_u32 v6, v7, 10, v6\n"
"v_lshlrev_b32 v26, 3, v7\n"
"buffer_load_dwordx2 v[140:141], v6, s[52:55], s71 offen offset:0\n"
"buffer_load_dwordx2 v[142:143], v6, s[52:55], s71 offen offset:32\n"
"buffer_load_dwordx2 v[156:157], v6, s[52:55], s71 offen offset:64\n"
"buffer_load_dwordx2 v[158:159], v6, s[52:55], s71 offen offset:96\n"
"v_lshlrev_b32 v5, 4, v25\n"
"v_lshrrev_b32 v26, 5, v21\n"
"v_lshlrev_b32 v26, 8, v26\n"
"v_lshl_add_u32 v26, v7, 4, v26\n"
"v_and_b32 v27, 8, v6\n"
"v_add_u32 v26, v26, v27\n"
"s_lshl_b32 s74, s75, 10\n"
"s_lshl_b32 s73, s3, 11\n"
"s_add_u32 s74, s74, s73\n"
"s_add_u32 s74, s74, 0x100000\n"
"buffer_load_dwordx2 v[236:237], v26, s[48:51], s74 offen offset:0 sc1\n"
"buffer_load_dwordx2 v[252:253], v26, s[48:51], s74 offen offset:512 sc1\n"
"buffer_load_dwordx2 v[18:19], v26, s[48:51], s74 offen offset:1024 sc1\n"
"buffer_load_dwordx2 v[12:13], v26, s[48:51], s74 offen offset:1536 sc1\n"
"v_xor_b32 v8, 0x2000, v4\n"
"v_xor_b32 v9, 0x2000, v3\n"
"v_xor_b32 v10, 0x2000, v2\n"
"s_waitcnt vmcnt(8) lgkmcnt(0)\n"
"s_barrier\n"
"ds_read_b64 v[16:17], v10\n"
"ds_read_b128 v[64:67], v8 offset:0\n"
"ds_read_b128 v[72:75], v8 offset:1024\n"
"ds_read_b128 v[80:83], v8 offset:2048\n"
"ds_read_b128 v[88:91], v8 offset:3072\n"
"ds_read_b128 v[96:99], v8 offset:4096\n"
"ds_read_b128 v[104:107], v8 offset:5120\n"
"ds_read_b128 v[112:115], v8 offset:6144\n"
"ds_read_b128 v[120:123], v8 offset:7168\n"
"ds_read_b128 v[128:131], v9 offset:32768\n"
"ds_read_b128 v[152:155], v9 offset:33792\n"
"ds_read_b128 v[160:163], v9 offset:34816\n"
"ds_read_b128 v[172:175], v9 offset:35840\n"
"ds_read_b128 v[224:227], v9 offset:36864\n"
"ds_read_b128 v[232:235], v9 offset:37888\n"
"ds_read_b128 v[240:243], v9 offset:38912\n"
"ds_read_b128 v[248:251], v9 offset:39936\n"
"s_waitcnt lgkmcnt(14)\n"
"v_add_f32 v14, v14, v16\n"
"v_add_f32 v15, v15, v17\n"
"v_add_f32 v24, v14, v15\n"
"v_mov_b32 v25, 0x5f800000\n"
"v_cmp_lt_f32 vcc, v24, v25\n"
"s_nop 0\n"
"s_cmp_eq_u64 vcc, -1\n"
"s_cselect_b32 s73, s35, 1\n"
"s_cmp_eq_u32 s73, 0\n"
"s_cbranch_scc0 L_slow%=\n"
"LT_slowret%=:\n"
"v_rcp_f32 v24, v14\n"
"v_rcp_f32 v26, v15\n"
"v_mov_b32 v25, v24\n"
"v_mov_b32 v27, v26\n"
"s_and_b32 s34, s65, 30\n"
"s_lshl_b32 s34, s34, 8\n"
"s_and_b32 s73, s65, 1\n"
"s_lshl_b32 s73, s73, 7\n"
"s_add_u32 s34, s34, s73\n"
"s_add_u32 s34, s34, 0x92000\n"
"v_and_b32 v11, 16, v21\n"
"v_lshlrev_b32 v11, 4, v11\n"
"v_lshl_add_u32 v11, v7, 3, v11\n"
"buffer_load_dwordx2 v[22:23], v11, s[48:51], s34 offen sc1\n"
"s_waitcnt lgkmcnt(8)\n"
"s_sub_u32 s73, s80, 0x100000\n"
"s_lshl_b32 s73, s73, 3\n"
"s_lshl_b32 s74, s65, 12\n"
"s_add_u32 s73, s73, s74\n"
"s_add_u32 s73, s73, 0x2400000\n"
"v_accvgpr_read_b32 v28, a64\n"
"v_accvgpr_read_b32 v29, a65\n"
"v_accvgpr_read_b32 v30, a66\n"
"v_accvgpr_read_b32 v31, a67\n"
"v_pk_add_f32 v[28:29], v[28:29], v[64:65]\n"
"v_pk_add_f32 v[30:31], v[30:31], v[66:67]\n"
"v_pk_mul_f32 v[28:29], v[28:29], v[24:25]\n"
"v_pk_mul_f32 v[30:31], v[30:31], v[24:25]\n"
"v_cvt_pk_bf16_f32 v192, v28, v29\n"
"v_cvt_pk_bf16_f32 v193, v30, v31\n"
"v_accvgpr_read_b32 v28, a68\n"
"v_accvgpr_read_b32 v29, a69\n"
"v_accvgpr_read_b32 v30, a70\n"
"v_accvgpr_read_b32 v31, a71\n"
"v_pk_add_f32 v[28:29], v[28:29], v[72:73]\n"
"v_pk_add_f32 v[30:31], v[30:31], v[74:75]\n"
"v_pk_mul_f32 v[28:29], v[28:29], v[24:25]\n"
"v_pk_mul_f32 v[30:31], v[30:31], v[24:25]\n"
"v_cvt_pk_bf16_f32 v194, v28, v29\n"
"v_cvt_pk_bf16_f32 v195, v30, v31\n"
"buffer_store_dwordx4 v[192:195], v1, s[48:51], s73 offen offset:0 sc1\n"
"v_accvgpr_read_b32 v28, a72\n"
"v_accvgpr_read_b32 v29, a73\n"
"v_accvgpr_read_b32 v30, a74\n"
"v_accvgpr_read_b32 v31, a75\n"
"v_pk_add_f32 v[28:29], v[28:29], v[80:81]\n"
"v_pk_add_f32 v[30:31], v[30:31], v[82:83]\n"
"v_pk_mul_f32 v[28:29], v[28:29], v[24:25]\n"
"v_pk_mul_f32 v[30:31], v[30:31], v[24:25]\n"
"v_cvt_pk_bf16_f32 v200, v28, v29\n"
"v_cvt_pk_bf16_f32 v201, v30, v31\n"
"v_accvgpr_read_b32 v28, a76\n"
"v_accvgpr_read_b32 v29, a77\n"
"v_accvgpr_read_b32 v30, a78\n"
"v_accvgpr_read_b32 v31, a79\n"
"v_pk_add_f32 v[28:29], v[28:29], v[88:89]\n"
"v_pk_add_f32 v[30:31], v[30:31], v[90:91]\n"
"v_pk_mul_f32 v[28:29], v[28:29], v[24:25]\n"
"v_pk_mul_f32 v[30:31], v[30:31], v[24:25]\n"
"v_cvt_pk_bf16_f32 v202, v28, v29\n"
"v_cvt_pk_bf16_f32 v203, v30, v31\n"
"buffer_store_dwordx4 v[200:203], v1, s[48:51], s73 offen offset:1024 sc1\n"
"v_accvgpr_read_b32 v28, a80\n"
"v_accvgpr_read_b32 v29, a81\n"
"v_accvgpr_read_b32 v30, a82\n"
"v_accvgpr_read_b32 v31, a83\n"
"v_pk_add_f32 v[28:29], v[28:29], v[96:97]\n"
"v_pk_add_f32 v[30:31], v[30:31], v[98:99]\n"
"v_pk_mul_f32 v[28:29], v[28:29], v[24:25]\n"
"v_pk_mul_f32 v[30:31], v[30:31], v[24:25]\n"
"v_cvt_pk_bf16_f32 v208, v28, v29\n"
"v_cvt_pk_bf16_f32 v209, v30, v31\n"
"v_accvgpr_read_b32 v28, a84\n"
"v_accvgpr_read_b32 v29, a85\n"
"v_accvgpr_read_b32 v30, a86\n"
"v_accvgpr_read_b32 v31, a87\n"
"v_pk_add_f32 v[28:29], v[28:29], v[104:105]\n"
"v_pk_add_f32 v[30:31], v[30:31], v[106:107]\n"
"v_pk_mul_f32 v[28:29], v[28:29], v[24:25]\n"
"v_pk_mul_f32 v[30:31], v[30:31], v[24:25]\n"
"v_cvt_pk_bf16_f32 v210, v28, v29\n"
"v_cvt_pk_bf16_f32 v211, v30, v31\n"
"buffer_store_dwordx4 v[208:211], v1, s[48:51], s73 offen offset:2048 sc1\n"
"v_accvgpr_read_b32 v28, a88\n"
"v_accvgpr_read_b32 v29, a89\n"
"v_accvgpr_read_b32 v30, a90\n"
"v_accvgpr_read_b32 v31, a91\n"
"v_pk_add_f32 v[28:29], v[28:29], v[112:113]\n"
"v_pk_add_f32 v[30:31], v[30:31], v[114:115]\n"
"v_pk_mul_f32 v[28:29], v[28:29], v[24:25]\n"
"v_pk_mul_f32 v[30:31], v[30:31], v[24:25]\n"
"v_cvt_pk_bf16_f32 v216, v28, v29\n"
"v_cvt_pk_bf16_f32 v217, v30, v31\n"
"v_accvgpr_read_b32 v28, a92\n"
"v_accvgpr_read_b32 v29, a93\n"
"v_accvgpr_read_b32 v30, a94\n"
"v_accvgpr_read_b32 v31, a95\n"
"v_pk_add_f32 v[28:29], v[28:29], v[120:121]\n"
"v_pk_add_f32 v[30:31], v[30:31], v[122:123]\n"
"v_pk_mul_f32 v[28:29], v[28:29], v[24:25]\n"
"v_pk_mul_f32 v[30:31], v[30:31], v[24:25]\n"
"v_cvt_pk_bf16_f32 v218, v28, v29\n"
"v_cvt_pk_bf16_f32 v219, v30, v31\n"
"buffer_store_dwordx4 v[216:219], v1, s[48:51], s73 offen offset:3072 sc1\n"
"s_waitcnt lgkmcnt(0)\n"
"s_barrier\n"
"s_sub_u32 s73, s81, 0x100000\n"
"s_lshl_b32 s73, s73, 3\n"
"s_lshl_b32 s74, s65, 12\n"
"s_add_u32 s73, s73, s74\n"
"s_add_u32 s73, s73, 0x2400000\n"
"v_accvgpr_read_b32 v28, a96\n"
"v_accvgpr_read_b32 v29, a97\n"
"v_accvgpr_read_b32 v30, a98\n"
"v_accvgpr_read_b32 v31, a99\n"
"v_pk_add_f32 v[28:29], v[28:29], v[128:129]\n"
"v_pk_add_f32 v[30:31], v[30:31], v[130:131]\n"
"v_pk_mul_f32 v[28:29], v[28:29], v[26:27]\n"
"v_pk_mul_f32 v[30:31], v[30:31], v[26:27]\n"
"v_cvt_pk_bf16_f32 v192, v28, v29\n"
"v_cvt_pk_bf16_f32 v193, v30, v31\n"
"v_accvgpr_read_b32 v28, a100\n"
"v_accvgpr_read_b32 v29, a101\n"
"v_accvgpr_read_b32 v30, a102\n"
"v_accvgpr_read_b32 v31, a103\n"
"v_pk_add_f32 v[28:29], v[28:29], v[152:153]\n"
"v_pk_add_f32 v[30:31], v[30:31], v[154:155]\n"
"v_pk_mul_f32 v[28:29], v[28:29], v[26:27]\n"
"v_pk_mul_f32 v[30:31], v[30:31], v[26:27]\n"
"v_cvt_pk_bf16_f32 v194, v28, v29\n"
"v_cvt_pk_bf16_f32 v195, v30, v31\n"
"buffer_store_dwordx4 v[192:195], v1, s[48:51], s73 offen offset:0 sc1\n"
"v_accvgpr_read_b32 v28, a104\n"
"v_accvgpr_read_b32 v29, a105\n"
"v_accvgpr_read_b32 v30, a106\n"
"v_accvgpr_read_b32 v31, a107\n"
"v_pk_add_f32 v[28:29], v[28:29], v[160:161]\n"
"v_pk_add_f32 v[30:31], v[30:31], v[162:163]\n"
"v_pk_mul_f32 v[28:29], v[28:29], v[26:27]\n"
"v_pk_mul_f32 v[30:31], v[30:31], v[26:27]\n"
"v_cvt_pk_bf16_f32 v200, v28, v29\n"
"v_cvt_pk_bf16_f32 v201, v30, v31\n"
"v_accvgpr_read_b32 v28, a108\n"
"v_accvgpr_read_b32 v29, a109\n"
"v_accvgpr_read_b32 v30, a110\n"
"v_accvgpr_read_b32 v31, a111\n"
"v_pk_add_f32 v[28:29], v[28:29], v[172:173]\n"
"v_pk_add_f32 v[30:31], v[30:31], v[174:175]\n"
"v_pk_mul_f32 v[28:29], v[28:29], v[26:27]\n"
"v_pk_mul_f32 v[30:31], v[30:31], v[26:27]\n"
"v_cvt_pk_bf16_f32 v202, v28, v29\n"
"v_cvt_pk_bf16_f32 v203, v30, v31\n"
"buffer_store_dwordx4 v[200:203], v1, s[48:51], s73 offen offset:1024 sc1\n"
"v_accvgpr_read_b32 v28, a112\n"
"v_accvgpr_read_b32 v29, a113\n"
"v_accvgpr_read_b32 v30, a114\n"
"v_accvgpr_read_b32 v31, a115\n"
"v_pk_add_f32 v[28:29], v[28:29], v[224:225]\n"
"v_pk_add_f32 v[30:31], v[30:31], v[226:227]\n"
"v_pk_mul_f32 v[28:29], v[28:29], v[26:27]\n"
"v_pk_mul_f32 v[30:31], v[30:31], v[26:27]\n"
"v_cvt_pk_bf16_f32 v208, v28, v29\n"
"v_cvt_pk_bf16_f32 v209, v30, v31\n"
"v_accvgpr_read_b32 v28, a116\n"
"v_accvgpr_read_b32 v29, a117\n"
"v_accvgpr_read_b32 v30, a118\n"
"v_accvgpr_read_b32 v31, a119\n"
"v_pk_add_f32 v[28:29], v[28:29], v[232:233]\n"
"v_pk_add_f32 v[30:31], v[30:31], v[234:235]\n"
"v_pk_mul_f32 v[28:29], v[28:29], v[26:27]\n"
"v_pk_mul_f32 v[30:31], v[30:31], v[26:27]\n"
"v_cvt_pk_bf16_f32 v210, v28, v29\n"
"v_cvt_pk_bf16_f32 v211, v30, v31\n"
"buffer_store_dwordx4 v[208:211], v1, s[48:51], s73 offen offset:2048 sc1\n"
"v_accvgpr_read_b32 v28, a120\n"
"v_accvgpr_read_b32 v29, a121\n"
"v_accvgpr_read_b32 v30, a122\n"
"v_accvgpr_read_b32 v31, a123\n"
"v_pk_add_f32 v[28:29], v[28:29], v[240:241]\n"
"v_pk_add_f32 v[30:31], v[30:31], v[242:243]\n"
"v_pk_mul_f32 v[28:29], v[28:29], v[26:27]\n"
"v_pk_mul_f32 v[30:31], v[30:31], v[26:27]\n"
"v_cvt_pk_bf16_f32 v216, v28, v29\n"
"v_cvt_pk_bf16_f32 v217, v30, v31\n"
"v_accvgpr_read_b32 v28, a124\n"
"v_accvgpr_read_b32 v29, a125\n"
"v_accvgpr_read_b32 v30, a126\n"
"v_accvgpr_read_b32 v31, a127\n"
"v_pk_add_f32 v[28:29], v[28:29], v[248:249]\n"
"v_pk_add_f32 v[30:31], v[30:31], v[250:251]\n"
"v_pk_mul_f32 v[28:29], v[28:29], v[26:27]\n"
"v_pk_mul_f32 v[30:31], v[30:31], v[26:27]\n"
"v_cvt_pk_bf16_f32 v218, v28, v29\n"
"v_cvt_pk_bf16_f32 v219, v30, v31\n"
"buffer_store_dwordx4 v[216:219], v1, s[48:51], s73 offen offset:3072 sc1\n"
"s_and_b32 s73, s65, 1\n"
"s_lshl_b32 s73, s73, 2\n"
"s_add_u32 s73, s73, s64\n"
"s_lshl_b32 s73, s73, 14\n"
"s_add_u32 s73, s73, 0x2400000\n"
"s_xor_b32 s74, s65, 0\n"
"s_lshl_b32 s74, s74, 18\n"
"s_add_u32 s78, s73, s74\n"
"s_add_u32 s79, s78, 0x20000\n"
"s_sub_u32 s60, s73, s74\n"
"s_add_u32 s60, s60, 0xfc0000\n"
"s_add_u32 s61, s60, 0x20000\n"
"s_xor_b32 s74, s65, 1\n"
"s_lshl_b32 s74, s74, 18\n"
"s_add_u32 s80, s73, s74\n"
"s_add_u32 s81, s80, 0x20000\n"
"s_sub_u32 s62, s73, s74\n"
"s_add_u32 s62, s62, 0xfc0000\n"
"s_add_u32 s63, s62, 0x20000\n"
"s_lshl_b32 s73, s3, 8\n"
"s_add_u32 s73, s73, 0x90000\n"
"buffer_load_dwordx4 v[224:227], v5, s[48:51], s73 offen offset:0 sc1\n"
"buffer_load_dwordx4 v[232:235], v5, s[48:51], s73 offen offset:64 sc1\n"
"buffer_load_dwordx4 v[240:243], v5, s[48:51], s73 offen offset:128 sc1\n"
"buffer_load_dwordx4 v[248:251], v5, s[48:51], s73 offen offset:192 sc1\n"
"s_waitcnt vmcnt(12)\n"
"v_cmp_eq_u64 vcc, s[68:69], v[22:23]\n"
"s_nop 0\n"
"s_cmp_eq_u64 vcc, -1\n"
"s_cbranch_scc0 L_cl_spin%=\n"
"LT_cl_ok%=:\n"
"s_add_u32 s89, s78, 0x3000\n"
"buffer_load_dwordx4 v[64:67], v1, s[48:51], s89 offen offset:2048 sc1\n"
"s_add_u32 s89, s79, 0x3000\n"
"buffer_load_dwordx4 v[72:75], v1, s[48:51], s89 offen offset:2048 sc1\n"
"s_add_u32 s89, s80, 0x3000\n"
"buffer_load_dwordx4 v[80:83], v1, s[48:51], s89 offen offset:2048 sc1\n"
"s_add_u32 s89, s81, 0x3000\n"
"buffer_load_dwordx4 v[88:91], v1, s[48:51], s89 offen offset:2048 sc1\n"
"s_add_u32 s89, s78, 0x3000\n"
"buffer_load_dwordx4 v[96:99], v1, s[48:51], s89 offen offset:3072 sc1\n"
"s_add_u32 s89, s79, 0x3000\n"
"buffer_load_dwordx4 v[104:107], v1, s[48:51], s89 offen offset:3072 sc1\n"
"s_add_u32 s89, s80, 0x3000\n"
"buffer_load_dwordx4 v[112:115], v1, s[48:51], s89 offen offset:3072 sc1\n"
"s_add_u32 s89, s81, 0x3000\n"
"buffer_load_dwordx4 v[120:123], v1, s[48:51], s89 offen offset:3072 sc1\n"
"s_add_u32 s89, s78, 0x2000\n"
"buffer_load_dwordx4 a[192:195], v1, s[48:51], s89 offen offset:2048 sc1\n"
"s_add_u32 s89, s79, 0x2000\n"
"buffer_load_dwordx4 a[196:199], v1, s[48:51], s89 offen offset:2048 sc1\n"
"s_add_u32 s89, s80, 0x2000\n"
"buffer_load_dwordx4 a[200:203], v1, s[48:51], s89 offen offset:2048 sc1\n"
"s_add_u32 s89, s81, 0x2000\n"
"buffer_load_dwordx4 a[204:207], v1, s[48:51], s89 offen offset:2048 sc1\n"
"v_cmp_eq_u32 vcc, 0, v21\n"
"v_mov_b32 v27, -1\n"
"s_lshl_b32 s73, s66, 2\n"
"s_add_u32 s73, s73, s64\n"
"v_cndmask_b32 v27, v27, 0, vcc\n"
"s_lshl_b32 s73, s73, 8\n"
"s_lshl_b32 s74, s65, 3\n"
"s_add_u32 s73, s73, s74\n"
"s_add_u32 s73, s73, 0x94000\n"
"v_mov_b32 v30, s68\n"
"v_mov_b32 v31, s69\n"
"s_waitcnt vmcnt(16)\n"
"buffer_store_dwordx2 v[30:31], v27, s[48:51], s73 offen sc1\n"
"s_cmp_eq_u32 s66, 7\n"
"s_cbranch_scc1 L_late7_%=\n"
"LT_late7r_%=:\n"
"s_mov_b32 s56, s28\n"
"s_and_b32 s57, s29, 0xffff\n"
"s_mov_b32 s58, 0x200000\n"
"s_mov_b32 s59, 0x20000\n"
"s_add_u32 s34, s34, 0x2000\n"
"s_lshl_b32 s73, s64, 11\n"
"s_sub_u32 s73, s73, s67\n"
"s_add_u32 s74, s73, s97\n"
"v_add_u32 v25, s74, v1\n"
"s_add_u32 s74, s73, s76\n"
"v_add_u32 v26, s74, v1\n"
"s_cmp_lt_u32 s64, 2\n"
"s_cselect_b32 s73, s95, s76\n"
"s_sub_u32 s73, s73, s67\n"
"s_and_b32 s74, s64, 1\n"
"s_lshl_b32 s74, s74, 12\n"
"s_add_u32 s73, s73, s74\n"
"s_add_u32 s73, s73, 0x8000\n"
"v_add_u32 v27, s73, v1\n"
"s_xor_b32 s73, s32, 8\n"
"s_lshl_b32 s52, s73, 14\n"
"s_lshl_b32 s74, s64, 11\n"
"s_add_u32 s52, s52, s74\n"
"s_add_u32 s52, s52, 0x3800000\n"
"s_lshl_b32 s54, s73, 6\n"
"s_lshl_b32 s74, s64, 3\n"
"s_add_u32 s54, s54, s74\n"
"s_add_u32 s54, s54, 0x96000\n"
"s_lshl_b32 s53, s32, 14\n"
"s_lshl_b32 s74, s64, 12\n"
"s_add_u32 s53, s53, s74\n"
"s_add_u32 s53, s53, 0x3800000\n"
"s_lshl_b32 s55, s32, 6\n"
"s_add_u32 s55, s55, 0x96000\n"
"v_cmp_eq_u32 vcc, 0, v21\n"
"v_mov_b32 v29, -1\n"
"v_mov_b32 v30, s68\n"
"v_cndmask_b32 v29, v29, 0, vcc\n"
"v_mov_b32 v31, s69\n"
"s_add_u32 s89, s78, 0x2000\n"
"buffer_load_dwordx4 a[208:211], v1, s[48:51], s89 offen offset:3072 sc1\n"
"s_add_u32 s89, s79, 0x2000\n"
"buffer_load_dwordx4 a[212:215], v1, s[48:51], s89 offen offset:3072 sc1\n"
"s_add_u32 s89, s80, 0x2000\n"
"buffer_load_dwordx4 a[216:219], v1, s[48:51], s89 offen offset:3072 sc1\n"
"s_add_u32 s89, s81, 0x2000\n"
"buffer_load_dwordx4 a[220:223], v1, s[48:51], s89 offen offset:3072 sc1\n"
"s_add_u32 s89, s78, 0x3000\n"
"buffer_load_dwordx4 v[192:195], v1, s[48:51], s89 offen offset:0 sc1\n"
"s_add_u32 s89, s79, 0x3000\n"
"buffer_load_dwordx4 v[200:203], v1, s[48:51], s89 offen offset:0 sc1\n"
"s_add_u32 s89, s80, 0x3000\n"
"buffer_load_dwordx4 v[208:211], v1, s[48:51], s89 offen offset:0 sc1\n"
"s_add_u32 s89, s81, 0x3000\n"
"buffer_load_dwordx4 v[216:219], v1, s[48:51], s89 offen offset:0 sc1\n"
"s_waitcnt vmcnt(17)\n"
"v_mfma_f32_16x16x32_bf16 a[64:67], v[36:39], v[64:67], 0\n"
"v_mov_b32 v28, s83\n"
"v_mul_f32 v28, 0xbfb8aa3b, v28\n"
"v_mfma_f32_16x16x32_bf16 a[80:83], v[36:39], v[72:75], 0\n"
"v_exp_f32 v28, v28\n"
"v_mfma_f32_16x16x32_bf16 a[96:99], v[36:39], v[80:83], 0\n"
"v_add_f32 v28, 1.0, v28\n"
"v_mfma_f32_16x16x32_bf16 a[112:115], v[36:39], v[88:91], 0\n"
"v_rcp_f32 v28, v28\n"
"v_mfma_f32_16x16x32_bf16 a[116:119], v[44:47], v[88:91], 0\n"
"v_cvt_pk_bf16_f32 v28, v28, v28\n"
"v_lshlrev_b32 v28, 16, v28\n"
"v_mfma_f32_16x16x32_bf16 a[100:103], v[44:47], v[80:83], 0\n"
"v_add_u32 v24, s75, v7\n"
"v_cmp_eq_u32 s[84:85], s82, v24\n"
"v_mfma_f32_16x16x32_bf16 a[84:87], v[44:47], v[72:75], 0\n"
"v_and_b32 v239, 0xffff0000, v237\n"
"v_lshlrev_b32 v238, 16, v237\n"
"v_mfma_f32_16x16x32_bf16 a[68:71], v[44:47], v[64:67], 0\n"
"v_and_b32 v237, 0xffff0000, v236\n"
"v_lshlrev_b32 v236, 16, v236\n"
"v_mfma_f32_16x16x32_bf16 a[72:75], v[52:55], v[64:67], 0\n"
"v_cvt_pk_bf16_f32 v224, v224, v225\n"
"v_cvt_pk_bf16_f32 v226, v226, v227\n"
"v_mfma_f32_16x16x32_bf16 a[88:91], v[52:55], v[72:75], 0\n"
"v_and_b32 v225, 0xffff0000, v224\n"
"v_lshlrev_b32 v224, 16, v224\n"
"v_mfma_f32_16x16x32_bf16 a[104:107], v[52:55], v[80:83], 0\n"
"v_and_b32 v227, 0xffff0000, v226\n"
"v_lshlrev_b32 v226, 16, v226\n"
"v_mfma_f32_16x16x32_bf16 a[120:123], v[52:55], v[88:91], 0\n"
"v_mul_f32 v224, v224, v28\n"
"v_mul_f32 v225, v225, v28\n"
"v_mfma_f32_16x16x32_bf16 a[124:127], v[60:63], v[88:91], 0\n"
"v_mul_f32 v226, v226, v28\n"
"v_mul_f32 v227, v227, v28\n"
"v_mfma_f32_16x16x32_bf16 a[108:111], v[60:63], v[80:83], 0\n"
"v_cvt_pk_bf16_f32 v224, v224, v225\n"
"v_cvt_pk_bf16_f32 v226, v226, v227\n"
"v_mfma_f32_16x16x32_bf16 a[92:95], v[60:63], v[72:75], 0\n"
"v_and_b32 v225, 0xffff0000, v224\n"
"v_lshlrev_b32 v224, 16, v224\n"
"v_mfma_f32_16x16x32_bf16 a[76:79], v[60:63], v[64:67], 0\n"
"v_and_b32 v227, 0xffff0000, v226\n"
"v_lshlrev_b32 v226, 16, v226\n"
"s_waitcnt vmcnt(13)\n"
"v_mfma_f32_16x16x32_bf16 a[64:67], v[76:79], v[96:99], a[64:67]\n"
"s_add_u32 s89, s78, 0x3000\n"
"v_cndmask_b32 v224, 0, v224, s[84:85]\n"
"v_mfma_f32_16x16x32_bf16 a[80:83], v[76:79], v[104:107], a[80:83]\n"
"buffer_load_dwordx4 v[64:67], v1, s[48:51], s89 offen offset:1024 sc1\n"
"v_mfma_f32_16x16x32_bf16 a[96:99], v[76:79], v[112:115], a[96:99]\n"
"v_cndmask_b32 v225, 0, v225, s[84:85]\n"
"v_cndmask_b32 v226, 0, v226, s[84:85]\n"
"v_mfma_f32_16x16x32_bf16 a[112:115], v[76:79], v[120:123], a[112:115]\n"
"s_add_u32 s89, s79, 0x3000\n"
"v_cndmask_b32 v227, 0, v227, s[84:85]\n"
"v_mfma_f32_16x16x32_bf16 a[116:119], v[92:95], v[120:123], a[116:119]\n"
"buffer_load_dwordx4 v[72:75], v1, s[48:51], s89 offen offset:1024 sc1\n"
"v_mfma_f32_16x16x32_bf16 a[100:103], v[92:95], v[112:115], a[100:103]\n"
"v_add_f32 v236, v236, v224\n"
"v_add_f32 v237, v237, v225\n"
"v_mfma_f32_16x16x32_bf16 a[84:87], v[92:95], v[104:107], a[84:87]\n"
"v_add_f32 v238, v238, v226\n"
"v_add_f32 v239, v239, v227\n"
"v_mfma_f32_16x16x32_bf16 a[68:71], v[92:95], v[96:99], a[68:71]\n"
"s_add_u32 s89, s80, 0x3000\n"
"v_cvt_pk_bf16_f32 v236, v236, v237\n"
"v_mfma_f32_16x16x32_bf16 a[72:75], v[108:111], v[96:99], a[72:75]\n"
"buffer_load_dwordx4 v[80:83], v1, s[48:51], s89 offen offset:1024 sc1\n"
"v_mfma_f32_16x16x32_bf16 a[88:91], v[108:111], v[104:107], a[88:91]\n"
"v_cvt_pk_bf16_f32 v238, v238, v239\n"
"v_and_b32 v237, 0xffff0000, v236\n"
"v_mfma_f32_16x16x32_bf16 a[104:107], v[108:111], v[112:115], a[104:107]\n"
"v_lshlrev_b32 v236, 16, v236\n"
"v_and_b32 v239, 0xffff0000, v238\n"
"v_mfma_f32_16x16x32_bf16 a[120:123], v[108:111], v[120:123], a[120:123]\n"
"s_add_u32 s89, s81, 0x3000\n"
"v_lshlrev_b32 v238, 16, v238\n"
"v_mfma_f32_16x16x32_bf16 a[124:127], v[124:127], v[120:123], a[124:127]\n"
"buffer_load_dwordx4 v[88:91], v1, s[48:51], s89 offen offset:1024 sc1\n"
"v_mfma_f32_16x16x32_bf16 a[108:111], v[124:127], v[112:115], a[108:111]\n"
"v_and_b32 v255, 0xffff0000, v253\n"
"v_lshlrev_b32 v254, 16, v253\n"
"v_mfma_f32_16x16x32_bf16 a[92:95], v[124:127], v[104:107], a[92:95]\n"
"v_and_b32 v253, 0xffff0000, v252\n"
"v_lshlrev_b32 v252, 16, v252\n"
"v_mfma_f32_16x16x32_bf16 a[76:79], v[124:127], v[96:99], a[76:79]\n"
"v_cvt_pk_bf16_f32 v232, v232, v233\n"
"v_cvt_pk_bf16_f32 v234, v234, v235\n"
"s_waitcnt vmcnt(13)\n"
"v_mfma_f32_16x16x32_bf16 a[64:67], v[136:139], a[192:195], a[64:67]\n"
"v_and_b32 v233, 0xffff0000, v232\n"
"v_lshlrev_b32 v232, 16, v232\n"
"v_mfma_f32_16x16x32_bf16 a[80:83], v[136:139], a[196:199], a[80:83]\n"
"buffer_load_dwordx4 v[96:99], v1, s[48:51], s78 offen offset:0 sc1\n"
"v_mfma_f32_16x16x32_bf16 a[96:99], v[136:139], a[200:203], a[96:99]\n"
"v_and_b32 v235, 0xffff0000, v234\n"
"v_lshlrev_b32 v234, 16, v234\n"
"v_mfma_f32_16x16x32_bf16 a[112:115], v[136:139], a[204:207], a[112:115]\n"
"v_mul_f32 v232, v232, v28\n"
"v_mul_f32 v233, v233, v28\n"
"v_mfma_f32_16x16x32_bf16 a[116:119], v[144:147], a[204:207], a[116:119]\n"
"buffer_load_dwordx4 v[104:107], v1, s[48:51], s79 offen offset:0 sc1\n"
"v_mfma_f32_16x16x32_bf16 a[100:103], v[144:147], a[200:203], a[100:103]\n"
"v_mul_f32 v234, v234, v28\n"
"v_mul_f32 v235, v235, v28\n"
"v_mfma_f32_16x16x32_bf16 a[84:87], v[144:147], a[196:199], a[84:87]\n"
"v_cvt_pk_bf16_f32 v232, v232, v233\n"
"v_cvt_pk_bf16_f32 v234, v234, v235\n"
"v_mfma_f32_16x16x32_bf16 a[68:71], v[144:147], a[192:195], a[68:71]\n"
"v_and_b32 v233, 0xffff0000, v232\n"
"v_lshlrev_b32 v232, 16, v232\n"
"v_mfma_f32_16x16x32_bf16 a[72:75], v[168:171], a[192:195], a[72:75]\n"
"buffer_load_dwordx4 v[112:115], v1, s[48:51], s80 offen offset:0 sc1\n"
"v_mfma_f32_16x16x32_bf16 a[88:91], v[168:171], a[196:199], a[88:91]\n"
"v_and_b32 v235, 0xffff0000, v234\n"
"v_lshlrev_b32 v234, 16, v234\n"
"v_mfma_f32_16x16x32_bf16 a[104:107], v[168:171], a[200:203], a[104:107]\n"
"v_cndmask_b32 v232, 0, v232, s[84:85]\n"
"v_cndmask_b32 v233, 0, v233, s[84:85]\n"
"v_mfma_f32_16x16x32_bf16 a[120:123], v[168:171], a[204:207], a[120:123]\n"
"v_cndmask_b32 v234, 0, v234, s[84:85]\n"
"v_cndmask_b32 v235, 0, v235, s[84:85]\n"
"v_mfma_f32_16x16x32_bf16 a[124:127], v[176:179], a[204:207], a[124:127]\n"
"buffer_load_dwordx4 v[120:123], v1, s[48:51], s81 offen offset:0 sc1\n"
"v_mfma_f32_16x16x32_bf16 a[108:111], v[176:179], a[200:203], a[108:111]\n"
"v_add_f32 v252, v252, v232\n"
"v_add_f32 v253, v253, v233\n"
"v_mfma_f32_16x16x32_bf16 a[92:95], v[176:179], a[196:199], a[92:95]\n"
"v_add_f32 v254, v254, v234\n"
"v_add_f32 v255, v255, v235\n"
"v_mfma_f32_16x16x32_bf16 a[76:79], v[176:179], a[192:195], a[76:79]\n"
"v_cvt_pk_bf16_f32 v252, v252, v253\n"
"v_cvt_pk_bf16_f32 v254, v254, v255\n"
"s_waitcnt vmcnt(12)\n"
"v_mfma_f32_16x16x32_bf16 a[64:67], v[184:187], a[208:211], a[64:67]\n"
"v_and_b32 v253, 0xffff0000, v252\n"
"v_lshlrev_b32 v252, 16, v252\n"
"v_mfma_f32_16x16x32_bf16 a[80:83], v[184:187], a[212:215], a[80:83]\n"
"buffer_load_dwordx4 a[192:195], v1, s[48:51], s78 offen offset:1024 sc1\n"
"v_mfma_f32_16x16x32_bf16 a[96:99], v[184:187], a[216:219], a[96:99]\n"
"v_and_b32 v255, 0xffff0000, v254\n"
"v_lshlrev_b32 v254, 16, v254\n"
"v_mfma_f32_16x16x32_bf16 a[112:115], v[184:187], a[220:223], a[112:115]\n"
"v_lshlrev_b32 v152, 16, v18\n"
"v_and_b32 v153, 0xffff0000, v18\n"
"v_mfma_f32_16x16x32_bf16 a[116:119], v[204:207], a[220:223], a[116:119]\n"
"buffer_load_dwordx4 a[196:199], v1, s[48:51], s79 offen offset:1024 sc1\n"
"v_mfma_f32_16x16x32_bf16 a[100:103], v[204:207], a[216:219], a[100:103]\n"
"v_lshlrev_b32 v154, 16, v19\n"
"v_and_b32 v155, 0xffff0000, v19\n"
"v_mfma_f32_16x16x32_bf16 a[84:87], v[204:207], a[212:215], a[84:87]\n"
"v_cvt_pk_bf16_f32 v240, v240, v241\n"
"v_cvt_pk_bf16_f32 v242, v242, v243\n"
"v_mfma_f32_16x16x32_bf16 a[68:71], v[204:207], a[208:211], a[68:71]\n"
"v_and_b32 v241, 0xffff0000, v240\n"
"v_lshlrev_b32 v240, 16, v240\n"
"v_mfma_f32_16x16x32_bf16 a[72:75], v[220:223], a[208:211], a[72:75]\n"
"buffer_load_dwordx4 a[200:203], v1, s[48:51], s80 offen offset:1024 sc1\n"
"v_mfma_f32_16x16x32_bf16 a[88:91], v[220:223], a[212:215], a[88:91]\n"
"v_and_b32 v243, 0xffff0000, v242\n"
"v_lshlrev_b32 v242, 16, v242\n"
"v_mfma_f32_16x16x32_bf16 a[104:107], v[220:223], a[216:219], a[104:107]\n"
"v_mul_f32 v240, v240, v28\n"
"v_mul_f32 v241, v241, v28\n"
"v_mfma_f32_16x16x32_bf16 a[120:123], v[220:223], a[220:223], a[120:123]\n"
"v_mul_f32 v242, v242, v28\n"
"v_mul_f32 v243, v243, v28\n"
"v_mfma_f32_16x16x32_bf16 a[124:127], v[188:191], a[220:223], a[124:127]\n"
"buffer_load_dwordx4 a[204:207], v1, s[48:51], s81 offen offset:1024 sc1\n"
"v_mfma_f32_16x16x32_bf16 a[108:111], v[188:191], a[216:219], a[108:111]\n"
"v_cvt_pk_bf16_f32 v240, v240, v241\n"
"v_cvt_pk_bf16_f32 v242, v242, v243\n"
"v_mfma_f32_16x16x32_bf16 a[92:95], v[188:191], a[212:215], a[92:95]\n"
"v_and_b32 v241, 0xffff0000, v240\n"
"v_lshlrev_b32 v240, 16, v240\n"
"v_mfma_f32_16x16x32_bf16 a[76:79], v[188:191], a[208:211], a[76:79]\n"
"v_and_b32 v243, 0xffff0000, v242\n"
"v_lshlrev_b32 v242, 16, v242\n"
"s_waitcnt vmcnt(12)\n"
"v_mfma_f32_16x16x32_bf16 a[64:67], v[32:35], v[192:195], a[64:67]\n"
"v_cndmask_b32 v240, 0, v240, s[84:85]\n"
"v_cndmask_b32 v241, 0, v241, s[84:85]\n"
"v_mfma_f32_16x16x32_bf16 a[80:83], v[32:35], v[200:203], a[80:83]\n"
"buffer_load_dwordx4 a[208:211], v1, s[48:51], s78 offen offset:2048 sc1\n"
"v_mfma_f32_16x16x32_bf16 a[96:99], v[32:35], v[208:211], a[96:99]\n"
"v_cndmask_b32 v242, 0, v242, s[84:85]\n"
"v_cndmask_b32 v243, 0, v243, s[84:85]\n"
"v_mfma_f32_16x16x32_bf16 a[112:115], v[32:35], v[216:219], a[112:115]\n"
"v_add_f32 v152, v152, v240\n"
"v_add_f32 v153, v153, v241\n"
"v_mfma_f32_16x16x32_bf16 a[116:119], v[68:71], v[216:219], a[116:119]\n"
"buffer_load_dwordx4 a[212:215], v1, s[48:51], s79 offen offset:2048 sc1\n"
"v_mfma_f32_16x16x32_bf16 a[100:103], v[68:71], v[208:211], a[100:103]\n"
"v_add_f32 v154, v154, v242\n"
"v_add_f32 v155, v155, v243\n"
"v_mfma_f32_16x16x32_bf16 a[84:87], v[68:71], v[200:203], a[84:87]\n"
"v_cvt_pk_bf16_f32 v152, v152, v153\n"
"v_cvt_pk_bf16_f32 v154, v154, v155\n"
"v_mfma_f32_16x16x32_bf16 a[68:71], v[68:71], v[192:195], a[68:71]\n"
"v_and_b32 v153, 0xffff0000, v152\n"
"v_lshlrev_b32 v152, 16, v152\n"
"v_mfma_f32_16x16x32_bf16 a[72:75], v[196:199], v[192:195], a[72:75]\n"
"buffer_load_dwordx4 a[216:219], v1, s[48:51], s80 offen offset:2048 sc1\n"
"v_mfma_f32_16x16x32_bf16 a[88:91], v[196:199], v[200:203], a[88:91]\n"
"v_and_b32 v155, 0xffff0000, v154\n"
"v_lshlrev_b32 v154, 16, v154\n"
"v_mfma_f32_16x16x32_bf16 a[104:107], v[196:199], v[208:211], a[104:107]\n"
"v_lshlrev_b32 v160, 16, v12\n"
"v_and_b32 v161, 0xffff0000, v12\n"
"v_mfma_f32_16x16x32_bf16 a[120:123], v[196:199], v[216:219], a[120:123]\n"
"v_lshlrev_b32 v162, 16, v13\n"
"v_and_b32 v163, 0xffff0000, v13\n"
"v_mfma_f32_16x16x32_bf16 a[124:127], v[132:135], v[216:219], a[124:127]\n"
"buffer_load_dwordx4 a[220:223], v1, s[48:51], s81 offen offset:2048 sc1\n"
"v_mfma_f32_16x16x32_bf16 a[108:111], v[132:135], v[208:211], a[108:111]\n"
"v_cvt_pk_bf16_f32 v248, v248, v249\n"
"v_cvt_pk_bf16_f32 v250, v250, v251\n"
"v_mfma_f32_16x16x32_bf16 a[92:95], v[132:135], v[200:203], a[92:95]\n"
"v_and_b32 v249, 0xffff0000, v248\n"
"v_lshlrev_b32 v248, 16, v248\n"
"v_mfma_f32_16x16x32_bf16 a[76:79], v[132:135], v[192:195], a[76:79]\n"
"v_and_b32 v251, 0xffff0000, v250\n"
"v_lshlrev_b32 v250, 16, v250\n"
"s_waitcnt vmcnt(12)\n"
"v_mfma_f32_16x16x32_bf16 a[64:67], v[40:43], v[64:67], a[64:67]\n"
"s_add_u32 s89, s78, 0x1000\n"
"v_mul_f32 v248, v248, v28\n"
"v_mfma_f32_16x16x32_bf16 a[80:83], v[40:43], v[72:75], a[80:83]\n"
"buffer_load_dwordx4 v[192:195], v1, s[48:51], s89 offen offset:2048 sc1\n"
"v_mfma_f32_16x16x32_bf16 a[96:99], v[40:43], v[80:83], a[96:99]\n"
"v_mul_f32 v249, v249, v28\n"
"v_mul_f32 v250, v250, v28\n"
"v_mfma_f32_16x16x32_bf16 a[112:115], v[40:43], v[88:91], a[112:115]\n"
"s_add_u32 s89, s79, 0x1000\n"
"v_mul_f32 v251, v251, v28\n"
"v_mfma_f32_16x16x32_bf16 a[116:119], v[84:87], v[88:91], a[116:119]\n"
"buffer_load_dwordx4 v[200:203], v1, s[48:51], s89 offen offset:2048 sc1\n"
"v_mfma_f32_16x16x32_bf16 a[100:103], v[84:87], v[80:83], a[100:103]\n"
"v_cvt_pk_bf16_f32 v248, v248, v249\n"
"v_cvt_pk_bf16_f32 v250, v250, v251\n"
"v_mfma_f32_16x16x32_bf16 a[84:87], v[84:87], v[72:75], a[84:87]\n"
"v_and_b32 v249, 0xffff0000, v248\n"
"v_lshlrev_b32 v248, 16, v248\n"
"v_mfma_f32_16x16x32_bf16 a[68:71], v[84:87], v[64:67], a[68:71]\n"
"s_add_u32 s89, s80, 0x1000\n"
"v_and_b32 v251, 0xffff0000, v250\n"
"v_mfma_f32_16x16x32_bf16 a[72:75], v[212:215], v[64:67], a[72:75]\n"
"buffer_load_dwordx4 v[208:211], v1, s[48:51], s89 offen offset:2048 sc1\n"
"v_mfma_f32_16x16x32_bf16 a[88:91], v[212:215], v[72:75], a[88:91]\n"
"v_lshlrev_b32 v250, 16, v250\n"
"v_cndmask_b32 v248, 0, v248, s[84:85]\n"
"v_mfma_f32_16x16x32_bf16 a[104:107], v[212:215], v[80:83], a[104:107]\n"
"v_cndmask_b32 v249, 0, v249, s[84:85]\n"
"v_cndmask_b32 v250, 0, v250, s[84:85]\n"
"v_mfma_f32_16x16x32_bf16 a[120:123], v[212:215], v[88:91], a[120:123]\n"
"s_add_u32 s89, s81, 0x1000\n"
"v_cndmask_b32 v251, 0, v251, s[84:85]\n"
"v_mfma_f32_16x16x32_bf16 a[124:127], v[148:151], v[88:91], a[124:127]\n"
"buffer_load_dwordx4 v[216:219], v1, s[48:51], s89 offen offset:2048 sc1\n"
"v_mfma_f32_16x16x32_bf16 a[108:111], v[148:151], v[80:83], a[108:111]\n"
"v_add_f32 v160, v160, v248\n"
"v_add_f32 v161, v161, v249\n"
"v_mfma_f32_16x16x32_bf16 a[92:95], v[148:151], v[72:75], a[92:95]\n"
"v_add_f32 v162, v162, v250\n"
"v_add_f32 v163, v163, v251\n"
"v_mfma_f32_16x16x32_bf16 a[76:79], v[148:151], v[64:67], a[76:79]\n"
"v_cvt_pk_bf16_f32 v160, v160, v161\n"
"v_cvt_pk_bf16_f32 v162, v162, v163\n"
"s_waitcnt vmcnt(12)\n"
"v_mfma_f32_16x16x32_bf16 a[64:67], a[128:131], v[96:99], a[64:67]\n"
"v_and_b32 v161, 0xffff0000, v160\n"
"v_lshlrev_b32 v160, 16, v160\n"
"v_mfma_f32_16x16x32_bf16 a[80:83], a[128:131], v[104:107], a[80:83]\n"
"buffer_load_dwordx4 v[64:67], v1, s[48:51], s78 offen offset:3072 sc1\n"
"v_mfma_f32_16x16x32_bf16 a[96:99], a[128:131], v[112:115], a[96:99]\n"
"buffer_load_dwordx4 v[72:75], v1, s[48:51], s79 offen offset:3072 sc1\n"
"v_mfma_f32_16x16x32_bf16 a[112:115], a[128:131], v[120:123], a[112:115]\n"
"v_and_b32 v163, 0xffff0000, v162\n"
"v_lshlrev_b32 v162, 16, v162\n"
"v_mfma_f32_16x16x32_bf16 a[116:119], a[132:135], v[120:123], a[116:119]\n"
"buffer_load_dwordx4 v[80:83], v1, s[48:51], s80 offen offset:3072 sc1\n"
"v_mfma_f32_16x16x32_bf16 a[100:103], a[132:135], v[112:115], a[100:103]\n"
"v_mul_f32 v128, 0xbfb8aa3b, v236\n"
"v_mul_f32 v129, 0xbfb8aa3b, v237\n"
"v_mfma_f32_16x16x32_bf16 a[84:87], a[132:135], v[104:107], a[84:87]\n"
"buffer_load_dwordx4 v[88:91], v1, s[48:51], s81 offen offset:3072 sc1\n"
"v_mfma_f32_16x16x32_bf16 a[68:71], a[132:135], v[96:99], a[68:71]\n"
"v_mul_f32 v130, 0xbfb8aa3b, v238\n"
"v_mul_f32 v131, 0xbfb8aa3b, v239\n"
"v_mfma_f32_16x16x32_bf16 a[72:75], a[136:139], v[96:99], a[72:75]\n"
"ds_read_b128 a[160:163], v2 offset:32768\n"
"v_mfma_f32_16x16x32_bf16 a[88:91], a[136:139], v[104:107], a[88:91]\n"
"v_exp_f32 v128, v128\n"
"v_mfma_f32_16x16x32_bf16 a[104:107], a[136:139], v[112:115], a[104:107]\n"
"ds_read_b128 a[164:167], v2 offset:33792\n"
"v_mfma_f32_16x16x32_bf16 a[120:123], a[136:139], v[120:123], a[120:123]\n"
"v_exp_f32 v129, v129\n"
"v_mfma_f32_16x16x32_bf16 a[124:127], a[140:143], v[120:123], a[124:127]\n"
"ds_read_b128 a[168:171], v2 offset:34816\n"
"v_mfma_f32_16x16x32_bf16 a[108:111], a[140:143], v[112:115], a[108:111]\n"
"v_exp_f32 v130, v130\n"
"v_mfma_f32_16x16x32_bf16 a[92:95], a[140:143], v[104:107], a[92:95]\n"
"ds_read_b128 a[172:175], v2 offset:35840\n"
"v_mfma_f32_16x16x32_bf16 a[76:79], a[140:143], v[96:99], a[76:79]\n"
"v_exp_f32 v131, v131\n"
"buffer_load_dwordx2 v[22:23], v11, s[48:51], s34 offen sc1\n"
"s_waitcnt vmcnt(13)\n"
"v_mfma_f32_16x16x32_bf16 a[64:67], a[144:147], a[192:195], a[64:67]\n"
"s_add_u32 s89, s78, 0x1000\n"
"v_add_f32 v128, 1.0, v128\n"
"v_mfma_f32_16x16x32_bf16 a[80:83], a[144:147], a[196:199], a[80:83]\n"
"buffer_load_dwordx4 v[96:99], v1, s[48:51], s89 offen offset:3072 sc1\n"
"v_mfma_f32_16x16x32_bf16 a[96:99], a[144:147], a[200:203], a[96:99]\n"
"v_add_f32 v129, 1.0, v129\n"
"v_add_f32 v130, 1.0, v130\n"
"v_mfma_f32_16x16x32_bf16 a[112:115], a[144:147], a[204:207], a[112:115]\n"
"s_add_u32 s89, s79, 0x1000\n"
"v_add_f32 v131, 1.0, v131\n"
"v_mfma_f32_16x16x32_bf16 a[116:119], a[148:151], a[204:207], a[116:119]\n"
"buffer_load_dwordx4 v[104:107], v1, s[48:51], s89 offen offset:3072 sc1\n"
"v_mfma_f32_16x16x32_bf16 a[100:103], a[148:151], a[200:203], a[100:103]\n"
"v_rcp_f32 v128, v128\n"
"v_mfma_f32_16x16x32_bf16 a[84:87], a[148:151], a[196:199], a[84:87]\n"
"v_rcp_f32 v129, v129\n"
"v_mfma_f32_16x16x32_bf16 a[68:71], a[148:151], a[192:195], a[68:71]\n"
"s_add_u32 s89, s80, 0x1000\n"
"v_mfma_f32_16x16x32_bf16 a[72:75], a[152:155], a[192:195], a[72:75]\n"
"buffer_load_dwordx4 v[112:115], v1, s[48:51], s89 offen offset:3072 sc1\n"
"v_mfma_f32_16x16x32_bf16 a[88:91], a[152:155], a[196:199], a[88:91]\n"
"v_rcp_f32 v130, v130\n"
"v_mfma_f32_16x16x32_bf16 a[104:107], a[152:155], a[200:203], a[104:107]\n"
"v_rcp_f32 v131, v131\n"
"v_mfma_f32_16x16x32_bf16 a[120:123], a[152:155], a[204:207], a[120:123]\n"
"s_add_u32 s89, s81, 0x1000\n"
"v_mul_f32 v236, v236, v128\n"
"v_mfma_f32_16x16x32_bf16 a[124:127], a[156:159], a[204:207], a[124:127]\n"
"buffer_load_dwordx4 v[120:123], v1, s[48:51], s89 offen offset:3072 sc1\n"
"v_mfma_f32_16x16x32_bf16 a[108:111], a[156:159], a[200:203], a[108:111]\n"
"v_mul_f32 v237, v237, v129\n"
"v_mul_f32 v238, v238, v130\n"
"v_mfma_f32_16x16x32_bf16 a[92:95], a[156:159], a[196:199], a[92:95]\n"
"v_mul_f32 v239, v239, v131\n"
"v_cvt_pk_bf16_f32 v236, v236, v237\n"
"v_mfma_f32_16x16x32_bf16 a[76:79], a[156:159], a[192:195], a[76:79]\n"
"v_cvt_pk_bf16_f32 v238, v238, v239\n"
"v_and_b32 v237, 0xffff0000, v236\n"
"s_waitcnt vmcnt(13) lgkmcnt(0)\n"
"v_mfma_f32_16x16x32_bf16 a[64:67], a[160:163], a[208:211], a[64:67]\n"
"s_add_u32 s89, s78, 0x1000\n"
"v_lshlrev_b32 v236, 16, v236\n"
"v_mfma_f32_16x16x32_bf16 a[80:83], a[160:163], a[212:215], a[80:83]\n"
"buffer_load_dwordx4 a[192:195], v1, s[48:51], s89 offen offset:0 sc1\n"
"s_add_u32 s89, s79, 0x1000\n"
"v_mfma_f32_16x16x32_bf16 a[96:99], a[160:163], a[216:219], a[96:99]\n"
"buffer_load_dwordx4 a[196:199], v1, s[48:51], s89 offen offset:0 sc1\n"
"v_mfma_f32_16x16x32_bf16 a[112:115], a[160:163], a[220:223], a[112:115]\n"
"s_add_u32 s89, s80, 0x1000\n"
"v_and_b32 v239, 0xffff0000, v238\n"
"v_mfma_f32_16x16x32_bf16 a[116:119], a[164:167], a[220:223], a[116:119]\n"
"buffer_load_dwordx4 a[200:203], v1, s[48:51], s89 offen offset:0 sc1\n"
"v_mfma_f32_16x16x32_bf16 a[100:103], a[164:167], a[216:219], a[100:103]\n"
"s_add_u32 s89, s81, 0x1000\n"
"v_lshlrev_b32 v238, 16, v238\n"
"v_mfma_f32_16x16x32_bf16 a[84:87], a[164:167], a[212:215], a[84:87]\n"
"buffer_load_dwordx4 a[204:207], v1, s[48:51], s89 offen offset:0 sc1\n"
"v_mfma_f32_16x16x32_bf16 a[68:71], a[164:167], a[208:211], a[68:71]\n"
"v_mul_f32 v128, 0xbfb8aa3b, v252\n"
"v_mul_f32 v129, 0xbfb8aa3b, v253\n"
"v_mfma_f32_16x16x32_bf16 a[72:75], a[168:171], a[208:211], a[72:75]\n"
"ds_read_b128 a[176:179], v2 offset:36864\n"
"v_mul_f32 v130, 0xbfb8aa3b, v254\n"
"v_mfma_f32_16x16x32_bf16 a[88:91], a[168:171], a[212:215], a[88:91]\n"
"v_mul_f32 v131, 0xbfb8aa3b, v255\n"
"v_mfma_f32_16x16x32_bf16 a[104:107], a[168:171], a[216:219], a[104:107]\n"
"ds_read_b128 a[180:183], v2 offset:37888\n"
"v_mfma_f32_16x16x32_bf16 a[120:123], a[168:171], a[220:223], a[120:123]\n"
"v_exp_f32 v128, v128\n"
"v_mfma_f32_16x16x32_bf16 a[124:127], a[172:175], a[220:223], a[124:127]\n"
"ds_read_b128 a[184:187], v2 offset:38912\n"
"v_mfma_f32_16x16x32_bf16 a[108:111], a[172:175], a[216:219], a[108:111]\n"
"v_exp_f32 v129, v129\n"
"v_mfma_f32_16x16x32_bf16 a[92:95], a[172:175], a[212:215], a[92:95]\n"
"ds_read_b128 a[188:191], v2 offset:39936\n"
"v_mfma_f32_16x16x32_bf16 a[76:79], a[172:175], a[208:211], a[76:79]\n"
"v_exp_f32 v130, v130\n"
"s_waitcnt vmcnt(13)\n"
"v_mfma_f32_16x16x32_bf16 a[64:67], a[0:3], v[192:195], a[64:67]\n"
"s_add_u32 s89, s78, 0x2000\n"
"v_mfma_f32_16x16x32_bf16 a[80:83], a[0:3], v[200:203], a[80:83]\n"
"buffer_load_dwordx4 a[208:211], v1, s[48:51], s89 offen offset:0 sc1\n"
"v_mfma_f32_16x16x32_bf16 a[96:99], a[0:3], v[208:211], a[96:99]\n"
"v_exp_f32 v131, v131\n"
"v_mfma_f32_16x16x32_bf16 a[112:115], a[0:3], v[216:219], a[112:115]\n"
"s_add_u32 s89, s79, 0x2000\n"
"v_add_f32 v128, 1.0, v128\n"
"v_mfma_f32_16x16x32_bf16 a[116:119], a[4:7], v[216:219], a[116:119]\n"
"buffer_load_dwordx4 a[212:215], v1, s[48:51], s89 offen offset:0 sc1\n"
"v_mfma_f32_16x16x32_bf16 a[100:103], a[4:7], v[208:211], a[100:103]\n"
"v_add_f32 v129, 1.0, v129\n"
"v_add_f32 v130, 1.0, v130\n"
"v_mfma_f32_16x16x32_bf16 a[84:87], a[4:7], v[200:203], a[84:87]\n"
"v_add_f32 v131, 1.0, v131\n"
"v_mfma_f32_16x16x32_bf16 a[68:71], a[4:7], v[192:195], a[68:71]\n"
"s_add_u32 s89, s80, 0x2000\n"
"v_mfma_f32_16x16x32_bf16 a[72:75], a[8:11], v[192:195], a[72:75]\n"
"buffer_load_dwordx4 a[216:219], v1, s[48:51], s89 offen offset:0 sc1\n"
"v_mfma_f32_16x16x32_bf16 a[88:91], a[8:11], v[200:203], a[88:91]\n"
"v_rcp_f32 v128, v128\n"
"v_mfma_f32_16x16x32_bf16 a[104:107], a[8:11], v[208:211], a[104:107]\n"
"v_rcp_f32 v129, v129\n"
"v_mfma_f32_16x16x32_bf16 a[120:123], a[8:11], v[216:219], a[120:123]\n"
"s_add_u32 s89, s81, 0x2000\n"
"v_mfma_f32_16x16x32_bf16 a[124:127], a[12:15], v[216:219], a[124:127]\n"
"buffer_load_dwordx4 a[220:223], v1, s[48:51], s89 offen offset:0 sc1\n"
"v_mfma_f32_16x16x32_bf16 a[108:111], a[12:15], v[208:211], a[108:111]\n"
"v_rcp_f32 v130, v130\n"
"v_mfma_f32_16x16x32_bf16 a[92:95], a[12:15], v[200:203], a[92:95]\n"
"v_rcp_f32 v131, v131\n"
"v_mfma_f32_16x16x32_bf16 a[76:79], a[12:15], v[192:195], a[76:79]\n"
"v_mul_f32 v252, v252, v128\n"
"v_mul_f32 v253, v253, v129\n"
"s_waitcnt vmcnt(12)\n"
"v_cmp_eq_u64 vcc, s[68:69], v[22:23]\n"
"s_nop 0\n"
"s_cmp_eq_u64 vcc, -1\n"
"s_cbranch_scc0 L_cx_spin%=\n"
"LT_cx_ok%=:\n"
"v_and_b32 v11, 7, v21\n"
"v_lshlrev_b32 v11, 3, v11\n"
"s_waitcnt vmcnt(13) lgkmcnt(0)\n"
"v_mfma_f32_16x16x32_bf16 a[64:67], a[176:179], v[64:67], a[64:67]\n"
"s_add_u32 s89, s78, 0x1000\n"
"v_mul_f32 v254, v254, v130\n"
"v_mfma_f32_16x16x32_bf16 a[80:83], a[176:179], v[72:75], a[80:83]\n"
"buffer_load_dwordx4 v[192:195], v1, s[48:51], s89 offen offset:1024 sc1\n"
"s_add_u32 s89, s79, 0x1000\n"
"v_mfma_f32_16x16x32_bf16 a[96:99], a[176:179], v[80:83], a[96:99]\n"
"buffer_load_dwordx4 v[200:203], v1, s[48:51], s89 offen offset:1024 sc1\n"
"v_mfma_f32_16x16x32_bf16 a[112:115], a[176:179], v[88:91], a[112:115]\n"
"s_add_u32 s89, s80, 0x1000\n"
"v_mul_f32 v255, v255, v131\n"
"v_mfma_f32_16x16x32_bf16 a[116:119], a[180:183], v[88:91], a[116:119]\n"
"buffer_load_dwordx4 v[208:211], v1, s[48:51], s89 offen offset:1024 sc1\n"
"v_mfma_f32_16x16x32_bf16 a[100:103], a[180:183], v[80:83], a[100:103]\n"
"s_add_u32 s89, s81, 0x1000\n"
"v_cvt_pk_bf16_f32 v252, v252, v253\n"
"v_mfma_f32_16x16x32_bf16 a[84:87], a[180:183], v[72:75], a[84:87]\n"
"buffer_load_dwordx4 v[216:219], v1, s[48:51], s89 offen offset:1024 sc1\n"
"v_mfma_f32_16x16x32_bf16 a[68:71], a[180:183], v[64:67], a[68:71]\n"
"v_cvt_pk_bf16_f32 v254, v254, v255\n"
"v_and_b32 v253, 0xffff0000, v252\n"
"v_mfma_f32_16x16x32_bf16 a[72:75], a[184:187], v[64:67], a[72:75]\n"
"ds_read_b128 a[160:163], v3 offset:0\n"
"v_lshlrev_b32 v252, 16, v252\n"
"v_mfma_f32_16x16x32_bf16 a[88:91], a[184:187], v[72:75], a[88:91]\n"
"v_and_b32 v255, 0xffff0000, v254\n"
"v_lshlrev_b32 v254, 16, v254\n"
"v_mfma_f32_16x16x32_bf16 a[104:107], a[184:187], v[80:83], a[104:107]\n"
"ds_read_b128 a[164:167], v3 offset:1024\n"
"v_mul_f32 v128, 0xbfb8aa3b, v152\n"
"v_mfma_f32_16x16x32_bf16 a[120:123], a[184:187], v[88:91], a[120:123]\n"
"v_mul_f32 v129, 0xbfb8aa3b, v153\n"
"v_mul_f32 v130, 0xbfb8aa3b, v154\n"
"v_mfma_f32_16x16x32_bf16 a[124:127], a[188:191], v[88:91], a[124:127]\n"
"ds_read_b128 a[168:171], v3 offset:2048\n"
"v_mul_f32 v131, 0xbfb8aa3b, v155\n"
"v_mfma_f32_16x16x32_bf16 a[108:111], a[188:191], v[80:83], a[108:111]\n"
"v_exp_f32 v128, v128\n"
"v_mfma_f32_16x16x32_bf16 a[92:95], a[188:191], v[72:75], a[92:95]\n"
"ds_read_b128 a[172:175], v3 offset:3072\n"
"v_mfma_f32_16x16x32_bf16 a[76:79], a[188:191], v[64:67], a[76:79]\n"
"v_exp_f32 v129, v129\n"
"s_waitcnt vmcnt(12)\n"
"v_mfma_f32_16x16x32_bf16 a[64:67], a[16:19], v[96:99], a[64:67]\n"
"s_add_u32 s89, s78, 0x2000\n"
"v_mfma_f32_16x16x32_bf16 a[80:83], a[16:19], v[104:107], a[80:83]\n"
"buffer_load_dwordx4 v[64:67], v1, s[48:51], s89 offen offset:1024 sc1\n"
"v_mfma_f32_16x16x32_bf16 a[96:99], a[16:19], v[112:115], a[96:99]\n"
"v_exp_f32 v130, v130\n"
"v_mfma_f32_16x16x32_bf16 a[112:115], a[16:19], v[120:123], a[112:115]\n"
"s_add_u32 s89, s79, 0x2000\n"
"v_mfma_f32_16x16x32_bf16 a[116:119], a[20:23], v[120:123], a[116:119]\n"
"buffer_load_dwordx4 v[72:75], v1, s[48:51], s89 offen offset:1024 sc1\n"
"v_mfma_f32_16x16x32_bf16 a[100:103], a[20:23], v[112:115], a[100:103]\n"
"v_exp_f32 v131, v131\n"
"v_mfma_f32_16x16x32_bf16 a[84:87], a[20:23], v[104:107], a[84:87]\n"
"v_add_f32 v128, 1.0, v128\n"
"v_add_f32 v129, 1.0, v129\n"
"v_mfma_f32_16x16x32_bf16 a[68:71], a[20:23], v[96:99], a[68:71]\n"
"s_add_u32 s89, s80, 0x2000\n"
"v_add_f32 v130, 1.0, v130\n"
"v_mfma_f32_16x16x32_bf16 a[72:75], a[24:27], v[96:99], a[72:75]\n"
"buffer_load_dwordx4 v[80:83], v1, s[48:51], s89 offen offset:1024 sc1\n"
"v_mfma_f32_16x16x32_bf16 a[88:91], a[24:27], v[104:107], a[88:91]\n"
"v_add_f32 v131, 1.0, v131\n"
"v_mfma_f32_16x16x32_bf16 a[104:107], a[24:27], v[112:115], a[104:107]\n"
"v_rcp_f32 v128, v128\n"
"v_mfma_f32_16x16x32_bf16 a[120:123], a[24:27], v[120:123], a[120:123]\n"
"s_add_u32 s89, s81, 0x2000\n"
"v_mfma_f32_16x16x32_bf16 a[124:127], a[28:31], v[120:123], a[124:127]\n"
"buffer_load_dwordx4 v[88:91], v1, s[48:51], s89 offen offset:1024 sc1\n"
"v_mfma_f32_16x16x32_bf16 a[108:111], a[28:31], v[112:115], a[108:111]\n"
"v_rcp_f32 v129, v129\n"
"v_mfma_f32_16x16x32_bf16 a[92:95], a[28:31], v[104:107], a[92:95]\n"
"v_rcp_f32 v130, v130\n"
"v_mfma_f32_16x16x32_bf16 a[76:79], a[28:31], v[96:99], a[76:79]\n"
"v_rcp_f32 v131, v131\n"
"s_waitcnt vmcnt(12) lgkmcnt(0)\n"
"v_mfma_f32_16x16x32_bf16 a[64:67], a[160:163], a[192:195], a[64:67]\n"
"s_add_u32 s89, s62, 0x3000\n"
"v_mul_f32 v152, v152, v128\n"
"v_mfma_f32_16x16x32_bf16 a[80:83], a[160:163], a[196:199], a[80:83]\n"
"buffer_load_dwordx4 v[96:99], v1, s[48:51], s89 offen offset:2048 sc1\n"
"s_add_u32 s89, s63, 0x3000\n"
"v_mfma_f32_16x16x32_bf16 a[96:99], a[160:163], a[200:203], a[96:99]\n"
"buffer_load_dwordx4 v[104:107], v1, s[48:51], s89 offen offset:2048 sc1\n"
"v_mfma_f32_16x16x32_bf16 a[112:115], a[160:163], a[204:207], a[112:115]\n"
"s_add_u32 s89, s62, 0x3000\n"
"v_mul_f32 v153, v153, v129\n"
"v_mfma_f32_16x16x32_bf16 a[116:119], a[164:167], a[204:207], a[116:119]\n"
"buffer_load_dwordx4 v[112:115], v1, s[48:51], s89 offen offset:3072 sc1\n"
"v_mfma_f32_16x16x32_bf16 a[100:103], a[164:167], a[200:203], a[100:103]\n"
"s_add_u32 s89, s63, 0x3000\n"
"v_mul_f32 v154, v154, v130\n"
"v_mfma_f32_16x16x32_bf16 a[84:87], a[164:167], a[196:199], a[84:87]\n"
"buffer_load_dwordx4 v[120:123], v1, s[48:51], s89 offen offset:3072 sc1\n"
"v_mfma_f32_16x16x32_bf16 a[68:71], a[164:167], a[192:195], a[68:71]\n"
"v_mul_f32 v155, v155, v131\n"
"v_cvt_pk_bf16_f32 v152, v152, v153\n"
"v_mfma_f32_16x16x32_bf16 a[72:75], a[168:171], a[192:195], a[72:75]\n"
"ds_read_b128 a[176:179], v3 offset:4096\n"
"v_cvt_pk_bf16_f32 v154, v154, v155\n"
"v_mfma_f32_16x16x32_bf16 a[88:91], a[168:171], a[196:199], a[88:91]\n"
"v_and_b32 v153, 0xffff0000, v152\n"
"v_lshlrev_b32 v152, 16, v152\n"
"v_mfma_f32_16x16x32_bf16 a[104:107], a[168:171], a[200:203], a[104:107]\n"
"ds_read_b128 a[180:183], v3 offset:5120\n"
"v_and_b32 v155, 0xffff0000, v154\n"
"v_mfma_f32_16x16x32_bf16 a[120:123], a[168:171], a[204:207], a[120:123]\n"
"v_lshlrev_b32 v154, 16, v154\n"
"v_mul_f32 v128, 0xbfb8aa3b, v160\n"
"v_mfma_f32_16x16x32_bf16 a[124:127], a[172:175], a[204:207], a[124:127]\n"
"ds_read_b128 a[184:187], v3 offset:6144\n"
"v_mul_f32 v129, 0xbfb8aa3b, v161\n"
"v_mfma_f32_16x16x32_bf16 a[108:111], a[172:175], a[200:203], a[108:111]\n"
"v_mul_f32 v130, 0xbfb8aa3b, v162\n"
"v_mul_f32 v131, 0xbfb8aa3b, v163\n"
"v_mfma_f32_16x16x32_bf16 a[92:95], a[172:175], a[196:199], a[92:95]\n"
"ds_read_b128 a[188:191], v3 offset:7168\n"
"v_mfma_f32_16x16x32_bf16 a[76:79], a[172:175], a[192:195], a[76:79]\n"
"v_exp_f32 v128, v128\n"
"s_waitcnt vmcnt(12)\n"
"v_mfma_f32_16x16x32_bf16 a[64:67], a[32:35], a[208:211], a[64:67]\n"
"s_add_u32 s89, s62, 0x2000\n"
"v_mfma_f32_16x16x32_bf16 a[80:83], a[32:35], a[212:215], a[80:83]\n"
"buffer_load_dwordx4 a[192:195], v1, s[48:51], s89 offen offset:2048 sc1\n"
"v_mfma_f32_16x16x32_bf16 a[96:99], a[32:35], a[216:219], a[96:99]\n"
"v_exp_f32 v129, v129\n"
"v_mfma_f32_16x16x32_bf16 a[112:115], a[32:35], a[220:223], a[112:115]\n"
"s_add_u32 s89, s63, 0x2000\n"
"v_mfma_f32_16x16x32_bf16 a[116:119], a[36:39], a[220:223], a[116:119]\n"
"buffer_load_dwordx4 a[196:199], v1, s[48:51], s89 offen offset:2048 sc1\n"
"v_mfma_f32_16x16x32_bf16 a[100:103], a[36:39], a[216:219], a[100:103]\n"
"v_exp_f32 v130, v130\n"
"v_mfma_f32_16x16x32_bf16 a[84:87], a[36:39], a[212:215], a[84:87]\n"
"v_exp_f32 v131, v131\n"
"v_mfma_f32_16x16x32_bf16 a[68:71], a[36:39], a[208:211], a[68:71]\n"
"s_add_u32 s89, s62, 0x2000\n"
"v_add_f32 v128, 1.0, v128\n"
"v_mfma_f32_16x16x32_bf16 a[72:75], a[40:43], a[208:211], a[72:75]\n"
"buffer_load_dwordx4 a[200:203], v1, s[48:51], s89 offen offset:3072 sc1\n"
"v_mfma_f32_16x16x32_bf16 a[88:91], a[40:43], a[212:215], a[88:91]\n"
"v_add_f32 v129, 1.0, v129\n"
"v_add_f32 v130, 1.0, v130\n"
"v_mfma_f32_16x16x32_bf16 a[104:107], a[40:43], a[216:219], a[104:107]\n"
"v_add_f32 v131, 1.0, v131\n"
"v_mfma_f32_16x16x32_bf16 a[120:123], a[40:43], a[220:223], a[120:123]\n"
"s_add_u32 s89, s63, 0x2000\n"
"v_mfma_f32_16x16x32_bf16 a[124:127], a[44:47], a[220:223], a[124:127]\n"
"buffer_load_dwordx4 a[204:207], v1, s[48:51], s89 offen offset:3072 sc1\n"
"v_mfma_f32_16x16x32_bf16 a[108:111], a[44:47], a[216:219], a[108:111]\n"
"v_rcp_f32 v128, v128\n"
"v_mfma_f32_16x16x32_bf16 a[92:95], a[44:47], a[212:215], a[92:95]\n"
"v_rcp_f32 v129, v129\n"
"v_mfma_f32_16x16x32_bf16 a[76:79], a[44:47], a[208:211], a[76:79]\n"
"v_rcp_f32 v130, v130\n"
"s_waitcnt vmcnt(12) lgkmcnt(0)\n"
"v_mfma_f32_16x16x32_bf16 a[64:67], a[176:179], v[192:195], a[64:67]\n"
"s_add_u32 s89, s62, 0x3000\n"
"v_mfma_f32_16x16x32_bf16 a[80:83], a[176:179], v[200:203], a[80:83]\n"
"buffer_load_dwordx4 a[208:211], v1, s[48:51], s89 offen offset:0 sc1\n"
"v_mfma_f32_16x16x32_bf16 a[96:99], a[176:179], v[208:211], a[96:99]\n"
"v_rcp_f32 v131, v131\n"
"v_mfma_f32_16x16x32_bf16 a[112:115], a[176:179], v[216:219], a[112:115]\n"
"s_add_u32 s89, s63, 0x3000\n"
"v_mul_f32 v160, v160, v128\n"
"v_mfma_f32_16x16x32_bf16 a[116:119], a[180:183], v[216:219], a[116:119]\n"
"buffer_load_dwordx4 a[212:215], v1, s[48:51], s89 offen offset:0 sc1\n"
"v_mfma_f32_16x16x32_bf16 a[100:103], a[180:183], v[208:211], a[100:103]\n"
"v_mul_f32 v161, v161, v129\n"
"v_mul_f32 v162, v162, v130\n"
"v_mfma_f32_16x16x32_bf16 a[84:87], a[180:183], v[200:203], a[84:87]\n"
"v_mul_f32 v163, v163, v131\n"
"v_cvt_pk_bf16_f32 v160, v160, v161\n"
"v_mfma_f32_16x16x32_bf16 a[68:71], a[180:183], v[192:195], a[68:71]\n"
"s_add_u32 s89, s62, 0x3000\n"
"v_cvt_pk_bf16_f32 v162, v162, v163\n"
"v_mfma_f32_16x16x32_bf16 a[72:75], a[184:187], v[192:195], a[72:75]\n"
"buffer_load_dwordx4 a[216:219], v1, s[48:51], s89 offen offset:1024 sc1\n"
"v_mfma_f32_16x16x32_bf16 a[88:91], a[184:187], v[200:203], a[88:91]\n"
"v_and_b32 v161, 0xffff0000, v160\n"
"v_lshlrev_b32 v160, 16, v160\n"
"v_mfma_f32_16x16x32_bf16 a[104:107], a[184:187], v[208:211], a[104:107]\n"
"v_and_b32 v163, 0xffff0000, v162\n"
"v_lshlrev_b32 v162, 16, v162\n"
"v_mfma_f32_16x16x32_bf16 a[120:123], a[184:187], v[216:219], a[120:123]\n"
"s_add_u32 s89, s63, 0x3000\n"
"v_mfma_f32_16x16x32_bf16 a[124:127], a[188:191], v[216:219], a[124:127]\n"
"buffer_load_dwordx4 a[220:223], v1, s[48:51], s89 offen offset:1024 sc1\n"
"v_mfma_f32_16x16x32_bf16 a[108:111], a[188:191], v[208:211], a[108:111]\n"
"v_mfma_f32_16x16x32_bf16 a[92:95], a[188:191], v[200:203], a[92:95]\n"
"v_mfma_f32_16x16x32_bf16 a[76:79], a[188:191], v[192:195], a[76:79]\n"
"s_waitcnt vmcnt(12)\n"
"v_mfma_f32_16x16x32_bf16 a[64:67], a[48:51], v[64:67], a[64:67]\n"
"v_mfma_f32_16x16x32_bf16 a[80:83], a[48:51], v[72:75], a[80:83]\n"
"buffer_load_dwordx4 v[192:195], v1, s[48:51], s62 offen offset:0 sc1\n"
"v_mfma_f32_16x16x32_bf16 a[96:99], a[48:51], v[80:83], a[96:99]\n"
"v_mfma_f32_16x16x32_bf16 a[112:115], a[48:51], v[88:91], a[112:115]\n"
"v_mfma_f32_16x16x32_bf16 a[116:119], a[52:55], v[88:91], a[116:119]\n"
"buffer_load_dwordx4 v[200:203], v1, s[48:51], s63 offen offset:0 sc1\n"
"v_mfma_f32_16x16x32_bf16 a[100:103], a[52:55], v[80:83], a[100:103]\n"
"v_mfma_f32_16x16x32_bf16 a[84:87], a[52:55], v[72:75], a[84:87]\n"
"v_mfma_f32_16x16x32_bf16 a[68:71], a[52:55], v[64:67], a[68:71]\n"
"v_mfma_f32_16x16x32_bf16 a[72:75], a[56:59], v[64:67], a[72:75]\n"
"buffer_load_dwordx4 v[208:211], v1, s[48:51], s62 offen offset:1024 sc1\n"
"v_mfma_f32_16x16x32_bf16 a[88:91], a[56:59], v[72:75], a[88:91]\n"
"v_mfma_f32_16x16x32_bf16 a[104:107], a[56:59], v[80:83], a[104:107]\n"
"v_mfma_f32_16x16x32_bf16 a[120:123], a[56:59], v[88:91], a[120:123]\n"
"v_mfma_f32_16x16x32_bf16 a[124:127], a[60:63], v[88:91], a[124:127]\n"
"buffer_load_dwordx4 v[216:219], v1, s[48:51], s63 offen offset:1024 sc1\n"
"v_mfma_f32_16x16x32_bf16 a[108:111], a[60:63], v[80:83], a[108:111]\n"
"v_mfma_f32_16x16x32_bf16 a[92:95], a[60:63], v[72:75], a[92:95]\n"
"v_mfma_f32_16x16x32_bf16 a[76:79], a[60:63], v[64:67], a[76:79]\n"
"s_waitcnt vmcnt(14)\n"
"v_mfma_f32_16x16x32_bf16 v[48:51], v[36:39], v[96:99], 0\n"
"v_mfma_f32_16x16x32_bf16 v[56:59], v[36:39], v[104:107], 0\n"
"buffer_load_dwordx4 v[64:67], v1, s[48:51], s62 offen offset:2048 sc1\n"
"v_mfma_f32_16x16x32_bf16 v[116:119], v[44:47], v[104:107], 0\n"
"ds_write_b128 v4, a[96:99] offset:0\n"
"v_mfma_f32_16x16x32_bf16 v[100:103], v[44:47], v[96:99], 0\n"
"v_mfma_f32_16x16x32_bf16 v[228:231], v[52:55], v[96:99], 0\n"
"buffer_load_dwordx4 v[72:75], v1, s[48:51], s63 offen offset:2048 sc1\n"
"v_mfma_f32_16x16x32_bf16 v[244:247], v[52:55], v[104:107], 0\n"
"v_mfma_f32_16x16x32_bf16 v[180:183], v[60:63], v[104:107], 0\n"
"ds_write_b128 v4, a[100:103] offset:1024\n"
"v_mfma_f32_16x16x32_bf16 v[164:167], v[60:63], v[96:99], 0\n"
"s_waitcnt vmcnt(14)\n"
"v_mfma_f32_16x16x32_bf16 v[48:51], v[76:79], v[112:115], v[48:51]\n"
"s_add_u32 s89, s62, 0x1000\n"
"v_mfma_f32_16x16x32_bf16 v[56:59], v[76:79], v[120:123], v[56:59]\n"
"buffer_load_dwordx4 v[80:83], v1, s[48:51], s89 offen offset:2048 sc1\n"
"v_mfma_f32_16x16x32_bf16 v[116:119], v[92:95], v[120:123], v[116:119]\n"
"ds_write_b128 v4, a[104:107] offset:2048\n"
"v_mfma_f32_16x16x32_bf16 v[100:103], v[92:95], v[112:115], v[100:103]\n"
"s_add_u32 s89, s63, 0x1000\n"
"v_mfma_f32_16x16x32_bf16 v[228:231], v[108:111], v[112:115], v[228:231]\n"
"buffer_load_dwordx4 v[88:91], v1, s[48:51], s89 offen offset:2048 sc1\n"
"v_mfma_f32_16x16x32_bf16 v[244:247], v[108:111], v[120:123], v[244:247]\n"
"v_mfma_f32_16x16x32_bf16 v[180:183], v[124:127], v[120:123], v[180:183]\n"
"ds_write_b128 v4, a[108:111] offset:3072\n"
"v_mfma_f32_16x16x32_bf16 v[164:167], v[124:127], v[112:115], v[164:167]\n"
"s_waitcnt vmcnt(14)\n"
"v_mfma_f32_16x16x32_bf16 v[48:51], v[136:139], a[192:195], v[48:51]\n"
"v_mfma_f32_16x16x32_bf16 v[56:59], v[136:139], a[196:199], v[56:59]\n"
"buffer_load_dwordx4 v[96:99], v1, s[48:51], s62 offen offset:3072 sc1\n"
"v_mfma_f32_16x16x32_bf16 v[116:119], v[144:147], a[196:199], v[116:119]\n"
"ds_write_b128 v4, a[112:115] offset:4096\n"
"v_mfma_f32_16x16x32_bf16 v[100:103], v[144:147], a[192:195], v[100:103]\n"
"v_mfma_f32_16x16x32_bf16 v[228:231], v[168:171], a[192:195], v[228:231]\n"
"buffer_load_dwordx4 v[104:107], v1, s[48:51], s63 offen offset:3072 sc1\n"
"v_mfma_f32_16x16x32_bf16 v[244:247], v[168:171], a[196:199], v[244:247]\n"
"v_mfma_f32_16x16x32_bf16 v[180:183], v[176:179], a[196:199], v[180:183]\n"
"ds_write_b128 v4, a[116:119] offset:5120\n"
"v_mfma_f32_16x16x32_bf16 v[164:167], v[176:179], a[192:195], v[164:167]\n"
"s_waitcnt vmcnt(14)\n"
"v_mfma_f32_16x16x32_bf16 v[48:51], v[184:187], a[200:203], v[48:51]\n"
"s_add_u32 s89, s62, 0x1000\n"
"v_mfma_f32_16x16x32_bf16 v[56:59], v[184:187], a[204:207], v[56:59]\n"
"buffer_load_dwordx4 v[112:115], v1, s[48:51], s89 offen offset:3072 sc1\n"
"v_mfma_f32_16x16x32_bf16 v[116:119], v[204:207], a[204:207], v[116:119]\n"
"ds_write_b128 v4, a[120:123] offset:6144\n"
"v_mfma_f32_16x16x32_bf16 v[100:103], v[204:207], a[200:203], v[100:103]\n"
"s_add_u32 s89, s63, 0x1000\n"
"v_mfma_f32_16x16x32_bf16 v[228:231], v[220:223], a[200:203], v[228:231]\n"
"buffer_load_dwordx4 v[120:123], v1, s[48:51], s89 offen offset:3072 sc1\n"
"v_mfma_f32_16x16x32_bf16 v[244:247], v[220:223], a[204:207], v[244:247]\n"
"v_mfma_f32_16x16x32_bf16 v[180:183], v[188:191], a[204:207], v[180:183]\n"
"ds_write_b128 v4, a[124:127] offset:7168\n"
"v_mfma_f32_16x16x32_bf16 v[164:167], v[188:191], a[200:203], v[164:167]\n"
"s_waitcnt vmcnt(14)\n"
"v_mfma_f32_16x16x32_bf16 v[48:51], v[32:35], a[208:211], v[48:51]\n"
"s_add_u32 s89, s62, 0x1000\n"
"v_mfma_f32_16x16x32_bf16 v[56:59], v[32:35], a[212:215], v[56:59]\n"
"buffer_load_dwordx4 a[192:195], v1, s[48:51], s89 offen offset:0 sc1\n"
"v_mfma_f32_16x16x32_bf16 v[116:119], v[68:71], a[212:215], v[116:119]\n"
"v_mfma_f32_16x16x32_bf16 v[100:103], v[68:71], a[208:211], v[100:103]\n"
"s_add_u32 s89, s63, 0x1000\n"
"v_mfma_f32_16x16x32_bf16 v[228:231], v[196:199], a[208:211], v[228:231]\n"
"buffer_load_dwordx4 a[196:199], v1, s[48:51], s89 offen offset:0 sc1\n"
"v_mfma_f32_16x16x32_bf16 v[244:247], v[196:199], a[212:215], v[244:247]\n"
"v_mfma_f32_16x16x32_bf16 v[180:183], v[132:135], a[212:215], v[180:183]\n"
"v_mfma_f32_16x16x32_bf16 v[164:167], v[132:135], a[208:211], v[164:167]\n"
"s_waitcnt lgkmcnt(0)\n"
"s_barrier\n"
"s_waitcnt vmcnt(14)\n"
"v_mfma_f32_16x16x32_bf16 v[48:51], v[40:43], a[216:219], v[48:51]\n"
"s_add_u32 s89, s62, 0x2000\n"
"v_mfma_f32_16x16x32_bf16 v[56:59], v[40:43], a[220:223], v[56:59]\n"
"buffer_load_dwordx4 a[200:203], v1, s[48:51], s89 offen offset:0 sc1\n"
"v_mfma_f32_16x16x32_bf16 v[116:119], v[84:87], a[220:223], v[116:119]\n"
"ds_read_b128 v[128:131], v25 offset:0\n"
"v_mfma_f32_16x16x32_bf16 v[100:103], v[84:87], a[216:219], v[100:103]\n"
"s_add_u32 s89, s63, 0x2000\n"
"v_mfma_f32_16x16x32_bf16 v[228:231], v[212:215], a[216:219], v[228:231]\n"
"buffer_load_dwordx4 a[204:207], v1, s[48:51], s89 offen offset:0 sc1\n"
"v_mfma_f32_16x16x32_bf16 v[244:247], v[212:215], a[220:223], v[244:247]\n"
"ds_read_b128 v[172:175], v25 offset:8192\n"
"v_mfma_f32_16x16x32_bf16 v[180:183], v[148:151], a[220:223], v[180:183]\n"
"ds_read_b128 v[224:227], v25 offset:16384\n"
"v_mfma_f32_16x16x32_bf16 v[164:167], v[148:151], a[216:219], v[164:167]\n"
"ds_read_b128 v[232:235], v25 offset:24576\n"
"s_waitcnt vmcnt(14)\n"
"v_mfma_f32_16x16x32_bf16 v[48:51], a[128:131], v[192:195], v[48:51]\n"
"s_add_u32 s89, s62, 0x1000\n"
"v_mfma_f32_16x16x32_bf16 v[56:59], a[128:131], v[200:203], v[56:59]\n"
"buffer_load_dwordx4 a[208:211], v1, s[48:51], s89 offen offset:1024 sc1\n"
"s_add_u32 s89, s63, 0x1000\n"
"v_mfma_f32_16x16x32_bf16 v[116:119], a[132:135], v[200:203], v[116:119]\n"
"buffer_load_dwordx4 a[212:215], v1, s[48:51], s89 offen offset:1024 sc1\n"
"v_mfma_f32_16x16x32_bf16 v[100:103], a[132:135], v[192:195], v[100:103]\n"
"ds_read_b128 a[160:163], v2 offset:32768\n"
"v_mfma_f32_16x16x32_bf16 v[228:231], a[136:139], v[192:195], v[228:231]\n"
"ds_read_b128 a[164:167], v2 offset:33792\n"
"v_mfma_f32_16x16x32_bf16 v[244:247], a[136:139], v[200:203], v[244:247]\n"
"ds_read_b128 a[168:171], v2 offset:34816\n"
"v_mfma_f32_16x16x32_bf16 v[180:183], a[140:143], v[200:203], v[180:183]\n"
"ds_read_b128 a[172:175], v2 offset:35840\n"
"v_mfma_f32_16x16x32_bf16 v[164:167], a[140:143], v[192:195], v[164:167]\n"
"ds_read_b128 v[240:243], v25 offset:1024\n"
"s_waitcnt vmcnt(14)\n"
"v_mfma_f32_16x16x32_bf16 v[48:51], a[144:147], v[208:211], v[48:51]\n"
"s_add_u32 s89, s62, 0x2000\n"
"v_mfma_f32_16x16x32_bf16 v[56:59], a[144:147], v[216:219], v[56:59]\n"
"buffer_load_dwordx4 a[216:219], v1, s[48:51], s89 offen offset:1024 sc1\n"
"v_mfma_f32_16x16x32_bf16 v[116:119], a[148:151], v[216:219], v[116:119]\n"
"ds_read_b128 v[248:251], v25 offset:9216\n"
"s_waitcnt lgkmcnt(6)\n"
"v_mfma_f32_16x16x32_bf16 v[100:103], a[148:151], v[208:211], v[100:103]\n"
"s_add_u32 s89, s63, 0x2000\n"
"v_add_f32 v128, v128, v172\n"
"v_mfma_f32_16x16x32_bf16 v[228:231], a[152:155], v[208:211], v[228:231]\n"
"buffer_load_dwordx4 a[220:223], v1, s[48:51], s89 offen offset:1024 sc1\n"
"v_mfma_f32_16x16x32_bf16 v[244:247], a[152:155], v[216:219], v[244:247]\n"
"v_add_f32 v129, v129, v173\n"
"v_add_f32 v130, v130, v174\n"
"v_mfma_f32_16x16x32_bf16 v[180:183], a[156:159], v[216:219], v[180:183]\n"
"v_add_f32 v131, v131, v175\n"
"v_add_f32 v224, v224, v232\n"
"v_mfma_f32_16x16x32_bf16 v[164:167], a[156:159], v[208:211], v[164:167]\n"
"v_add_f32 v225, v225, v233\n"
"v_add_f32 v226, v226, v234\n"
"s_waitcnt vmcnt(14) lgkmcnt(2)\n"
"v_mfma_f32_16x16x32_bf16 v[48:51], a[160:163], v[64:67], v[48:51]\n"
"s_add_u32 s89, s60, 0x3000\n"
"v_add_f32 v227, v227, v235\n"
"v_mfma_f32_16x16x32_bf16 v[56:59], a[160:163], v[72:75], v[56:59]\n"
"buffer_load_dwordx4 v[192:195], v1, s[48:51], s89 offen offset:2048 sc1\n"
"s_add_u32 s89, s61, 0x3000\n"
"v_mfma_f32_16x16x32_bf16 v[116:119], a[164:167], v[72:75], v[116:119]\n"
"buffer_load_dwordx4 v[200:203], v1, s[48:51], s89 offen offset:2048 sc1\n"
"ds_write_b128 v3, a[64:67] offset:32768\n"
"v_mfma_f32_16x16x32_bf16 v[100:103], a[164:167], v[64:67], v[100:103]\n"
"ds_read_b128 a[176:179], v2 offset:36864\n"
"v_mfma_f32_16x16x32_bf16 v[228:231], a[168:171], v[64:67], v[228:231]\n"
"ds_read_b128 a[180:183], v2 offset:37888\n"
"v_mfma_f32_16x16x32_bf16 v[244:247], a[168:171], v[72:75], v[244:247]\n"
"ds_read_b128 a[184:187], v2 offset:38912\n"
"v_mfma_f32_16x16x32_bf16 v[180:183], a[172:175], v[72:75], v[180:183]\n"
"ds_read_b128 a[188:191], v2 offset:39936\n"
"ds_write_b128 v3, a[68:71] offset:33792\n"
"v_mfma_f32_16x16x32_bf16 v[164:167], a[172:175], v[64:67], v[164:167]\n"
"ds_read_b128 v[172:175], v25 offset:17408\n"
"s_waitcnt vmcnt(14)\n"
"v_mfma_f32_16x16x32_bf16 v[48:51], a[0:3], v[80:83], v[48:51]\n"
"s_add_u32 s89, s60, 0x3000\n"
"v_mfma_f32_16x16x32_bf16 v[56:59], a[0:3], v[88:91], v[56:59]\n"
"buffer_load_dwordx4 v[208:211], v1, s[48:51], s89 offen offset:3072 sc1\n"
"v_mfma_f32_16x16x32_bf16 v[116:119], a[4:7], v[88:91], v[116:119]\n"
"ds_write_b128 v3, a[72:75] offset:34816\n"
"v_mfma_f32_16x16x32_bf16 v[100:103], a[4:7], v[80:83], v[100:103]\n"
"s_add_u32 s89, s61, 0x3000\n"
"v_mfma_f32_16x16x32_bf16 v[228:231], a[8:11], v[80:83], v[228:231]\n"
"buffer_load_dwordx4 v[216:219], v1, s[48:51], s89 offen offset:3072 sc1\n"
"v_mfma_f32_16x16x32_bf16 v[244:247], a[8:11], v[88:91], v[244:247]\n"
"ds_read_b128 v[232:235], v25 offset:25600\n"
"v_add_f32 v128, v128, v224\n"
"v_mfma_f32_16x16x32_bf16 v[180:183], a[12:15], v[88:91], v[180:183]\n"
"ds_write_b128 v3, a[76:79] offset:35840\n"
"v_mfma_f32_16x16x32_bf16 v[164:167], a[12:15], v[80:83], v[164:167]\n"
"v_add_f32 v129, v129, v225\n"
"v_add_f32 v130, v130, v226\n"
"s_waitcnt vmcnt(14) lgkmcnt(5)\n"
"v_mfma_f32_16x16x32_bf16 v[48:51], a[176:179], v[96:99], v[48:51]\n"
"s_add_u32 s89, s60, 0x2000\n"
"v_add_f32 v131, v131, v227\n"
"v_mfma_f32_16x16x32_bf16 v[56:59], a[176:179], v[104:107], v[56:59]\n"
"buffer_load_dwordx4 v[64:67], v1, s[48:51], s89 offen offset:2048 sc1\n"
"s_add_u32 s89, s61, 0x2000\n"
"v_mfma_f32_16x16x32_bf16 v[116:119], a[180:183], v[104:107], v[116:119]\n"
"buffer_load_dwordx4 v[72:75], v1, s[48:51], s89 offen offset:2048 sc1\n"
"ds_write_b128 v3, a[80:83] offset:36864\n"
"v_mfma_f32_16x16x32_bf16 v[100:103], a[180:183], v[96:99], v[100:103]\n"
"ds_read_b128 a[160:163], v3 offset:0\n"
"v_mfma_f32_16x16x32_bf16 v[228:231], a[184:187], v[96:99], v[228:231]\n"
"ds_read_b128 a[164:167], v3 offset:1024\n"
"v_mfma_f32_16x16x32_bf16 v[244:247], a[184:187], v[104:107], v[244:247]\n"
"ds_read_b128 a[168:171], v3 offset:2048\n"
"v_mfma_f32_16x16x32_bf16 v[180:183], a[188:191], v[104:107], v[180:183]\n"
"ds_read_b128 a[172:175], v3 offset:3072\n"
"ds_write_b128 v3, a[84:87] offset:37888\n"
"v_mfma_f32_16x16x32_bf16 v[164:167], a[188:191], v[96:99], v[164:167]\n"
"buffer_store_dwordx4 v[128:131], v1, s[48:51], s52 offen sc1\n"
"s_waitcnt vmcnt(15)\n"
"v_mfma_f32_16x16x32_bf16 v[48:51], a[16:19], v[112:115], v[48:51]\n"
"s_add_u32 s89, s60, 0x2000\n"
"s_waitcnt lgkmcnt(7)\n"
"v_mfma_f32_16x16x32_bf16 v[56:59], a[16:19], v[120:123], v[56:59]\n"
"buffer_load_dwordx4 v[80:83], v1, s[48:51], s89 offen offset:3072 sc1\n"
"v_mfma_f32_16x16x32_bf16 v[116:119], a[20:23], v[120:123], v[116:119]\n"
"ds_write_b128 v3, a[88:91] offset:38912\n"
"v_mfma_f32_16x16x32_bf16 v[100:103], a[20:23], v[112:115], v[100:103]\n"
"s_add_u32 s89, s61, 0x2000\n"
"v_add_f32 v240, v240, v248\n"
"v_mfma_f32_16x16x32_bf16 v[228:231], a[24:27], v[112:115], v[228:231]\n"
"buffer_load_dwordx4 v[88:91], v1, s[48:51], s89 offen offset:3072 sc1\n"
"v_mfma_f32_16x16x32_bf16 v[244:247], a[24:27], v[120:123], v[244:247]\n"
"v_add_f32 v241, v241, v249\n"
"v_add_f32 v242, v242, v250\n"
"v_mfma_f32_16x16x32_bf16 v[180:183], a[28:31], v[120:123], v[180:183]\n"
"ds_write_b128 v3, a[92:95] offset:39936\n"
"v_mfma_f32_16x16x32_bf16 v[164:167], a[28:31], v[112:115], v[164:167]\n"
"v_add_f32 v243, v243, v251\n"
"v_add_f32 v172, v172, v232\n"
"s_waitcnt vmcnt(15) lgkmcnt(3)\n"
"v_mfma_f32_16x16x32_bf16 v[48:51], a[160:163], a[192:195], v[48:51]\n"
"s_add_u32 s89, s60, 0x3000\n"
"v_add_f32 v173, v173, v233\n"
"v_mfma_f32_16x16x32_bf16 v[56:59], a[160:163], a[196:199], v[56:59]\n"
"buffer_load_dwordx4 v[96:99], v1, s[48:51], s89 offen offset:0 sc1\n"
"s_add_u32 s89, s61, 0x3000\n"
"v_mfma_f32_16x16x32_bf16 v[116:119], a[164:167], a[196:199], v[116:119]\n"
"buffer_load_dwordx4 v[104:107], v1, s[48:51], s89 offen offset:0 sc1\n"
"v_mfma_f32_16x16x32_bf16 v[100:103], a[164:167], a[192:195], v[100:103]\n"
"ds_read_b128 a[176:179], v3 offset:4096\n"
"v_add_f32 v174, v174, v234\n"
"v_mfma_f32_16x16x32_bf16 v[228:231], a[168:171], a[192:195], v[228:231]\n"
"ds_read_b128 a[180:183], v3 offset:5120\n"
"v_add_f32 v175, v175, v235\n"
"v_mfma_f32_16x16x32_bf16 v[244:247], a[168:171], a[196:199], v[244:247]\n"
"ds_read_b128 a[184:187], v3 offset:6144\n"
"v_add_f32 v240, v240, v172\n"
"v_mfma_f32_16x16x32_bf16 v[180:183], a[172:175], a[196:199], v[180:183]\n"
"ds_read_b128 a[188:191], v3 offset:7168\n"
"v_add_f32 v241, v241, v173\n"
"v_mfma_f32_16x16x32_bf16 v[164:167], a[172:175], a[192:195], v[164:167]\n"
"v_add_f32 v242, v242, v174\n"
"v_add_f32 v243, v243, v175\n"
"s_waitcnt vmcnt(15)\n"
"v_mfma_f32_16x16x32_bf16 v[48:51], a[32:35], a[200:203], v[48:51]\n"
"s_add_u32 s89, s60, 0x3000\n"
"v_mfma_f32_16x16x32_bf16 v[56:59], a[32:35], a[204:207], v[56:59]\n"
"buffer_load_dwordx4 v[112:115], v1, s[48:51], s89 offen offset:1024 sc1\n"
"v_mfma_f32_16x16x32_bf16 v[116:119], a[36:39], a[204:207], v[116:119]\n"
"buffer_store_dwordx4 v[240:243], v1, s[48:51], s52 offen offset:1024 sc1\n"
"v_mfma_f32_16x16x32_bf16 v[100:103], a[36:39], a[200:203], v[100:103]\n"
"s_add_u32 s89, s61, 0x3000\n"
"v_mfma_f32_16x16x32_bf16 v[228:231], a[40:43], a[200:203], v[228:231]\n"
"buffer_load_dwordx4 v[120:123], v1, s[48:51], s89 offen offset:1024 sc1\n"
"v_mfma_f32_16x16x32_bf16 v[244:247], a[40:43], a[204:207], v[244:247]\n"
"v_mfma_f32_16x16x32_bf16 v[180:183], a[44:47], a[204:207], v[180:183]\n"
"v_mfma_f32_16x16x32_bf16 v[164:167], a[44:47], a[200:203], v[164:167]\n"
"s_waitcnt vmcnt(16) lgkmcnt(0)\n"
"v_mfma_f32_16x16x32_bf16 v[48:51], a[176:179], a[208:211], v[48:51]\n"
"s_waitcnt vmcnt(1)\n"
"v_mfma_f32_16x16x32_bf16 v[56:59], a[176:179], a[212:215], v[56:59]\n"
"buffer_load_dwordx4 a[192:195], v1, s[48:51], s60 offen offset:0 sc1\n"
"v_mfma_f32_16x16x32_bf16 v[116:119], a[180:183], a[212:215], v[116:119]\n"
"buffer_store_dwordx2 v[30:31], v29, s[48:51], s54 offen sc1\n"
"v_mfma_f32_16x16x32_bf16 v[100:103], a[180:183], a[208:211], v[100:103]\n"
"s_add_u32 s52, s52, 0x2000\n"
"s_add_u32 s54, s54, 32\n"
"v_mfma_f32_16x16x32_bf16 v[228:231], a[184:187], a[208:211], v[228:231]\n"
"buffer_load_dwordx4 a[196:199], v1, s[48:51], s61 offen offset:0 sc1\n"
"v_mfma_f32_16x16x32_bf16 v[244:247], a[184:187], a[212:215], v[244:247]\n"
"v_mfma_f32_16x16x32_bf16 v[180:183], a[188:191], a[212:215], v[180:183]\n"
"v_mfma_f32_16x16x32_bf16 v[164:167], a[188:191], a[208:211], v[164:167]\n"
"s_waitcnt vmcnt(17)\n"
"v_mfma_f32_16x16x32_bf16 v[48:51], a[48:51], a[216:219], v[48:51]\n"
"v_mfma_f32_16x16x32_bf16 v[56:59], a[48:51], a[220:223], v[56:59]\n"
"buffer_load_dwordx4 a[200:203], v1, s[48:51], s60 offen offset:1024 sc1\n"
"v_mfma_f32_16x16x32_bf16 v[116:119], a[52:55], a[220:223], v[116:119]\n"
"v_mfma_f32_16x16x32_bf16 v[100:103], a[52:55], a[216:219], v[100:103]\n"
"v_mfma_f32_16x16x32_bf16 v[228:231], a[56:59], a[216:219], v[228:231]\n"
"buffer_load_dwordx4 a[204:207], v1, s[48:51], s61 offen offset:1024 sc1\n"
"v_mfma_f32_16x16x32_bf16 v[244:247], a[56:59], a[220:223], v[244:247]\n"
"v_mfma_f32_16x16x32_bf16 v[180:183], a[60:63], a[220:223], v[180:183]\n"
"v_mfma_f32_16x16x32_bf16 v[164:167], a[60:63], a[216:219], v[164:167]\n"
"s_waitcnt vmcnt(17)\n"
"v_mfma_f32_16x16x32_bf16 a[224:227], v[36:39], v[192:195], 0\n"
"v_mfma_f32_16x16x32_bf16 a[240:243], v[36:39], v[200:203], 0\n"
"buffer_load_dwordx4 a[208:211], v1, s[48:51], s60 offen offset:2048 sc1\n"
"v_mfma_f32_16x16x32_bf16 a[244:247], v[44:47], v[200:203], 0\n"
"buffer_load_dwordx4 a[212:215], v1, s[48:51], s61 offen offset:2048 sc1\n"
"ds_write_b128 v2, v[48:51] offset:0\n"
"v_mfma_f32_16x16x32_bf16 a[228:231], v[44:47], v[192:195], 0\n"
"s_add_u32 s89, s60, 0x1000\n"
"v_mfma_f32_16x16x32_bf16 a[232:235], v[52:55], v[192:195], 0\n"
"buffer_load_dwordx4 a[216:219], v1, s[48:51], s89 offen offset:2048 sc1\n"
"v_mfma_f32_16x16x32_bf16 a[248:251], v[52:55], v[200:203], 0\n"
"s_add_u32 s89, s61, 0x1000\n"
"v_mfma_f32_16x16x32_bf16 a[252:255], v[60:63], v[200:203], 0\n"
"buffer_load_dwordx4 a[220:223], v1, s[48:51], s89 offen offset:2048 sc1\n"
"ds_write_b128 v2, v[100:103] offset:1024\n"
"v_mfma_f32_16x16x32_bf16 a[236:239], v[60:63], v[192:195], 0\n"
"s_waitcnt vmcnt(19)\n"
"v_mfma_f32_16x16x32_bf16 a[224:227], v[76:79], v[208:211], a[224:227]\n"
"v_mfma_f32_16x16x32_bf16 a[240:243], v[76:79], v[216:219], a[240:243]\n"
"buffer_load_dwordx4 v[192:195], v1, s[48:51], s60 offen offset:3072 sc1\n"
"v_mfma_f32_16x16x32_bf16 a[244:247], v[92:95], v[216:219], a[244:247]\n"
"buffer_load_dwordx4 v[200:203], v1, s[48:51], s61 offen offset:3072 sc1\n"
"ds_write_b128 v2, v[228:231] offset:2048\n"
"v_mfma_f32_16x16x32_bf16 a[228:231], v[92:95], v[208:211], a[228:231]\n"
"s_add_u32 s89, s60, 0x1000\n"
"v_mfma_f32_16x16x32_bf16 a[232:235], v[108:111], v[208:211], a[232:235]\n"
"buffer_load_dwordx4 v[36:39], v1, s[48:51], s89 offen offset:3072 sc1\n"
"v_mfma_f32_16x16x32_bf16 a[248:251], v[108:111], v[216:219], a[248:251]\n"
"s_add_u32 s89, s61, 0x1000\n"
"v_mfma_f32_16x16x32_bf16 a[252:255], v[124:127], v[216:219], a[252:255]\n"
"buffer_load_dwordx4 v[44:47], v1, s[48:51], s89 offen offset:3072 sc1\n"
"ds_write_b128 v2, v[164:167] offset:3072\n"
"v_mfma_f32_16x16x32_bf16 a[236:239], v[124:127], v[208:211], a[236:239]\n"
"s_waitcnt vmcnt(21)\n"
"v_mfma_f32_16x16x32_bf16 a[224:227], v[136:139], v[64:67], a[224:227]\n"
"s_add_u32 s89, s60, 0x1000\n"
"v_mfma_f32_16x16x32_bf16 a[240:243], v[136:139], v[72:75], a[240:243]\n"
"buffer_load_dwordx4 v[52:55], v1, s[48:51], s89 offen offset:0 sc1\n"
"s_add_u32 s89, s61, 0x1000\n"
"v_mfma_f32_16x16x32_bf16 a[244:247], v[144:147], v[72:75], a[244:247]\n"
"buffer_load_dwordx4 v[60:63], v1, s[48:51], s89 offen offset:0 sc1\n"
"ds_write_b128 v2, v[56:59] offset:4096\n"
"v_mfma_f32_16x16x32_bf16 a[228:231], v[144:147], v[64:67], a[228:231]\n"
"s_add_u32 s89, s60, 0x2000\n"
"v_mfma_f32_16x16x32_bf16 a[232:235], v[168:171], v[64:67], a[232:235]\n"
"buffer_load_dwordx4 v[208:211], v1, s[48:51], s89 offen offset:0 sc1\n"
"v_mfma_f32_16x16x32_bf16 a[248:251], v[168:171], v[72:75], a[248:251]\n"
"s_add_u32 s89, s61, 0x2000\n"
"v_mfma_f32_16x16x32_bf16 a[252:255], v[176:179], v[72:75], a[252:255]\n"
"buffer_load_dwordx4 v[216:219], v1, s[48:51], s89 offen offset:0 sc1\n"
"ds_write_b128 v2, v[116:119] offset:5120\n"
"v_mfma_f32_16x16x32_bf16 a[236:239], v[176:179], v[64:67], a[236:239]\n"
"s_waitcnt vmcnt(22)\n"
"v_mfma_f32_16x16x32_bf16 a[224:227], v[184:187], v[80:83], a[224:227]\n"
"s_add_u32 s89, s60, 0x1000\n"
"v_mfma_f32_16x16x32_bf16 a[240:243], v[184:187], v[88:91], a[240:243]\n"
"buffer_load_dwordx4 v[76:79], v1, s[48:51], s89 offen offset:1024 sc1\n"
"s_add_u32 s89, s61, 0x1000\n"
"v_mfma_f32_16x16x32_bf16 a[244:247], v[204:207], v[88:91], a[244:247]\n"
"buffer_load_dwordx4 v[92:95], v1, s[48:51], s89 offen offset:1024 sc1\n"
"ds_write_b128 v2, v[244:247] offset:6144\n"
"v_mfma_f32_16x16x32_bf16 a[228:231], v[204:207], v[80:83], a[228:231]\n"
"s_add_u32 s89, s60, 0x2000\n"
"v_mfma_f32_16x16x32_bf16 a[232:235], v[220:223], v[80:83], a[232:235]\n"
"buffer_load_dwordx4 v[108:111], v1, s[48:51], s89 offen offset:1024 sc1\n"
"v_mfma_f32_16x16x32_bf16 a[248:251], v[220:223], v[88:91], a[248:251]\n"
"s_add_u32 s89, s61, 0x2000\n"
"v_mfma_f32_16x16x32_bf16 a[252:255], v[188:191], v[88:91], a[252:255]\n"
"buffer_load_dwordx4 v[124:127], v1, s[48:51], s89 offen offset:1024 sc1\n"
"ds_write_b128 v2, v[180:183] offset:7168\n"
"v_mfma_f32_16x16x32_bf16 a[236:239], v[188:191], v[80:83], a[236:239]\n"
"s_waitcnt lgkmcnt(0)\n"
"s_barrier\n"
"ds_read_b128 v[128:131], v26 offset:0\n"
"ds_read_b128 v[172:175], v26 offset:8192\n"
"ds_read_b128 v[224:227], v26 offset:16384\n"
"ds_read_b128 v[232:235], v26 offset:24576\n"
"ds_read_b128 v[240:243], v26 offset:1024\n"
"ds_read_b128 v[248:251], v26 offset:9216\n"
"s_waitcnt lgkmcnt(2)\n"
"v_add_f32 v128, v128, v172\n"
"v_add_f32 v129, v129, v173\n"
"v_add_f32 v130, v130, v174\n"
"v_add_f32 v131, v131, v175\n"
"v_add_f32 v224, v224, v232\n"
"v_add_f32 v225, v225, v233\n"
"v_add_f32 v226, v226, v234\n"
"v_add_f32 v227, v227, v235\n"
"ds_read_b128 v[172:175], v26 offset:17408\n"
"ds_read_b128 v[232:235], v26 offset:25600\n"
"v_add_f32 v128, v128, v224\n"
"v_add_f32 v129, v129, v225\n"
"v_add_f32 v130, v130, v226\n"
"v_add_f32 v131, v131, v227\n"
"buffer_store_dwordx4 v[128:131], v1, s[48:51], s52 offen sc1\n"
"s_waitcnt lgkmcnt(0)\n"
"v_add_f32 v240, v240, v248\n"
"v_add_f32 v241, v241, v249\n"
"v_add_f32 v242, v242, v250\n"
"v_add_f32 v243, v243, v251\n"
"v_add_f32 v172, v172, v232\n"
"v_add_f32 v173, v173, v233\n"
"v_add_f32 v174, v174, v234\n"
"v_add_f32 v175, v175, v235\n"
"v_add_f32 v240, v240, v172\n"
"v_add_f32 v241, v241, v173\n"
"v_add_f32 v242, v242, v174\n"
"v_add_f32 v243, v243, v175\n"
"buffer_store_dwordx4 v[240:243], v1, s[48:51], s52 offen offset:1024 sc1\n"
"s_waitcnt vmcnt(26)\n"
"v_mfma_f32_16x16x32_bf16 a[224:227], v[32:35], v[96:99], a[224:227]\n"
"v_mfma_f32_16x16x32_bf16 a[240:243], v[32:35], v[104:107], a[240:243]\n"
"v_mfma_f32_16x16x32_bf16 a[244:247], v[68:71], v[104:107], a[244:247]\n"
"v_mfma_f32_16x16x32_bf16 a[228:231], v[68:71], v[96:99], a[228:231]\n"
"v_mfma_f32_16x16x32_bf16 a[232:235], v[196:199], v[96:99], a[232:235]\n"
"v_mfma_f32_16x16x32_bf16 a[248:251], v[196:199], v[104:107], a[248:251]\n"
"v_mfma_f32_16x16x32_bf16 a[252:255], v[132:135], v[104:107], a[252:255]\n"
"v_mfma_f32_16x16x32_bf16 a[236:239], v[132:135], v[96:99], a[236:239]\n"
"s_waitcnt vmcnt(23)\n"
"v_mfma_f32_16x16x32_bf16 a[224:227], v[40:43], v[112:115], a[224:227]\n"
"v_mfma_f32_16x16x32_bf16 a[240:243], v[40:43], v[120:123], a[240:243]\n"
"v_mfma_f32_16x16x32_bf16 a[244:247], v[84:87], v[120:123], a[244:247]\n"
"v_mfma_f32_16x16x32_bf16 a[228:231], v[84:87], v[112:115], a[228:231]\n"
"v_mfma_f32_16x16x32_bf16 a[232:235], v[212:215], v[112:115], a[232:235]\n"
"v_mfma_f32_16x16x32_bf16 a[248:251], v[212:215], v[120:123], a[248:251]\n"
"v_mfma_f32_16x16x32_bf16 a[252:255], v[148:151], v[120:123], a[252:255]\n"
"v_mfma_f32_16x16x32_bf16 a[236:239], v[148:151], v[112:115], a[236:239]\n"
"s_waitcnt vmcnt(20)\n"
"v_mfma_f32_16x16x32_bf16 a[224:227], a[128:131], a[192:195], a[224:227]\n"
"v_mfma_f32_16x16x32_bf16 a[240:243], a[128:131], a[196:199], a[240:243]\n"
"ds_read_b128 a[160:163], v2 offset:32768\n"
"v_mfma_f32_16x16x32_bf16 a[244:247], a[132:135], a[196:199], a[244:247]\n"
"ds_read_b128 a[164:167], v2 offset:33792\n"
"v_mfma_f32_16x16x32_bf16 a[228:231], a[132:135], a[192:195], a[228:231]\n"
"v_mfma_f32_16x16x32_bf16 a[232:235], a[136:139], a[192:195], a[232:235]\n"
"ds_read_b128 a[168:171], v2 offset:34816\n"
"v_mfma_f32_16x16x32_bf16 a[248:251], a[136:139], a[196:199], a[248:251]\n"
"v_mfma_f32_16x16x32_bf16 a[252:255], a[140:143], a[196:199], a[252:255]\n"
"ds_read_b128 a[172:175], v2 offset:35840\n"
"v_mfma_f32_16x16x32_bf16 a[236:239], a[140:143], a[192:195], a[236:239]\n"
"s_waitcnt vmcnt(18)\n"
"v_mfma_f32_16x16x32_bf16 a[224:227], a[144:147], a[200:203], a[224:227]\n"
"v_mfma_f32_16x16x32_bf16 a[240:243], a[144:147], a[204:207], a[240:243]\n"
"v_mfma_f32_16x16x32_bf16 a[244:247], a[148:151], a[204:207], a[244:247]\n"
"v_mfma_f32_16x16x32_bf16 a[228:231], a[148:151], a[200:203], a[228:231]\n"
"v_mfma_f32_16x16x32_bf16 a[232:235], a[152:155], a[200:203], a[232:235]\n"
"v_mfma_f32_16x16x32_bf16 a[248:251], a[152:155], a[204:207], a[248:251]\n"
"v_mfma_f32_16x16x32_bf16 a[252:255], a[156:159], a[204:207], a[252:255]\n"
"v_mfma_f32_16x16x32_bf16 a[236:239], a[156:159], a[200:203], a[236:239]\n"
"s_waitcnt vmcnt(16) lgkmcnt(0)\n"
"v_mfma_f32_16x16x32_bf16 a[224:227], a[160:163], a[208:211], a[224:227]\n"
"s_waitcnt vmcnt(0)\n"
"v_mfma_f32_16x16x32_bf16 a[240:243], a[160:163], a[212:215], a[240:243]\n"
"ds_read_b128 a[176:179], v2 offset:36864\n"
"v_mfma_f32_16x16x32_bf16 a[244:247], a[164:167], a[212:215], a[244:247]\n"
"ds_read_b128 a[180:183], v2 offset:37888\n"
"v_mfma_f32_16x16x32_bf16 a[228:231], a[164:167], a[208:211], a[228:231]\n"
"buffer_store_dwordx2 v[30:31], v29, s[48:51], s54 offen sc1\n"
"v_mfma_f32_16x16x32_bf16 a[232:235], a[168:171], a[208:211], a[232:235]\n"
"ds_read_b128 a[184:187], v2 offset:38912\n"
"v_mfma_f32_16x16x32_bf16 a[248:251], a[168:171], a[212:215], a[248:251]\n"
"v_mfma_f32_16x16x32_bf16 a[252:255], a[172:175], a[212:215], a[252:255]\n"
"ds_read_b128 a[188:191], v2 offset:39936\n"
"v_mfma_f32_16x16x32_bf16 a[236:239], a[172:175], a[208:211], a[236:239]\n"
"s_waitcnt vmcnt(15)\n"
"v_mfma_f32_16x16x32_bf16 a[224:227], a[0:3], a[216:219], a[224:227]\n"
"v_mfma_f32_16x16x32_bf16 a[240:243], a[0:3], a[220:223], a[240:243]\n"
"v_mfma_f32_16x16x32_bf16 a[244:247], a[4:7], a[220:223], a[244:247]\n"
"v_mfma_f32_16x16x32_bf16 a[228:231], a[4:7], a[216:219], a[228:231]\n"
"v_mfma_f32_16x16x32_bf16 a[232:235], a[8:11], a[216:219], a[232:235]\n"
"v_mfma_f32_16x16x32_bf16 a[248:251], a[8:11], a[220:223], a[248:251]\n"
"v_mfma_f32_16x16x32_bf16 a[252:255], a[12:15], a[220:223], a[252:255]\n"
"v_mfma_f32_16x16x32_bf16 a[236:239], a[12:15], a[216:219], a[236:239]\n"
"s_waitcnt vmcnt(13) lgkmcnt(0)\n"
"v_mfma_f32_16x16x32_bf16 a[224:227], a[176:179], v[192:195], a[224:227]\n"
"v_mfma_f32_16x16x32_bf16 a[240:243], a[176:179], v[200:203], a[240:243]\n"
"ds_read_b128 a[160:163], v3 offset:0\n"
"v_mfma_f32_16x16x32_bf16 a[244:247], a[180:183], v[200:203], a[244:247]\n"
"ds_read_b128 a[164:167], v3 offset:1024\n"
"v_mfma_f32_16x16x32_bf16 a[228:231], a[180:183], v[192:195], a[228:231]\n"
"v_mfma_f32_16x16x32_bf16 a[232:235], a[184:187], v[192:195], a[232:235]\n"
"ds_read_b128 a[168:171], v3 offset:2048\n"
"v_mfma_f32_16x16x32_bf16 a[248:251], a[184:187], v[200:203], a[248:251]\n"
"v_mfma_f32_16x16x32_bf16 a[252:255], a[188:191], v[200:203], a[252:255]\n"
"ds_read_b128 a[172:175], v3 offset:3072\n"
"v_mfma_f32_16x16x32_bf16 a[236:239], a[188:191], v[192:195], a[236:239]\n"
"s_waitcnt vmcnt(11)\n"
"v_mfma_f32_16x16x32_bf16 a[224:227], a[16:19], v[36:39], a[224:227]\n"
"v_mfma_f32_16x16x32_bf16 a[240:243], a[16:19], v[44:47], a[240:243]\n"
"v_mfma_f32_16x16x32_bf16 a[244:247], a[20:23], v[44:47], a[244:247]\n"
"v_mfma_f32_16x16x32_bf16 a[228:231], a[20:23], v[36:39], a[228:231]\n"
"v_mfma_f32_16x16x32_bf16 a[232:235], a[24:27], v[36:39], a[232:235]\n"
"v_mfma_f32_16x16x32_bf16 a[248:251], a[24:27], v[44:47], a[248:251]\n"
"v_mfma_f32_16x16x32_bf16 a[252:255], a[28:31], v[44:47], a[252:255]\n"
"v_mfma_f32_16x16x32_bf16 a[236:239], a[28:31], v[36:39], a[236:239]\n"
"buffer_load_dwordx2 v[22:23], v11, s[48:51], s55 offen sc1\n"
"s_waitcnt vmcnt(10) lgkmcnt(0)\n"
"v_mfma_f32_16x16x32_bf16 a[224:227], a[160:163], v[52:55], a[224:227]\n"
"v_mfma_f32_16x16x32_bf16 a[240:243], a[160:163], v[60:63], a[240:243]\n"
"ds_read_b128 a[176:179], v3 offset:4096\n"
"v_mfma_f32_16x16x32_bf16 a[244:247], a[164:167], v[60:63], a[244:247]\n"
"ds_read_b128 a[180:183], v3 offset:5120\n"
"v_mfma_f32_16x16x32_bf16 a[228:231], a[164:167], v[52:55], a[228:231]\n"
"v_mfma_f32_16x16x32_bf16 a[232:235], a[168:171], v[52:55], a[232:235]\n"
"ds_read_b128 a[184:187], v3 offset:6144\n"
"v_mfma_f32_16x16x32_bf16 a[248:251], a[168:171], v[60:63], a[248:251]\n"
"v_mfma_f32_16x16x32_bf16 a[252:255], a[172:175], v[60:63], a[252:255]\n"
"ds_read_b128 a[188:191], v3 offset:7168\n"
"v_mfma_f32_16x16x32_bf16 a[236:239], a[172:175], v[52:55], a[236:239]\n"
"s_waitcnt vmcnt(8)\n"
"v_mfma_f32_16x16x32_bf16 a[224:227], a[32:35], v[208:211], a[224:227]\n"
"v_mfma_f32_16x16x32_bf16 a[240:243], a[32:35], v[216:219], a[240:243]\n"
"v_mfma_f32_16x16x32_bf16 a[244:247], a[36:39], v[216:219], a[244:247]\n"
"v_mfma_f32_16x16x32_bf16 a[228:231], a[36:39], v[208:211], a[228:231]\n"
"v_mfma_f32_16x16x32_bf16 a[232:235], a[40:43], v[208:211], a[232:235]\n"
"v_mfma_f32_16x16x32_bf16 a[248:251], a[40:43], v[216:219], a[248:251]\n"
"v_mfma_f32_16x16x32_bf16 a[252:255], a[44:47], v[216:219], a[252:255]\n"
"v_mfma_f32_16x16x32_bf16 a[236:239], a[44:47], v[208:211], a[236:239]\n"
"s_waitcnt vmcnt(6) lgkmcnt(0)\n"
"v_mfma_f32_16x16x32_bf16 a[224:227], a[176:179], v[76:79], a[224:227]\n"
"v_mfma_f32_16x16x32_bf16 a[240:243], a[176:179], v[92:95], a[240:243]\n"
"v_mfma_f32_16x16x32_bf16 a[244:247], a[180:183], v[92:95], a[244:247]\n"
"v_mfma_f32_16x16x32_bf16 a[228:231], a[180:183], v[76:79], a[228:231]\n"
"v_mfma_f32_16x16x32_bf16 a[232:235], a[184:187], v[76:79], a[232:235]\n"
"v_mfma_f32_16x16x32_bf16 a[248:251], a[184:187], v[92:95], a[248:251]\n"
"v_mfma_f32_16x16x32_bf16 a[252:255], a[188:191], v[92:95], a[252:255]\n"
"v_mfma_f32_16x16x32_bf16 a[236:239], a[188:191], v[76:79], a[236:239]\n"
"s_waitcnt vmcnt(0)\n"
"v_cmp_eq_u64 vcc, s[68:69], v[22:23]\n"
"s_nop 0\n"
"s_cmp_eq_u64 vcc, -1\n"
"s_cbranch_scc0 L_xf_spin%=\n"
"LT_xf_ok%=:\n"
"buffer_load_dwordx4 v[32:35], v1, s[48:51], s53 offen offset:0 sc1\n"
"buffer_load_dwordx4 v[68:71], v1, s[48:51], s53 offen offset:1024 sc1\n"
"buffer_load_dwordx4 v[196:199], v1, s[48:51], s53 offen offset:2048 sc1\n"
"buffer_load_dwordx4 v[132:135], v1, s[48:51], s53 offen offset:3072 sc1\n"
"s_waitcnt vmcnt(8)\n"
"v_mfma_f32_16x16x32_bf16 a[224:227], a[48:51], v[108:111], a[224:227]\n"
"v_mfma_f32_16x16x32_bf16 a[240:243], a[48:51], v[124:127], a[240:243]\n"
"v_mfma_f32_16x16x32_bf16 a[244:247], a[52:55], v[124:127], a[244:247]\n"
"v_mfma_f32_16x16x32_bf16 a[228:231], a[52:55], v[108:111], a[228:231]\n"
"v_mfma_f32_16x16x32_bf16 a[232:235], a[56:59], v[108:111], a[232:235]\n"
"v_mfma_f32_16x16x32_bf16 a[248:251], a[56:59], v[124:127], a[248:251]\n"
"v_mfma_f32_16x16x32_bf16 a[252:255], a[60:63], v[124:127], a[252:255]\n"
"v_mfma_f32_16x16x32_bf16 a[236:239], a[60:63], v[108:111], a[236:239]\n"
"s_nop 0\n"
"ds_write_b128 v2, a[224:227] offset:32768\n"
"s_nop 1\n"
"ds_write_b128 v2, a[228:231] offset:33792\n"
"ds_write_b128 v2, a[232:235] offset:34816\n"
"s_nop 1\n"
"ds_write_b128 v2, a[236:239] offset:35840\n"
"ds_write_b128 v2, a[240:243] offset:36864\n"
"ds_write_b128 v2, a[244:247] offset:37888\n"
"ds_write_b128 v2, a[248:251] offset:38912\n"
"ds_write_b128 v2, a[252:255] offset:39936\n"
"v_lshlrev_b32 v76, 16, v140\n"
"v_and_b32 v77, 0xffff0000, v140\n"
"v_lshlrev_b32 v78, 16, v141\n"
"v_and_b32 v79, 0xffff0000, v141\n"
"v_lshlrev_b32 v92, 16, v142\n"
"v_and_b32 v93, 0xffff0000, v142\n"
"v_lshlrev_b32 v94, 16, v143\n"
"v_and_b32 v95, 0xffff0000, v143\n"
"v_lshlrev_b32 v108, 16, v156\n"
"v_and_b32 v109, 0xffff0000, v156\n"
"v_lshlrev_b32 v110, 16, v157\n"
"v_and_b32 v111, 0xffff0000, v157\n"
"v_lshlrev_b32 v124, 16, v158\n"
"v_and_b32 v125, 0xffff0000, v158\n"
"v_lshlrev_b32 v126, 16, v159\n"
"v_and_b32 v127, 0xffff0000, v159\n"
"s_waitcnt lgkmcnt(0)\n"
"s_barrier\n"
"ds_read_b128 v[64:67], v27 offset:0\n"
"ds_read_b128 v[72:75], v27 offset:8192\n"
"ds_read_b128 v[80:83], v27 offset:16384\n"
"ds_read_b128 v[88:91], v27 offset:24576\n"
"ds_read_b128 v[96:99], v27 offset:1024\n"
"ds_read_b128 v[104:107], v27 offset:9216\n"
"ds_read_b128 v[112:115], v27 offset:17408\n"
"ds_read_b128 v[120:123], v27 offset:25600\n"
"ds_read_b128 v[192:195], v27 offset:2048\n"
"ds_read_b128 v[200:203], v27 offset:10240\n"
"ds_read_b128 v[208:211], v27 offset:18432\n"
"ds_read_b128 v[216:219], v27 offset:26624\n"
"ds_read_b128 v[224:227], v27 offset:3072\n"
"ds_read_b128 v[232:235], v27 offset:11264\n"
"ds_read_b128 v[240:243], v27 offset:19456\n"
"ds_read_b128 v[248:251], v27 offset:27648\n"
"s_waitcnt vmcnt(3) lgkmcnt(12)\n"
"v_pk_add_f32 v[64:65], v[64:65], v[72:73]\n"
"v_pk_add_f32 v[66:67], v[66:67], v[74:75]\n"
"v_pk_add_f32 v[64:65], v[64:65], v[80:81]\n"
"v_pk_add_f32 v[66:67], v[66:67], v[82:83]\n"
"v_pk_add_f32 v[64:65], v[64:65], v[88:89]\n"
"v_pk_add_f32 v[66:67], v[66:67], v[90:91]\n"
"v_pk_add_f32 v[64:65], v[64:65], v[32:33]\n"
"v_pk_add_f32 v[66:67], v[66:67], v[34:35]\n"
"v_cvt_pk_bf16_f32 v64, v64, v65\n"
"v_cvt_pk_bf16_f32 v66, v66, v67\n"
"v_and_b32 v65, 0xffff0000, v64\n"
"v_lshlrev_b32 v64, 16, v64\n"
"v_and_b32 v67, 0xffff0000, v66\n"
"v_lshlrev_b32 v66, 16, v66\n"
"v_pk_mul_f32 v[64:65], v[64:65], v[236:237]\n"
"v_pk_mul_f32 v[66:67], v[66:67], v[238:239]\n"
"v_cvt_pk_bf16_f32 v64, v64, v65\n"
"v_cvt_pk_bf16_f32 v66, v66, v67\n"
"v_and_b32 v65, 0xffff0000, v64\n"
"v_lshlrev_b32 v64, 16, v64\n"
"v_and_b32 v67, 0xffff0000, v66\n"
"v_lshlrev_b32 v66, 16, v66\n"
"v_pk_add_f32 v[64:65], v[64:65], v[76:77]\n"
"v_pk_add_f32 v[66:67], v[66:67], v[78:79]\n"
"v_cvt_pk_bf16_f32 v64, v64, v65\n"
"v_cvt_pk_bf16_f32 v65, v66, v67\n"
"buffer_store_dwordx2 v[64:65], v6, s[56:59], s71 offen offset:0\n"
"s_waitcnt vmcnt(3) lgkmcnt(8)\n"
"v_pk_add_f32 v[96:97], v[96:97], v[104:105]\n"
"v_pk_add_f32 v[98:99], v[98:99], v[106:107]\n"
"v_pk_add_f32 v[96:97], v[96:97], v[112:113]\n"
"v_pk_add_f32 v[98:99], v[98:99], v[114:115]\n"
"v_pk_add_f32 v[96:97], v[96:97], v[120:121]\n"
"v_pk_add_f32 v[98:99], v[98:99], v[122:123]\n"
"v_pk_add_f32 v[96:97], v[96:97], v[68:69]\n"
"v_pk_add_f32 v[98:99], v[98:99], v[70:71]\n"
"v_cvt_pk_bf16_f32 v96, v96, v97\n"
"v_cvt_pk_bf16_f32 v98, v98, v99\n"
"v_and_b32 v97, 0xffff0000, v96\n"
"v_lshlrev_b32 v96, 16, v96\n"
"v_and_b32 v99, 0xffff0000, v98\n"
"v_lshlrev_b32 v98, 16, v98\n"
"v_pk_mul_f32 v[96:97], v[96:97], v[252:253]\n"
"v_pk_mul_f32 v[98:99], v[98:99], v[254:255]\n"
"v_cvt_pk_bf16_f32 v96, v96, v97\n"
"v_cvt_pk_bf16_f32 v98, v98, v99\n"
"v_and_b32 v97, 0xffff0000, v96\n"
"v_lshlrev_b32 v96, 16, v96\n"
"v_and_b32 v99, 0xffff0000, v98\n"
"v_lshlrev_b32 v98, 16, v98\n"
"v_pk_add_f32 v[96:97], v[96:97], v[92:93]\n"
"v_pk_add_f32 v[98:99], v[98:99], v[94:95]\n"
"v_cvt_pk_bf16_f32 v96, v96, v97\n"
"v_cvt_pk_bf16_f32 v97, v98, v99\n"
"buffer_store_dwordx2 v[96:97], v6, s[56:59], s71 offen offset:32\n"
"s_waitcnt vmcnt(3) lgkmcnt(4)\n"
"v_pk_add_f32 v[192:193], v[192:193], v[200:201]\n"
"v_pk_add_f32 v[194:195], v[194:195], v[202:203]\n"
"v_pk_add_f32 v[192:193], v[192:193], v[208:209]\n"
"v_pk_add_f32 v[194:195], v[194:195], v[210:211]\n"
"v_pk_add_f32 v[192:193], v[192:193], v[216:217]\n"
"v_pk_add_f32 v[194:195], v[194:195], v[218:219]\n"
"v_pk_add_f32 v[192:193], v[192:193], v[196:197]\n"
"v_pk_add_f32 v[194:195], v[194:195], v[198:199]\n"
"v_cvt_pk_bf16_f32 v192, v192, v193\n"
"v_cvt_pk_bf16_f32 v194, v194, v195\n"
"v_and_b32 v193, 0xffff0000, v192\n"
"v_lshlrev_b32 v192, 16, v192\n"
"v_and_b32 v195, 0xffff0000, v194\n"
"v_lshlrev_b32 v194, 16, v194\n"
"v_pk_mul_f32 v[192:193], v[192:193], v[152:153]\n"
"v_pk_mul_f32 v[194:195], v[194:195], v[154:155]\n"
"v_cvt_pk_bf16_f32 v192, v192, v193\n"
"v_cvt_pk_bf16_f32 v194, v194, v195\n"
"v_and_b32 v193, 0xffff0000, v192\n"
"v_lshlrev_b32 v192, 16, v192\n"
"v_and_b32 v195, 0xffff0000, v194\n"
"v_lshlrev_b32 v194, 16, v194\n"
"v_pk_add_f32 v[192:193], v[192:193], v[108:109]\n"
"v_pk_add_f32 v[194:195], v[194:195], v[110:111]\n"
"v_cvt_pk_bf16_f32 v192, v192, v193\n"
"v_cvt_pk_bf16_f32 v193, v194, v195\n"
"buffer_store_dwordx2 v[192:193], v6, s[56:59], s71 offen offset:64\n"
"s_waitcnt vmcnt(3) lgkmcnt(0)\n"
"v_pk_add_f32 v[224:225], v[224:225], v[232:233]\n"
"v_pk_add_f32 v[226:227], v[226:227], v[234:235]\n"
"v_pk_add_f32 v[224:225], v[224:225], v[240:241]\n"
"v_pk_add_f32 v[226:227], v[226:227], v[242:243]\n"
"v_pk_add_f32 v[224:225], v[224:225], v[248:249]\n"
"v_pk_add_f32 v[226:227], v[226:227], v[250:251]\n"
"v_pk_add_f32 v[224:225], v[224:225], v[132:133]\n"
"v_pk_add_f32 v[226:227], v[226:227], v[134:135]\n"
"v_cvt_pk_bf16_f32 v224, v224, v225\n"
"v_cvt_pk_bf16_f32 v226, v226, v227\n"
"v_and_b32 v225, 0xffff0000, v224\n"
"v_lshlrev_b32 v224, 16, v224\n"
"v_and_b32 v227, 0xffff0000, v226\n"
"v_lshlrev_b32 v226, 16, v226\n"
"v_pk_mul_f32 v[224:225], v[224:225], v[160:161]\n"
"v_pk_mul_f32 v[226:227], v[226:227], v[162:163]\n"
"v_cvt_pk_bf16_f32 v224, v224, v225\n"
"v_cvt_pk_bf16_f32 v226, v226, v227\n"
"v_and_b32 v225, 0xffff0000, v224\n"
"v_lshlrev_b32 v224, 16, v224\n"
"v_and_b32 v227, 0xffff0000, v226\n"
"v_lshlrev_b32 v226, 16, v226\n"
"v_pk_add_f32 v[224:225], v[224:225], v[124:125]\n"
"v_pk_add_f32 v[226:227], v[226:227], v[126:127]\n"
"v_cvt_pk_bf16_f32 v224, v224, v225\n"
"v_cvt_pk_bf16_f32 v225, v226, v227\n"
"buffer_store_dwordx2 v[224:225], v6, s[56:59], s71 offen offset:96\n"
"s_branch L_end_%=\n"
"L_pad0_%=:\n"
"s_branch L_padn0_%=\n"
"L_hand_%=:\n"
"s_cmp_eq_u32 s94, 2\n"
"s_cbranch_scc1 L_ev2_%=\n"
"s_cmp_eq_u32 s94, 3\n"
"s_cbranch_scc1 L_ev3_%=\n"
"s_cmp_eq_u32 s94, 1\n"
"s_cbranch_scc1 L_ev1_%=\n"
"s_mov_b32 s89, s95\n"
"s_mov_b32 s94, 1\n"
"s_lshl_b32 s88, s66, 1\n"
"s_sub_u32 s88, 11, s88\n"
"s_cbranch_scc1 L_ev1_%=\n"
"s_add_u32 s6, s6, 8\n"
"s_addc_u32 s7, s7, 0\n"
"s_setpc_b64 s[6:7]\n"
"L_pad1_%=:\n"
"s_branch L_padn1_%=\n"
"L_ev1_%=:\n"
"s_lshl_b32 s88, s32, 3\n"
"s_add_u32 s88, s88, 0x90800\n"
"s_cmp_eq_u32 s66, 0\n"
"s_cselect_b32 s88, s88, 0x40000000\n"
"v_mov_b32 v10, s68\n"
"v_mov_b32 v11, s69\n"
"buffer_store_dwordx2 v[10:11], v20, s[48:51], s88 offen sc1\n"
"buffer_load_dwordx2 v[18:19], v6, s[48:51], s34 offen sc1\n"
"s_add_u32 m0, s67, 0x20000\n"
"s_and_b32 s93, s65, 1\n"
"s_lshl_b32 s93, s93, 2\n"
"s_add_u32 s93, s93, s64\n"
"s_lshl_b32 s93, s93, 16\n"
"s_lshl_b32 s90, s3, 19\n"
"s_add_u32 s90, s90, s93\n"
"s_add_u32 s90, s90, 0x340e000\n"
"buffer_load_dwordx4 v1, s[48:51], s90 offen offset:0 sc1 lds\n"
"buffer_load_dwordx4 v1, s[48:51], s90 offen offset:1024 sc1 lds\n"
"buffer_load_dwordx4 v1, s[48:51], s90 offen offset:2048 sc1 lds\n"
"buffer_load_dwordx4 v1, s[48:51], s90 offen offset:3072 sc1 lds\n"
"s_mov_b32 s88, 1\n"
"s_mov_b32 s94, 2\n"
"s_add_u32 s6, s6, 8\n"
"s_addc_u32 s7, s7, 0\n"
"s_setpc_b64 s[6:7]\n"
"L_pad2_%=:\n"
"s_branch L_padn2_%=\n"
"L_ev2_%=:\n"
"v_cmp_eq_u64 vcc, s[68:69], v[18:19]\n"
"s_add_u32 m0, s67, 0x21000\n"
"s_and_b32 s93, s65, 1\n"
"s_lshl_b32 s93, s93, 2\n"
"s_add_u32 s93, s93, s64\n"
"s_lshl_b32 s93, s93, 16\n"
"s_lshl_b32 s90, s3, 19\n"
"s_add_u32 s90, s90, s93\n"
"s_add_u32 s90, s90, 0x340f000\n"
"buffer_load_dwordx4 v1, s[48:51], s90 offen offset:0 sc1 lds\n"
"buffer_load_dwordx4 v1, s[48:51], s90 offen offset:1024 sc1 lds\n"
"buffer_load_dwordx4 v1, s[48:51], s90 offen offset:2048 sc1 lds\n"
"buffer_load_dwordx4 v1, s[48:51], s90 offen offset:3072 sc1 lds\n"
"s_lshl_b32 s88, s66, 1\n"
"s_sub_u32 s88, 13, s88\n"
"s_mov_b32 s94, 3\n"
"s_cmp_eq_u64 vcc, -1\n"
"s_cbranch_scc0 L_ev2s_%=\n"
"L_ev2r_%=:\n"
"s_add_u32 s6, s6, 8\n"
"s_addc_u32 s7, s7, 0\n"
"s_setpc_b64 s[6:7]\n"
"L_pad3_%=:\n"
"s_branch L_padn3_%=\n"
"L_ev3_%=:\n"
"s_and_b32 s90, s65, 1\n"
"s_lshl_b32 s90, s90, 2\n"
"s_add_u32 s90, s90, s64\n"
"s_lshl_b32 s90, s90, 16\n"
"s_lshl_b32 s89, s3, 19\n"
"s_add_u32 s89, s89, s90\n"
"s_add_u32 s89, s89, 0x33fe000\n"
"s_sub_u32 s89, s89, s67\n"
"s_mov_b32 s91, 0x2000\n"
"s_mov_b32 s88, 0x7fffffff\n"
"s_lshl_b32 s34, s3, 5\n"
"s_add_u32 s34, s34, 0x90800\n"
"v_and_b32 v6, 24, v6\n"
"buffer_load_dwordx2 v[18:19], v6, s[48:51], s34 offen sc1\n"
"v_cmp_eq_u32 vcc, 0, v21\n"
"v_mov_b32 v27, -1\n"
"s_lshl_b32 s73, s66, 2\n"
"s_add_u32 s73, s73, s64\n"
"v_cndmask_b32 v27, v27, 0, vcc\n"
"s_lshl_b32 s73, s73, 8\n"
"s_lshl_b32 s74, s65, 3\n"
"s_add_u32 s73, s73, s74\n"
"s_add_u32 s73, s73, 0x92000\n"
"v_mov_b32 v30, s68\n"
"v_mov_b32 v31, s69\n"
"s_cmp_eq_u32 s35, 0\n"
"s_cselect_b32 s73, s73, 0x40000000\n"
"s_cmp_lt_u32 s66, 6\n"
"s_cselect_b32 s73, s73, 0x40000000\n"
"buffer_store_dwordx2 v[30:31], v27, s[48:51], s73 offen sc1\n"
"s_add_u32 s6, s6, 8\n"
"s_addc_u32 s7, s7, 0\n"
"s_setpc_b64 s[6:7]\n"
"L_ev2s_%=:\n"
"s_mov_b32 s93, 0x40000\n"
"L_ev2b_%=:\n"
"s_sleep 1\n"
"buffer_load_dwordx2 v[18:19], v6, s[48:51], s34 offen sc1\n"
"s_waitcnt vmcnt(0)\n"
"v_cmp_eq_u64 vcc, s[68:69], v[18:19]\n"
"s_nop 0\n"
"s_cmp_eq_u64 vcc, -1\n"
"s_cbranch_scc1 L_ev2r_%=\n"
"s_sub_u32 s93, s93, 1\n"
"s_cbranch_scc0 L_ev2b_%=\n"
"s_branch L_ev2r_%=\n"
"L_AB_%=:\n"
"s_mov_b32 s97, 0x18000\n"
"s_waitcnt lgkmcnt(5)\n"
"v_mfma_f32_16x16x32_bf16 v[64:67], a[128:131], v[32:35], v[204:207]\n"
"v_exp_f32 v112, v112\n"
"v_mfma_f32_16x16x32_bf16 v[64:67], a[132:135], v[68:71], v[64:67]\n"
"ds_read_b128 a[180:183], v9 offset:13312\n"
"v_mfma_f32_16x16x32_bf16 v[64:67], a[136:139], v[196:199], v[64:67]\n"
"v_exp_f32 v113, v113\n"
"v_mfma_f32_16x16x32_bf16 v[64:67], a[140:143], v[132:135], v[64:67]\n"
"v_cvt_pk_bf16_f32 v203, v106, v107\n"
"v_add_f32 v14, v14, v112\n"
"v_mfma_f32_16x16x32_bf16 v[72:75], a[140:143], v[148:151], v[220:223]\n"
"v_exp_f32 v114, v114\n"
"v_mfma_f32_16x16x32_bf16 v[72:75], a[136:139], v[212:215], v[72:75]\n"
"ds_read_b128 a[184:187], v9 offset:14336\n"
"v_add_f32 v14, v14, v113\n"
"v_mfma_f32_16x16x32_bf16 v[72:75], a[132:135], v[84:87], v[72:75]\n"
"v_exp_f32 v115, v115\n"
"v_mfma_f32_16x16x32_bf16 v[72:75], a[128:131], v[40:43], v[72:75]\n"
"v_cvt_pk_bf16_f32 v210, v112, v113\n"
"v_add_f32 v14, v14, v114\n"
"v_add_u32 v9, 0x0, v1\n"
"v_mfma_f32_16x16x32_bf16 v[80:83], a[128:131], v[48:51], v[236:239]\n"
"v_exp_f32 v120, v120\n"
"v_mfma_f32_16x16x32_bf16 v[80:83], a[132:135], v[100:103], v[80:83]\n"
"ds_read_b128 v[172:175], v1 offset:15360\n"
"v_add_f32 v14, v14, v115\n"
"v_mfma_f32_16x16x32_bf16 v[80:83], a[136:139], v[228:231], v[80:83]\n"
"v_exp_f32 v121, v121\n"
"v_mfma_f32_16x16x32_bf16 v[80:83], a[140:143], v[164:167], v[80:83]\n"
"v_cvt_pk_bf16_f32 v211, v114, v115\n"
"v_add_f32 v15, v15, v120\n"
"v_mfma_f32_16x16x32_bf16 v[152:155], v[128:131], v[192:195], v[152:155]\n"
"s_waitcnt vmcnt(8) lgkmcnt(1)\n"
"s_barrier\n"
"v_mfma_f32_16x16x32_bf16 v[88:91], a[140:143], v[180:183], v[252:255]\n"
"v_exp_f32 v122, v122\n"
"v_mfma_f32_16x16x32_bf16 v[88:91], a[136:139], v[244:247], v[88:91]\n"
"ds_read_b128 a[140:143], v1 offset:19456\n"
"v_add_f32 v15, v15, v121\n"
"v_mfma_f32_16x16x32_bf16 v[88:91], a[132:135], v[116:119], v[88:91]\n"
"v_exp_f32 v123, v123\n"
"v_mfma_f32_16x16x32_bf16 v[88:91], a[128:131], v[56:59], v[88:91]\n"
"v_cvt_pk_bf16_f32 v218, v120, v121\n"
"v_add_f32 v15, v15, v122\n"
"v_mfma_f32_16x16x32_bf16 v[96:99], a[144:147], v[32:35], v[204:207]\n"
"v_exp_f32 v64, v64\n"
"v_mfma_f32_16x16x32_bf16 v[96:99], a[148:151], v[68:71], v[96:99]\n"
"ds_read_b128 a[136:139], v1 offset:18432\n"
"v_add_f32 v15, v15, v123\n"
"v_mfma_f32_16x16x32_bf16 v[96:99], a[152:155], v[196:199], v[96:99]\n"
"v_exp_f32 v65, v65\n"
"v_mfma_f32_16x16x32_bf16 v[96:99], a[156:159], v[132:135], v[96:99]\n"
"v_cvt_pk_bf16_f32 v219, v122, v123\n"
"v_add_f32 v12, v12, v64\n"
"v_mfma_f32_16x16x32_bf16 v[104:107], a[156:159], v[148:151], v[220:223]\n"
"v_exp_f32 v66, v66\n"
"v_mfma_f32_16x16x32_bf16 v[104:107], a[152:155], v[212:215], v[104:107]\n"
"ds_read_b128 a[132:135], v1 offset:17408\n"
"v_add_f32 v12, v12, v65\n"
"v_mfma_f32_16x16x32_bf16 v[104:107], a[148:151], v[84:87], v[104:107]\n"
"v_exp_f32 v67, v67\n"
"v_mfma_f32_16x16x32_bf16 v[104:107], a[144:147], v[40:43], v[104:107]\n"
"v_cvt_pk_bf16_f32 v224, v64, v65\n"
"v_add_f32 v12, v12, v66\n"
"v_mfma_f32_16x16x32_bf16 v[112:115], a[144:147], v[48:51], v[236:239]\n"
"v_exp_f32 v72, v72\n"
"v_mfma_f32_16x16x32_bf16 v[112:115], a[148:151], v[100:103], v[112:115]\n"
"ds_read_b128 a[128:131], v1 offset:16384\n"
"v_add_f32 v12, v12, v67\n"
"v_mfma_f32_16x16x32_bf16 v[112:115], a[152:155], v[228:231], v[112:115]\n"
"v_exp_f32 v73, v73\n"
"v_mfma_f32_16x16x32_bf16 v[112:115], a[156:159], v[164:167], v[112:115]\n"
"v_cvt_pk_bf16_f32 v225, v66, v67\n"
"v_add_f32 v13, v13, v72\n"
"v_mfma_f32_16x16x32_bf16 v[120:123], a[156:159], v[180:183], v[252:255]\n"
"v_exp_f32 v74, v74\n"
"v_mfma_f32_16x16x32_bf16 v[120:123], a[152:155], v[244:247], v[120:123]\n"
"ds_read_b128 a[156:159], v1 offset:23552\n"
"v_add_f32 v13, v13, v73\n"
"v_mfma_f32_16x16x32_bf16 v[120:123], a[148:151], v[116:119], v[120:123]\n"
"v_exp_f32 v75, v75\n"
"v_mfma_f32_16x16x32_bf16 v[120:123], a[144:147], v[56:59], v[120:123]\n"
"v_cvt_pk_bf16_f32 v232, v72, v73\n"
"v_add_f32 v13, v13, v74\n"
"v_mfma_f32_16x16x32_bf16 a[192:195], a[160:163], v[192:195], a[192:195]\n"
"v_exp_f32 v80, v80\n"
"v_mfma_f32_16x16x32_bf16 a[224:227], a[160:163], v[200:203], a[224:227]\n"
"ds_read_b128 a[152:155], v1 offset:22528\n"
"v_add_f32 v13, v13, v75\n"
"v_mfma_f32_16x16x32_bf16 a[64:67], a[160:163], v[208:211], a[64:67]\n"
"v_exp_f32 v81, v81\n"
"v_mfma_f32_16x16x32_bf16 a[96:99], a[160:163], v[216:219], a[96:99]\n"
"v_cvt_pk_bf16_f32 v233, v74, v75\n"
"v_add_f32 v14, v14, v80\n"
"v_mfma_f32_16x16x32_bf16 a[100:103], a[164:167], v[216:219], a[100:103]\n"
"v_exp_f32 v82, v82\n"
"v_mfma_f32_16x16x32_bf16 a[68:71], a[164:167], v[208:211], a[68:71]\n"
"ds_read_b128 a[148:151], v1 offset:21504\n"
"v_add_f32 v14, v14, v81\n"
"v_mfma_f32_16x16x32_bf16 a[228:231], a[164:167], v[200:203], a[228:231]\n"
"v_exp_f32 v83, v83\n"
"v_mfma_f32_16x16x32_bf16 a[196:199], a[164:167], v[192:195], a[196:199]\n"
"v_cvt_pk_bf16_f32 v240, v80, v81\n"
"v_add_f32 v14, v14, v82\n"
"v_mfma_f32_16x16x32_bf16 a[200:203], a[168:171], v[192:195], a[200:203]\n"
"v_exp_f32 v88, v88\n"
"v_mov_b32 v5, v1\n"
"v_mfma_f32_16x16x32_bf16 v[160:163], v[128:131], v[200:203], v[160:163]\n"
"v_mfma_f32_16x16x32_bf16 a[232:235], a[168:171], v[200:203], a[232:235]\n"
"ds_read_b128 a[144:147], v1 offset:20480\n"
"v_add_f32 v14, v14, v83\n"
"v_mfma_f32_16x16x32_bf16 a[72:75], a[168:171], v[208:211], a[72:75]\n"
"v_exp_f32 v89, v89\n"
"v_mfma_f32_16x16x32_bf16 a[104:107], a[168:171], v[216:219], a[104:107]\n"
"v_cvt_pk_bf16_f32 v241, v82, v83\n"
"v_add_f32 v15, v15, v88\n"
"v_mfma_f32_16x16x32_bf16 a[108:111], a[172:175], v[216:219], a[108:111]\n"
"v_exp_f32 v90, v90\n"
"v_add_u32 v23, 0x8000, v1\n"
"v_mfma_f32_16x16x32_bf16 a[76:79], a[172:175], v[208:211], a[76:79]\n"
"ds_read_b128 a[160:163], v1 offset:8192\n"
"v_add_f32 v15, v15, v89\n"
"v_mfma_f32_16x16x32_bf16 a[236:239], a[172:175], v[200:203], a[236:239]\n"
"v_exp_f32 v91, v91\n"
"v_mfma_f32_16x16x32_bf16 a[204:207], a[172:175], v[192:195], a[204:207]\n"
"v_cvt_pk_bf16_f32 v248, v88, v89\n"
"v_add_f32 v15, v15, v90\n"
"v_mfma_f32_16x16x32_bf16 a[208:211], a[176:179], v[192:195], a[208:211]\n"
"v_exp_f32 v96, v96\n"
"v_mfma_f32_16x16x32_bf16 a[240:243], a[176:179], v[200:203], a[240:243]\n"
"ds_read_b128 a[164:167], v1 offset:9216\n"
"v_add_f32 v15, v15, v91\n"
"v_mfma_f32_16x16x32_bf16 a[80:83], a[176:179], v[208:211], a[80:83]\n"
"v_exp_f32 v97, v97\n"
"v_mfma_f32_16x16x32_bf16 a[112:115], a[176:179], v[216:219], a[112:115]\n"
"v_cvt_pk_bf16_f32 v249, v90, v91\n"
"v_add_f32 v12, v12, v96\n"
"v_mfma_f32_16x16x32_bf16 a[116:119], a[180:183], v[216:219], a[116:119]\n"
"v_exp_f32 v98, v98\n"
"v_mfma_f32_16x16x32_bf16 a[84:87], a[180:183], v[208:211], a[84:87]\n"
"ds_read_b128 a[168:171], v1 offset:10240\n"
"v_add_f32 v12, v12, v97\n"
"v_mfma_f32_16x16x32_bf16 a[244:247], a[180:183], v[200:203], a[244:247]\n"
"v_exp_f32 v99, v99\n"
"v_mfma_f32_16x16x32_bf16 a[212:215], a[180:183], v[192:195], a[212:215]\n"
"v_cvt_pk_bf16_f32 v226, v96, v97\n"
"v_add_f32 v12, v12, v98\n"
"v_mfma_f32_16x16x32_bf16 a[216:219], a[184:187], v[192:195], a[216:219]\n"
"v_exp_f32 v104, v104\n"
"v_mfma_f32_16x16x32_bf16 a[248:251], a[184:187], v[200:203], a[248:251]\n"
"ds_read_b128 a[172:175], v1 offset:11264\n"
"v_add_f32 v12, v12, v99\n"
"v_mfma_f32_16x16x32_bf16 a[88:91], a[184:187], v[208:211], a[88:91]\n"
"v_exp_f32 v105, v105\n"
"v_mfma_f32_16x16x32_bf16 a[120:123], a[184:187], v[216:219], a[120:123]\n"
"v_cvt_pk_bf16_f32 v227, v98, v99\n"
"v_add_f32 v13, v13, v104\n"
"v_mfma_f32_16x16x32_bf16 a[124:127], a[188:191], v[216:219], a[124:127]\n"
"v_exp_f32 v106, v106\n"
"v_mfma_f32_16x16x32_bf16 a[92:95], a[188:191], v[208:211], a[92:95]\n"
"ds_read_b128 a[176:179], v1 offset:12288\n"
"v_add_f32 v13, v13, v105\n"
"v_mfma_f32_16x16x32_bf16 a[252:255], a[188:191], v[200:203], a[252:255]\n"
"v_exp_f32 v107, v107\n"
"v_mfma_f32_16x16x32_bf16 a[220:223], a[188:191], v[192:195], a[220:223]\n"
"v_cvt_pk_bf16_f32 v234, v104, v105\n"
"v_add_f32 v13, v13, v106\n"
"s_cmp_eq_u32 s66, 7\n"
"s_cbranch_scc1 L_pre7_%=\n"
".p2align 6\n"
"L_A_%=:\n"
"s_waitcnt lgkmcnt(5)\n"
"v_mfma_f32_16x16x32_bf16 v[64:67], a[128:131], v[32:35], v[204:207]\n"
"v_exp_f32 v112, v112\n"
"v_mfma_f32_16x16x32_bf16 v[64:67], a[132:135], v[68:71], v[64:67]\n"
"ds_read_b128 a[180:183], v9 offset:13312\n"
"v_add_f32 v13, v13, v107\n"
"v_mfma_f32_16x16x32_bf16 v[64:67], a[136:139], v[196:199], v[64:67]\n"
"v_exp_f32 v113, v113\n"
"v_mfma_f32_16x16x32_bf16 v[64:67], a[140:143], v[132:135], v[64:67]\n"
"v_cvt_pk_bf16_f32 v235, v106, v107\n"
"v_add_f32 v14, v14, v112\n"
"v_mfma_f32_16x16x32_bf16 v[72:75], a[140:143], v[148:151], v[220:223]\n"
"v_exp_f32 v114, v114\n"
"v_mfma_f32_16x16x32_bf16 v[72:75], a[136:139], v[212:215], v[72:75]\n"
"ds_read_b128 a[184:187], v9 offset:14336\n"
"v_add_f32 v14, v14, v113\n"
"v_mfma_f32_16x16x32_bf16 v[72:75], a[132:135], v[84:87], v[72:75]\n"
"v_exp_f32 v115, v115\n"
"v_mfma_f32_16x16x32_bf16 v[72:75], a[128:131], v[40:43], v[72:75]\n"
"v_cvt_pk_bf16_f32 v242, v112, v113\n"
"v_add_f32 v14, v14, v114\n"
"v_add_u32 v9, 0x4000, v5\n"
"v_mfma_f32_16x16x32_bf16 v[80:83], a[128:131], v[48:51], v[236:239]\n"
"v_exp_f32 v120, v120\n"
"v_mfma_f32_16x16x32_bf16 v[80:83], a[132:135], v[100:103], v[80:83]\n"
"ds_read_b128 a[188:191], v5 offset:31744\n"
"v_add_f32 v14, v14, v115\n"
"v_mfma_f32_16x16x32_bf16 v[80:83], a[136:139], v[228:231], v[80:83]\n"
"v_exp_f32 v121, v121\n"
"v_mfma_f32_16x16x32_bf16 v[80:83], a[140:143], v[164:167], v[80:83]\n"
"v_cvt_pk_bf16_f32 v243, v114, v115\n"
"v_add_f32 v15, v15, v120\n"
"s_waitcnt lgkmcnt(1)\n"
"v_mfma_f32_16x16x32_bf16 v[88:91], a[140:143], v[180:183], v[252:255]\n"
"v_exp_f32 v122, v122\n"
"v_mfma_f32_16x16x32_bf16 v[88:91], a[136:139], v[244:247], v[88:91]\n"
"ds_read_b128 a[140:143], v23 offset:3072\n"
"v_add_f32 v15, v15, v121\n"
"v_mfma_f32_16x16x32_bf16 v[88:91], a[132:135], v[116:119], v[88:91]\n"
"v_exp_f32 v123, v123\n"
"v_mfma_f32_16x16x32_bf16 v[88:91], a[128:131], v[56:59], v[88:91]\n"
"v_cvt_pk_bf16_f32 v250, v120, v121\n"
"v_add_f32 v15, v15, v122\n"
"v_mfma_f32_16x16x32_bf16 v[96:99], a[144:147], v[32:35], v[204:207]\n"
"v_exp_f32 v64, v64\n"
"v_mfma_f32_16x16x32_bf16 v[96:99], a[148:151], v[68:71], v[96:99]\n"
"ds_read_b128 a[136:139], v23 offset:2048\n"
"v_add_f32 v15, v15, v123\n"
"v_mfma_f32_16x16x32_bf16 v[96:99], a[152:155], v[196:199], v[96:99]\n"
"v_exp_f32 v65, v65\n"
"v_mfma_f32_16x16x32_bf16 v[96:99], a[156:159], v[132:135], v[96:99]\n"
"v_cvt_pk_bf16_f32 v251, v122, v123\n"
"v_add_f32 v12, v12, v64\n"
"v_mfma_f32_16x16x32_bf16 v[104:107], a[156:159], v[148:151], v[220:223]\n"
"v_exp_f32 v66, v66\n"
"v_mfma_f32_16x16x32_bf16 v[104:107], a[152:155], v[212:215], v[104:107]\n"
"ds_read_b128 a[132:135], v23 offset:1024\n"
"v_add_f32 v12, v12, v65\n"
"v_mfma_f32_16x16x32_bf16 v[104:107], a[148:151], v[84:87], v[104:107]\n"
"v_exp_f32 v67, v67\n"
"s_getpc_b64 s[6:7]\n"
"s_sub_u32 s88, s88, 1\n"
"s_cbranch_scc1 L_hand_%=\n"
"s_add_u32 s89, s89, s91\n"
"s_add_u32 m0, s67, s97\n"
"v_mfma_f32_16x16x32_bf16 v[104:107], a[144:147], v[40:43], v[104:107]\n"
"buffer_load_dwordx4 v8, s[48:51], s89 offen offset:0 sc1 lds\n"
"v_cvt_pk_bf16_f32 v192, v64, v65\n"
"v_add_f32 v12, v12, v66\n"
"v_mfma_f32_16x16x32_bf16 v[112:115], a[144:147], v[48:51], v[236:239]\n"
"v_exp_f32 v72, v72\n"
"v_mfma_f32_16x16x32_bf16 v[112:115], a[148:151], v[100:103], v[112:115]\n"
"ds_read_b128 a[128:131], v23 offset:0\n"
"v_add_f32 v12, v12, v67\n"
"v_mfma_f32_16x16x32_bf16 v[112:115], a[152:155], v[228:231], v[112:115]\n"
"v_exp_f32 v73, v73\n"
"v_mfma_f32_16x16x32_bf16 v[112:115], a[156:159], v[164:167], v[112:115]\n"
"v_cvt_pk_bf16_f32 v193, v66, v67\n"
"v_add_f32 v13, v13, v72\n"
"v_mfma_f32_16x16x32_bf16 v[120:123], a[156:159], v[180:183], v[252:255]\n"
"v_exp_f32 v74, v74\n"
"v_mfma_f32_16x16x32_bf16 v[120:123], a[152:155], v[244:247], v[120:123]\n"
"ds_read_b128 a[156:159], v23 offset:7168\n"
"v_add_f32 v13, v13, v73\n"
"v_mfma_f32_16x16x32_bf16 v[120:123], a[148:151], v[116:119], v[120:123]\n"
"v_exp_f32 v75, v75\n"
"v_mfma_f32_16x16x32_bf16 v[120:123], a[144:147], v[56:59], v[120:123]\n"
"v_cvt_pk_bf16_f32 v200, v72, v73\n"
"v_add_f32 v13, v13, v74\n"
"v_mfma_f32_16x16x32_bf16 a[0:3], a[160:163], v[224:227], a[0:3]\n"
"v_exp_f32 v80, v80\n"
"v_mfma_f32_16x16x32_bf16 a[32:35], a[160:163], v[232:235], a[32:35]\n"
"ds_read_b128 a[152:155], v23 offset:6144\n"
"v_add_f32 v13, v13, v75\n"
"v_mfma_f32_16x16x32_bf16 a[64:67], a[160:163], v[240:243], a[64:67]\n"
"v_exp_f32 v81, v81\n"
"v_mfma_f32_16x16x32_bf16 a[96:99], a[160:163], v[248:251], a[96:99]\n"
"buffer_load_dwordx4 v8, s[48:51], s89 offen offset:1024 sc1 lds\n"
"v_cvt_pk_bf16_f32 v201, v74, v75\n"
"v_add_f32 v14, v14, v80\n"
"v_mfma_f32_16x16x32_bf16 a[100:103], a[164:167], v[248:251], a[100:103]\n"
"v_exp_f32 v82, v82\n"
"v_mfma_f32_16x16x32_bf16 a[68:71], a[164:167], v[240:243], a[68:71]\n"
"ds_read_b128 a[148:151], v23 offset:5120\n"
"v_add_f32 v14, v14, v81\n"
"v_mfma_f32_16x16x32_bf16 a[36:39], a[164:167], v[232:235], a[36:39]\n"
"v_exp_f32 v83, v83\n"
"v_mfma_f32_16x16x32_bf16 a[4:7], a[164:167], v[224:227], a[4:7]\n"
"v_cvt_pk_bf16_f32 v208, v80, v81\n"
"v_add_f32 v14, v14, v82\n"
"v_mfma_f32_16x16x32_bf16 a[8:11], a[168:171], v[224:227], a[8:11]\n"
"v_exp_f32 v88, v88\n"
"v_mfma_f32_16x16x32_bf16 a[40:43], a[168:171], v[232:235], a[40:43]\n"
"ds_read_b128 a[144:147], v23 offset:4096\n"
"v_add_f32 v14, v14, v83\n"
"v_mfma_f32_16x16x32_bf16 a[72:75], a[168:171], v[240:243], a[72:75]\n"
"v_exp_f32 v89, v89\n"
"v_mfma_f32_16x16x32_bf16 a[104:107], a[168:171], v[248:251], a[104:107]\n"
"v_cvt_pk_bf16_f32 v209, v82, v83\n"
"v_add_f32 v15, v15, v88\n"
"v_mfma_f32_16x16x32_bf16 a[108:111], a[172:175], v[248:251], a[108:111]\n"
"v_exp_f32 v90, v90\n"
"v_mfma_f32_16x16x32_bf16 a[76:79], a[172:175], v[240:243], a[76:79]\n"
"ds_read_b128 a[160:163], v5 offset:24576\n"
"v_add_f32 v15, v15, v89\n"
"v_mfma_f32_16x16x32_bf16 a[44:47], a[172:175], v[232:235], a[44:47]\n"
"v_exp_f32 v91, v91\n"
"v_mfma_f32_16x16x32_bf16 a[12:15], a[172:175], v[224:227], a[12:15]\n"
"buffer_load_dwordx4 v8, s[48:51], s89 offen offset:2048 sc1 lds\n"
"v_cvt_pk_bf16_f32 v216, v88, v89\n"
"v_add_f32 v15, v15, v90\n"
"v_mfma_f32_16x16x32_bf16 a[16:19], a[176:179], v[224:227], a[16:19]\n"
"v_exp_f32 v96, v96\n"
"v_mfma_f32_16x16x32_bf16 a[48:51], a[176:179], v[232:235], a[48:51]\n"
"ds_read_b128 a[164:167], v5 offset:25600\n"
"v_add_f32 v15, v15, v91\n"
"v_mfma_f32_16x16x32_bf16 a[80:83], a[176:179], v[240:243], a[80:83]\n"
"v_exp_f32 v97, v97\n"
"v_mfma_f32_16x16x32_bf16 a[112:115], a[176:179], v[248:251], a[112:115]\n"
"v_cvt_pk_bf16_f32 v217, v90, v91\n"
"v_add_f32 v12, v12, v96\n"
"v_mfma_f32_16x16x32_bf16 a[116:119], a[180:183], v[248:251], a[116:119]\n"
"v_exp_f32 v98, v98\n"
"v_mfma_f32_16x16x32_bf16 a[84:87], a[180:183], v[240:243], a[84:87]\n"
"ds_read_b128 a[168:171], v5 offset:26624\n"
"v_add_f32 v12, v12, v97\n"
"v_mfma_f32_16x16x32_bf16 a[52:55], a[180:183], v[232:235], a[52:55]\n"
"v_exp_f32 v99, v99\n"
"v_mfma_f32_16x16x32_bf16 a[20:23], a[180:183], v[224:227], a[20:23]\n"
"v_cvt_pk_bf16_f32 v194, v96, v97\n"
"v_add_f32 v12, v12, v98\n"
"v_mfma_f32_16x16x32_bf16 a[24:27], a[184:187], v[224:227], a[24:27]\n"
"v_exp_f32 v104, v104\n"
"v_mfma_f32_16x16x32_bf16 a[56:59], a[184:187], v[232:235], a[56:59]\n"
"ds_read_b128 a[172:175], v5 offset:27648\n"
"v_add_f32 v12, v12, v99\n"
"v_mfma_f32_16x16x32_bf16 a[88:91], a[184:187], v[240:243], a[88:91]\n"
"v_exp_f32 v105, v105\n"
"v_mfma_f32_16x16x32_bf16 a[120:123], a[184:187], v[248:251], a[120:123]\n"
"v_cvt_pk_bf16_f32 v195, v98, v99\n"
"v_add_f32 v13, v13, v104\n"
"v_mfma_f32_16x16x32_bf16 a[124:127], v[172:175], v[248:251], a[124:127]\n"
"v_exp_f32 v106, v106\n"
"v_mfma_f32_16x16x32_bf16 a[92:95], v[172:175], v[240:243], a[92:95]\n"
"ds_read_b128 a[176:179], v5 offset:28672\n"
"v_add_f32 v13, v13, v105\n"
"v_mfma_f32_16x16x32_bf16 a[60:63], v[172:175], v[232:235], a[60:63]\n"
"v_exp_f32 v107, v107\n"
"v_mfma_f32_16x16x32_bf16 a[28:31], v[172:175], v[224:227], a[28:31]\n"
"buffer_load_dwordx4 v8, s[48:51], s89 offen offset:3072 sc1 lds\n"
"v_cvt_pk_bf16_f32 v202, v104, v105\n"
"v_add_f32 v13, v13, v106\n"
"s_sub_u32 s96, s96, 1\n"
"s_cmp_eq_u32 s96, 0\n"
"s_cbranch_scc1 L_Aexit_%=\n"
"s_waitcnt lgkmcnt(5)\n"
"v_mfma_f32_16x16x32_bf16 v[64:67], a[128:131], v[32:35], v[204:207]\n"
"v_exp_f32 v112, v112\n"
"v_mfma_f32_16x16x32_bf16 v[64:67], a[132:135], v[68:71], v[64:67]\n"
"ds_read_b128 a[180:183], v9 offset:13312\n"
"v_add_f32 v13, v13, v107\n"
"v_mfma_f32_16x16x32_bf16 v[64:67], a[136:139], v[196:199], v[64:67]\n"
"v_exp_f32 v113, v113\n"
"v_mfma_f32_16x16x32_bf16 v[64:67], a[140:143], v[132:135], v[64:67]\n"
"v_cvt_pk_bf16_f32 v203, v106, v107\n"
"v_add_f32 v14, v14, v112\n"
"v_mfma_f32_16x16x32_bf16 v[72:75], a[140:143], v[148:151], v[220:223]\n"
"v_exp_f32 v114, v114\n"
"v_mfma_f32_16x16x32_bf16 v[72:75], a[136:139], v[212:215], v[72:75]\n"
"ds_read_b128 a[184:187], v9 offset:14336\n"
"v_add_f32 v14, v14, v113\n"
"v_mfma_f32_16x16x32_bf16 v[72:75], a[132:135], v[84:87], v[72:75]\n"
"v_exp_f32 v115, v115\n"
"v_mfma_f32_16x16x32_bf16 v[72:75], a[128:131], v[40:43], v[72:75]\n"
"v_cvt_pk_bf16_f32 v210, v112, v113\n"
"v_add_f32 v14, v14, v114\n"
"v_add_u32 v9, 0x0, v23\n"
"v_mfma_f32_16x16x32_bf16 v[80:83], a[128:131], v[48:51], v[236:239]\n"
"v_exp_f32 v120, v120\n"
"v_mfma_f32_16x16x32_bf16 v[80:83], a[132:135], v[100:103], v[80:83]\n"
"ds_read_b128 v[172:175], v23 offset:15360\n"
"v_add_f32 v14, v14, v115\n"
"v_mfma_f32_16x16x32_bf16 v[80:83], a[136:139], v[228:231], v[80:83]\n"
"v_exp_f32 v121, v121\n"
"v_mfma_f32_16x16x32_bf16 v[80:83], a[140:143], v[164:167], v[80:83]\n"
"v_cvt_pk_bf16_f32 v211, v114, v115\n"
"v_add_f32 v15, v15, v120\n"
"s_waitcnt vmcnt(4) lgkmcnt(1)\n"
"s_barrier\n"
"v_mfma_f32_16x16x32_bf16 v[88:91], a[140:143], v[180:183], v[252:255]\n"
"v_exp_f32 v122, v122\n"
"v_mfma_f32_16x16x32_bf16 v[88:91], a[136:139], v[244:247], v[88:91]\n"
"ds_read_b128 a[140:143], v23 offset:19456\n"
"v_add_f32 v15, v15, v121\n"
"v_mfma_f32_16x16x32_bf16 v[88:91], a[132:135], v[116:119], v[88:91]\n"
"v_exp_f32 v123, v123\n"
"v_mfma_f32_16x16x32_bf16 v[88:91], a[128:131], v[56:59], v[88:91]\n"
"v_cvt_pk_bf16_f32 v218, v120, v121\n"
"v_add_f32 v15, v15, v122\n"
"v_mfma_f32_16x16x32_bf16 v[96:99], a[144:147], v[32:35], v[204:207]\n"
"v_exp_f32 v64, v64\n"
"v_mfma_f32_16x16x32_bf16 v[96:99], a[148:151], v[68:71], v[96:99]\n"
"ds_read_b128 a[136:139], v23 offset:18432\n"
"v_add_f32 v15, v15, v123\n"
"v_mfma_f32_16x16x32_bf16 v[96:99], a[152:155], v[196:199], v[96:99]\n"
"v_exp_f32 v65, v65\n"
"v_mfma_f32_16x16x32_bf16 v[96:99], a[156:159], v[132:135], v[96:99]\n"
"v_cvt_pk_bf16_f32 v219, v122, v123\n"
"v_add_f32 v12, v12, v64\n"
"v_mfma_f32_16x16x32_bf16 v[104:107], a[156:159], v[148:151], v[220:223]\n"
"v_exp_f32 v66, v66\n"
"v_mfma_f32_16x16x32_bf16 v[104:107], a[152:155], v[212:215], v[104:107]\n"
"ds_read_b128 a[132:135], v23 offset:17408\n"
"v_add_f32 v12, v12, v65\n"
"v_mfma_f32_16x16x32_bf16 v[104:107], a[148:151], v[84:87], v[104:107]\n"
"v_exp_f32 v67, v67\n"
"s_add_u32 s90, s89, 0x1000\n"
"s_add_u32 m0, s70, s97\n"
"v_mfma_f32_16x16x32_bf16 v[104:107], a[144:147], v[40:43], v[104:107]\n"
"buffer_load_dwordx4 v8, s[48:51], s90 offen offset:0 sc1 lds\n"
"v_cvt_pk_bf16_f32 v224, v64, v65\n"
"v_add_f32 v12, v12, v66\n"
"v_mfma_f32_16x16x32_bf16 v[112:115], a[144:147], v[48:51], v[236:239]\n"
"v_exp_f32 v72, v72\n"
"v_mfma_f32_16x16x32_bf16 v[112:115], a[148:151], v[100:103], v[112:115]\n"
"ds_read_b128 a[128:131], v23 offset:16384\n"
"v_add_f32 v12, v12, v67\n"
"v_mfma_f32_16x16x32_bf16 v[112:115], a[152:155], v[228:231], v[112:115]\n"
"v_exp_f32 v73, v73\n"
"v_mfma_f32_16x16x32_bf16 v[112:115], a[156:159], v[164:167], v[112:115]\n"
"v_cvt_pk_bf16_f32 v225, v66, v67\n"
"v_add_f32 v13, v13, v72\n"
"v_mfma_f32_16x16x32_bf16 v[120:123], a[156:159], v[180:183], v[252:255]\n"
"v_exp_f32 v74, v74\n"
"v_mfma_f32_16x16x32_bf16 v[120:123], a[152:155], v[244:247], v[120:123]\n"
"ds_read_b128 a[156:159], v23 offset:23552\n"
"v_add_f32 v13, v13, v73\n"
"v_mfma_f32_16x16x32_bf16 v[120:123], a[148:151], v[116:119], v[120:123]\n"
"v_exp_f32 v75, v75\n"
"v_mfma_f32_16x16x32_bf16 v[120:123], a[144:147], v[56:59], v[120:123]\n"
"v_cvt_pk_bf16_f32 v232, v72, v73\n"
"v_add_f32 v13, v13, v74\n"
"v_mfma_f32_16x16x32_bf16 a[0:3], a[160:163], v[192:195], a[0:3]\n"
"v_exp_f32 v80, v80\n"
"v_mfma_f32_16x16x32_bf16 a[32:35], a[160:163], v[200:203], a[32:35]\n"
"ds_read_b128 a[152:155], v23 offset:22528\n"
"v_add_f32 v13, v13, v75\n"
"v_mfma_f32_16x16x32_bf16 a[64:67], a[160:163], v[208:211], a[64:67]\n"
"v_exp_f32 v81, v81\n"
"v_mfma_f32_16x16x32_bf16 a[96:99], a[160:163], v[216:219], a[96:99]\n"
"buffer_load_dwordx4 v8, s[48:51], s90 offen offset:1024 sc1 lds\n"
"v_cvt_pk_bf16_f32 v233, v74, v75\n"
"v_add_f32 v14, v14, v80\n"
"v_mfma_f32_16x16x32_bf16 a[100:103], a[164:167], v[216:219], a[100:103]\n"
"v_exp_f32 v82, v82\n"
"v_mfma_f32_16x16x32_bf16 a[68:71], a[164:167], v[208:211], a[68:71]\n"
"ds_read_b128 a[148:151], v23 offset:21504\n"
"v_add_f32 v14, v14, v81\n"
"v_mfma_f32_16x16x32_bf16 a[36:39], a[164:167], v[200:203], a[36:39]\n"
"v_exp_f32 v83, v83\n"
"v_mfma_f32_16x16x32_bf16 a[4:7], a[164:167], v[192:195], a[4:7]\n"
"v_cvt_pk_bf16_f32 v240, v80, v81\n"
"v_add_f32 v14, v14, v82\n"
"v_mfma_f32_16x16x32_bf16 a[8:11], a[168:171], v[192:195], a[8:11]\n"
"v_exp_f32 v88, v88\n"
"v_mov_b32 v5, v23\n"
"v_mfma_f32_16x16x32_bf16 a[40:43], a[168:171], v[200:203], a[40:43]\n"
"ds_read_b128 a[144:147], v23 offset:20480\n"
"v_add_f32 v14, v14, v83\n"
"v_mfma_f32_16x16x32_bf16 a[72:75], a[168:171], v[208:211], a[72:75]\n"
"v_exp_f32 v89, v89\n"
"v_mfma_f32_16x16x32_bf16 a[104:107], a[168:171], v[216:219], a[104:107]\n"
"v_cvt_pk_bf16_f32 v241, v82, v83\n"
"v_add_f32 v15, v15, v88\n"
"v_mfma_f32_16x16x32_bf16 a[108:111], a[172:175], v[216:219], a[108:111]\n"
"v_exp_f32 v90, v90\n"
"v_mfma_f32_16x16x32_bf16 a[76:79], a[172:175], v[208:211], a[76:79]\n"
"ds_read_b128 a[160:163], v23 offset:8192\n"
"v_add_f32 v15, v15, v89\n"
"v_mfma_f32_16x16x32_bf16 a[44:47], a[172:175], v[200:203], a[44:47]\n"
"v_exp_f32 v91, v91\n"
"v_mfma_f32_16x16x32_bf16 a[12:15], a[172:175], v[192:195], a[12:15]\n"
"buffer_load_dwordx4 v8, s[48:51], s90 offen offset:2048 sc1 lds\n"
"v_cvt_pk_bf16_f32 v248, v88, v89\n"
"v_add_f32 v15, v15, v90\n"
"v_mfma_f32_16x16x32_bf16 a[16:19], a[176:179], v[192:195], a[16:19]\n"
"v_exp_f32 v96, v96\n"
"v_mfma_f32_16x16x32_bf16 a[48:51], a[176:179], v[200:203], a[48:51]\n"
"ds_read_b128 a[164:167], v23 offset:9216\n"
"v_add_f32 v15, v15, v91\n"
"v_mfma_f32_16x16x32_bf16 a[80:83], a[176:179], v[208:211], a[80:83]\n"
"v_exp_f32 v97, v97\n"
"v_mfma_f32_16x16x32_bf16 a[112:115], a[176:179], v[216:219], a[112:115]\n"
"v_cvt_pk_bf16_f32 v249, v90, v91\n"
"v_add_f32 v12, v12, v96\n"
"v_mfma_f32_16x16x32_bf16 a[116:119], a[180:183], v[216:219], a[116:119]\n"
"v_exp_f32 v98, v98\n"
"v_mfma_f32_16x16x32_bf16 a[84:87], a[180:183], v[208:211], a[84:87]\n"
"ds_read_b128 a[168:171], v23 offset:10240\n"
"v_add_f32 v12, v12, v97\n"
"v_mfma_f32_16x16x32_bf16 a[52:55], a[180:183], v[200:203], a[52:55]\n"
"v_exp_f32 v99, v99\n"
"v_mfma_f32_16x16x32_bf16 a[20:23], a[180:183], v[192:195], a[20:23]\n"
"v_cvt_pk_bf16_f32 v226, v96, v97\n"
"v_add_f32 v12, v12, v98\n"
"v_mfma_f32_16x16x32_bf16 a[24:27], a[184:187], v[192:195], a[24:27]\n"
"v_exp_f32 v104, v104\n"
"v_mfma_f32_16x16x32_bf16 a[56:59], a[184:187], v[200:203], a[56:59]\n"
"ds_read_b128 a[172:175], v23 offset:11264\n"
"v_add_f32 v12, v12, v99\n"
"v_mfma_f32_16x16x32_bf16 a[88:91], a[184:187], v[208:211], a[88:91]\n"
"v_exp_f32 v105, v105\n"
"s_add_u32 s97, s97, 0x8000\n"
"v_mfma_f32_16x16x32_bf16 a[120:123], a[184:187], v[216:219], a[120:123]\n"
"v_cvt_pk_bf16_f32 v227, v98, v99\n"
"v_add_f32 v13, v13, v104\n"
"v_mfma_f32_16x16x32_bf16 a[124:127], a[188:191], v[216:219], a[124:127]\n"
"v_exp_f32 v106, v106\n"
"s_and_b32 s97, s97, 0x1ffff\n"
"v_mfma_f32_16x16x32_bf16 a[92:95], a[188:191], v[208:211], a[92:95]\n"
"ds_read_b128 a[176:179], v23 offset:12288\n"
"v_add_f32 v13, v13, v105\n"
"v_mfma_f32_16x16x32_bf16 a[60:63], a[188:191], v[200:203], a[60:63]\n"
"v_exp_f32 v107, v107\n"
"v_add_u32 v23, 0x8000, v23\n"
"v_mfma_f32_16x16x32_bf16 a[28:31], a[188:191], v[192:195], a[28:31]\n"
"buffer_load_dwordx4 v8, s[48:51], s90 offen offset:3072 sc1 lds\n"
"v_cvt_pk_bf16_f32 v234, v104, v105\n"
"v_add_f32 v13, v13, v106\n"
"v_and_b32 v23, 0x1ffff, v23\n"
"s_cmp_eq_u32 s96, 1\n"
"s_cbranch_scc0 L_A_%=\n"
"v_add_u32 v23, s72, v23\n"
"s_branch L_A_%=\n"
"L_Aexit_%=:\n"
"s_cmp_eq_u32 s66, 7\n"
"s_cbranch_scc1 L_drainA_%=\n"
"s_lshl_b32 s96, s66, 1\n"
"s_sub_u32 s96, 13, s96\n"
"s_and_b32 s73, s66, 1\n"
"s_cbranch_scc0 L_BA0_%=\n"
"s_waitcnt lgkmcnt(5)\n"
"v_mfma_f32_16x16x32_bf16 v[64:67], a[128:131], v[36:39], v[140:143]\n"
"v_exp_f32 v112, v112\n"
"v_mfma_f32_16x16x32_bf16 v[64:67], a[132:135], v[44:47], v[64:67]\n"
"ds_read_b128 a[180:183], v9 offset:13312\n"
"v_add_f32 v13, v13, v107\n"
"v_mfma_f32_16x16x32_bf16 v[64:67], a[136:139], v[52:55], v[64:67]\n"
"v_exp_f32 v113, v113\n"
"v_mfma_f32_16x16x32_bf16 v[64:67], a[140:143], v[60:63], v[64:67]\n"
"v_cvt_pk_bf16_f32 v203, v106, v107\n"
"v_add_f32 v14, v14, v112\n"
"v_mfma_f32_16x16x32_bf16 v[72:75], a[140:143], v[124:127], v[156:159]\n"
"v_exp_f32 v114, v114\n"
"v_mfma_f32_16x16x32_bf16 v[72:75], a[136:139], v[108:111], v[72:75]\n"
"ds_read_b128 a[184:187], v9 offset:14336\n"
"v_add_f32 v14, v14, v113\n"
"v_mfma_f32_16x16x32_bf16 v[72:75], a[132:135], v[92:95], v[72:75]\n"
"v_exp_f32 v115, v115\n"
"v_mfma_f32_16x16x32_bf16 v[72:75], a[128:131], v[76:79], v[72:75]\n"
"v_cvt_pk_bf16_f32 v210, v112, v113\n"
"v_add_f32 v14, v14, v114\n"
"v_add_u32 v9, 0x0, v4\n"
"v_mfma_f32_16x16x32_bf16 v[80:83], a[128:131], v[48:51], v[236:239]\n"
"v_exp_f32 v120, v120\n"
"v_mfma_f32_16x16x32_bf16 v[80:83], a[132:135], v[100:103], v[80:83]\n"
"ds_read_b128 v[172:175], v4 offset:15360\n"
"v_add_f32 v14, v14, v115\n"
"v_mfma_f32_16x16x32_bf16 v[80:83], a[136:139], v[228:231], v[80:83]\n"
"v_exp_f32 v121, v121\n"
"v_mfma_f32_16x16x32_bf16 v[80:83], a[140:143], v[164:167], v[80:83]\n"
"v_cvt_pk_bf16_f32 v211, v114, v115\n"
"v_add_f32 v15, v15, v120\n"
"s_waitcnt vmcnt(4) lgkmcnt(1)\n"
"s_barrier\n"
"v_mfma_f32_16x16x32_bf16 v[88:91], a[140:143], v[180:183], v[252:255]\n"
"v_exp_f32 v122, v122\n"
"v_mfma_f32_16x16x32_bf16 v[88:91], a[136:139], v[244:247], v[88:91]\n"
"ds_read_b128 a[140:143], v4 offset:35840\n"
"v_add_f32 v15, v15, v121\n"
"v_mfma_f32_16x16x32_bf16 v[88:91], a[132:135], v[116:119], v[88:91]\n"
"v_exp_f32 v123, v123\n"
"s_add_u32 s90, s89, 0x1000\n"
"s_add_u32 m0, s67, 0x1000\n"
"v_mfma_f32_16x16x32_bf16 v[88:91], a[128:131], v[56:59], v[88:91]\n"
"buffer_load_dwordx4 v8, s[48:51], s90 offen offset:0 sc1 lds\n"
"v_cvt_pk_bf16_f32 v218, v120, v121\n"
"v_add_f32 v15, v15, v122\n"
"v_mfma_f32_16x16x32_bf16 v[96:99], a[144:147], v[36:39], v[140:143]\n"
"v_exp_f32 v64, v64\n"
"v_mfma_f32_16x16x32_bf16 v[96:99], a[148:151], v[44:47], v[96:99]\n"
"ds_read_b128 a[136:139], v4 offset:34816\n"
"v_add_f32 v15, v15, v123\n"
"v_mfma_f32_16x16x32_bf16 v[96:99], a[152:155], v[52:55], v[96:99]\n"
"v_exp_f32 v65, v65\n"
"v_mfma_f32_16x16x32_bf16 v[96:99], a[156:159], v[60:63], v[96:99]\n"
"ds_read_b128 a[132:135], v4 offset:33792\n"
"v_cvt_pk_bf16_f32 v219, v122, v123\n"
"v_mfma_f32_16x16x32_bf16 v[104:107], a[156:159], v[124:127], v[156:159]\n"
"v_exp_f32 v66, v66\n"
"v_mfma_f32_16x16x32_bf16 v[104:107], a[152:155], v[108:111], v[104:107]\n"
"buffer_load_dwordx4 v8, s[48:51], s90 offen offset:1024 sc1 lds\n"
"v_mfma_f32_16x16x32_bf16 v[104:107], a[148:151], v[92:95], v[104:107]\n"
"v_exp_f32 v67, v67\n"
"v_mfma_f32_16x16x32_bf16 v[104:107], a[144:147], v[76:79], v[104:107]\n"
"ds_read_b128 a[128:131], v4 offset:32768\n"
"v_cvt_pk_bf16_f32 v224, v64, v65\n"
"v_mfma_f32_16x16x32_bf16 v[112:115], a[144:147], v[48:51], v[236:239]\n"
"v_exp_f32 v72, v72\n"
"v_mfma_f32_16x16x32_bf16 v[112:115], a[148:151], v[100:103], v[112:115]\n"
"buffer_load_dwordx4 v8, s[48:51], s90 offen offset:2048 sc1 lds\n"
"v_mfma_f32_16x16x32_bf16 v[112:115], a[152:155], v[228:231], v[112:115]\n"
"v_exp_f32 v73, v73\n"
"v_mfma_f32_16x16x32_bf16 v[112:115], a[156:159], v[164:167], v[112:115]\n"
"buffer_load_dwordx4 v8, s[48:51], s90 offen offset:3072 sc1 lds\n"
"v_cvt_pk_bf16_f32 v225, v66, v67\n"
"v_mfma_f32_16x16x32_bf16 v[120:123], a[156:159], v[180:183], v[252:255]\n"
"v_exp_f32 v74, v74\n"
"s_getpc_b64 s[6:7]\n"
"s_sub_u32 s88, s88, 1\n"
"s_cbranch_scc1 L_hand_%=\n"
"s_add_u32 s89, s89, s91\n"
"s_add_u32 m0, s67, 0x8000\n"
"v_mfma_f32_16x16x32_bf16 v[120:123], a[152:155], v[244:247], v[120:123]\n"
"buffer_load_dwordx4 v8, s[48:51], s89 offen offset:0 sc1 lds\n"
"v_mfma_f32_16x16x32_bf16 v[120:123], a[148:151], v[116:119], v[120:123]\n"
"v_exp_f32 v75, v75\n"
"v_mfma_f32_16x16x32_bf16 v[120:123], a[144:147], v[56:59], v[120:123]\n"
"ds_read_b128 a[156:159], v4 offset:39936\n"
"v_cvt_pk_bf16_f32 v232, v72, v73\n"
"v_mfma_f32_16x16x32_bf16 a[0:3], a[160:163], v[192:195], a[0:3]\n"
"v_exp_f32 v80, v80\n"
"v_mfma_f32_16x16x32_bf16 a[32:35], a[160:163], v[200:203], a[32:35]\n"
"buffer_load_dwordx4 v8, s[48:51], s89 offen offset:1024 sc1 lds\n"
"v_mfma_f32_16x16x32_bf16 a[64:67], a[160:163], v[208:211], a[64:67]\n"
"v_exp_f32 v81, v81\n"
"v_mfma_f32_16x16x32_bf16 a[96:99], a[160:163], v[216:219], a[96:99]\n"
"v_cvt_pk_bf16_f32 v233, v74, v75\n"
"v_add_f32 v14, v14, v80\n"
"v_mfma_f32_16x16x32_bf16 a[100:103], a[164:167], v[216:219], a[100:103]\n"
"v_exp_f32 v82, v82\n"
"v_mfma_f32_16x16x32_bf16 a[68:71], a[164:167], v[208:211], a[68:71]\n"
"ds_read_b128 a[152:155], v4 offset:38912\n"
"v_add_f32 v14, v14, v81\n"
"v_mfma_f32_16x16x32_bf16 a[36:39], a[164:167], v[200:203], a[36:39]\n"
"v_exp_f32 v83, v83\n"
"v_mfma_f32_16x16x32_bf16 a[4:7], a[164:167], v[192:195], a[4:7]\n"
"buffer_load_dwordx4 v8, s[48:51], s89 offen offset:2048 sc1 lds\n"
"v_cvt_pk_bf16_f32 v240, v80, v81\n"
"v_add_f32 v14, v14, v82\n"
"v_mfma_f32_16x16x32_bf16 a[8:11], a[168:171], v[192:195], a[8:11]\n"
"v_exp_f32 v88, v88\n"
"v_mfma_f32_16x16x32_bf16 a[40:43], a[168:171], v[200:203], a[40:43]\n"
"ds_read_b128 a[148:151], v4 offset:37888\n"
"v_add_f32 v14, v14, v83\n"
"v_mfma_f32_16x16x32_bf16 a[72:75], a[168:171], v[208:211], a[72:75]\n"
"v_exp_f32 v89, v89\n"
"v_mfma_f32_16x16x32_bf16 a[104:107], a[168:171], v[216:219], a[104:107]\n"
"buffer_load_dwordx4 v8, s[48:51], s89 offen offset:3072 sc1 lds\n"
"v_cvt_pk_bf16_f32 v241, v82, v83\n"
"v_add_f32 v15, v15, v88\n"
"v_mfma_f32_16x16x32_bf16 a[108:111], a[172:175], v[216:219], a[108:111]\n"
"v_exp_f32 v90, v90\n"
"v_mfma_f32_16x16x32_bf16 a[76:79], a[172:175], v[208:211], a[76:79]\n"
"ds_read_b128 a[144:147], v4 offset:36864\n"
"v_add_f32 v15, v15, v89\n"
"v_mfma_f32_16x16x32_bf16 a[44:47], a[172:175], v[200:203], a[44:47]\n"
"v_exp_f32 v91, v91\n"
"s_add_u32 s90, s89, 0x1000\n"
"s_add_u32 m0, s67, 0x9000\n"
"v_mfma_f32_16x16x32_bf16 a[12:15], a[172:175], v[192:195], a[12:15]\n"
"buffer_load_dwordx4 v8, s[48:51], s90 offen offset:0 sc1 lds\n"
"v_cvt_pk_bf16_f32 v248, v88, v89\n"
"v_add_f32 v15, v15, v90\n"
"v_mfma_f32_16x16x32_bf16 a[16:19], a[176:179], v[192:195], a[16:19]\n"
"v_exp_f32 v96, v96\n"
"v_mfma_f32_16x16x32_bf16 a[48:51], a[176:179], v[200:203], a[48:51]\n"
"ds_read_b128 a[160:163], v4 offset:8192\n"
"v_add_f32 v15, v15, v91\n"
"v_mfma_f32_16x16x32_bf16 a[80:83], a[176:179], v[208:211], a[80:83]\n"
"v_exp_f32 v97, v97\n"
"v_mfma_f32_16x16x32_bf16 a[112:115], a[176:179], v[216:219], a[112:115]\n"
"ds_read_b128 a[164:167], v4 offset:9216\n"
"v_cvt_pk_bf16_f32 v249, v90, v91\n"
"v_mfma_f32_16x16x32_bf16 a[116:119], a[180:183], v[216:219], a[116:119]\n"
"v_exp_f32 v98, v98\n"
"v_mfma_f32_16x16x32_bf16 a[84:87], a[180:183], v[208:211], a[84:87]\n"
"buffer_load_dwordx4 v8, s[48:51], s90 offen offset:1024 sc1 lds\n"
"v_mfma_f32_16x16x32_bf16 a[52:55], a[180:183], v[200:203], a[52:55]\n"
"v_exp_f32 v99, v99\n"
"v_mfma_f32_16x16x32_bf16 a[20:23], a[180:183], v[192:195], a[20:23]\n"
"ds_read_b128 a[168:171], v4 offset:10240\n"
"v_cvt_pk_bf16_f32 v226, v96, v97\n"
"v_mfma_f32_16x16x32_bf16 a[24:27], a[184:187], v[192:195], a[24:27]\n"
"v_exp_f32 v104, v104\n"
"v_mfma_f32_16x16x32_bf16 a[56:59], a[184:187], v[200:203], a[56:59]\n"
"buffer_load_dwordx4 v8, s[48:51], s90 offen offset:2048 sc1 lds\n"
"v_mfma_f32_16x16x32_bf16 a[88:91], a[184:187], v[208:211], a[88:91]\n"
"v_exp_f32 v105, v105\n"
"v_mfma_f32_16x16x32_bf16 a[120:123], a[184:187], v[216:219], a[120:123]\n"
"ds_read_b128 a[172:175], v4 offset:11264\n"
"v_cvt_pk_bf16_f32 v227, v98, v99\n"
"v_mfma_f32_16x16x32_bf16 a[124:127], a[188:191], v[216:219], a[124:127]\n"
"v_exp_f32 v106, v106\n"
"v_mfma_f32_16x16x32_bf16 a[92:95], a[188:191], v[208:211], a[92:95]\n"
"buffer_load_dwordx4 v8, s[48:51], s90 offen offset:3072 sc1 lds\n"
"v_mfma_f32_16x16x32_bf16 a[60:63], a[188:191], v[200:203], a[60:63]\n"
"v_exp_f32 v107, v107\n"
"v_mfma_f32_16x16x32_bf16 a[28:31], a[188:191], v[192:195], a[28:31]\n"
"ds_read_b128 a[176:179], v4 offset:12288\n"
"v_cvt_pk_bf16_f32 v234, v104, v105\n"
"v_mov_b32 v188, v12\n"
"v_mov_b32 v189, v13\n"
"s_nop 0\n"
"v_permlane16_swap_b32 v12, v188\n"
"v_permlane16_swap_b32 v13, v189\n"
"v_add_f32 v12, v12, v188\n"
"v_add_f32 v13, v13, v189\n"
"v_mov_b32 v188, v12\n"
"v_mov_b32 v189, v13\n"
"s_nop 0\n"
"v_permlane32_swap_b32 v12, v188\n"
"v_permlane32_swap_b32 v13, v189\n"
"v_add_f32 v12, v12, v188\n"
"v_add_f32 v13, v13, v189\n"
"v_add_f32 v188, v12, v13\n"
"v_mov_b32 v189, 0x5f800000\n"
"v_cmp_lt_f32 vcc, v188, v189\n"
"s_nop 0\n"
"s_cmp_eq_u64 vcc, -1\n"
"s_cselect_b32 s35, 0, 1\n"
"v_rcp_f32 v12, v12\n"
"v_rcp_f32 v7, v13\n"
"v_mov_b32 v13, v12\n"
"s_sub_u32 s73, s78, 0x100000\n"
"s_lshl_b32 s73, s73, 3\n"
"s_lshl_b32 s74, s65, 12\n"
"s_add_u32 s73, s73, s74\n"
"s_add_u32 s73, s73, 0x2400000\n"
"v_accvgpr_read_b32 v188, a0\n"
"v_accvgpr_read_b32 v189, a1\n"
"v_accvgpr_read_b32 v190, a2\n"
"v_accvgpr_read_b32 v191, a3\n"
"v_pk_mul_f32 v[188:189], v[188:189], v[12:13]\n"
"v_pk_mul_f32 v[190:191], v[190:191], v[12:13]\n"
"v_cvt_pk_bf16_f32 v24, v188, v189\n"
"v_cvt_pk_bf16_f32 v25, v190, v191\n"
"v_accvgpr_read_b32 v188, a4\n"
"v_accvgpr_read_b32 v189, a5\n"
"v_accvgpr_read_b32 v190, a6\n"
"v_accvgpr_read_b32 v191, a7\n"
"v_pk_mul_f32 v[188:189], v[188:189], v[12:13]\n"
"v_pk_mul_f32 v[190:191], v[190:191], v[12:13]\n"
"v_cvt_pk_bf16_f32 v26, v188, v189\n"
"v_cvt_pk_bf16_f32 v27, v190, v191\n"
"buffer_store_dwordx4 v[24:27], v1, s[48:51], s73 offen offset:0 sc1\n"
"v_accvgpr_read_b32 v188, a8\n"
"v_accvgpr_read_b32 v189, a9\n"
"v_accvgpr_read_b32 v190, a10\n"
"v_accvgpr_read_b32 v191, a11\n"
"v_pk_mul_f32 v[188:189], v[188:189], v[12:13]\n"
"v_pk_mul_f32 v[190:191], v[190:191], v[12:13]\n"
"v_cvt_pk_bf16_f32 v28, v188, v189\n"
"v_cvt_pk_bf16_f32 v29, v190, v191\n"
"v_accvgpr_read_b32 v188, a12\n"
"v_accvgpr_read_b32 v189, a13\n"
"v_accvgpr_read_b32 v190, a14\n"
"v_accvgpr_read_b32 v191, a15\n"
"v_pk_mul_f32 v[188:189], v[188:189], v[12:13]\n"
"v_pk_mul_f32 v[190:191], v[190:191], v[12:13]\n"
"v_cvt_pk_bf16_f32 v30, v188, v189\n"
"v_cvt_pk_bf16_f32 v31, v190, v191\n"
"buffer_store_dwordx4 v[28:31], v1, s[48:51], s73 offen offset:1024 sc1\n"
"v_accvgpr_read_b32 v188, a16\n"
"v_accvgpr_read_b32 v189, a17\n"
"v_accvgpr_read_b32 v190, a18\n"
"v_accvgpr_read_b32 v191, a19\n"
"v_pk_mul_f32 v[188:189], v[188:189], v[12:13]\n"
"v_pk_mul_f32 v[190:191], v[190:191], v[12:13]\n"
"v_cvt_pk_bf16_f32 v24, v188, v189\n"
"v_cvt_pk_bf16_f32 v25, v190, v191\n"
"v_accvgpr_read_b32 v188, a20\n"
"v_accvgpr_read_b32 v189, a21\n"
"v_accvgpr_read_b32 v190, a22\n"
"v_accvgpr_read_b32 v191, a23\n"
"v_pk_mul_f32 v[188:189], v[188:189], v[12:13]\n"
"v_pk_mul_f32 v[190:191], v[190:191], v[12:13]\n"
"v_cvt_pk_bf16_f32 v26, v188, v189\n"
"v_cvt_pk_bf16_f32 v27, v190, v191\n"
"buffer_store_dwordx4 v[24:27], v1, s[48:51], s73 offen offset:2048 sc1\n"
"v_accvgpr_read_b32 v188, a24\n"
"v_accvgpr_read_b32 v189, a25\n"
"v_accvgpr_read_b32 v190, a26\n"
"v_accvgpr_read_b32 v191, a27\n"
"v_pk_mul_f32 v[188:189], v[188:189], v[12:13]\n"
"v_pk_mul_f32 v[190:191], v[190:191], v[12:13]\n"
"v_cvt_pk_bf16_f32 v28, v188, v189\n"
"v_cvt_pk_bf16_f32 v29, v190, v191\n"
"v_accvgpr_read_b32 v188, a28\n"
"v_accvgpr_read_b32 v189, a29\n"
"v_accvgpr_read_b32 v190, a30\n"
"v_accvgpr_read_b32 v191, a31\n"
"v_pk_mul_f32 v[188:189], v[188:189], v[12:13]\n"
"v_pk_mul_f32 v[190:191], v[190:191], v[12:13]\n"
"v_cvt_pk_bf16_f32 v30, v188, v189\n"
"v_cvt_pk_bf16_f32 v31, v190, v191\n"
"buffer_store_dwordx4 v[28:31], v1, s[48:51], s73 offen offset:3072 sc1\n"
"v_mov_b32 v12, v7\n"
"v_mov_b32 v13, v7\n"
"s_sub_u32 s73, s79, 0x100000\n"
"s_lshl_b32 s73, s73, 3\n"
"s_lshl_b32 s74, s65, 12\n"
"s_add_u32 s73, s73, s74\n"
"s_add_u32 s73, s73, 0x2400000\n"
"v_accvgpr_read_b32 v188, a32\n"
"v_accvgpr_read_b32 v189, a33\n"
"v_accvgpr_read_b32 v190, a34\n"
"v_accvgpr_read_b32 v191, a35\n"
"v_pk_mul_f32 v[188:189], v[188:189], v[12:13]\n"
"v_pk_mul_f32 v[190:191], v[190:191], v[12:13]\n"
"v_cvt_pk_bf16_f32 v24, v188, v189\n"
"v_cvt_pk_bf16_f32 v25, v190, v191\n"
"v_accvgpr_read_b32 v188, a36\n"
"v_accvgpr_read_b32 v189, a37\n"
"v_accvgpr_read_b32 v190, a38\n"
"v_accvgpr_read_b32 v191, a39\n"
"v_pk_mul_f32 v[188:189], v[188:189], v[12:13]\n"
"v_pk_mul_f32 v[190:191], v[190:191], v[12:13]\n"
"v_cvt_pk_bf16_f32 v26, v188, v189\n"
"v_cvt_pk_bf16_f32 v27, v190, v191\n"
"buffer_store_dwordx4 v[24:27], v1, s[48:51], s73 offen offset:0 sc1\n"
"v_accvgpr_read_b32 v188, a40\n"
"v_accvgpr_read_b32 v189, a41\n"
"v_accvgpr_read_b32 v190, a42\n"
"v_accvgpr_read_b32 v191, a43\n"
"v_pk_mul_f32 v[188:189], v[188:189], v[12:13]\n"
"v_pk_mul_f32 v[190:191], v[190:191], v[12:13]\n"
"v_cvt_pk_bf16_f32 v28, v188, v189\n"
"v_cvt_pk_bf16_f32 v29, v190, v191\n"
"v_accvgpr_read_b32 v188, a44\n"
"v_accvgpr_read_b32 v189, a45\n"
"v_accvgpr_read_b32 v190, a46\n"
"v_accvgpr_read_b32 v191, a47\n"
"v_pk_mul_f32 v[188:189], v[188:189], v[12:13]\n"
"v_pk_mul_f32 v[190:191], v[190:191], v[12:13]\n"
"v_cvt_pk_bf16_f32 v30, v188, v189\n"
"v_cvt_pk_bf16_f32 v31, v190, v191\n"
"buffer_store_dwordx4 v[28:31], v1, s[48:51], s73 offen offset:1024 sc1\n"
"v_accvgpr_read_b32 v188, a48\n"
"v_accvgpr_read_b32 v189, a49\n"
"v_accvgpr_read_b32 v190, a50\n"
"v_accvgpr_read_b32 v191, a51\n"
"v_pk_mul_f32 v[188:189], v[188:189], v[12:13]\n"
"v_pk_mul_f32 v[190:191], v[190:191], v[12:13]\n"
"v_cvt_pk_bf16_f32 v24, v188, v189\n"
"v_cvt_pk_bf16_f32 v25, v190, v191\n"
"v_accvgpr_read_b32 v188, a52\n"
"v_accvgpr_read_b32 v189, a53\n"
"v_accvgpr_read_b32 v190, a54\n"
"v_accvgpr_read_b32 v191, a55\n"
"v_pk_mul_f32 v[188:189], v[188:189], v[12:13]\n"
"v_pk_mul_f32 v[190:191], v[190:191], v[12:13]\n"
"v_cvt_pk_bf16_f32 v26, v188, v189\n"
"v_cvt_pk_bf16_f32 v27, v190, v191\n"
"buffer_store_dwordx4 v[24:27], v1, s[48:51], s73 offen offset:2048 sc1\n"
"v_accvgpr_read_b32 v188, a56\n"
"v_accvgpr_read_b32 v189, a57\n"
"v_accvgpr_read_b32 v190, a58\n"
"v_accvgpr_read_b32 v191, a59\n"
"v_pk_mul_f32 v[188:189], v[188:189], v[12:13]\n"
"v_pk_mul_f32 v[190:191], v[190:191], v[12:13]\n"
"v_cvt_pk_bf16_f32 v28, v188, v189\n"
"v_cvt_pk_bf16_f32 v29, v190, v191\n"
"v_accvgpr_read_b32 v188, a60\n"
"v_accvgpr_read_b32 v189, a61\n"
"v_accvgpr_read_b32 v190, a62\n"
"v_accvgpr_read_b32 v191, a63\n"
"v_pk_mul_f32 v[188:189], v[188:189], v[12:13]\n"
"v_pk_mul_f32 v[190:191], v[190:191], v[12:13]\n"
"v_cvt_pk_bf16_f32 v30, v188, v189\n"
"v_cvt_pk_bf16_f32 v31, v190, v191\n"
"buffer_store_dwordx4 v[28:31], v1, s[48:51], s73 offen offset:3072 sc1\n"
"s_and_b32 s73, s65, 1\n"
"s_lshl_b32 s73, s73, 2\n"
"s_add_u32 s73, s73, s64\n"
"s_lshl_b32 s73, s73, 16\n"
"s_lshl_b32 s92, s3, 19\n"
"s_add_u32 s92, s92, s73\n"
"s_add_u32 s92, s92, 0x3400000\n"
"s_cmp_eq_u32 s35, 0\n"
"s_cbranch_scc0 L_bad3_%=\n"
"s_waitcnt lgkmcnt(5)\n"
"v_mfma_f32_16x16x32_bf16 v[64:67], a[128:131], v[36:39], v[140:143]\n"
"v_exp_f32 v112, v112\n"
"v_mfma_f32_16x16x32_bf16 v[64:67], a[132:135], v[44:47], v[64:67]\n"
"ds_read_b128 a[180:183], v9 offset:13312\n"
"s_add_u32 s73, s92, 0x6000\n"
"v_mfma_f32_16x16x32_bf16 v[64:67], a[136:139], v[52:55], v[64:67]\n"
"buffer_load_dwordx4 a[0:3], v1, s[48:51], s73 offen offset:0 sc1\n"
"v_exp_f32 v113, v113\n"
"v_mfma_f32_16x16x32_bf16 v[64:67], a[140:143], v[60:63], v[64:67]\n"
"v_cvt_pk_bf16_f32 v235, v106, v107\n"
"v_add_f32 v14, v14, v112\n"
"v_mfma_f32_16x16x32_bf16 v[72:75], a[140:143], v[124:127], v[156:159]\n"
"v_exp_f32 v114, v114\n"
"v_mfma_f32_16x16x32_bf16 v[72:75], a[136:139], v[108:111], v[72:75]\n"
"ds_read_b128 a[184:187], v9 offset:14336\n"
"v_add_f32 v14, v14, v113\n"
"v_mfma_f32_16x16x32_bf16 v[72:75], a[132:135], v[92:95], v[72:75]\n"
"buffer_load_dwordx4 a[4:7], v1, s[48:51], s73 offen offset:1024 sc1\n"
"v_exp_f32 v115, v115\n"
"v_mfma_f32_16x16x32_bf16 v[72:75], a[128:131], v[76:79], v[72:75]\n"
"v_cvt_pk_bf16_f32 v242, v112, v113\n"
"v_add_f32 v14, v14, v114\n"
"v_mfma_f32_16x16x32_bf16 v[80:83], a[128:131], v[48:51], v[236:239]\n"
"v_exp_f32 v120, v120\n"
"v_mfma_f32_16x16x32_bf16 v[80:83], a[132:135], v[100:103], v[80:83]\n"
"ds_read_b128 a[188:191], v4 offset:48128\n"
"v_add_f32 v14, v14, v115\n"
"v_mfma_f32_16x16x32_bf16 v[80:83], a[136:139], v[228:231], v[80:83]\n"
"v_exp_f32 v121, v121\n"
"v_mfma_f32_16x16x32_bf16 v[80:83], a[140:143], v[164:167], v[80:83]\n"
"v_cvt_pk_bf16_f32 v243, v114, v115\n"
"v_add_f32 v15, v15, v120\n"
"v_mfma_f32_16x16x32_bf16 v[152:155], v[128:131], v[224:227], v[152:155]\n"
"s_waitcnt vmcnt(8) lgkmcnt(1)\n"
"s_barrier\n"
"v_mfma_f32_16x16x32_bf16 v[88:91], a[140:143], v[180:183], v[252:255]\n"
"v_exp_f32 v122, v122\n"
"v_mfma_f32_16x16x32_bf16 v[88:91], a[136:139], v[244:247], v[88:91]\n"
"ds_read_b128 a[140:143], v3 offset:3072\n"
"v_add_f32 v15, v15, v121\n"
"v_mfma_f32_16x16x32_bf16 v[88:91], a[132:135], v[116:119], v[88:91]\n"
"buffer_load_dwordx4 a[8:11], v1, s[48:51], s73 offen offset:2048 sc1\n"
"v_exp_f32 v123, v123\n"
"v_mfma_f32_16x16x32_bf16 v[88:91], a[128:131], v[56:59], v[88:91]\n"
"v_cvt_pk_bf16_f32 v250, v120, v121\n"
"v_add_f32 v15, v15, v122\n"
"v_mfma_f32_16x16x32_bf16 v[96:99], a[144:147], v[36:39], v[140:143]\n"
"v_exp_f32 v64, v64\n"
"v_mfma_f32_16x16x32_bf16 v[96:99], a[148:151], v[44:47], v[96:99]\n"
"ds_read_b128 a[136:139], v3 offset:2048\n"
"v_add_f32 v15, v15, v123\n"
"v_mfma_f32_16x16x32_bf16 v[96:99], a[152:155], v[52:55], v[96:99]\n"
"buffer_load_dwordx4 a[12:15], v1, s[48:51], s73 offen offset:3072 sc1\n"
"v_exp_f32 v65, v65\n"
"v_mfma_f32_16x16x32_bf16 v[96:99], a[156:159], v[60:63], v[96:99]\n"
"ds_read_b128 a[132:135], v3 offset:1024\n"
"v_cvt_pk_bf16_f32 v251, v122, v123\n"
"v_mfma_f32_16x16x32_bf16 v[160:163], v[128:131], v[232:235], v[160:163]\n"
"s_getpc_b64 s[6:7]\n"
"s_sub_u32 s88, s88, 1\n"
"s_cbranch_scc1 L_hand_%=\n"
"s_add_u32 s89, s89, s91\n"
"s_add_u32 m0, s67, 0x10000\n"
"v_mfma_f32_16x16x32_bf16 v[104:107], a[156:159], v[124:127], v[156:159]\n"
"v_exp_f32 v66, v66\n"
"v_mfma_f32_16x16x32_bf16 v[104:107], a[152:155], v[108:111], v[104:107]\n"
"buffer_load_dwordx4 v8, s[48:51], s89 offen offset:0 sc1 lds\n"
"v_mfma_f32_16x16x32_bf16 v[104:107], a[148:151], v[92:95], v[104:107]\n"
"v_exp_f32 v67, v67\n"
"v_mfma_f32_16x16x32_bf16 v[104:107], a[144:147], v[76:79], v[104:107]\n"
"ds_read_b128 a[128:131], v3 offset:0\n"
"v_cvt_pk_bf16_f32 v192, v64, v65\n"
"v_mfma_f32_16x16x32_bf16 v[112:115], a[144:147], v[48:51], v[236:239]\n"
"v_exp_f32 v72, v72\n"
"v_mfma_f32_16x16x32_bf16 v[112:115], a[148:151], v[100:103], v[112:115]\n"
"buffer_load_dwordx4 v8, s[48:51], s89 offen offset:1024 sc1 lds\n"
"s_add_u32 s73, s92, 0x7000\n"
"v_mfma_f32_16x16x32_bf16 v[112:115], a[152:155], v[228:231], v[112:115]\n"
"buffer_load_dwordx4 a[16:19], v1, s[48:51], s73 offen offset:0 sc1\n"
"v_exp_f32 v73, v73\n"
"v_mfma_f32_16x16x32_bf16 v[112:115], a[156:159], v[164:167], v[112:115]\n"
"v_cvt_pk_bf16_f32 v193, v66, v67\n"
"v_add_u32 v9, 0x8000, v4\n"
"v_mfma_f32_16x16x32_bf16 v[120:123], a[156:159], v[180:183], v[252:255]\n"
"v_exp_f32 v74, v74\n"
"v_mfma_f32_16x16x32_bf16 v[120:123], a[152:155], v[244:247], v[120:123]\n"
"buffer_load_dwordx4 v8, s[48:51], s89 offen offset:2048 sc1 lds\n"
"v_mfma_f32_16x16x32_bf16 v[120:123], a[148:151], v[116:119], v[120:123]\n"
"v_exp_f32 v75, v75\n"
"v_mfma_f32_16x16x32_bf16 v[120:123], a[144:147], v[56:59], v[120:123]\n"
"ds_read_b128 a[156:159], v3 offset:7168\n"
"v_cvt_pk_bf16_f32 v200, v72, v73\n"
"v_mfma_f32_16x16x32_bf16 a[192:195], a[160:163], v[224:227], a[192:195]\n"
"v_exp_f32 v80, v80\n"
"v_mfma_f32_16x16x32_bf16 a[224:227], a[160:163], v[232:235], a[224:227]\n"
"buffer_load_dwordx4 v8, s[48:51], s89 offen offset:3072 sc1 lds\n"
"v_mfma_f32_16x16x32_bf16 a[64:67], a[160:163], v[240:243], a[64:67]\n"
"buffer_load_dwordx4 a[20:23], v1, s[48:51], s73 offen offset:1024 sc1\n"
"v_exp_f32 v81, v81\n"
"v_mfma_f32_16x16x32_bf16 a[96:99], a[160:163], v[248:251], a[96:99]\n"
"v_cvt_pk_bf16_f32 v201, v74, v75\n"
"v_add_f32 v14, v14, v80\n"
"v_mfma_f32_16x16x32_bf16 a[100:103], a[164:167], v[248:251], a[100:103]\n"
"v_exp_f32 v82, v82\n"
"v_mfma_f32_16x16x32_bf16 a[68:71], a[164:167], v[240:243], a[68:71]\n"
"ds_read_b128 a[152:155], v3 offset:6144\n"
"v_add_f32 v14, v14, v81\n"
"v_mfma_f32_16x16x32_bf16 a[228:231], a[164:167], v[232:235], a[228:231]\n"
"buffer_load_dwordx4 a[24:27], v1, s[48:51], s73 offen offset:2048 sc1\n"
"v_exp_f32 v83, v83\n"
"v_mfma_f32_16x16x32_bf16 a[196:199], a[164:167], v[224:227], a[196:199]\n"
"v_cvt_pk_bf16_f32 v208, v80, v81\n"
"v_add_f32 v14, v14, v82\n"
"v_mfma_f32_16x16x32_bf16 a[200:203], a[168:171], v[224:227], a[200:203]\n"
"v_exp_f32 v88, v88\n"
"v_mfma_f32_16x16x32_bf16 a[232:235], a[168:171], v[232:235], a[232:235]\n"
"ds_read_b128 a[148:151], v3 offset:5120\n"
"v_add_f32 v14, v14, v83\n"
"s_add_u32 s90, s89, 0x1000\n"
"s_add_u32 m0, s67, 0x11000\n"
"v_mfma_f32_16x16x32_bf16 a[72:75], a[168:171], v[240:243], a[72:75]\n"
"buffer_load_dwordx4 a[28:31], v1, s[48:51], s73 offen offset:3072 sc1\n"
"v_exp_f32 v89, v89\n"
"v_mfma_f32_16x16x32_bf16 a[104:107], a[168:171], v[248:251], a[104:107]\n"
"buffer_load_dwordx4 v8, s[48:51], s90 offen offset:0 sc1 lds\n"
"v_cvt_pk_bf16_f32 v209, v82, v83\n"
"v_add_f32 v15, v15, v88\n"
"v_mfma_f32_16x16x32_bf16 a[108:111], a[172:175], v[248:251], a[108:111]\n"
"v_exp_f32 v90, v90\n"
"v_mfma_f32_16x16x32_bf16 a[76:79], a[172:175], v[240:243], a[76:79]\n"
"ds_read_b128 a[144:147], v3 offset:4096\n"
"v_add_f32 v15, v15, v89\n"
"s_add_u32 s73, s92, 0x8000\n"
"v_mfma_f32_16x16x32_bf16 a[236:239], a[172:175], v[232:235], a[236:239]\n"
"buffer_load_dwordx4 a[32:35], v1, s[48:51], s73 offen offset:0 sc1\n"
"v_exp_f32 v91, v91\n"
"v_mfma_f32_16x16x32_bf16 a[204:207], a[172:175], v[224:227], a[204:207]\n"
"v_cvt_pk_bf16_f32 v216, v88, v89\n"
"v_add_f32 v15, v15, v90\n"
"v_mfma_f32_16x16x32_bf16 a[208:211], a[176:179], v[224:227], a[208:211]\n"
"v_exp_f32 v96, v96\n"
"v_mfma_f32_16x16x32_bf16 a[240:243], a[176:179], v[232:235], a[240:243]\n"
"ds_read_b128 a[160:163], v4 offset:40960\n"
"v_add_f32 v15, v15, v91\n"
"v_mfma_f32_16x16x32_bf16 a[80:83], a[176:179], v[240:243], a[80:83]\n"
"buffer_load_dwordx4 a[36:39], v1, s[48:51], s73 offen offset:1024 sc1\n"
"v_exp_f32 v97, v97\n"
"v_mfma_f32_16x16x32_bf16 a[112:115], a[176:179], v[248:251], a[112:115]\n"
"ds_read_b128 a[164:167], v4 offset:41984\n"
"v_cvt_pk_bf16_f32 v217, v90, v91\n"
"v_mfma_f32_16x16x32_bf16 a[116:119], a[180:183], v[248:251], a[116:119]\n"
"v_exp_f32 v98, v98\n"
"v_mfma_f32_16x16x32_bf16 a[84:87], a[180:183], v[240:243], a[84:87]\n"
"buffer_load_dwordx4 v8, s[48:51], s90 offen offset:1024 sc1 lds\n"
"v_mfma_f32_16x16x32_bf16 a[244:247], a[180:183], v[232:235], a[244:247]\n"
"v_exp_f32 v99, v99\n"
"v_mfma_f32_16x16x32_bf16 a[212:215], a[180:183], v[224:227], a[212:215]\n"
"ds_read_b128 a[168:171], v4 offset:43008\n"
"v_cvt_pk_bf16_f32 v194, v96, v97\n"
"v_mfma_f32_16x16x32_bf16 a[216:219], a[184:187], v[224:227], a[216:219]\n"
"v_exp_f32 v104, v104\n"
"v_mfma_f32_16x16x32_bf16 a[248:251], a[184:187], v[232:235], a[248:251]\n"
"buffer_load_dwordx4 v8, s[48:51], s90 offen offset:2048 sc1 lds\n"
"v_mfma_f32_16x16x32_bf16 a[88:91], a[184:187], v[240:243], a[88:91]\n"
"v_exp_f32 v105, v105\n"
"v_mfma_f32_16x16x32_bf16 a[120:123], a[184:187], v[248:251], a[120:123]\n"
"ds_read_b128 a[172:175], v4 offset:44032\n"
"v_cvt_pk_bf16_f32 v195, v98, v99\n"
"v_mfma_f32_16x16x32_bf16 a[124:127], v[172:175], v[248:251], a[124:127]\n"
"v_exp_f32 v106, v106\n"
"v_mfma_f32_16x16x32_bf16 a[92:95], v[172:175], v[240:243], a[92:95]\n"
"buffer_load_dwordx4 v8, s[48:51], s90 offen offset:3072 sc1 lds\n"
"v_mfma_f32_16x16x32_bf16 a[252:255], v[172:175], v[232:235], a[252:255]\n"
"buffer_load_dwordx4 a[40:43], v1, s[48:51], s73 offen offset:2048 sc1\n"
"v_exp_f32 v107, v107\n"
"v_mfma_f32_16x16x32_bf16 a[220:223], v[172:175], v[224:227], a[220:223]\n"
"ds_read_b128 a[176:179], v4 offset:45056\n"
"v_cvt_pk_bf16_f32 v202, v104, v105\n"
"s_sub_u32 s96, s96, 1\n"
"s_cbranch_scc1 L_drainB_%=\n"
"s_waitcnt lgkmcnt(5)\n"
"v_mfma_f32_16x16x32_bf16 v[64:67], a[128:131], v[36:39], v[140:143]\n"
"v_exp_f32 v112, v112\n"
"v_mfma_f32_16x16x32_bf16 v[64:67], a[132:135], v[44:47], v[64:67]\n"
"ds_read_b128 a[180:183], v9 offset:13312\n"
"v_mfma_f32_16x16x32_bf16 v[64:67], a[136:139], v[52:55], v[64:67]\n"
"buffer_load_dwordx4 a[44:47], v1, s[48:51], s73 offen offset:3072 sc1\n"
"v_exp_f32 v113, v113\n"
"v_mfma_f32_16x16x32_bf16 v[64:67], a[140:143], v[60:63], v[64:67]\n"
"v_cvt_pk_bf16_f32 v203, v106, v107\n"
"v_add_f32 v14, v14, v112\n"
"v_mfma_f32_16x16x32_bf16 v[72:75], a[140:143], v[124:127], v[156:159]\n"
"v_exp_f32 v114, v114\n"
"v_mfma_f32_16x16x32_bf16 v[72:75], a[136:139], v[108:111], v[72:75]\n"
"ds_read_b128 a[184:187], v9 offset:14336\n"
"v_add_f32 v14, v14, v113\n"
"s_add_u32 s73, s92, 0x9000\n"
"v_mfma_f32_16x16x32_bf16 v[72:75], a[132:135], v[92:95], v[72:75]\n"
"buffer_load_dwordx4 a[48:51], v1, s[48:51], s73 offen offset:0 sc1\n"
"v_exp_f32 v115, v115\n"
"v_mfma_f32_16x16x32_bf16 v[72:75], a[128:131], v[76:79], v[72:75]\n"
"v_cvt_pk_bf16_f32 v210, v112, v113\n"
"v_add_f32 v14, v14, v114\n"
"v_mfma_f32_16x16x32_bf16 v[80:83], a[128:131], v[48:51], v[236:239]\n"
"v_exp_f32 v120, v120\n"
"v_mfma_f32_16x16x32_bf16 v[80:83], a[132:135], v[100:103], v[80:83]\n"
"ds_read_b128 v[172:175], v3 offset:15360\n"
"v_add_f32 v14, v14, v115\n"
"v_mfma_f32_16x16x32_bf16 v[80:83], a[136:139], v[228:231], v[80:83]\n"
"v_exp_f32 v121, v121\n"
"v_mfma_f32_16x16x32_bf16 v[80:83], a[140:143], v[164:167], v[80:83]\n"
"v_cvt_pk_bf16_f32 v211, v114, v115\n"
"v_add_f32 v15, v15, v120\n"
"v_mfma_f32_16x16x32_bf16 v[152:155], v[128:131], v[192:195], v[152:155]\n"
"s_waitcnt vmcnt(8) lgkmcnt(1)\n"
"s_barrier\n"
"v_mfma_f32_16x16x32_bf16 v[88:91], a[140:143], v[180:183], v[252:255]\n"
"v_exp_f32 v122, v122\n"
"v_mfma_f32_16x16x32_bf16 v[88:91], a[136:139], v[244:247], v[88:91]\n"
"ds_read_b128 a[140:143], v3 offset:35840\n"
"v_add_f32 v15, v15, v121\n"
"v_mfma_f32_16x16x32_bf16 v[88:91], a[132:135], v[116:119], v[88:91]\n"
"buffer_load_dwordx4 a[52:55], v1, s[48:51], s73 offen offset:1024 sc1\n"
"v_exp_f32 v123, v123\n"
"v_mfma_f32_16x16x32_bf16 v[88:91], a[128:131], v[56:59], v[88:91]\n"
"v_cvt_pk_bf16_f32 v218, v120, v121\n"
"v_add_f32 v15, v15, v122\n"
"v_mfma_f32_16x16x32_bf16 v[96:99], a[144:147], v[36:39], v[140:143]\n"
"v_exp_f32 v64, v64\n"
"v_mfma_f32_16x16x32_bf16 v[96:99], a[148:151], v[44:47], v[96:99]\n"
"ds_read_b128 a[136:139], v3 offset:34816\n"
"v_add_f32 v15, v15, v123\n"
"v_mfma_f32_16x16x32_bf16 v[96:99], a[152:155], v[52:55], v[96:99]\n"
"buffer_load_dwordx4 a[56:59], v1, s[48:51], s73 offen offset:2048 sc1\n"
"v_exp_f32 v65, v65\n"
"v_mfma_f32_16x16x32_bf16 v[96:99], a[156:159], v[60:63], v[96:99]\n"
"ds_read_b128 a[132:135], v3 offset:33792\n"
"v_cvt_pk_bf16_f32 v219, v122, v123\n"
"v_mfma_f32_16x16x32_bf16 v[160:163], v[128:131], v[200:203], v[160:163]\n"
"s_getpc_b64 s[6:7]\n"
"s_sub_u32 s88, s88, 1\n"
"s_cbranch_scc1 L_hand_%=\n"
"s_add_u32 s89, s89, s91\n"
"s_add_u32 m0, s67, 0x18000\n"
"v_mfma_f32_16x16x32_bf16 v[104:107], a[156:159], v[124:127], v[156:159]\n"
"v_exp_f32 v66, v66\n"
"v_mfma_f32_16x16x32_bf16 v[104:107], a[152:155], v[108:111], v[104:107]\n"
"buffer_load_dwordx4 v8, s[48:51], s89 offen offset:0 sc1 lds\n"
"v_mfma_f32_16x16x32_bf16 v[104:107], a[148:151], v[92:95], v[104:107]\n"
"v_exp_f32 v67, v67\n"
"v_mfma_f32_16x16x32_bf16 v[104:107], a[144:147], v[76:79], v[104:107]\n"
"ds_read_b128 a[128:131], v3 offset:32768\n"
"v_cvt_pk_bf16_f32 v224, v64, v65\n"
"v_mfma_f32_16x16x32_bf16 v[112:115], a[144:147], v[48:51], v[236:239]\n"
"v_exp_f32 v72, v72\n"
"v_mfma_f32_16x16x32_bf16 v[112:115], a[148:151], v[100:103], v[112:115]\n"
"buffer_load_dwordx4 v8, s[48:51], s89 offen offset:1024 sc1 lds\n"
"v_mfma_f32_16x16x32_bf16 v[112:115], a[152:155], v[228:231], v[112:115]\n"
"v_exp_f32 v73, v73\n"
"v_mfma_f32_16x16x32_bf16 v[112:115], a[156:159], v[164:167], v[112:115]\n"
"v_cvt_pk_bf16_f32 v225, v66, v67\n"
"v_add_u32 v9, 0x0, v3\n"
"v_mfma_f32_16x16x32_bf16 v[120:123], a[156:159], v[180:183], v[252:255]\n"
"v_exp_f32 v74, v74\n"
"v_mfma_f32_16x16x32_bf16 v[120:123], a[152:155], v[244:247], v[120:123]\n"
"buffer_load_dwordx4 v8, s[48:51], s89 offen offset:2048 sc1 lds\n"
"v_mfma_f32_16x16x32_bf16 v[120:123], a[148:151], v[116:119], v[120:123]\n"
"v_exp_f32 v75, v75\n"
"v_mfma_f32_16x16x32_bf16 v[120:123], a[144:147], v[56:59], v[120:123]\n"
"ds_read_b128 a[156:159], v3 offset:39936\n"
"v_cvt_pk_bf16_f32 v232, v72, v73\n"
"v_mfma_f32_16x16x32_bf16 a[192:195], a[160:163], v[192:195], a[192:195]\n"
"v_exp_f32 v80, v80\n"
"v_mfma_f32_16x16x32_bf16 a[224:227], a[160:163], v[200:203], a[224:227]\n"
"buffer_load_dwordx4 v8, s[48:51], s89 offen offset:3072 sc1 lds\n"
"v_mfma_f32_16x16x32_bf16 a[64:67], a[160:163], v[208:211], a[64:67]\n"
"buffer_load_dwordx4 a[60:63], v1, s[48:51], s73 offen offset:3072 sc1\n"
"v_exp_f32 v81, v81\n"
"v_mfma_f32_16x16x32_bf16 a[96:99], a[160:163], v[216:219], a[96:99]\n"
"v_cvt_pk_bf16_f32 v233, v74, v75\n"
"v_add_f32 v14, v14, v80\n"
"v_mfma_f32_16x16x32_bf16 a[100:103], a[164:167], v[216:219], a[100:103]\n"
"v_exp_f32 v82, v82\n"
"v_mfma_f32_16x16x32_bf16 a[68:71], a[164:167], v[208:211], a[68:71]\n"
"ds_read_b128 a[152:155], v3 offset:38912\n"
"v_add_f32 v14, v14, v81\n"
"s_add_u32 s73, s92, 0xa000\n"
"v_mfma_f32_16x16x32_bf16 a[228:231], a[164:167], v[200:203], a[228:231]\n"
"buffer_load_dwordx4 v[136:139], v1, s[48:51], s73 offen offset:0 sc1\n"
"v_exp_f32 v83, v83\n"
"v_mfma_f32_16x16x32_bf16 a[196:199], a[164:167], v[192:195], a[196:199]\n"
"v_cvt_pk_bf16_f32 v240, v80, v81\n"
"v_add_f32 v14, v14, v82\n"
"v_mfma_f32_16x16x32_bf16 a[200:203], a[168:171], v[192:195], a[200:203]\n"
"v_exp_f32 v88, v88\n"
"v_mfma_f32_16x16x32_bf16 a[232:235], a[168:171], v[200:203], a[232:235]\n"
"ds_read_b128 a[148:151], v3 offset:37888\n"
"v_add_f32 v14, v14, v83\n"
"s_add_u32 s90, s89, 0x1000\n"
"s_add_u32 m0, s67, 0x19000\n"
"v_mfma_f32_16x16x32_bf16 a[72:75], a[168:171], v[208:211], a[72:75]\n"
"buffer_load_dwordx4 v[144:147], v1, s[48:51], s73 offen offset:1024 sc1\n"
"v_exp_f32 v89, v89\n"
"v_mfma_f32_16x16x32_bf16 a[104:107], a[168:171], v[216:219], a[104:107]\n"
"buffer_load_dwordx4 v8, s[48:51], s90 offen offset:0 sc1 lds\n"
"v_cvt_pk_bf16_f32 v241, v82, v83\n"
"v_add_f32 v15, v15, v88\n"
"v_mfma_f32_16x16x32_bf16 a[108:111], a[172:175], v[216:219], a[108:111]\n"
"v_exp_f32 v90, v90\n"
"v_mfma_f32_16x16x32_bf16 a[76:79], a[172:175], v[208:211], a[76:79]\n"
"ds_read_b128 a[144:147], v3 offset:36864\n"
"v_add_f32 v15, v15, v89\n"
"v_mfma_f32_16x16x32_bf16 a[236:239], a[172:175], v[200:203], a[236:239]\n"
"buffer_load_dwordx4 v[168:171], v1, s[48:51], s73 offen offset:2048 sc1\n"
"v_exp_f32 v91, v91\n"
"v_mfma_f32_16x16x32_bf16 a[204:207], a[172:175], v[192:195], a[204:207]\n"
"v_cvt_pk_bf16_f32 v248, v88, v89\n"
"v_add_f32 v15, v15, v90\n"
"v_mfma_f32_16x16x32_bf16 a[208:211], a[176:179], v[192:195], a[208:211]\n"
"v_exp_f32 v96, v96\n"
"v_mfma_f32_16x16x32_bf16 a[240:243], a[176:179], v[200:203], a[240:243]\n"
"ds_read_b128 a[160:163], v3 offset:8192\n"
"v_add_f32 v15, v15, v91\n"
"v_mfma_f32_16x16x32_bf16 a[80:83], a[176:179], v[208:211], a[80:83]\n"
"buffer_load_dwordx4 v[176:179], v1, s[48:51], s73 offen offset:3072 sc1\n"
"v_exp_f32 v97, v97\n"
"v_mfma_f32_16x16x32_bf16 a[112:115], a[176:179], v[216:219], a[112:115]\n"
"ds_read_b128 a[164:167], v3 offset:9216\n"
"v_cvt_pk_bf16_f32 v249, v90, v91\n"
"v_mfma_f32_16x16x32_bf16 a[116:119], a[180:183], v[216:219], a[116:119]\n"
"v_exp_f32 v98, v98\n"
"v_mfma_f32_16x16x32_bf16 a[84:87], a[180:183], v[208:211], a[84:87]\n"
"buffer_load_dwordx4 v8, s[48:51], s90 offen offset:1024 sc1 lds\n"
"v_mfma_f32_16x16x32_bf16 a[244:247], a[180:183], v[200:203], a[244:247]\n"
"v_exp_f32 v99, v99\n"
"v_mfma_f32_16x16x32_bf16 a[212:215], a[180:183], v[192:195], a[212:215]\n"
"ds_read_b128 a[168:171], v3 offset:10240\n"
"v_cvt_pk_bf16_f32 v226, v96, v97\n"
"v_mfma_f32_16x16x32_bf16 a[216:219], a[184:187], v[192:195], a[216:219]\n"
"v_exp_f32 v104, v104\n"
"v_mfma_f32_16x16x32_bf16 a[248:251], a[184:187], v[200:203], a[248:251]\n"
"buffer_load_dwordx4 v8, s[48:51], s90 offen offset:2048 sc1 lds\n"
"v_mfma_f32_16x16x32_bf16 a[88:91], a[184:187], v[208:211], a[88:91]\n"
"v_exp_f32 v105, v105\n"
"v_mfma_f32_16x16x32_bf16 a[120:123], a[184:187], v[216:219], a[120:123]\n"
"ds_read_b128 a[172:175], v3 offset:11264\n"
"v_cvt_pk_bf16_f32 v227, v98, v99\n"
"v_mfma_f32_16x16x32_bf16 a[124:127], a[188:191], v[216:219], a[124:127]\n"
"v_exp_f32 v106, v106\n"
"v_mfma_f32_16x16x32_bf16 a[92:95], a[188:191], v[208:211], a[92:95]\n"
"buffer_load_dwordx4 v8, s[48:51], s90 offen offset:3072 sc1 lds\n"
"s_add_u32 s73, s92, 0xb000\n"
"v_mfma_f32_16x16x32_bf16 a[252:255], a[188:191], v[200:203], a[252:255]\n"
"buffer_load_dwordx4 v[184:187], v1, s[48:51], s73 offen offset:0 sc1\n"
"v_exp_f32 v107, v107\n"
"v_mfma_f32_16x16x32_bf16 a[220:223], a[188:191], v[192:195], a[220:223]\n"
"ds_read_b128 a[176:179], v3 offset:12288\n"
"v_cvt_pk_bf16_f32 v234, v104, v105\n"
"s_waitcnt lgkmcnt(5)\n"
"v_mfma_f32_16x16x32_bf16 v[64:67], a[128:131], v[36:39], v[140:143]\n"
"v_exp_f32 v112, v112\n"
"v_mfma_f32_16x16x32_bf16 v[64:67], a[132:135], v[44:47], v[64:67]\n"
"ds_read_b128 a[180:183], v9 offset:13312\n"
"v_mfma_f32_16x16x32_bf16 v[64:67], a[136:139], v[52:55], v[64:67]\n"
"buffer_load_dwordx4 v[204:207], v1, s[48:51], s73 offen offset:1024 sc1\n"
"v_exp_f32 v113, v113\n"
"v_mfma_f32_16x16x32_bf16 v[64:67], a[140:143], v[60:63], v[64:67]\n"
"v_cvt_pk_bf16_f32 v235, v106, v107\n"
"v_add_f32 v14, v14, v112\n"
"v_mfma_f32_16x16x32_bf16 v[72:75], a[140:143], v[124:127], v[156:159]\n"
"v_exp_f32 v114, v114\n"
"v_mfma_f32_16x16x32_bf16 v[72:75], a[136:139], v[108:111], v[72:75]\n"
"ds_read_b128 a[184:187], v9 offset:14336\n"
"v_add_f32 v14, v14, v113\n"
"v_mfma_f32_16x16x32_bf16 v[72:75], a[132:135], v[92:95], v[72:75]\n"
"buffer_load_dwordx4 v[220:223], v1, s[48:51], s73 offen offset:2048 sc1\n"
"v_exp_f32 v115, v115\n"
"v_mfma_f32_16x16x32_bf16 v[72:75], a[128:131], v[76:79], v[72:75]\n"
"v_cvt_pk_bf16_f32 v242, v112, v113\n"
"v_add_f32 v14, v14, v114\n"
"v_mfma_f32_16x16x32_bf16 v[80:83], a[128:131], v[48:51], v[236:239]\n"
"v_exp_f32 v120, v120\n"
"v_mfma_f32_16x16x32_bf16 v[80:83], a[132:135], v[100:103], v[80:83]\n"
"ds_read_b128 a[188:191], v3 offset:48128\n"
"v_add_f32 v14, v14, v115\n"
"v_mfma_f32_16x16x32_bf16 v[80:83], a[136:139], v[228:231], v[80:83]\n"
"v_exp_f32 v121, v121\n"
"v_mfma_f32_16x16x32_bf16 v[80:83], a[140:143], v[164:167], v[80:83]\n"
"v_cvt_pk_bf16_f32 v243, v114, v115\n"
"v_add_f32 v15, v15, v120\n"
"v_mfma_f32_16x16x32_bf16 v[152:155], v[128:131], v[224:227], v[152:155]\n"
"s_waitcnt vmcnt(8) lgkmcnt(1)\n"
"s_barrier\n"
"v_mfma_f32_16x16x32_bf16 v[88:91], a[140:143], v[180:183], v[252:255]\n"
"v_exp_f32 v122, v122\n"
"v_mfma_f32_16x16x32_bf16 v[88:91], a[136:139], v[244:247], v[88:91]\n"
"ds_read_b128 a[140:143], v4 offset:3072\n"
"v_add_f32 v15, v15, v121\n"
"v_mfma_f32_16x16x32_bf16 v[88:91], a[132:135], v[116:119], v[88:91]\n"
"buffer_load_dwordx4 v[188:191], v1, s[48:51], s73 offen offset:3072 sc1\n"
"v_exp_f32 v123, v123\n"
"v_mfma_f32_16x16x32_bf16 v[88:91], a[128:131], v[56:59], v[88:91]\n"
"v_cvt_pk_bf16_f32 v250, v120, v121\n"
"v_add_f32 v15, v15, v122\n"
"v_mfma_f32_16x16x32_bf16 v[96:99], a[144:147], v[36:39], v[140:143]\n"
"v_exp_f32 v64, v64\n"
"v_mfma_f32_16x16x32_bf16 v[96:99], a[148:151], v[44:47], v[96:99]\n"
"ds_read_b128 a[136:139], v4 offset:2048\n"
"v_add_f32 v15, v15, v123\n"
"s_add_u32 s73, s92, 0xc000\n"
"v_mfma_f32_16x16x32_bf16 v[96:99], a[152:155], v[52:55], v[96:99]\n"
"buffer_load_dwordx4 v[32:35], v1, s[48:51], s73 offen offset:0 sc1\n"
"v_exp_f32 v65, v65\n"
"v_mfma_f32_16x16x32_bf16 v[96:99], a[156:159], v[60:63], v[96:99]\n"
"ds_read_b128 a[132:135], v4 offset:1024\n"
"v_cvt_pk_bf16_f32 v251, v122, v123\n"
"v_mfma_f32_16x16x32_bf16 v[160:163], v[128:131], v[232:235], v[160:163]\n"
"s_getpc_b64 s[6:7]\n"
"s_sub_u32 s88, s88, 1\n"
"s_cbranch_scc1 L_hand_%=\n"
"s_add_u32 s89, s89, s91\n"
"s_add_u32 m0, s67, 0x0\n"
"v_mfma_f32_16x16x32_bf16 v[104:107], a[156:159], v[124:127], v[156:159]\n"
"v_exp_f32 v66, v66\n"
"v_mfma_f32_16x16x32_bf16 v[104:107], a[152:155], v[108:111], v[104:107]\n"
"buffer_load_dwordx4 v8, s[48:51], s89 offen offset:0 sc1 lds\n"
"v_mfma_f32_16x16x32_bf16 v[104:107], a[148:151], v[92:95], v[104:107]\n"
"v_exp_f32 v67, v67\n"
"v_mfma_f32_16x16x32_bf16 v[104:107], a[144:147], v[76:79], v[104:107]\n"
"ds_read_b128 a[128:131], v4 offset:0\n"
"v_cvt_pk_bf16_f32 v192, v64, v65\n"
"v_mfma_f32_16x16x32_bf16 v[112:115], a[144:147], v[48:51], v[236:239]\n"
"v_exp_f32 v72, v72\n"
"v_mfma_f32_16x16x32_bf16 v[112:115], a[148:151], v[100:103], v[112:115]\n"
"buffer_load_dwordx4 v8, s[48:51], s89 offen offset:1024 sc1 lds\n"
"v_mfma_f32_16x16x32_bf16 v[112:115], a[152:155], v[228:231], v[112:115]\n"
"buffer_load_dwordx4 v[68:71], v1, s[48:51], s73 offen offset:1024 sc1\n"
"v_exp_f32 v73, v73\n"
"v_mfma_f32_16x16x32_bf16 v[112:115], a[156:159], v[164:167], v[112:115]\n"
"v_cvt_pk_bf16_f32 v193, v66, v67\n"
"v_add_u32 v9, 0x8000, v3\n"
"v_mfma_f32_16x16x32_bf16 v[120:123], a[156:159], v[180:183], v[252:255]\n"
"v_exp_f32 v74, v74\n"
"v_mfma_f32_16x16x32_bf16 v[120:123], a[152:155], v[244:247], v[120:123]\n"
"buffer_load_dwordx4 v8, s[48:51], s89 offen offset:2048 sc1 lds\n"
"v_mfma_f32_16x16x32_bf16 v[120:123], a[148:151], v[116:119], v[120:123]\n"
"v_exp_f32 v75, v75\n"
"v_mfma_f32_16x16x32_bf16 v[120:123], a[144:147], v[56:59], v[120:123]\n"
"ds_read_b128 a[156:159], v4 offset:7168\n"
"v_cvt_pk_bf16_f32 v200, v72, v73\n"
"v_mfma_f32_16x16x32_bf16 a[192:195], a[160:163], v[224:227], a[192:195]\n"
"v_exp_f32 v80, v80\n"
"v_mfma_f32_16x16x32_bf16 a[224:227], a[160:163], v[232:235], a[224:227]\n"
"buffer_load_dwordx4 v8, s[48:51], s89 offen offset:3072 sc1 lds\n"
"v_mfma_f32_16x16x32_bf16 a[64:67], a[160:163], v[240:243], a[64:67]\n"
"buffer_load_dwordx4 v[196:199], v1, s[48:51], s73 offen offset:2048 sc1\n"
"v_exp_f32 v81, v81\n"
"v_mfma_f32_16x16x32_bf16 a[96:99], a[160:163], v[248:251], a[96:99]\n"
"v_cvt_pk_bf16_f32 v201, v74, v75\n"
"v_add_f32 v14, v14, v80\n"
"v_mfma_f32_16x16x32_bf16 a[100:103], a[164:167], v[248:251], a[100:103]\n"
"v_exp_f32 v82, v82\n"
"v_mfma_f32_16x16x32_bf16 a[68:71], a[164:167], v[240:243], a[68:71]\n"
"ds_read_b128 a[152:155], v4 offset:6144\n"
"v_add_f32 v14, v14, v81\n"
"v_mfma_f32_16x16x32_bf16 a[228:231], a[164:167], v[232:235], a[228:231]\n"
"buffer_load_dwordx4 v[132:135], v1, s[48:51], s73 offen offset:3072 sc1\n"
"v_exp_f32 v83, v83\n"
"v_mfma_f32_16x16x32_bf16 a[196:199], a[164:167], v[224:227], a[196:199]\n"
"v_cvt_pk_bf16_f32 v208, v80, v81\n"
"v_add_f32 v14, v14, v82\n"
"v_mfma_f32_16x16x32_bf16 a[200:203], a[168:171], v[224:227], a[200:203]\n"
"v_exp_f32 v88, v88\n"
"v_mfma_f32_16x16x32_bf16 a[232:235], a[168:171], v[232:235], a[232:235]\n"
"ds_read_b128 a[148:151], v4 offset:5120\n"
"v_add_f32 v14, v14, v83\n"
"s_add_u32 s90, s89, 0x1000\n"
"s_add_u32 m0, s67, 0x1000\n"
"s_add_u32 s73, s92, 0xd000\n"
"v_mfma_f32_16x16x32_bf16 a[72:75], a[168:171], v[240:243], a[72:75]\n"
"buffer_load_dwordx4 v[40:43], v1, s[48:51], s73 offen offset:0 sc1\n"
"v_exp_f32 v89, v89\n"
"v_mfma_f32_16x16x32_bf16 a[104:107], a[168:171], v[248:251], a[104:107]\n"
"buffer_load_dwordx4 v8, s[48:51], s90 offen offset:0 sc1 lds\n"
"v_cvt_pk_bf16_f32 v209, v82, v83\n"
"v_add_f32 v15, v15, v88\n"
"v_mfma_f32_16x16x32_bf16 a[108:111], a[172:175], v[248:251], a[108:111]\n"
"v_exp_f32 v90, v90\n"
"v_mfma_f32_16x16x32_bf16 a[76:79], a[172:175], v[240:243], a[76:79]\n"
"ds_read_b128 a[144:147], v4 offset:4096\n"
"v_add_f32 v15, v15, v89\n"
"v_mfma_f32_16x16x32_bf16 a[236:239], a[172:175], v[232:235], a[236:239]\n"
"buffer_load_dwordx4 v[84:87], v1, s[48:51], s73 offen offset:1024 sc1\n"
"v_exp_f32 v91, v91\n"
"v_mfma_f32_16x16x32_bf16 a[204:207], a[172:175], v[224:227], a[204:207]\n"
"v_cvt_pk_bf16_f32 v216, v88, v89\n"
"v_add_f32 v15, v15, v90\n"
"v_mfma_f32_16x16x32_bf16 a[208:211], a[176:179], v[224:227], a[208:211]\n"
"v_exp_f32 v96, v96\n"
"v_mfma_f32_16x16x32_bf16 a[240:243], a[176:179], v[232:235], a[240:243]\n"
"ds_read_b128 a[160:163], v3 offset:40960\n"
"v_add_f32 v15, v15, v91\n"
"v_mfma_f32_16x16x32_bf16 a[80:83], a[176:179], v[240:243], a[80:83]\n"
"buffer_load_dwordx4 v[212:215], v1, s[48:51], s73 offen offset:2048 sc1\n"
"v_exp_f32 v97, v97\n"
"v_mfma_f32_16x16x32_bf16 a[112:115], a[176:179], v[248:251], a[112:115]\n"
"ds_read_b128 a[164:167], v3 offset:41984\n"
"v_cvt_pk_bf16_f32 v217, v90, v91\n"
"v_mfma_f32_16x16x32_bf16 a[116:119], a[180:183], v[248:251], a[116:119]\n"
"v_exp_f32 v98, v98\n"
"v_mfma_f32_16x16x32_bf16 a[84:87], a[180:183], v[240:243], a[84:87]\n"
"buffer_load_dwordx4 v8, s[48:51], s90 offen offset:1024 sc1 lds\n"
"v_mfma_f32_16x16x32_bf16 a[244:247], a[180:183], v[232:235], a[244:247]\n"
"v_exp_f32 v99, v99\n"
"v_mfma_f32_16x16x32_bf16 a[212:215], a[180:183], v[224:227], a[212:215]\n"
"ds_read_b128 a[168:171], v3 offset:43008\n"
"v_cvt_pk_bf16_f32 v194, v96, v97\n"
"v_mfma_f32_16x16x32_bf16 a[216:219], a[184:187], v[224:227], a[216:219]\n"
"v_exp_f32 v104, v104\n"
"v_mfma_f32_16x16x32_bf16 a[248:251], a[184:187], v[232:235], a[248:251]\n"
"buffer_load_dwordx4 v8, s[48:51], s90 offen offset:2048 sc1 lds\n"
"v_mfma_f32_16x16x32_bf16 a[88:91], a[184:187], v[240:243], a[88:91]\n"
"v_exp_f32 v105, v105\n"
"v_mfma_f32_16x16x32_bf16 a[120:123], a[184:187], v[248:251], a[120:123]\n"
"ds_read_b128 a[172:175], v3 offset:44032\n"
"v_cvt_pk_bf16_f32 v195, v98, v99\n"
"v_mfma_f32_16x16x32_bf16 a[124:127], v[172:175], v[248:251], a[124:127]\n"
"v_exp_f32 v106, v106\n"
"v_mfma_f32_16x16x32_bf16 a[92:95], v[172:175], v[240:243], a[92:95]\n"
"buffer_load_dwordx4 v8, s[48:51], s90 offen offset:3072 sc1 lds\n"
"v_mfma_f32_16x16x32_bf16 a[252:255], v[172:175], v[232:235], a[252:255]\n"
"buffer_load_dwordx4 v[148:151], v1, s[48:51], s73 offen offset:3072 sc1\n"
"v_exp_f32 v107, v107\n"
"v_mfma_f32_16x16x32_bf16 a[220:223], v[172:175], v[224:227], a[220:223]\n"
"ds_read_b128 a[176:179], v3 offset:45056\n"
"v_cvt_pk_bf16_f32 v202, v104, v105\n"
"s_sub_u32 s96, s96, 1\n"
"s_cbranch_scc1 L_drainB_%=\n"
"s_branch L_B2_%=\n"
"L_BA0_%=:\n"
"s_waitcnt lgkmcnt(5)\n"
"v_mfma_f32_16x16x32_bf16 v[64:67], a[128:131], v[36:39], v[140:143]\n"
"v_exp_f32 v112, v112\n"
"v_mfma_f32_16x16x32_bf16 v[64:67], a[132:135], v[44:47], v[64:67]\n"
"ds_read_b128 a[180:183], v9 offset:13312\n"
"v_add_f32 v13, v13, v107\n"
"v_mfma_f32_16x16x32_bf16 v[64:67], a[136:139], v[52:55], v[64:67]\n"
"v_exp_f32 v113, v113\n"
"v_mfma_f32_16x16x32_bf16 v[64:67], a[140:143], v[60:63], v[64:67]\n"
"v_cvt_pk_bf16_f32 v203, v106, v107\n"
"v_add_f32 v14, v14, v112\n"
"v_mfma_f32_16x16x32_bf16 v[72:75], a[140:143], v[124:127], v[156:159]\n"
"v_exp_f32 v114, v114\n"
"v_mfma_f32_16x16x32_bf16 v[72:75], a[136:139], v[108:111], v[72:75]\n"
"ds_read_b128 a[184:187], v9 offset:14336\n"
"v_add_f32 v14, v14, v113\n"
"v_mfma_f32_16x16x32_bf16 v[72:75], a[132:135], v[92:95], v[72:75]\n"
"v_exp_f32 v115, v115\n"
"v_mfma_f32_16x16x32_bf16 v[72:75], a[128:131], v[76:79], v[72:75]\n"
"v_cvt_pk_bf16_f32 v210, v112, v113\n"
"v_add_f32 v14, v14, v114\n"
"v_add_u32 v9, 0x0, v3\n"
"v_mfma_f32_16x16x32_bf16 v[80:83], a[128:131], v[48:51], v[236:239]\n"
"v_exp_f32 v120, v120\n"
"v_mfma_f32_16x16x32_bf16 v[80:83], a[132:135], v[100:103], v[80:83]\n"
"ds_read_b128 v[172:175], v3 offset:15360\n"
"v_add_f32 v14, v14, v115\n"
"v_mfma_f32_16x16x32_bf16 v[80:83], a[136:139], v[228:231], v[80:83]\n"
"v_exp_f32 v121, v121\n"
"v_mfma_f32_16x16x32_bf16 v[80:83], a[140:143], v[164:167], v[80:83]\n"
"v_cvt_pk_bf16_f32 v211, v114, v115\n"
"v_add_f32 v15, v15, v120\n"
"s_waitcnt vmcnt(4) lgkmcnt(1)\n"
"s_barrier\n"
"v_mfma_f32_16x16x32_bf16 v[88:91], a[140:143], v[180:183], v[252:255]\n"
"v_exp_f32 v122, v122\n"
"v_mfma_f32_16x16x32_bf16 v[88:91], a[136:139], v[244:247], v[88:91]\n"
"ds_read_b128 a[140:143], v3 offset:35840\n"
"v_add_f32 v15, v15, v121\n"
"v_mfma_f32_16x16x32_bf16 v[88:91], a[132:135], v[116:119], v[88:91]\n"
"v_exp_f32 v123, v123\n"
"s_add_u32 s90, s89, 0x1000\n"
"s_add_u32 m0, s67, 0x11000\n"
"v_mfma_f32_16x16x32_bf16 v[88:91], a[128:131], v[56:59], v[88:91]\n"
"buffer_load_dwordx4 v8, s[48:51], s90 offen offset:0 sc1 lds\n"
"v_cvt_pk_bf16_f32 v218, v120, v121\n"
"v_add_f32 v15, v15, v122\n"
"v_mfma_f32_16x16x32_bf16 v[96:99], a[144:147], v[36:39], v[140:143]\n"
"v_exp_f32 v64, v64\n"
"v_mfma_f32_16x16x32_bf16 v[96:99], a[148:151], v[44:47], v[96:99]\n"
"ds_read_b128 a[136:139], v3 offset:34816\n"
"v_add_f32 v15, v15, v123\n"
"v_mfma_f32_16x16x32_bf16 v[96:99], a[152:155], v[52:55], v[96:99]\n"
"v_exp_f32 v65, v65\n"
"v_mfma_f32_16x16x32_bf16 v[96:99], a[156:159], v[60:63], v[96:99]\n"
"ds_read_b128 a[132:135], v3 offset:33792\n"
"v_cvt_pk_bf16_f32 v219, v122, v123\n"
"v_mfma_f32_16x16x32_bf16 v[104:107], a[156:159], v[124:127], v[156:159]\n"
"v_exp_f32 v66, v66\n"
"v_mfma_f32_16x16x32_bf16 v[104:107], a[152:155], v[108:111], v[104:107]\n"
"buffer_load_dwordx4 v8, s[48:51], s90 offen offset:1024 sc1 lds\n"
"v_mfma_f32_16x16x32_bf16 v[104:107], a[148:151], v[92:95], v[104:107]\n"
"v_exp_f32 v67, v67\n"
"v_mfma_f32_16x16x32_bf16 v[104:107], a[144:147], v[76:79], v[104:107]\n"
"ds_read_b128 a[128:131], v3 offset:32768\n"
"v_cvt_pk_bf16_f32 v224, v64, v65\n"
"v_mfma_f32_16x16x32_bf16 v[112:115], a[144:147], v[48:51], v[236:239]\n"
"v_exp_f32 v72, v72\n"
"v_mfma_f32_16x16x32_bf16 v[112:115], a[148:151], v[100:103], v[112:115]\n"
"buffer_load_dwordx4 v8, s[48:51], s90 offen offset:2048 sc1 lds\n"
"v_mfma_f32_16x16x32_bf16 v[112:115], a[152:155], v[228:231], v[112:115]\n"
"v_exp_f32 v73, v73\n"
"v_mfma_f32_16x16x32_bf16 v[112:115], a[156:159], v[164:167], v[112:115]\n"
"buffer_load_dwordx4 v8, s[48:51], s90 offen offset:3072 sc1 lds\n"
"v_cvt_pk_bf16_f32 v225, v66, v67\n"
"v_mfma_f32_16x16x32_bf16 v[120:123], a[156:159], v[180:183], v[252:255]\n"
"v_exp_f32 v74, v74\n"
"s_getpc_b64 s[6:7]\n"
"s_sub_u32 s88, s88, 1\n"
"s_cbranch_scc1 L_hand_%=\n"
"s_add_u32 s89, s89, s91\n"
"s_add_u32 m0, s67, 0x18000\n"
"v_mfma_f32_16x16x32_bf16 v[120:123], a[152:155], v[244:247], v[120:123]\n"
"buffer_load_dwordx4 v8, s[48:51], s89 offen offset:0 sc1 lds\n"
"v_mfma_f32_16x16x32_bf16 v[120:123], a[148:151], v[116:119], v[120:123]\n"
"v_exp_f32 v75, v75\n"
"v_mfma_f32_16x16x32_bf16 v[120:123], a[144:147], v[56:59], v[120:123]\n"
"ds_read_b128 a[156:159], v3 offset:39936\n"
"v_cvt_pk_bf16_f32 v232, v72, v73\n"
"v_mfma_f32_16x16x32_bf16 a[0:3], a[160:163], v[192:195], a[0:3]\n"
"v_exp_f32 v80, v80\n"
"v_mfma_f32_16x16x32_bf16 a[32:35], a[160:163], v[200:203], a[32:35]\n"
"buffer_load_dwordx4 v8, s[48:51], s89 offen offset:1024 sc1 lds\n"
"v_mfma_f32_16x16x32_bf16 a[64:67], a[160:163], v[208:211], a[64:67]\n"
"v_exp_f32 v81, v81\n"
"v_mfma_f32_16x16x32_bf16 a[96:99], a[160:163], v[216:219], a[96:99]\n"
"v_cvt_pk_bf16_f32 v233, v74, v75\n"
"v_add_f32 v14, v14, v80\n"
"v_mfma_f32_16x16x32_bf16 a[100:103], a[164:167], v[216:219], a[100:103]\n"
"v_exp_f32 v82, v82\n"
"v_mfma_f32_16x16x32_bf16 a[68:71], a[164:167], v[208:211], a[68:71]\n"
"ds_read_b128 a[152:155], v3 offset:38912\n"
"v_add_f32 v14, v14, v81\n"
"v_mfma_f32_16x16x32_bf16 a[36:39], a[164:167], v[200:203], a[36:39]\n"
"v_exp_f32 v83, v83\n"
"v_mfma_f32_16x16x32_bf16 a[4:7], a[164:167], v[192:195], a[4:7]\n"
"buffer_load_dwordx4 v8, s[48:51], s89 offen offset:2048 sc1 lds\n"
"v_cvt_pk_bf16_f32 v240, v80, v81\n"
"v_add_f32 v14, v14, v82\n"
"v_mfma_f32_16x16x32_bf16 a[8:11], a[168:171], v[192:195], a[8:11]\n"
"v_exp_f32 v88, v88\n"
"v_mfma_f32_16x16x32_bf16 a[40:43], a[168:171], v[200:203], a[40:43]\n"
"ds_read_b128 a[148:151], v3 offset:37888\n"
"v_add_f32 v14, v14, v83\n"
"v_mfma_f32_16x16x32_bf16 a[72:75], a[168:171], v[208:211], a[72:75]\n"
"v_exp_f32 v89, v89\n"
"v_mfma_f32_16x16x32_bf16 a[104:107], a[168:171], v[216:219], a[104:107]\n"
"buffer_load_dwordx4 v8, s[48:51], s89 offen offset:3072 sc1 lds\n"
"v_cvt_pk_bf16_f32 v241, v82, v83\n"
"v_add_f32 v15, v15, v88\n"
"v_mfma_f32_16x16x32_bf16 a[108:111], a[172:175], v[216:219], a[108:111]\n"
"v_exp_f32 v90, v90\n"
"v_mfma_f32_16x16x32_bf16 a[76:79], a[172:175], v[208:211], a[76:79]\n"
"ds_read_b128 a[144:147], v3 offset:36864\n"
"v_add_f32 v15, v15, v89\n"
"v_mfma_f32_16x16x32_bf16 a[44:47], a[172:175], v[200:203], a[44:47]\n"
"v_exp_f32 v91, v91\n"
"s_add_u32 s90, s89, 0x1000\n"
"s_add_u32 m0, s67, 0x19000\n"
"v_mfma_f32_16x16x32_bf16 a[12:15], a[172:175], v[192:195], a[12:15]\n"
"buffer_load_dwordx4 v8, s[48:51], s90 offen offset:0 sc1 lds\n"
"v_cvt_pk_bf16_f32 v248, v88, v89\n"
"v_add_f32 v15, v15, v90\n"
"v_mfma_f32_16x16x32_bf16 a[16:19], a[176:179], v[192:195], a[16:19]\n"
"v_exp_f32 v96, v96\n"
"v_mfma_f32_16x16x32_bf16 a[48:51], a[176:179], v[200:203], a[48:51]\n"
"ds_read_b128 a[160:163], v3 offset:8192\n"
"v_add_f32 v15, v15, v91\n"
"v_mfma_f32_16x16x32_bf16 a[80:83], a[176:179], v[208:211], a[80:83]\n"
"v_exp_f32 v97, v97\n"
"v_mfma_f32_16x16x32_bf16 a[112:115], a[176:179], v[216:219], a[112:115]\n"
"ds_read_b128 a[164:167], v3 offset:9216\n"
"v_cvt_pk_bf16_f32 v249, v90, v91\n"
"v_mfma_f32_16x16x32_bf16 a[116:119], a[180:183], v[216:219], a[116:119]\n"
"v_exp_f32 v98, v98\n"
"v_mfma_f32_16x16x32_bf16 a[84:87], a[180:183], v[208:211], a[84:87]\n"
"buffer_load_dwordx4 v8, s[48:51], s90 offen offset:1024 sc1 lds\n"
"v_mfma_f32_16x16x32_bf16 a[52:55], a[180:183], v[200:203], a[52:55]\n"
"v_exp_f32 v99, v99\n"
"v_mfma_f32_16x16x32_bf16 a[20:23], a[180:183], v[192:195], a[20:23]\n"
"ds_read_b128 a[168:171], v3 offset:10240\n"
"v_cvt_pk_bf16_f32 v226, v96, v97\n"
"v_mfma_f32_16x16x32_bf16 a[24:27], a[184:187], v[192:195], a[24:27]\n"
"v_exp_f32 v104, v104\n"
"v_mfma_f32_16x16x32_bf16 a[56:59], a[184:187], v[200:203], a[56:59]\n"
"buffer_load_dwordx4 v8, s[48:51], s90 offen offset:2048 sc1 lds\n"
"v_mfma_f32_16x16x32_bf16 a[88:91], a[184:187], v[208:211], a[88:91]\n"
"v_exp_f32 v105, v105\n"
"v_mfma_f32_16x16x32_bf16 a[120:123], a[184:187], v[216:219], a[120:123]\n"
"ds_read_b128 a[172:175], v3 offset:11264\n"
"v_cvt_pk_bf16_f32 v227, v98, v99\n"
"v_mfma_f32_16x16x32_bf16 a[124:127], a[188:191], v[216:219], a[124:127]\n"
"v_exp_f32 v106, v106\n"
"v_mfma_f32_16x16x32_bf16 a[92:95], a[188:191], v[208:211], a[92:95]\n"
"buffer_load_dwordx4 v8, s[48:51], s90 offen offset:3072 sc1 lds\n"
"v_mfma_f32_16x16x32_bf16 a[60:63], a[188:191], v[200:203], a[60:63]\n"
"v_exp_f32 v107, v107\n"
"v_mfma_f32_16x16x32_bf16 a[28:31], a[188:191], v[192:195], a[28:31]\n"
"ds_read_b128 a[176:179], v3 offset:12288\n"
"v_cvt_pk_bf16_f32 v234, v104, v105\n"
"s_branch L_fin1_%=\n"
"L_drainA_%=:\n"
"ds_read_b128 a[180:183], v9 offset:13312\n"
"ds_read_b128 a[184:187], v9 offset:14336\n"
"v_add_f32 v13, v13, v107\n"
"v_cvt_pk_bf16_f32 v203, v106, v107\n"
"v_exp_f32 v112, v112\n"
"s_nop 0\n"
"v_add_f32 v14, v14, v112\n"
"v_exp_f32 v113, v113\n"
"s_nop 0\n"
"v_add_f32 v14, v14, v113\n"
"v_cvt_pk_bf16_f32 v210, v112, v113\n"
"v_exp_f32 v114, v114\n"
"s_nop 0\n"
"v_add_f32 v14, v14, v114\n"
"v_exp_f32 v115, v115\n"
"s_nop 0\n"
"v_add_f32 v14, v14, v115\n"
"v_cvt_pk_bf16_f32 v211, v114, v115\n"
"v_exp_f32 v120, v120\n"
"s_nop 0\n"
"v_add_f32 v15, v15, v120\n"
"v_exp_f32 v121, v121\n"
"s_nop 0\n"
"v_add_f32 v15, v15, v121\n"
"v_cvt_pk_bf16_f32 v218, v120, v121\n"
"v_exp_f32 v122, v122\n"
"s_nop 0\n"
"v_add_f32 v15, v15, v122\n"
"v_exp_f32 v123, v123\n"
"s_nop 0\n"
"v_add_f32 v15, v15, v123\n"
"v_cvt_pk_bf16_f32 v219, v122, v123\n"
"s_waitcnt lgkmcnt(0)\n"
"v_mfma_f32_16x16x32_bf16 a[0:3], a[160:163], v[192:195], a[0:3]\n"
"v_mfma_f32_16x16x32_bf16 a[32:35], a[160:163], v[200:203], a[32:35]\n"
"v_mfma_f32_16x16x32_bf16 a[4:7], a[164:167], v[192:195], a[4:7]\n"
"s_add_u32 s90, s89, 0x1000\n"
"v_mfma_f32_16x16x32_bf16 a[36:39], a[164:167], v[200:203], a[36:39]\n"
"v_mfma_f32_16x16x32_bf16 a[8:11], a[168:171], v[192:195], a[8:11]\n"
"v_mfma_f32_16x16x32_bf16 a[40:43], a[168:171], v[200:203], a[40:43]\n"
"v_mfma_f32_16x16x32_bf16 a[12:15], a[172:175], v[192:195], a[12:15]\n"
"s_add_u32 m0, s70, s97\n"
"v_mfma_f32_16x16x32_bf16 a[44:47], a[172:175], v[200:203], a[44:47]\n"
"v_mfma_f32_16x16x32_bf16 a[16:19], a[176:179], v[192:195], a[16:19]\n"
"v_mfma_f32_16x16x32_bf16 a[48:51], a[176:179], v[200:203], a[48:51]\n"
"v_mfma_f32_16x16x32_bf16 a[20:23], a[180:183], v[192:195], a[20:23]\n"
"buffer_load_dwordx4 v8, s[48:51], s90 offen offset:0 sc1 lds\n"
"v_mfma_f32_16x16x32_bf16 a[52:55], a[180:183], v[200:203], a[52:55]\n"
"v_mfma_f32_16x16x32_bf16 a[24:27], a[184:187], v[192:195], a[24:27]\n"
"v_mfma_f32_16x16x32_bf16 a[56:59], a[184:187], v[200:203], a[56:59]\n"
"v_mfma_f32_16x16x32_bf16 a[28:31], a[188:191], v[192:195], a[28:31]\n"
"buffer_load_dwordx4 v8, s[48:51], s90 offen offset:1024 sc1 lds\n"
"v_mfma_f32_16x16x32_bf16 a[60:63], a[188:191], v[200:203], a[60:63]\n"
"v_mfma_f32_16x16x32_bf16 a[64:67], a[160:163], v[208:211], a[64:67]\n"
"v_mfma_f32_16x16x32_bf16 a[96:99], a[160:163], v[216:219], a[96:99]\n"
"v_mfma_f32_16x16x32_bf16 a[68:71], a[164:167], v[208:211], a[68:71]\n"
"buffer_load_dwordx4 v8, s[48:51], s90 offen offset:2048 sc1 lds\n"
"v_mfma_f32_16x16x32_bf16 a[100:103], a[164:167], v[216:219], a[100:103]\n"
"v_mfma_f32_16x16x32_bf16 a[72:75], a[168:171], v[208:211], a[72:75]\n"
"v_mfma_f32_16x16x32_bf16 a[104:107], a[168:171], v[216:219], a[104:107]\n"
"v_mfma_f32_16x16x32_bf16 a[76:79], a[172:175], v[208:211], a[76:79]\n"
"buffer_load_dwordx4 v8, s[48:51], s90 offen offset:3072 sc1 lds\n"
"v_mfma_f32_16x16x32_bf16 a[108:111], a[172:175], v[216:219], a[108:111]\n"
"v_mfma_f32_16x16x32_bf16 a[80:83], a[176:179], v[208:211], a[80:83]\n"
"v_mfma_f32_16x16x32_bf16 a[112:115], a[176:179], v[216:219], a[112:115]\n"
"v_mfma_f32_16x16x32_bf16 a[84:87], a[180:183], v[208:211], a[84:87]\n"
"v_mfma_f32_16x16x32_bf16 a[116:119], a[180:183], v[216:219], a[116:119]\n"
"v_mfma_f32_16x16x32_bf16 a[88:91], a[184:187], v[208:211], a[88:91]\n"
"v_mfma_f32_16x16x32_bf16 a[120:123], a[184:187], v[216:219], a[120:123]\n"
"v_mfma_f32_16x16x32_bf16 a[92:95], a[188:191], v[208:211], a[92:95]\n"
"v_mfma_f32_16x16x32_bf16 a[124:127], a[188:191], v[216:219], a[124:127]\n"
"v_mov_b32 v188, v12\n"
"v_mov_b32 v189, v13\n"
"s_nop 0\n"
"v_permlane16_swap_b32 v12, v188\n"
"v_permlane16_swap_b32 v13, v189\n"
"v_add_f32 v12, v12, v188\n"
"v_add_f32 v13, v13, v189\n"
"v_mov_b32 v188, v12\n"
"v_mov_b32 v189, v13\n"
"s_nop 0\n"
"v_permlane32_swap_b32 v12, v188\n"
"v_permlane32_swap_b32 v13, v189\n"
"v_add_f32 v12, v12, v188\n"
"v_add_f32 v13, v13, v189\n"
"v_add_f32 v188, v12, v13\n"
"v_mov_b32 v189, 0x5f800000\n"
"v_cmp_lt_f32 vcc, v188, v189\n"
"s_nop 0\n"
"s_cmp_eq_u64 vcc, -1\n"
"s_cselect_b32 s35, 0, 1\n"
"v_rcp_f32 v12, v12\n"
"v_rcp_f32 v7, v13\n"
"v_mov_b32 v13, v12\n"
"s_sub_u32 s73, s78, 0x100000\n"
"s_lshl_b32 s73, s73, 3\n"
"s_lshl_b32 s74, s65, 12\n"
"s_add_u32 s73, s73, s74\n"
"s_add_u32 s73, s73, 0x2400000\n"
"v_accvgpr_read_b32 v188, a0\n"
"v_accvgpr_read_b32 v189, a1\n"
"v_accvgpr_read_b32 v190, a2\n"
"v_accvgpr_read_b32 v191, a3\n"
"v_pk_mul_f32 v[188:189], v[188:189], v[12:13]\n"
"v_pk_mul_f32 v[190:191], v[190:191], v[12:13]\n"
"v_cvt_pk_bf16_f32 v24, v188, v189\n"
"v_cvt_pk_bf16_f32 v25, v190, v191\n"
"v_accvgpr_read_b32 v188, a4\n"
"v_accvgpr_read_b32 v189, a5\n"
"v_accvgpr_read_b32 v190, a6\n"
"v_accvgpr_read_b32 v191, a7\n"
"v_pk_mul_f32 v[188:189], v[188:189], v[12:13]\n"
"v_pk_mul_f32 v[190:191], v[190:191], v[12:13]\n"
"v_cvt_pk_bf16_f32 v26, v188, v189\n"
"v_cvt_pk_bf16_f32 v27, v190, v191\n"
"buffer_store_dwordx4 v[24:27], v1, s[48:51], s73 offen offset:0 sc1\n"
"v_accvgpr_read_b32 v188, a8\n"
"v_accvgpr_read_b32 v189, a9\n"
"v_accvgpr_read_b32 v190, a10\n"
"v_accvgpr_read_b32 v191, a11\n"
"v_pk_mul_f32 v[188:189], v[188:189], v[12:13]\n"
"v_pk_mul_f32 v[190:191], v[190:191], v[12:13]\n"
"v_cvt_pk_bf16_f32 v28, v188, v189\n"
"v_cvt_pk_bf16_f32 v29, v190, v191\n"
"v_accvgpr_read_b32 v188, a12\n"
"v_accvgpr_read_b32 v189, a13\n"
"v_accvgpr_read_b32 v190, a14\n"
"v_accvgpr_read_b32 v191, a15\n"
"v_pk_mul_f32 v[188:189], v[188:189], v[12:13]\n"
"v_pk_mul_f32 v[190:191], v[190:191], v[12:13]\n"
"v_cvt_pk_bf16_f32 v30, v188, v189\n"
"v_cvt_pk_bf16_f32 v31, v190, v191\n"
"buffer_store_dwordx4 v[28:31], v1, s[48:51], s73 offen offset:1024 sc1\n"
"v_accvgpr_read_b32 v188, a16\n"
"v_accvgpr_read_b32 v189, a17\n"
"v_accvgpr_read_b32 v190, a18\n"
"v_accvgpr_read_b32 v191, a19\n"
"v_pk_mul_f32 v[188:189], v[188:189], v[12:13]\n"
"v_pk_mul_f32 v[190:191], v[190:191], v[12:13]\n"
"v_cvt_pk_bf16_f32 v24, v188, v189\n"
"v_cvt_pk_bf16_f32 v25, v190, v191\n"
"v_accvgpr_read_b32 v188, a20\n"
"v_accvgpr_read_b32 v189, a21\n"
"v_accvgpr_read_b32 v190, a22\n"
"v_accvgpr_read_b32 v191, a23\n"
"v_pk_mul_f32 v[188:189], v[188:189], v[12:13]\n"
"v_pk_mul_f32 v[190:191], v[190:191], v[12:13]\n"
"v_cvt_pk_bf16_f32 v26, v188, v189\n"
"v_cvt_pk_bf16_f32 v27, v190, v191\n"
"buffer_store_dwordx4 v[24:27], v1, s[48:51], s73 offen offset:2048 sc1\n"
"v_accvgpr_read_b32 v188, a24\n"
"v_accvgpr_read_b32 v189, a25\n"
"v_accvgpr_read_b32 v190, a26\n"
"v_accvgpr_read_b32 v191, a27\n"
"v_pk_mul_f32 v[188:189], v[188:189], v[12:13]\n"
"v_pk_mul_f32 v[190:191], v[190:191], v[12:13]\n"
"v_cvt_pk_bf16_f32 v28, v188, v189\n"
"v_cvt_pk_bf16_f32 v29, v190, v191\n"
"v_accvgpr_read_b32 v188, a28\n"
"v_accvgpr_read_b32 v189, a29\n"
"v_accvgpr_read_b32 v190, a30\n"
"v_accvgpr_read_b32 v191, a31\n"
"v_pk_mul_f32 v[188:189], v[188:189], v[12:13]\n"
"v_pk_mul_f32 v[190:191], v[190:191], v[12:13]\n"
"v_cvt_pk_bf16_f32 v30, v188, v189\n"
"v_cvt_pk_bf16_f32 v31, v190, v191\n"
"buffer_store_dwordx4 v[28:31], v1, s[48:51], s73 offen offset:3072 sc1\n"
"v_mov_b32 v12, v7\n"
"v_mov_b32 v13, v7\n"
"s_sub_u32 s73, s79, 0x100000\n"
"s_lshl_b32 s73, s73, 3\n"
"s_lshl_b32 s74, s65, 12\n"
"s_add_u32 s73, s73, s74\n"
"s_add_u32 s73, s73, 0x2400000\n"
"v_accvgpr_read_b32 v188, a32\n"
"v_accvgpr_read_b32 v189, a33\n"
"v_accvgpr_read_b32 v190, a34\n"
"v_accvgpr_read_b32 v191, a35\n"
"v_pk_mul_f32 v[188:189], v[188:189], v[12:13]\n"
"v_pk_mul_f32 v[190:191], v[190:191], v[12:13]\n"
"v_cvt_pk_bf16_f32 v24, v188, v189\n"
"v_cvt_pk_bf16_f32 v25, v190, v191\n"
"v_accvgpr_read_b32 v188, a36\n"
"v_accvgpr_read_b32 v189, a37\n"
"v_accvgpr_read_b32 v190, a38\n"
"v_accvgpr_read_b32 v191, a39\n"
"v_pk_mul_f32 v[188:189], v[188:189], v[12:13]\n"
"v_pk_mul_f32 v[190:191], v[190:191], v[12:13]\n"
"v_cvt_pk_bf16_f32 v26, v188, v189\n"
"v_cvt_pk_bf16_f32 v27, v190, v191\n"
"buffer_store_dwordx4 v[24:27], v1, s[48:51], s73 offen offset:0 sc1\n"
"v_accvgpr_read_b32 v188, a40\n"
"v_accvgpr_read_b32 v189, a41\n"
"v_accvgpr_read_b32 v190, a42\n"
"v_accvgpr_read_b32 v191, a43\n"
"v_pk_mul_f32 v[188:189], v[188:189], v[12:13]\n"
"v_pk_mul_f32 v[190:191], v[190:191], v[12:13]\n"
"v_cvt_pk_bf16_f32 v28, v188, v189\n"
"v_cvt_pk_bf16_f32 v29, v190, v191\n"
"v_accvgpr_read_b32 v188, a44\n"
"v_accvgpr_read_b32 v189, a45\n"
"v_accvgpr_read_b32 v190, a46\n"
"v_accvgpr_read_b32 v191, a47\n"
"v_pk_mul_f32 v[188:189], v[188:189], v[12:13]\n"
"v_pk_mul_f32 v[190:191], v[190:191], v[12:13]\n"
"v_cvt_pk_bf16_f32 v30, v188, v189\n"
"v_cvt_pk_bf16_f32 v31, v190, v191\n"
"buffer_store_dwordx4 v[28:31], v1, s[48:51], s73 offen offset:1024 sc1\n"
"v_accvgpr_read_b32 v188, a48\n"
"v_accvgpr_read_b32 v189, a49\n"
"v_accvgpr_read_b32 v190, a50\n"
"v_accvgpr_read_b32 v191, a51\n"
"v_pk_mul_f32 v[188:189], v[188:189], v[12:13]\n"
"v_pk_mul_f32 v[190:191], v[190:191], v[12:13]\n"
"v_cvt_pk_bf16_f32 v24, v188, v189\n"
"v_cvt_pk_bf16_f32 v25, v190, v191\n"
"v_accvgpr_read_b32 v188, a52\n"
"v_accvgpr_read_b32 v189, a53\n"
"v_accvgpr_read_b32 v190, a54\n"
"v_accvgpr_read_b32 v191, a55\n"
"v_pk_mul_f32 v[188:189], v[188:189], v[12:13]\n"
"v_pk_mul_f32 v[190:191], v[190:191], v[12:13]\n"
"v_cvt_pk_bf16_f32 v26, v188, v189\n"
"v_cvt_pk_bf16_f32 v27, v190, v191\n"
"buffer_store_dwordx4 v[24:27], v1, s[48:51], s73 offen offset:2048 sc1\n"
"v_accvgpr_read_b32 v188, a56\n"
"v_accvgpr_read_b32 v189, a57\n"
"v_accvgpr_read_b32 v190, a58\n"
"v_accvgpr_read_b32 v191, a59\n"
"v_pk_mul_f32 v[188:189], v[188:189], v[12:13]\n"
"v_pk_mul_f32 v[190:191], v[190:191], v[12:13]\n"
"v_cvt_pk_bf16_f32 v28, v188, v189\n"
"v_cvt_pk_bf16_f32 v29, v190, v191\n"
"v_accvgpr_read_b32 v188, a60\n"
"v_accvgpr_read_b32 v189, a61\n"
"v_accvgpr_read_b32 v190, a62\n"
"v_accvgpr_read_b32 v191, a63\n"
"v_pk_mul_f32 v[188:189], v[188:189], v[12:13]\n"
"v_pk_mul_f32 v[190:191], v[190:191], v[12:13]\n"
"v_cvt_pk_bf16_f32 v30, v188, v189\n"
"v_cvt_pk_bf16_f32 v31, v190, v191\n"
"buffer_store_dwordx4 v[28:31], v1, s[48:51], s73 offen offset:3072 sc1\n"
"s_and_b32 s73, s65, 1\n"
"s_lshl_b32 s73, s73, 2\n"
"s_add_u32 s73, s73, s64\n"
"s_lshl_b32 s73, s73, 16\n"
"s_lshl_b32 s92, s3, 19\n"
"s_add_u32 s92, s92, s73\n"
"s_add_u32 s92, s92, 0x3400000\n"
"s_waitcnt vmcnt(24)\n"
"v_cmp_eq_u64 vcc, s[68:69], v[18:19]\n"
"s_nop 0\n"
"s_cmp_eq_u64 vcc, -1\n"
"s_cbranch_scc0 L_ya_spin%=\n"
"LT_ya_ok%=:\n"
"s_waitcnt lgkmcnt(0)\n"
"s_barrier\n"
"s_and_b32 s73, s66, 1\n"
"s_lshl_b32 s73, s73, 16\n"
"s_add_u32 s76, s73, s67\n"
"s_xor_b32 s95, s76, 0x10000\n"
"s_add_u32 s97, s67, 0x20000\n"
"v_add_u32 v4, s97, v1\n"
"v_add_u32 v3, s95, v1\n"
"v_add_u32 v2, s76, v1\n"
"ds_read_b128 a[128:131], v2 offset:0\n"
"ds_read_b128 a[132:135], v2 offset:1024\n"
"ds_read_b128 a[136:139], v2 offset:2048\n"
"ds_read_b128 a[140:143], v2 offset:3072\n"
"ds_read_b128 a[144:147], v2 offset:4096\n"
"ds_read_b128 a[148:151], v2 offset:5120\n"
"ds_read_b128 a[152:155], v2 offset:6144\n"
"ds_read_b128 a[156:159], v2 offset:7168\n"
"ds_write_b128 v4, a[192:195] offset:0\n"
"ds_write_b128 v3, a[224:227] offset:32768\n"
"ds_write_b128 v4, a[196:199] offset:1024\n"
"ds_write_b128 v3, a[228:231] offset:33792\n"
"ds_write_b128 v4, a[200:203] offset:2048\n"
"ds_write_b128 v3, a[232:235] offset:34816\n"
"ds_write_b128 v4, a[204:207] offset:3072\n"
"ds_write_b128 v3, a[236:239] offset:35840\n"
"ds_write_b128 v4, a[208:211] offset:4096\n"
"ds_write_b128 v3, a[240:243] offset:36864\n"
"ds_write_b128 v4, a[212:215] offset:5120\n"
"ds_write_b128 v3, a[244:247] offset:37888\n"
"ds_write_b128 v4, a[216:219] offset:6144\n"
"ds_write_b128 v3, a[248:251] offset:38912\n"
"ds_write_b128 v4, a[220:223] offset:7168\n"
"ds_write_b128 v3, a[252:255] offset:39936\n"
"s_waitcnt vmcnt(0)\n"
"v_cmp_eq_u32 vcc, 0, v21\n"
"v_mov_b32 v27, -1\n"
"s_lshl_b32 s73, s66, 2\n"
"s_add_u32 s73, s73, s64\n"
"v_cndmask_b32 v27, v27, 0, vcc\n"
"s_lshl_b32 s73, s73, 8\n"
"s_lshl_b32 s74, s65, 3\n"
"s_add_u32 s73, s73, s74\n"
"s_add_u32 s73, s73, 0x92000\n"
"v_mov_b32 v30, s68\n"
"v_mov_b32 v31, s69\n"
"s_cmp_eq_u32 s35, 0\n"
"s_cselect_b32 s73, s73, 0x40000000\n"
"buffer_store_dwordx2 v[30:31], v27, s[48:51], s73 offen sc1\n"
"s_branch L_fin_%=\n"
"L_late7_%=:\n"
"s_add_u32 s73, s92, 0x6000\n"
"buffer_load_dwordx4 a[0:3], v1, s[48:51], s73 offen offset:0 sc1\n"
"buffer_load_dwordx4 a[4:7], v1, s[48:51], s73 offen offset:1024 sc1\n"
"buffer_load_dwordx4 a[8:11], v1, s[48:51], s73 offen offset:2048 sc1\n"
"buffer_load_dwordx4 a[12:15], v1, s[48:51], s73 offen offset:3072 sc1\n"
"s_add_u32 s73, s92, 0x7000\n"
"buffer_load_dwordx4 a[16:19], v1, s[48:51], s73 offen offset:0 sc1\n"
"buffer_load_dwordx4 a[20:23], v1, s[48:51], s73 offen offset:1024 sc1\n"
"buffer_load_dwordx4 a[24:27], v1, s[48:51], s73 offen offset:2048 sc1\n"
"buffer_load_dwordx4 a[28:31], v1, s[48:51], s73 offen offset:3072 sc1\n"
"s_add_u32 s73, s92, 0x8000\n"
"buffer_load_dwordx4 a[32:35], v1, s[48:51], s73 offen offset:0 sc1\n"
"buffer_load_dwordx4 a[36:39], v1, s[48:51], s73 offen offset:1024 sc1\n"
"buffer_load_dwordx4 a[40:43], v1, s[48:51], s73 offen offset:2048 sc1\n"
"buffer_load_dwordx4 a[44:47], v1, s[48:51], s73 offen offset:3072 sc1\n"
"s_add_u32 s73, s92, 0x9000\n"
"buffer_load_dwordx4 a[48:51], v1, s[48:51], s73 offen offset:0 sc1\n"
"buffer_load_dwordx4 a[52:55], v1, s[48:51], s73 offen offset:1024 sc1\n"
"buffer_load_dwordx4 a[56:59], v1, s[48:51], s73 offen offset:2048 sc1\n"
"buffer_load_dwordx4 a[60:63], v1, s[48:51], s73 offen offset:3072 sc1\n"
"s_add_u32 s73, s92, 0xb000\n"
"buffer_load_dwordx4 v[184:187], v1, s[48:51], s73 offen offset:0 sc1\n"
"buffer_load_dwordx4 v[204:207], v1, s[48:51], s73 offen offset:1024 sc1\n"
"buffer_load_dwordx4 v[220:223], v1, s[48:51], s73 offen offset:2048 sc1\n"
"buffer_load_dwordx4 v[188:191], v1, s[48:51], s73 offen offset:3072 sc1\n"
"s_add_u32 s73, s92, 0xc000\n"
"buffer_load_dwordx4 v[32:35], v1, s[48:51], s73 offen offset:0 sc1\n"
"buffer_load_dwordx4 v[68:71], v1, s[48:51], s73 offen offset:1024 sc1\n"
"buffer_load_dwordx4 v[196:199], v1, s[48:51], s73 offen offset:2048 sc1\n"
"buffer_load_dwordx4 v[132:135], v1, s[48:51], s73 offen offset:3072 sc1\n"
"s_add_u32 s73, s92, 0xd000\n"
"buffer_load_dwordx4 v[40:43], v1, s[48:51], s73 offen offset:0 sc1\n"
"buffer_load_dwordx4 v[84:87], v1, s[48:51], s73 offen offset:1024 sc1\n"
"buffer_load_dwordx4 v[212:215], v1, s[48:51], s73 offen offset:2048 sc1\n"
"buffer_load_dwordx4 v[148:151], v1, s[48:51], s73 offen offset:3072 sc1\n"
"s_waitcnt vmcnt(20)\n"
"s_branch LT_late7r_%=\n"
"L_bad1_%=:\n"
"s_add_u32 s73, s92, 0x6000\n"
"buffer_load_dwordx4 a[0:3], v1, s[48:51], s73 offen offset:0 sc1\n"
"buffer_load_dwordx4 a[4:7], v1, s[48:51], s73 offen offset:1024 sc1\n"
"buffer_load_dwordx4 a[8:11], v1, s[48:51], s73 offen offset:2048 sc1\n"
"buffer_load_dwordx4 a[12:15], v1, s[48:51], s73 offen offset:3072 sc1\n"
"s_add_u32 s73, s92, 0x7000\n"
"buffer_load_dwordx4 a[16:19], v1, s[48:51], s73 offen offset:0 sc1\n"
"buffer_load_dwordx4 a[20:23], v1, s[48:51], s73 offen offset:1024 sc1\n"
"buffer_load_dwordx4 a[24:27], v1, s[48:51], s73 offen offset:2048 sc1\n"
"buffer_load_dwordx4 a[28:31], v1, s[48:51], s73 offen offset:3072 sc1\n"
"s_add_u32 s73, s92, 0x8000\n"
"buffer_load_dwordx4 a[32:35], v1, s[48:51], s73 offen offset:0 sc1\n"
"buffer_load_dwordx4 a[36:39], v1, s[48:51], s73 offen offset:1024 sc1\n"
"buffer_load_dwordx4 a[40:43], v1, s[48:51], s73 offen offset:2048 sc1\n"
"buffer_load_dwordx4 a[44:47], v1, s[48:51], s73 offen offset:3072 sc1\n"
"s_add_u32 s73, s92, 0x9000\n"
"buffer_load_dwordx4 a[48:51], v1, s[48:51], s73 offen offset:0 sc1\n"
"buffer_load_dwordx4 a[52:55], v1, s[48:51], s73 offen offset:1024 sc1\n"
"buffer_load_dwordx4 a[56:59], v1, s[48:51], s73 offen offset:2048 sc1\n"
"buffer_load_dwordx4 a[60:63], v1, s[48:51], s73 offen offset:3072 sc1\n"
"s_add_u32 s73, s92, 0xa000\n"
"buffer_load_dwordx4 v[136:139], v1, s[48:51], s73 offen offset:0 sc1\n"
"buffer_load_dwordx4 v[144:147], v1, s[48:51], s73 offen offset:1024 sc1\n"
"buffer_load_dwordx4 v[168:171], v1, s[48:51], s73 offen offset:2048 sc1\n"
"buffer_load_dwordx4 v[176:179], v1, s[48:51], s73 offen offset:3072 sc1\n"
"s_add_u32 s73, s92, 0xb000\n"
"buffer_load_dwordx4 v[184:187], v1, s[48:51], s73 offen offset:0 sc1\n"
"buffer_load_dwordx4 v[204:207], v1, s[48:51], s73 offen offset:1024 sc1\n"
"buffer_load_dwordx4 v[220:223], v1, s[48:51], s73 offen offset:2048 sc1\n"
"buffer_load_dwordx4 v[188:191], v1, s[48:51], s73 offen offset:3072 sc1\n"
"s_branch L_B1_%=\n"
"L_bad3_%=:\n"
"s_add_u32 s73, s92, 0x6000\n"
"buffer_load_dwordx4 a[0:3], v1, s[48:51], s73 offen offset:0 sc1\n"
"buffer_load_dwordx4 a[4:7], v1, s[48:51], s73 offen offset:1024 sc1\n"
"buffer_load_dwordx4 a[8:11], v1, s[48:51], s73 offen offset:2048 sc1\n"
"buffer_load_dwordx4 a[12:15], v1, s[48:51], s73 offen offset:3072 sc1\n"
"s_add_u32 s73, s92, 0x7000\n"
"buffer_load_dwordx4 a[16:19], v1, s[48:51], s73 offen offset:0 sc1\n"
"buffer_load_dwordx4 a[20:23], v1, s[48:51], s73 offen offset:1024 sc1\n"
"buffer_load_dwordx4 a[24:27], v1, s[48:51], s73 offen offset:2048 sc1\n"
"buffer_load_dwordx4 a[28:31], v1, s[48:51], s73 offen offset:3072 sc1\n"
"s_add_u32 s73, s92, 0x8000\n"
"buffer_load_dwordx4 a[32:35], v1, s[48:51], s73 offen offset:0 sc1\n"
"buffer_load_dwordx4 a[36:39], v1, s[48:51], s73 offen offset:1024 sc1\n"
"buffer_load_dwordx4 a[40:43], v1, s[48:51], s73 offen offset:2048 sc1\n"
"buffer_load_dwordx4 a[44:47], v1, s[48:51], s73 offen offset:3072 sc1\n"
"s_add_u32 s73, s92, 0x9000\n"
"buffer_load_dwordx4 a[48:51], v1, s[48:51], s73 offen offset:0 sc1\n"
"buffer_load_dwordx4 a[52:55], v1, s[48:51], s73 offen offset:1024 sc1\n"
"buffer_load_dwordx4 a[56:59], v1, s[48:51], s73 offen offset:2048 sc1\n"
"buffer_load_dwordx4 a[60:63], v1, s[48:51], s73 offen offset:3072 sc1\n"
"s_add_u32 s73, s92, 0xa000\n"
"buffer_load_dwordx4 v[136:139], v1, s[48:51], s73 offen offset:0 sc1\n"
"buffer_load_dwordx4 v[144:147], v1, s[48:51], s73 offen offset:1024 sc1\n"
"buffer_load_dwordx4 v[168:171], v1, s[48:51], s73 offen offset:2048 sc1\n"
"buffer_load_dwordx4 v[176:179], v1, s[48:51], s73 offen offset:3072 sc1\n"
"s_add_u32 s73, s92, 0xb000\n"
"buffer_load_dwordx4 v[184:187], v1, s[48:51], s73 offen offset:0 sc1\n"
"buffer_load_dwordx4 v[204:207], v1, s[48:51], s73 offen offset:1024 sc1\n"
"buffer_load_dwordx4 v[220:223], v1, s[48:51], s73 offen offset:2048 sc1\n"
"buffer_load_dwordx4 v[188:191], v1, s[48:51], s73 offen offset:3072 sc1\n"
"s_branch L_B3_%=\n"
"L_pre7_%=:\n"
"s_and_b32 s73, s65, 1\n"
"s_lshl_b32 s73, s73, 2\n"
"s_add_u32 s73, s73, s64\n"
"s_lshl_b32 s73, s73, 16\n"
"s_lshl_b32 s92, s3, 19\n"
"s_add_u32 s92, s92, s73\n"
"s_add_u32 s92, s92, 0x3400000\n"
"s_add_u32 s73, s92, 0xe000\n"
"buffer_load_dwordx4 v[36:39], v1, s[48:51], s73 offen offset:0 sc1\n"
"buffer_load_dwordx4 v[44:47], v1, s[48:51], s73 offen offset:1024 sc1\n"
"buffer_load_dwordx4 v[52:55], v1, s[48:51], s73 offen offset:2048 sc1\n"
"buffer_load_dwordx4 v[60:63], v1, s[48:51], s73 offen offset:3072 sc1\n"
"s_add_u32 s73, s92, 0xf000\n"
"buffer_load_dwordx4 v[76:79], v1, s[48:51], s73 offen offset:0 sc1\n"
"buffer_load_dwordx4 v[92:95], v1, s[48:51], s73 offen offset:1024 sc1\n"
"buffer_load_dwordx4 v[108:111], v1, s[48:51], s73 offen offset:2048 sc1\n"
"buffer_load_dwordx4 v[124:127], v1, s[48:51], s73 offen offset:3072 sc1\n"
"s_add_u32 s73, s92, 0xa000\n"
"buffer_load_dwordx4 v[136:139], v1, s[48:51], s73 offen offset:0 sc1\n"
"buffer_load_dwordx4 v[144:147], v1, s[48:51], s73 offen offset:1024 sc1\n"
"buffer_load_dwordx4 v[168:171], v1, s[48:51], s73 offen offset:2048 sc1\n"
"buffer_load_dwordx4 v[176:179], v1, s[48:51], s73 offen offset:3072 sc1\n"
"s_branch L_A_%=\n"
"L_kv0_spin%=:\n"
"s_mov_b32 s93, 0x40000\n"
"L_kv0_sp2%=:\n"
"s_sleep 1\n"
"buffer_load_dwordx2 v[28:29], v6, s[48:51], s34 offen sc1\n"
"s_waitcnt vmcnt(0)\n"
"v_cmp_eq_u64 vcc, s[68:69], v[28:29]\n"
"s_nop 0\n"
"s_cmp_eq_u64 vcc, -1\n"
"s_cbranch_scc1 LT_kv0_ok%=\n"
"s_sub_u32 s93, s93, 1\n"
"s_cbranch_scc0 L_kv0_sp2%=\n"
"s_branch LT_kv0_ok%=\n"
"L_kv1_spin%=:\n"
"s_mov_b32 s93, 0x40000\n"
"L_kv1_sp2%=:\n"
"s_sleep 1\n"
"buffer_load_dwordx2 v[28:29], v6, s[48:51], s34 offen sc1\n"
"s_waitcnt vmcnt(0)\n"
"v_cmp_eq_u64 vcc, s[68:69], v[28:29]\n"
"s_nop 0\n"
"s_cmp_eq_u64 vcc, -1\n"
"s_cbranch_scc1 LT_kv1_ok%=\n"
"s_sub_u32 s93, s93, 1\n"
"s_cbranch_scc0 L_kv1_sp2%=\n"
"s_branch LT_kv1_ok%=\n"
"L_yb_spin%=:\n"
"s_mov_b32 s93, 0x40000\n"
"L_yb_sp2%=:\n"
"s_sleep 1\n"
"buffer_load_dwordx2 v[18:19], v6, s[48:51], s34 offen sc1\n"
"s_waitcnt vmcnt(0)\n"
"v_cmp_eq_u64 vcc, s[68:69], v[18:19]\n"
"s_nop 0\n"
"s_cmp_eq_u64 vcc, -1\n"
"s_cbranch_scc1 LT_yb_ok%=\n"
"s_sub_u32 s93, s93, 1\n"
"s_cbranch_scc0 L_yb_sp2%=\n"
"s_branch LT_yb_ok%=\n"
"L_cl_spin%=:\n"
"s_mov_b32 s93, 0x40000\n"
"L_cl_sp2%=:\n"
"s_sleep 1\n"
"buffer_load_dwordx2 v[22:23], v11, s[48:51], s34 offen sc1\n"
"s_waitcnt vmcnt(0)\n"
"v_cmp_eq_u64 vcc, s[68:69], v[22:23]\n"
"s_nop 0\n"
"s_cmp_eq_u64 vcc, -1\n"
"s_cbranch_scc1 LT_cl_ok%=\n"
"s_sub_u32 s93, s93, 1\n"
"s_cbranch_scc0 L_cl_sp2%=\n"
"s_branch LT_cl_ok%=\n"
"L_cx_spin%=:\n"
"s_mov_b32 s93, 0x40000\n"
"L_cx_sp2%=:\n"
"s_sleep 1\n"
"buffer_load_dwordx2 v[22:23], v11, s[48:51], s34 offen sc1\n"
"s_waitcnt vmcnt(0)\n"
"v_cmp_eq_u64 vcc, s[68:69], v[22:23]\n"
"s_nop 0\n"
"s_cmp_eq_u64 vcc, -1\n"
"s_cbranch_scc1 LT_cx_ok%=\n"
"s_sub_u32 s93, s93, 1\n"
"s_cbranch_scc0 L_cx_sp2%=\n"
"s_branch LT_cx_ok%=\n"
"L_xf_spin%=:\n"
"s_mov_b32 s93, 0x40000\n"
"L_xf_sp2%=:\n"
"s_sleep 1\n"
"buffer_load_dwordx2 v[22:23], v11, s[48:51], s55 offen sc1\n"
"s_waitcnt vmcnt(0)\n"
"v_cmp_eq_u64 vcc, s[68:69], v[22:23]\n"
"s_nop 0\n"
"s_cmp_eq_u64 vcc, -1\n"
"s_cbranch_scc1 LT_xf_ok%=\n"
"s_sub_u32 s93, s93, 1\n"
"s_cbranch_scc0 L_xf_sp2%=\n"
"s_branch LT_xf_ok%=\n"
"L_ya_spin%=:\n"
"s_mov_b32 s93, 0x40000\n"
"L_ya_sp2%=:\n"
"s_sleep 1\n"
"buffer_load_dwordx2 v[18:19], v6, s[48:51], s34 offen sc1\n"
"s_waitcnt vmcnt(0)\n"
"v_cmp_eq_u64 vcc, s[68:69], v[18:19]\n"
"s_nop 0\n"
"s_cmp_eq_u64 vcc, -1\n"
"s_cbranch_scc1 LT_ya_ok%=\n"
"s_sub_u32 s93, s93, 1\n"
"s_cbranch_scc0 L_ya_sp2%=\n"
"s_branch LT_ya_ok%=\n"
"L_ff0_spin%=:\n"
"s_mov_b32 s93, 0x40000\n"
"L_ff0_sp2%=:\n"
"s_sleep 1\n"
"buffer_load_dwordx4 v[188:191], v31, s[48:51], s75 offen sc1\n"
"buffer_load_dwordx4 v[184:187], v31, s[48:51], s75 offen offset:16 sc1\n"
"s_waitcnt vmcnt(0)\n"
"v_cmp_eq_u64 vcc, s[68:69], v[188:189]\n"
"v_cmp_eq_u64 s[84:85], s[68:69], v[190:191]\n"
"s_nop 0\n"
"s_and_b64 vcc, vcc, s[84:85]\n"
"v_cmp_eq_u64 s[84:85], s[68:69], v[184:185]\n"
"s_nop 0\n"
"s_and_b64 vcc, vcc, s[84:85]\n"
"v_cmp_eq_u64 s[84:85], s[68:69], v[186:187]\n"
"s_nop 0\n"
"s_and_b64 vcc, vcc, s[84:85]\n"
"s_cmp_eq_u64 vcc, -1\n"
"s_cbranch_scc1 LT_ff0_ok%=\n"
"s_sub_u32 s93, s93, 1\n"
"s_cbranch_scc0 L_ff0_sp2%=\n"
"s_branch LT_ff0_ok%=\n"
"L_ff1_spin%=:\n"
"s_mov_b32 s93, 0x40000\n"
"L_ff1_sp2%=:\n"
"s_sleep 1\n"
"buffer_load_dwordx4 v[188:191], v31, s[48:51], s75 offen sc1\n"
"buffer_load_dwordx4 v[184:187], v31, s[48:51], s75 offen offset:16 sc1\n"
"s_waitcnt vmcnt(0)\n"
"v_cmp_eq_u64 vcc, s[68:69], v[188:189]\n"
"v_cmp_eq_u64 s[84:85], s[68:69], v[190:191]\n"
"s_nop 0\n"
"s_and_b64 vcc, vcc, s[84:85]\n"
"v_cmp_eq_u64 s[84:85], s[68:69], v[184:185]\n"
"s_nop 0\n"
"s_and_b64 vcc, vcc, s[84:85]\n"
"v_cmp_eq_u64 s[84:85], s[68:69], v[186:187]\n"
"s_nop 0\n"
"s_and_b64 vcc, vcc, s[84:85]\n"
"s_cmp_eq_u64 vcc, -1\n"
"s_cbranch_scc1 LT_ff1_ok%=\n"
"s_sub_u32 s93, s93, 1\n"
"s_cbranch_scc0 L_ff1_sp2%=\n"
"s_branch LT_ff1_ok%=\n"
"L_ln_spin%=:\n"
"s_mov_b32 s93, 0x40000\n"
"L_ln_sp2%=:\n"
"s_sleep 1\n"
"buffer_load_dwordx4 v[32:35], v1, s[48:51], s73 offen offset:0 sc1\n"
"buffer_load_dwordx4 v[36:39], v1, s[48:51], s73 offen offset:1024 sc1\n"
"buffer_load_dwordx4 v[40:43], v1, s[48:51], s73 offen offset:2048 sc1\n"
"buffer_load_dwordx4 v[44:47], v1, s[48:51], s73 offen offset:3072 sc1\n"
"buffer_load_dwordx4 v[48:51], v1, s[48:51], s74 offen offset:0 sc1\n"
"buffer_load_dwordx4 v[52:55], v1, s[48:51], s74 offen offset:1024 sc1\n"
"buffer_load_dwordx4 v[56:59], v1, s[48:51], s74 offen offset:2048 sc1\n"
"buffer_load_dwordx4 v[60:63], v1, s[48:51], s74 offen offset:3072 sc1\n"
"s_waitcnt vmcnt(0)\n"
"v_min3_u32 v24, v33, v35, v37\n"
"v_min3_u32 v24, v24, v39, v41\n"
"v_min3_u32 v24, v24, v43, v45\n"
"v_min3_u32 v24, v24, v47, v49\n"
"v_min3_u32 v24, v24, v51, v53\n"
"v_min3_u32 v24, v24, v55, v57\n"
"v_min3_u32 v24, v24, v59, v61\n"
"v_min3_u32 v24, v24, v63, v63\n"
"v_max3_u32 v25, v33, v35, v37\n"
"v_max3_u32 v25, v25, v39, v41\n"
"v_max3_u32 v25, v25, v43, v45\n"
"v_max3_u32 v25, v25, v47, v49\n"
"v_max3_u32 v25, v25, v51, v53\n"
"v_max3_u32 v25, v25, v55, v57\n"
"v_max3_u32 v25, v25, v59, v61\n"
"v_max3_u32 v25, v25, v63, v63\n"
"v_max3_u32 v26, v32, v34, v36\n"
"v_max3_u32 v26, v26, v38, v40\n"
"v_max3_u32 v26, v26, v42, v44\n"
"v_max3_u32 v26, v26, v46, v48\n"
"v_max3_u32 v26, v26, v50, v52\n"
"v_max3_u32 v26, v26, v54, v56\n"
"v_max3_u32 v26, v26, v58, v60\n"
"v_max3_u32 v26, v26, v62, v62\n"
"v_xor_b32 v24, s68, v24\n"
"v_xor_b32 v25, s68, v25\n"
"v_or_b32 v24, v24, v25\n"
"v_cmp_eq_u32 vcc, 0, v24\n"
"s_nop 0\n"
"s_cmp_eq_u64 vcc, -1\n"
"s_cbranch_scc1 LT_ln_ok%=\n"
"s_sub_u32 s93, s93, 1\n"
"s_cbranch_scc0 L_ln_sp2%=\n"
"s_branch LT_ln_ok%=\n"
"L_slow%=:\n"
"s_waitcnt vmcnt(0) lgkmcnt(0)\n"
"v_mov_b32 v220, v12\n"
"v_mov_b32 v221, v13\n"
"v_mov_b32 v12, 0\n"
"v_accvgpr_write_b32 a192, 0\n"
"v_accvgpr_write_b32 a193, 0\n"
"v_accvgpr_write_b32 a194, 0\n"
"v_accvgpr_write_b32 a195, 0\n"
"v_accvgpr_write_b32 a196, 0\n"
"v_accvgpr_write_b32 a197, 0\n"
"v_accvgpr_write_b32 a198, 0\n"
"v_accvgpr_write_b32 a199, 0\n"
"v_accvgpr_write_b32 a200, 0\n"
"v_accvgpr_write_b32 a201, 0\n"
"v_accvgpr_write_b32 a202, 0\n"
"v_accvgpr_write_b32 a203, 0\n"
"v_accvgpr_write_b32 a204, 0\n"
"v_accvgpr_write_b32 a205, 0\n"
"v_accvgpr_write_b32 a206, 0\n"
"v_accvgpr_write_b32 a207, 0\n"
"v_accvgpr_write_b32 a208, 0\n"
"v_accvgpr_write_b32 a209, 0\n"
"v_accvgpr_write_b32 a210, 0\n"
"v_accvgpr_write_b32 a211, 0\n"
"v_accvgpr_write_b32 a212, 0\n"
"v_accvgpr_write_b32 a213, 0\n"
"v_accvgpr_write_b32 a214, 0\n"
"v_accvgpr_write_b32 a215, 0\n"
"v_accvgpr_write_b32 a216, 0\n"
"v_accvgpr_write_b32 a217, 0\n"
"v_accvgpr_write_b32 a218, 0\n"
"v_accvgpr_write_b32 a219, 0\n"
"v_accvgpr_write_b32 a220, 0\n"
"v_accvgpr_write_b32 a221, 0\n"
"v_accvgpr_write_b32 a222, 0\n"
"v_accvgpr_write_b32 a223, 0\n"
"v_mov_b32 v13, 0\n"
"v_accvgpr_write_b32 a224, 0\n"
"v_accvgpr_write_b32 a225, 0\n"
"v_accvgpr_write_b32 a226, 0\n"
"v_accvgpr_write_b32 a227, 0\n"
"v_accvgpr_write_b32 a228, 0\n"
"v_accvgpr_write_b32 a229, 0\n"
"v_accvgpr_write_b32 a230, 0\n"
"v_accvgpr_write_b32 a231, 0\n"
"v_accvgpr_write_b32 a232, 0\n"
"v_accvgpr_write_b32 a233, 0\n"
"v_accvgpr_write_b32 a234, 0\n"
"v_accvgpr_write_b32 a235, 0\n"
"v_accvgpr_write_b32 a236, 0\n"
"v_accvgpr_write_b32 a237, 0\n"
"v_accvgpr_write_b32 a238, 0\n"
"v_accvgpr_write_b32 a239, 0\n"
"v_accvgpr_write_b32 a240, 0\n"
"v_accvgpr_write_b32 a241, 0\n"
"v_accvgpr_write_b32 a242, 0\n"
"v_accvgpr_write_b32 a243, 0\n"
"v_accvgpr_write_b32 a244, 0\n"
"v_accvgpr_write_b32 a245, 0\n"
"v_accvgpr_write_b32 a246, 0\n"
"v_accvgpr_write_b32 a247, 0\n"
"v_accvgpr_write_b32 a248, 0\n"
"v_accvgpr_write_b32 a249, 0\n"
"v_accvgpr_write_b32 a250, 0\n"
"v_accvgpr_write_b32 a251, 0\n"
"v_accvgpr_write_b32 a252, 0\n"
"v_accvgpr_write_b32 a253, 0\n"
"v_accvgpr_write_b32 a254, 0\n"
"v_accvgpr_write_b32 a255, 0\n"
"v_mov_b32 v14, 0\n"
"v_accvgpr_write_b32 a64, 0\n"
"v_accvgpr_write_b32 a65, 0\n"
"v_accvgpr_write_b32 a66, 0\n"
"v_accvgpr_write_b32 a67, 0\n"
"v_accvgpr_write_b32 a68, 0\n"
"v_accvgpr_write_b32 a69, 0\n"
"v_accvgpr_write_b32 a70, 0\n"
"v_accvgpr_write_b32 a71, 0\n"
"v_accvgpr_write_b32 a72, 0\n"
"v_accvgpr_write_b32 a73, 0\n"
"v_accvgpr_write_b32 a74, 0\n"
"v_accvgpr_write_b32 a75, 0\n"
"v_accvgpr_write_b32 a76, 0\n"
"v_accvgpr_write_b32 a77, 0\n"
"v_accvgpr_write_b32 a78, 0\n"
"v_accvgpr_write_b32 a79, 0\n"
"v_accvgpr_write_b32 a80, 0\n"
"v_accvgpr_write_b32 a81, 0\n"
"v_accvgpr_write_b32 a82, 0\n"
"v_accvgpr_write_b32 a83, 0\n"
"v_accvgpr_write_b32 a84, 0\n"
"v_accvgpr_write_b32 a85, 0\n"
"v_accvgpr_write_b32 a86, 0\n"
"v_accvgpr_write_b32 a87, 0\n"
"v_accvgpr_write_b32 a88, 0\n"
"v_accvgpr_write_b32 a89, 0\n"
"v_accvgpr_write_b32 a90, 0\n"
"v_accvgpr_write_b32 a91, 0\n"
"v_accvgpr_write_b32 a92, 0\n"
"v_accvgpr_write_b32 a93, 0\n"
"v_accvgpr_write_b32 a94, 0\n"
"v_accvgpr_write_b32 a95, 0\n"
"v_mov_b32 v15, 0\n"
"v_accvgpr_write_b32 a96, 0\n"
"v_accvgpr_write_b32 a97, 0\n"
"v_accvgpr_write_b32 a98, 0\n"
"v_accvgpr_write_b32 a99, 0\n"
"v_accvgpr_write_b32 a100, 0\n"
"v_accvgpr_write_b32 a101, 0\n"
"v_accvgpr_write_b32 a102, 0\n"
"v_accvgpr_write_b32 a103, 0\n"
"v_accvgpr_write_b32 a104, 0\n"
"v_accvgpr_write_b32 a105, 0\n"
"v_accvgpr_write_b32 a106, 0\n"
"v_accvgpr_write_b32 a107, 0\n"
"v_accvgpr_write_b32 a108, 0\n"
"v_accvgpr_write_b32 a109, 0\n"
"v_accvgpr_write_b32 a110, 0\n"
"v_accvgpr_write_b32 a111, 0\n"
"v_accvgpr_write_b32 a112, 0\n"
"v_accvgpr_write_b32 a113, 0\n"
"v_accvgpr_write_b32 a114, 0\n"
"v_accvgpr_write_b32 a115, 0\n"
"v_accvgpr_write_b32 a116, 0\n"
"v_accvgpr_write_b32 a117, 0\n"
"v_accvgpr_write_b32 a118, 0\n"
"v_accvgpr_write_b32 a119, 0\n"
"v_accvgpr_write_b32 a120, 0\n"
"v_accvgpr_write_b32 a121, 0\n"
"v_accvgpr_write_b32 a122, 0\n"
"v_accvgpr_write_b32 a123, 0\n"
"v_accvgpr_write_b32 a124, 0\n"
"v_accvgpr_write_b32 a125, 0\n"
"v_accvgpr_write_b32 a126, 0\n"
"v_accvgpr_write_b32 a127, 0\n"
"v_mov_b32 v136, 0xf149f2ca\n"
"v_mov_b32 v137, 0xf149f2ca\n"
"v_mov_b32 v138, 0xf149f2ca\n"
"v_mov_b32 v139, 0xf149f2ca\n"
"v_mov_b32 v204, 0xff800000\n"
"v_and_b32 v205, 15, v21\n"
"v_lshrrev_b32 v206, 4, v21\n"
"v_lshlrev_b32 v206, 2, v206\n"
"v_sub_u32 v205, v205, v206\n"
"s_sub_u32 s73, s78, 0x100000\n"
"s_lshr_b32 s73, s73, 10\n"
"v_add_u32 v184, s73, v205\n"
"s_sub_u32 s73, s79, 0x100000\n"
"s_lshr_b32 s73, s73, 10\n"
"v_add_u32 v185, s73, v205\n"
"s_sub_u32 s73, s80, 0x100000\n"
"s_lshr_b32 s73, s73, 10\n"
"v_add_u32 v186, s73, v205\n"
"s_sub_u32 s73, s81, 0x100000\n"
"s_lshr_b32 s73, s73, 10\n"
"v_add_u32 v187, s73, v205\n"
"s_lshr_b32 s93, s73, 5\n"
"s_add_u32 s93, s93, 1\n"
"s_lshl_b32 s73, s65, 20\n"
"s_add_u32 s73, s73, 0x400000\n"
"L_slowloop%=:\n"
"buffer_load_dwordx4 v[36:39], v1, s[48:51], s73 offen offset:0 sc1\n"
"buffer_load_dwordx4 v[44:47], v1, s[48:51], s73 offen offset:1024 sc1\n"
"buffer_load_dwordx4 v[52:55], v1, s[48:51], s73 offen offset:2048 sc1\n"
"buffer_load_dwordx4 v[60:63], v1, s[48:51], s73 offen offset:3072 sc1\n"
"s_add_u32 s73, s73, 0x1000\n"
"buffer_load_dwordx4 v[76:79], v1, s[48:51], s73 offen offset:0 sc1\n"
"buffer_load_dwordx4 v[92:95], v1, s[48:51], s73 offen offset:1024 sc1\n"
"buffer_load_dwordx4 v[108:111], v1, s[48:51], s73 offen offset:2048 sc1\n"
"buffer_load_dwordx4 v[124:127], v1, s[48:51], s73 offen offset:3072 sc1\n"
"s_add_u32 s73, s73, 0x1000\n"
"s_waitcnt vmcnt(0)\n"
"v_mfma_f32_16x16x32_bf16 v[64:67], v[36:39], v[32:35], 0\n"
"v_mfma_f32_16x16x32_bf16 v[72:75], v[36:39], v[40:43], 0\n"
"v_mfma_f32_16x16x32_bf16 v[80:83], v[36:39], v[48:51], 0\n"
"v_mfma_f32_16x16x32_bf16 v[88:91], v[36:39], v[56:59], 0\n"
"v_mfma_f32_16x16x32_bf16 v[64:67], v[44:47], v[68:71], v[64:67]\n"
"v_mfma_f32_16x16x32_bf16 v[72:75], v[44:47], v[84:87], v[72:75]\n"
"v_mfma_f32_16x16x32_bf16 v[80:83], v[44:47], v[100:103], v[80:83]\n"
"v_mfma_f32_16x16x32_bf16 v[88:91], v[44:47], v[116:119], v[88:91]\n"
"v_mfma_f32_16x16x32_bf16 v[64:67], v[52:55], v[196:199], v[64:67]\n"
"v_mfma_f32_16x16x32_bf16 v[72:75], v[52:55], v[212:215], v[72:75]\n"
"v_mfma_f32_16x16x32_bf16 v[80:83], v[52:55], v[228:231], v[80:83]\n"
"v_mfma_f32_16x16x32_bf16 v[88:91], v[52:55], v[244:247], v[88:91]\n"
"v_mfma_f32_16x16x32_bf16 v[64:67], v[60:63], v[132:135], v[64:67]\n"
"v_mfma_f32_16x16x32_bf16 v[72:75], v[60:63], v[148:151], v[72:75]\n"
"v_mfma_f32_16x16x32_bf16 v[80:83], v[60:63], v[164:167], v[80:83]\n"
"v_mfma_f32_16x16x32_bf16 v[88:91], v[60:63], v[180:183], v[88:91]\n"
"v_mfma_f32_16x16x32_bf16 v[96:99], v[76:79], v[32:35], 0\n"
"v_mfma_f32_16x16x32_bf16 v[104:107], v[76:79], v[40:43], 0\n"
"v_mfma_f32_16x16x32_bf16 v[112:115], v[76:79], v[48:51], 0\n"
"v_mfma_f32_16x16x32_bf16 v[120:123], v[76:79], v[56:59], 0\n"
"v_mfma_f32_16x16x32_bf16 v[96:99], v[92:95], v[68:71], v[96:99]\n"
"v_mfma_f32_16x16x32_bf16 v[104:107], v[92:95], v[84:87], v[104:107]\n"
"v_mfma_f32_16x16x32_bf16 v[112:115], v[92:95], v[100:103], v[112:115]\n"
"v_mfma_f32_16x16x32_bf16 v[120:123], v[92:95], v[116:119], v[120:123]\n"
"v_mfma_f32_16x16x32_bf16 v[96:99], v[108:111], v[196:199], v[96:99]\n"
"v_mfma_f32_16x16x32_bf16 v[104:107], v[108:111], v[212:215], v[104:107]\n"
"v_mfma_f32_16x16x32_bf16 v[112:115], v[108:111], v[228:231], v[112:115]\n"
"v_mfma_f32_16x16x32_bf16 v[120:123], v[108:111], v[244:247], v[120:123]\n"
"v_mfma_f32_16x16x32_bf16 v[96:99], v[124:127], v[132:135], v[96:99]\n"
"v_mfma_f32_16x16x32_bf16 v[104:107], v[124:127], v[148:151], v[104:107]\n"
"v_mfma_f32_16x16x32_bf16 v[112:115], v[124:127], v[164:167], v[112:115]\n"
"v_mfma_f32_16x16x32_bf16 v[120:123], v[124:127], v[180:183], v[120:123]\n"
"s_nop 7\n"
"buffer_load_dwordx4 v[36:39], v1, s[48:51], s73 offen offset:0 sc1\n"
"buffer_load_dwordx4 v[44:47], v1, s[48:51], s73 offen offset:1024 sc1\n"
"buffer_load_dwordx4 v[52:55], v1, s[48:51], s73 offen offset:2048 sc1\n"
"buffer_load_dwordx4 v[60:63], v1, s[48:51], s73 offen offset:3072 sc1\n"
"s_add_u32 s73, s73, 0x1000\n"
"buffer_load_dwordx4 v[76:79], v1, s[48:51], s73 offen offset:0 sc1\n"
"buffer_load_dwordx4 v[92:95], v1, s[48:51], s73 offen offset:1024 sc1\n"
"buffer_load_dwordx4 v[108:111], v1, s[48:51], s73 offen offset:2048 sc1\n"
"buffer_load_dwordx4 v[124:127], v1, s[48:51], s73 offen offset:3072 sc1\n"
"s_add_u32 s73, s73, 0x1000\n"
"v_cmp_le_i32 vcc, 0, v184\n"
"s_nop 1\n"
"v_cndmask_b32 v64, v204, v64, vcc\n"
"v_cmp_le_i32 vcc, 1, v184\n"
"s_nop 1\n"
"v_cndmask_b32 v65, v204, v65, vcc\n"
"v_cmp_le_i32 vcc, 2, v184\n"
"s_nop 1\n"
"v_cndmask_b32 v66, v204, v66, vcc\n"
"v_cmp_le_i32 vcc, 3, v184\n"
"s_nop 1\n"
"v_cndmask_b32 v67, v204, v67, vcc\n"
"v_cmp_le_i32 vcc, 0, v185\n"
"s_nop 1\n"
"v_cndmask_b32 v72, v204, v72, vcc\n"
"v_cmp_le_i32 vcc, 1, v185\n"
"s_nop 1\n"
"v_cndmask_b32 v73, v204, v73, vcc\n"
"v_cmp_le_i32 vcc, 2, v185\n"
"s_nop 1\n"
"v_cndmask_b32 v74, v204, v74, vcc\n"
"v_cmp_le_i32 vcc, 3, v185\n"
"s_nop 1\n"
"v_cndmask_b32 v75, v204, v75, vcc\n"
"v_cmp_le_i32 vcc, 0, v186\n"
"s_nop 1\n"
"v_cndmask_b32 v80, v204, v80, vcc\n"
"v_cmp_le_i32 vcc, 1, v186\n"
"s_nop 1\n"
"v_cndmask_b32 v81, v204, v81, vcc\n"
"v_cmp_le_i32 vcc, 2, v186\n"
"s_nop 1\n"
"v_cndmask_b32 v82, v204, v82, vcc\n"
"v_cmp_le_i32 vcc, 3, v186\n"
"s_nop 1\n"
"v_cndmask_b32 v83, v204, v83, vcc\n"
"v_cmp_le_i32 vcc, 0, v187\n"
"s_nop 1\n"
"v_cndmask_b32 v88, v204, v88, vcc\n"
"v_cmp_le_i32 vcc, 1, v187\n"
"s_nop 1\n"
"v_cndmask_b32 v89, v204, v89, vcc\n"
"v_cmp_le_i32 vcc, 2, v187\n"
"s_nop 1\n"
"v_cndmask_b32 v90, v204, v90, vcc\n"
"v_cmp_le_i32 vcc, 3, v187\n"
"s_nop 1\n"
"v_cndmask_b32 v91, v204, v91, vcc\n"
"v_cmp_le_i32 vcc, 16, v184\n"
"s_nop 1\n"
"v_cndmask_b32 v96, v204, v96, vcc\n"
"v_cmp_le_i32 vcc, 17, v184\n"
"s_nop 1\n"
"v_cndmask_b32 v97, v204, v97, vcc\n"
"v_cmp_le_i32 vcc, 18, v184\n"
"s_nop 1\n"
"v_cndmask_b32 v98, v204, v98, vcc\n"
"v_cmp_le_i32 vcc, 19, v184\n"
"s_nop 1\n"
"v_cndmask_b32 v99, v204, v99, vcc\n"
"v_cmp_le_i32 vcc, 16, v185\n"
"s_nop 1\n"
"v_cndmask_b32 v104, v204, v104, vcc\n"
"v_cmp_le_i32 vcc, 17, v185\n"
"s_nop 1\n"
"v_cndmask_b32 v105, v204, v105, vcc\n"
"v_cmp_le_i32 vcc, 18, v185\n"
"s_nop 1\n"
"v_cndmask_b32 v106, v204, v106, vcc\n"
"v_cmp_le_i32 vcc, 19, v185\n"
"s_nop 1\n"
"v_cndmask_b32 v107, v204, v107, vcc\n"
"v_cmp_le_i32 vcc, 16, v186\n"
"s_nop 1\n"
"v_cndmask_b32 v112, v204, v112, vcc\n"
"v_cmp_le_i32 vcc, 17, v186\n"
"s_nop 1\n"
"v_cndmask_b32 v113, v204, v113, vcc\n"
"v_cmp_le_i32 vcc, 18, v186\n"
"s_nop 1\n"
"v_cndmask_b32 v114, v204, v114, vcc\n"
"v_cmp_le_i32 vcc, 19, v186\n"
"s_nop 1\n"
"v_cndmask_b32 v115, v204, v115, vcc\n"
"v_cmp_le_i32 vcc, 16, v187\n"
"s_nop 1\n"
"v_cndmask_b32 v120, v204, v120, vcc\n"
"v_cmp_le_i32 vcc, 17, v187\n"
"s_nop 1\n"
"v_cndmask_b32 v121, v204, v121, vcc\n"
"v_cmp_le_i32 vcc, 18, v187\n"
"s_nop 1\n"
"v_cndmask_b32 v122, v204, v122, vcc\n"
"v_cmp_le_i32 vcc, 19, v187\n"
"s_nop 1\n"
"v_cndmask_b32 v123, v204, v123, vcc\n"
"v_mov_b32 v144, v136\n"
"v_max3_f32 v144, v144, v64, v65\n"
"v_max3_f32 v144, v144, v66, v67\n"
"v_max3_f32 v144, v144, v96, v97\n"
"v_max3_f32 v144, v144, v98, v99\n"
"v_mov_b32 v168, v144\n"
"s_nop 1\n"
"v_permlane16_swap_b32 v144, v168\n"
"v_max_f32 v144, v144, v168\n"
"v_mov_b32 v168, v144\n"
"s_nop 1\n"
"v_permlane32_swap_b32 v144, v168\n"
"v_max_f32 v144, v144, v168\n"
"v_sub_f32 v176, v136, v144\n"
"v_exp_f32 v176, v176\n"
"v_mov_b32 v136, v144\n"
"v_mul_f32 v12, v12, v176\n"
"v_sub_f32 v64, v64, v136\n"
"v_exp_f32 v64, v64\n"
"s_nop 0\n"
"v_add_f32 v12, v12, v64\n"
"v_sub_f32 v65, v65, v136\n"
"v_exp_f32 v65, v65\n"
"s_nop 0\n"
"v_add_f32 v12, v12, v65\n"
"v_sub_f32 v66, v66, v136\n"
"v_exp_f32 v66, v66\n"
"s_nop 0\n"
"v_add_f32 v12, v12, v66\n"
"v_sub_f32 v67, v67, v136\n"
"v_exp_f32 v67, v67\n"
"s_nop 0\n"
"v_add_f32 v12, v12, v67\n"
"v_cvt_pk_bf16_f32 v192, v64, v65\n"
"v_cvt_pk_bf16_f32 v193, v66, v67\n"
"v_sub_f32 v96, v96, v136\n"
"v_exp_f32 v96, v96\n"
"s_nop 0\n"
"v_add_f32 v12, v12, v96\n"
"v_sub_f32 v97, v97, v136\n"
"v_exp_f32 v97, v97\n"
"s_nop 0\n"
"v_add_f32 v12, v12, v97\n"
"v_sub_f32 v98, v98, v136\n"
"v_exp_f32 v98, v98\n"
"s_nop 0\n"
"v_add_f32 v12, v12, v98\n"
"v_sub_f32 v99, v99, v136\n"
"v_exp_f32 v99, v99\n"
"s_nop 0\n"
"v_add_f32 v12, v12, v99\n"
"v_cvt_pk_bf16_f32 v194, v96, v97\n"
"v_cvt_pk_bf16_f32 v195, v98, v99\n"
"v_accvgpr_read_b32 v168, a192\n"
"v_mul_f32 v168, v168, v176\n"
"v_accvgpr_write_b32 a192, v168\n"
"v_accvgpr_read_b32 v168, a193\n"
"v_mul_f32 v168, v168, v176\n"
"v_accvgpr_write_b32 a193, v168\n"
"v_accvgpr_read_b32 v168, a194\n"
"v_mul_f32 v168, v168, v176\n"
"v_accvgpr_write_b32 a194, v168\n"
"v_accvgpr_read_b32 v168, a195\n"
"v_mul_f32 v168, v168, v176\n"
"v_accvgpr_write_b32 a195, v168\n"
"v_accvgpr_read_b32 v168, a196\n"
"v_mul_f32 v168, v168, v176\n"
"v_accvgpr_write_b32 a196, v168\n"
"v_accvgpr_read_b32 v168, a197\n"
"v_mul_f32 v168, v168, v176\n"
"v_accvgpr_write_b32 a197, v168\n"
"v_accvgpr_read_b32 v168, a198\n"
"v_mul_f32 v168, v168, v176\n"
"v_accvgpr_write_b32 a198, v168\n"
"v_accvgpr_read_b32 v168, a199\n"
"v_mul_f32 v168, v168, v176\n"
"v_accvgpr_write_b32 a199, v168\n"
"v_accvgpr_read_b32 v168, a200\n"
"v_mul_f32 v168, v168, v176\n"
"v_accvgpr_write_b32 a200, v168\n"
"v_accvgpr_read_b32 v168, a201\n"
"v_mul_f32 v168, v168, v176\n"
"v_accvgpr_write_b32 a201, v168\n"
"v_accvgpr_read_b32 v168, a202\n"
"v_mul_f32 v168, v168, v176\n"
"v_accvgpr_write_b32 a202, v168\n"
"v_accvgpr_read_b32 v168, a203\n"
"v_mul_f32 v168, v168, v176\n"
"v_accvgpr_write_b32 a203, v168\n"
"v_accvgpr_read_b32 v168, a204\n"
"v_mul_f32 v168, v168, v176\n"
"v_accvgpr_write_b32 a204, v168\n"
"v_accvgpr_read_b32 v168, a205\n"
"v_mul_f32 v168, v168, v176\n"
"v_accvgpr_write_b32 a205, v168\n"
"v_accvgpr_read_b32 v168, a206\n"
"v_mul_f32 v168, v168, v176\n"
"v_accvgpr_write_b32 a206, v168\n"
"v_accvgpr_read_b32 v168, a207\n"
"v_mul_f32 v168, v168, v176\n"
"v_accvgpr_write_b32 a207, v168\n"
"v_accvgpr_read_b32 v168, a208\n"
"v_mul_f32 v168, v168, v176\n"
"v_accvgpr_write_b32 a208, v168\n"
"v_accvgpr_read_b32 v168, a209\n"
"v_mul_f32 v168, v168, v176\n"
"v_accvgpr_write_b32 a209, v168\n"
"v_accvgpr_read_b32 v168, a210\n"
"v_mul_f32 v168, v168, v176\n"
"v_accvgpr_write_b32 a210, v168\n"
"v_accvgpr_read_b32 v168, a211\n"
"v_mul_f32 v168, v168, v176\n"
"v_accvgpr_write_b32 a211, v168\n"
"v_accvgpr_read_b32 v168, a212\n"
"v_mul_f32 v168, v168, v176\n"
"v_accvgpr_write_b32 a212, v168\n"
"v_accvgpr_read_b32 v168, a213\n"
"v_mul_f32 v168, v168, v176\n"
"v_accvgpr_write_b32 a213, v168\n"
"v_accvgpr_read_b32 v168, a214\n"
"v_mul_f32 v168, v168, v176\n"
"v_accvgpr_write_b32 a214, v168\n"
"v_accvgpr_read_b32 v168, a215\n"
"v_mul_f32 v168, v168, v176\n"
"v_accvgpr_write_b32 a215, v168\n"
"v_accvgpr_read_b32 v168, a216\n"
"v_mul_f32 v168, v168, v176\n"
"v_accvgpr_write_b32 a216, v168\n"
"v_accvgpr_read_b32 v168, a217\n"
"v_mul_f32 v168, v168, v176\n"
"v_accvgpr_write_b32 a217, v168\n"
"v_accvgpr_read_b32 v168, a218\n"
"v_mul_f32 v168, v168, v176\n"
"v_accvgpr_write_b32 a218, v168\n"
"v_accvgpr_read_b32 v168, a219\n"
"v_mul_f32 v168, v168, v176\n"
"v_accvgpr_write_b32 a219, v168\n"
"v_accvgpr_read_b32 v168, a220\n"
"v_mul_f32 v168, v168, v176\n"
"v_accvgpr_write_b32 a220, v168\n"
"v_accvgpr_read_b32 v168, a221\n"
"v_mul_f32 v168, v168, v176\n"
"v_accvgpr_write_b32 a221, v168\n"
"v_accvgpr_read_b32 v168, a222\n"
"v_mul_f32 v168, v168, v176\n"
"v_accvgpr_write_b32 a222, v168\n"
"v_accvgpr_read_b32 v168, a223\n"
"v_mul_f32 v168, v168, v176\n"
"v_accvgpr_write_b32 a223, v168\n"
"v_subrev_u32 v184, 32, v184\n"
"v_mov_b32 v145, v137\n"
"v_max3_f32 v145, v145, v72, v73\n"
"v_max3_f32 v145, v145, v74, v75\n"
"v_max3_f32 v145, v145, v104, v105\n"
"v_max3_f32 v145, v145, v106, v107\n"
"v_mov_b32 v169, v145\n"
"s_nop 1\n"
"v_permlane16_swap_b32 v145, v169\n"
"v_max_f32 v145, v145, v169\n"
"v_mov_b32 v169, v145\n"
"s_nop 1\n"
"v_permlane32_swap_b32 v145, v169\n"
"v_max_f32 v145, v145, v169\n"
"v_sub_f32 v177, v137, v145\n"
"v_exp_f32 v177, v177\n"
"v_mov_b32 v137, v145\n"
"v_mul_f32 v13, v13, v177\n"
"v_sub_f32 v72, v72, v137\n"
"v_exp_f32 v72, v72\n"
"s_nop 0\n"
"v_add_f32 v13, v13, v72\n"
"v_sub_f32 v73, v73, v137\n"
"v_exp_f32 v73, v73\n"
"s_nop 0\n"
"v_add_f32 v13, v13, v73\n"
"v_sub_f32 v74, v74, v137\n"
"v_exp_f32 v74, v74\n"
"s_nop 0\n"
"v_add_f32 v13, v13, v74\n"
"v_sub_f32 v75, v75, v137\n"
"v_exp_f32 v75, v75\n"
"s_nop 0\n"
"v_add_f32 v13, v13, v75\n"
"v_cvt_pk_bf16_f32 v200, v72, v73\n"
"v_cvt_pk_bf16_f32 v201, v74, v75\n"
"v_sub_f32 v104, v104, v137\n"
"v_exp_f32 v104, v104\n"
"s_nop 0\n"
"v_add_f32 v13, v13, v104\n"
"v_sub_f32 v105, v105, v137\n"
"v_exp_f32 v105, v105\n"
"s_nop 0\n"
"v_add_f32 v13, v13, v105\n"
"v_sub_f32 v106, v106, v137\n"
"v_exp_f32 v106, v106\n"
"s_nop 0\n"
"v_add_f32 v13, v13, v106\n"
"v_sub_f32 v107, v107, v137\n"
"v_exp_f32 v107, v107\n"
"s_nop 0\n"
"v_add_f32 v13, v13, v107\n"
"v_cvt_pk_bf16_f32 v202, v104, v105\n"
"v_cvt_pk_bf16_f32 v203, v106, v107\n"
"v_accvgpr_read_b32 v169, a224\n"
"v_mul_f32 v169, v169, v177\n"
"v_accvgpr_write_b32 a224, v169\n"
"v_accvgpr_read_b32 v169, a225\n"
"v_mul_f32 v169, v169, v177\n"
"v_accvgpr_write_b32 a225, v169\n"
"v_accvgpr_read_b32 v169, a226\n"
"v_mul_f32 v169, v169, v177\n"
"v_accvgpr_write_b32 a226, v169\n"
"v_accvgpr_read_b32 v169, a227\n"
"v_mul_f32 v169, v169, v177\n"
"v_accvgpr_write_b32 a227, v169\n"
"v_accvgpr_read_b32 v169, a228\n"
"v_mul_f32 v169, v169, v177\n"
"v_accvgpr_write_b32 a228, v169\n"
"v_accvgpr_read_b32 v169, a229\n"
"v_mul_f32 v169, v169, v177\n"
"v_accvgpr_write_b32 a229, v169\n"
"v_accvgpr_read_b32 v169, a230\n"
"v_mul_f32 v169, v169, v177\n"
"v_accvgpr_write_b32 a230, v169\n"
"v_accvgpr_read_b32 v169, a231\n"
"v_mul_f32 v169, v169, v177\n"
"v_accvgpr_write_b32 a231, v169\n"
"v_accvgpr_read_b32 v169, a232\n"
"v_mul_f32 v169, v169, v177\n"
"v_accvgpr_write_b32 a232, v169\n"
"v_accvgpr_read_b32 v169, a233\n"
"v_mul_f32 v169, v169, v177\n"
"v_accvgpr_write_b32 a233, v169\n"
"v_accvgpr_read_b32 v169, a234\n"
"v_mul_f32 v169, v169, v177\n"
"v_accvgpr_write_b32 a234, v169\n"
"v_accvgpr_read_b32 v169, a235\n"
"v_mul_f32 v169, v169, v177\n"
"v_accvgpr_write_b32 a235, v169\n"
"v_accvgpr_read_b32 v169, a236\n"
"v_mul_f32 v169, v169, v177\n"
"v_accvgpr_write_b32 a236, v169\n"
"v_accvgpr_read_b32 v169, a237\n"
"v_mul_f32 v169, v169, v177\n"
"v_accvgpr_write_b32 a237, v169\n"
"v_accvgpr_read_b32 v169, a238\n"
"v_mul_f32 v169, v169, v177\n"
"v_accvgpr_write_b32 a238, v169\n"
"v_accvgpr_read_b32 v169, a239\n"
"v_mul_f32 v169, v169, v177\n"
"v_accvgpr_write_b32 a239, v169\n"
"v_accvgpr_read_b32 v169, a240\n"
"v_mul_f32 v169, v169, v177\n"
"v_accvgpr_write_b32 a240, v169\n"
"v_accvgpr_read_b32 v169, a241\n"
"v_mul_f32 v169, v169, v177\n"
"v_accvgpr_write_b32 a241, v169\n"
"v_accvgpr_read_b32 v169, a242\n"
"v_mul_f32 v169, v169, v177\n"
"v_accvgpr_write_b32 a242, v169\n"
"v_accvgpr_read_b32 v169, a243\n"
"v_mul_f32 v169, v169, v177\n"
"v_accvgpr_write_b32 a243, v169\n"
"v_accvgpr_read_b32 v169, a244\n"
"v_mul_f32 v169, v169, v177\n"
"v_accvgpr_write_b32 a244, v169\n"
"v_accvgpr_read_b32 v169, a245\n"
"v_mul_f32 v169, v169, v177\n"
"v_accvgpr_write_b32 a245, v169\n"
"v_accvgpr_read_b32 v169, a246\n"
"v_mul_f32 v169, v169, v177\n"
"v_accvgpr_write_b32 a246, v169\n"
"v_accvgpr_read_b32 v169, a247\n"
"v_mul_f32 v169, v169, v177\n"
"v_accvgpr_write_b32 a247, v169\n"
"v_accvgpr_read_b32 v169, a248\n"
"v_mul_f32 v169, v169, v177\n"
"v_accvgpr_write_b32 a248, v169\n"
"v_accvgpr_read_b32 v169, a249\n"
"v_mul_f32 v169, v169, v177\n"
"v_accvgpr_write_b32 a249, v169\n"
"v_accvgpr_read_b32 v169, a250\n"
"v_mul_f32 v169, v169, v177\n"
"v_accvgpr_write_b32 a250, v169\n"
"v_accvgpr_read_b32 v169, a251\n"
"v_mul_f32 v169, v169, v177\n"
"v_accvgpr_write_b32 a251, v169\n"
"v_accvgpr_read_b32 v169, a252\n"
"v_mul_f32 v169, v169, v177\n"
"v_accvgpr_write_b32 a252, v169\n"
"v_accvgpr_read_b32 v169, a253\n"
"v_mul_f32 v169, v169, v177\n"
"v_accvgpr_write_b32 a253, v169\n"
"v_accvgpr_read_b32 v169, a254\n"
"v_mul_f32 v169, v169, v177\n"
"v_accvgpr_write_b32 a254, v169\n"
"v_accvgpr_read_b32 v169, a255\n"
"v_mul_f32 v169, v169, v177\n"
"v_accvgpr_write_b32 a255, v169\n"
"v_subrev_u32 v185, 32, v185\n"
"v_mov_b32 v146, v138\n"
"v_max3_f32 v146, v146, v80, v81\n"
"v_max3_f32 v146, v146, v82, v83\n"
"v_max3_f32 v146, v146, v112, v113\n"
"v_max3_f32 v146, v146, v114, v115\n"
"v_mov_b32 v170, v146\n"
"s_nop 1\n"
"v_permlane16_swap_b32 v146, v170\n"
"v_max_f32 v146, v146, v170\n"
"v_mov_b32 v170, v146\n"
"s_nop 1\n"
"v_permlane32_swap_b32 v146, v170\n"
"v_max_f32 v146, v146, v170\n"
"v_sub_f32 v178, v138, v146\n"
"v_exp_f32 v178, v178\n"
"v_mov_b32 v138, v146\n"
"v_mul_f32 v14, v14, v178\n"
"v_sub_f32 v80, v80, v138\n"
"v_exp_f32 v80, v80\n"
"s_nop 0\n"
"v_add_f32 v14, v14, v80\n"
"v_sub_f32 v81, v81, v138\n"
"v_exp_f32 v81, v81\n"
"s_nop 0\n"
"v_add_f32 v14, v14, v81\n"
"v_sub_f32 v82, v82, v138\n"
"v_exp_f32 v82, v82\n"
"s_nop 0\n"
"v_add_f32 v14, v14, v82\n"
"v_sub_f32 v83, v83, v138\n"
"v_exp_f32 v83, v83\n"
"s_nop 0\n"
"v_add_f32 v14, v14, v83\n"
"v_cvt_pk_bf16_f32 v208, v80, v81\n"
"v_cvt_pk_bf16_f32 v209, v82, v83\n"
"v_sub_f32 v112, v112, v138\n"
"v_exp_f32 v112, v112\n"
"s_nop 0\n"
"v_add_f32 v14, v14, v112\n"
"v_sub_f32 v113, v113, v138\n"
"v_exp_f32 v113, v113\n"
"s_nop 0\n"
"v_add_f32 v14, v14, v113\n"
"v_sub_f32 v114, v114, v138\n"
"v_exp_f32 v114, v114\n"
"s_nop 0\n"
"v_add_f32 v14, v14, v114\n"
"v_sub_f32 v115, v115, v138\n"
"v_exp_f32 v115, v115\n"
"s_nop 0\n"
"v_add_f32 v14, v14, v115\n"
"v_cvt_pk_bf16_f32 v210, v112, v113\n"
"v_cvt_pk_bf16_f32 v211, v114, v115\n"
"v_accvgpr_read_b32 v170, a64\n"
"v_mul_f32 v170, v170, v178\n"
"v_accvgpr_write_b32 a64, v170\n"
"v_accvgpr_read_b32 v170, a65\n"
"v_mul_f32 v170, v170, v178\n"
"v_accvgpr_write_b32 a65, v170\n"
"v_accvgpr_read_b32 v170, a66\n"
"v_mul_f32 v170, v170, v178\n"
"v_accvgpr_write_b32 a66, v170\n"
"v_accvgpr_read_b32 v170, a67\n"
"v_mul_f32 v170, v170, v178\n"
"v_accvgpr_write_b32 a67, v170\n"
"v_accvgpr_read_b32 v170, a68\n"
"v_mul_f32 v170, v170, v178\n"
"v_accvgpr_write_b32 a68, v170\n"
"v_accvgpr_read_b32 v170, a69\n"
"v_mul_f32 v170, v170, v178\n"
"v_accvgpr_write_b32 a69, v170\n"
"v_accvgpr_read_b32 v170, a70\n"
"v_mul_f32 v170, v170, v178\n"
"v_accvgpr_write_b32 a70, v170\n"
"v_accvgpr_read_b32 v170, a71\n"
"v_mul_f32 v170, v170, v178\n"
"v_accvgpr_write_b32 a71, v170\n"
"v_accvgpr_read_b32 v170, a72\n"
"v_mul_f32 v170, v170, v178\n"
"v_accvgpr_write_b32 a72, v170\n"
"v_accvgpr_read_b32 v170, a73\n"
"v_mul_f32 v170, v170, v178\n"
"v_accvgpr_write_b32 a73, v170\n"
"v_accvgpr_read_b32 v170, a74\n"
"v_mul_f32 v170, v170, v178\n"
"v_accvgpr_write_b32 a74, v170\n"
"v_accvgpr_read_b32 v170, a75\n"
"v_mul_f32 v170, v170, v178\n"
"v_accvgpr_write_b32 a75, v170\n"
"v_accvgpr_read_b32 v170, a76\n"
"v_mul_f32 v170, v170, v178\n"
"v_accvgpr_write_b32 a76, v170\n"
"v_accvgpr_read_b32 v170, a77\n"
"v_mul_f32 v170, v170, v178\n"
"v_accvgpr_write_b32 a77, v170\n"
"v_accvgpr_read_b32 v170, a78\n"
"v_mul_f32 v170, v170, v178\n"
"v_accvgpr_write_b32 a78, v170\n"
"v_accvgpr_read_b32 v170, a79\n"
"v_mul_f32 v170, v170, v178\n"
"v_accvgpr_write_b32 a79, v170\n"
"v_accvgpr_read_b32 v170, a80\n"
"v_mul_f32 v170, v170, v178\n"
"v_accvgpr_write_b32 a80, v170\n"
"v_accvgpr_read_b32 v170, a81\n"
"v_mul_f32 v170, v170, v178\n"
"v_accvgpr_write_b32 a81, v170\n"
"v_accvgpr_read_b32 v170, a82\n"
"v_mul_f32 v170, v170, v178\n"
"v_accvgpr_write_b32 a82, v170\n"
"v_accvgpr_read_b32 v170, a83\n"
"v_mul_f32 v170, v170, v178\n"
"v_accvgpr_write_b32 a83, v170\n"
"v_accvgpr_read_b32 v170, a84\n"
"v_mul_f32 v170, v170, v178\n"
"v_accvgpr_write_b32 a84, v170\n"
"v_accvgpr_read_b32 v170, a85\n"
"v_mul_f32 v170, v170, v178\n"
"v_accvgpr_write_b32 a85, v170\n"
"v_accvgpr_read_b32 v170, a86\n"
"v_mul_f32 v170, v170, v178\n"
"v_accvgpr_write_b32 a86, v170\n"
"v_accvgpr_read_b32 v170, a87\n"
"v_mul_f32 v170, v170, v178\n"
"v_accvgpr_write_b32 a87, v170\n"
"v_accvgpr_read_b32 v170, a88\n"
"v_mul_f32 v170, v170, v178\n"
"v_accvgpr_write_b32 a88, v170\n"
"v_accvgpr_read_b32 v170, a89\n"
"v_mul_f32 v170, v170, v178\n"
"v_accvgpr_write_b32 a89, v170\n"
"v_accvgpr_read_b32 v170, a90\n"
"v_mul_f32 v170, v170, v178\n"
"v_accvgpr_write_b32 a90, v170\n"
"v_accvgpr_read_b32 v170, a91\n"
"v_mul_f32 v170, v170, v178\n"
"v_accvgpr_write_b32 a91, v170\n"
"v_accvgpr_read_b32 v170, a92\n"
"v_mul_f32 v170, v170, v178\n"
"v_accvgpr_write_b32 a92, v170\n"
"v_accvgpr_read_b32 v170, a93\n"
"v_mul_f32 v170, v170, v178\n"
"v_accvgpr_write_b32 a93, v170\n"
"v_accvgpr_read_b32 v170, a94\n"
"v_mul_f32 v170, v170, v178\n"
"v_accvgpr_write_b32 a94, v170\n"
"v_accvgpr_read_b32 v170, a95\n"
"v_mul_f32 v170, v170, v178\n"
"v_accvgpr_write_b32 a95, v170\n"
"v_subrev_u32 v186, 32, v186\n"
"v_mov_b32 v147, v139\n"
"v_max3_f32 v147, v147, v88, v89\n"
"v_max3_f32 v147, v147, v90, v91\n"
"v_max3_f32 v147, v147, v120, v121\n"
"v_max3_f32 v147, v147, v122, v123\n"
"v_mov_b32 v171, v147\n"
"s_nop 1\n"
"v_permlane16_swap_b32 v147, v171\n"
"v_max_f32 v147, v147, v171\n"
"v_mov_b32 v171, v147\n"
"s_nop 1\n"
"v_permlane32_swap_b32 v147, v171\n"
"v_max_f32 v147, v147, v171\n"
"v_sub_f32 v179, v139, v147\n"
"v_exp_f32 v179, v179\n"
"v_mov_b32 v139, v147\n"
"v_mul_f32 v15, v15, v179\n"
"v_sub_f32 v88, v88, v139\n"
"v_exp_f32 v88, v88\n"
"s_nop 0\n"
"v_add_f32 v15, v15, v88\n"
"v_sub_f32 v89, v89, v139\n"
"v_exp_f32 v89, v89\n"
"s_nop 0\n"
"v_add_f32 v15, v15, v89\n"
"v_sub_f32 v90, v90, v139\n"
"v_exp_f32 v90, v90\n"
"s_nop 0\n"
"v_add_f32 v15, v15, v90\n"
"v_sub_f32 v91, v91, v139\n"
"v_exp_f32 v91, v91\n"
"s_nop 0\n"
"v_add_f32 v15, v15, v91\n"
"v_cvt_pk_bf16_f32 v216, v88, v89\n"
"v_cvt_pk_bf16_f32 v217, v90, v91\n"
"v_sub_f32 v120, v120, v139\n"
"v_exp_f32 v120, v120\n"
"s_nop 0\n"
"v_add_f32 v15, v15, v120\n"
"v_sub_f32 v121, v121, v139\n"
"v_exp_f32 v121, v121\n"
"s_nop 0\n"
"v_add_f32 v15, v15, v121\n"
"v_sub_f32 v122, v122, v139\n"
"v_exp_f32 v122, v122\n"
"s_nop 0\n"
"v_add_f32 v15, v15, v122\n"
"v_sub_f32 v123, v123, v139\n"
"v_exp_f32 v123, v123\n"
"s_nop 0\n"
"v_add_f32 v15, v15, v123\n"
"v_cvt_pk_bf16_f32 v218, v120, v121\n"
"v_cvt_pk_bf16_f32 v219, v122, v123\n"
"v_accvgpr_read_b32 v171, a96\n"
"v_mul_f32 v171, v171, v179\n"
"v_accvgpr_write_b32 a96, v171\n"
"v_accvgpr_read_b32 v171, a97\n"
"v_mul_f32 v171, v171, v179\n"
"v_accvgpr_write_b32 a97, v171\n"
"v_accvgpr_read_b32 v171, a98\n"
"v_mul_f32 v171, v171, v179\n"
"v_accvgpr_write_b32 a98, v171\n"
"v_accvgpr_read_b32 v171, a99\n"
"v_mul_f32 v171, v171, v179\n"
"v_accvgpr_write_b32 a99, v171\n"
"v_accvgpr_read_b32 v171, a100\n"
"v_mul_f32 v171, v171, v179\n"
"v_accvgpr_write_b32 a100, v171\n"
"v_accvgpr_read_b32 v171, a101\n"
"v_mul_f32 v171, v171, v179\n"
"v_accvgpr_write_b32 a101, v171\n"
"v_accvgpr_read_b32 v171, a102\n"
"v_mul_f32 v171, v171, v179\n"
"v_accvgpr_write_b32 a102, v171\n"
"v_accvgpr_read_b32 v171, a103\n"
"v_mul_f32 v171, v171, v179\n"
"v_accvgpr_write_b32 a103, v171\n"
"v_accvgpr_read_b32 v171, a104\n"
"v_mul_f32 v171, v171, v179\n"
"v_accvgpr_write_b32 a104, v171\n"
"v_accvgpr_read_b32 v171, a105\n"
"v_mul_f32 v171, v171, v179\n"
"v_accvgpr_write_b32 a105, v171\n"
"v_accvgpr_read_b32 v171, a106\n"
"v_mul_f32 v171, v171, v179\n"
"v_accvgpr_write_b32 a106, v171\n"
"v_accvgpr_read_b32 v171, a107\n"
"v_mul_f32 v171, v171, v179\n"
"v_accvgpr_write_b32 a107, v171\n"
"v_accvgpr_read_b32 v171, a108\n"
"v_mul_f32 v171, v171, v179\n"
"v_accvgpr_write_b32 a108, v171\n"
"v_accvgpr_read_b32 v171, a109\n"
"v_mul_f32 v171, v171, v179\n"
"v_accvgpr_write_b32 a109, v171\n"
"v_accvgpr_read_b32 v171, a110\n"
"v_mul_f32 v171, v171, v179\n"
"v_accvgpr_write_b32 a110, v171\n"
"v_accvgpr_read_b32 v171, a111\n"
"v_mul_f32 v171, v171, v179\n"
"v_accvgpr_write_b32 a111, v171\n"
"v_accvgpr_read_b32 v171, a112\n"
"v_mul_f32 v171, v171, v179\n"
"v_accvgpr_write_b32 a112, v171\n"
"v_accvgpr_read_b32 v171, a113\n"
"v_mul_f32 v171, v171, v179\n"
"v_accvgpr_write_b32 a113, v171\n"
"v_accvgpr_read_b32 v171, a114\n"
"v_mul_f32 v171, v171, v179\n"
"v_accvgpr_write_b32 a114, v171\n"
"v_accvgpr_read_b32 v171, a115\n"
"v_mul_f32 v171, v171, v179\n"
"v_accvgpr_write_b32 a115, v171\n"
"v_accvgpr_read_b32 v171, a116\n"
"v_mul_f32 v171, v171, v179\n"
"v_accvgpr_write_b32 a116, v171\n"
"v_accvgpr_read_b32 v171, a117\n"
"v_mul_f32 v171, v171, v179\n"
"v_accvgpr_write_b32 a117, v171\n"
"v_accvgpr_read_b32 v171, a118\n"
"v_mul_f32 v171, v171, v179\n"
"v_accvgpr_write_b32 a118, v171\n"
"v_accvgpr_read_b32 v171, a119\n"
"v_mul_f32 v171, v171, v179\n"
"v_accvgpr_write_b32 a119, v171\n"
"v_accvgpr_read_b32 v171, a120\n"
"v_mul_f32 v171, v171, v179\n"
"v_accvgpr_write_b32 a120, v171\n"
"v_accvgpr_read_b32 v171, a121\n"
"v_mul_f32 v171, v171, v179\n"
"v_accvgpr_write_b32 a121, v171\n"
"v_accvgpr_read_b32 v171, a122\n"
"v_mul_f32 v171, v171, v179\n"
"v_accvgpr_write_b32 a122, v171\n"
"v_accvgpr_read_b32 v171, a123\n"
"v_mul_f32 v171, v171, v179\n"
"v_accvgpr_write_b32 a123, v171\n"
"v_accvgpr_read_b32 v171, a124\n"
"v_mul_f32 v171, v171, v179\n"
"v_accvgpr_write_b32 a124, v171\n"
"v_accvgpr_read_b32 v171, a125\n"
"v_mul_f32 v171, v171, v179\n"
"v_accvgpr_write_b32 a125, v171\n"
"v_accvgpr_read_b32 v171, a126\n"
"v_mul_f32 v171, v171, v179\n"
"v_accvgpr_write_b32 a126, v171\n"
"v_accvgpr_read_b32 v171, a127\n"
"v_mul_f32 v171, v171, v179\n"
"v_accvgpr_write_b32 a127, v171\n"
"v_subrev_u32 v187, 32, v187\n"
"s_waitcnt vmcnt(0)\n"
"v_mfma_f32_16x16x32_bf16 a[192:195], v[36:39], v[192:195], a[192:195]\n"
"v_mfma_f32_16x16x32_bf16 a[224:227], v[36:39], v[200:203], a[224:227]\n"
"v_mfma_f32_16x16x32_bf16 a[64:67], v[36:39], v[208:211], a[64:67]\n"
"v_mfma_f32_16x16x32_bf16 a[96:99], v[36:39], v[216:219], a[96:99]\n"
"v_mfma_f32_16x16x32_bf16 a[196:199], v[44:47], v[192:195], a[196:199]\n"
"v_mfma_f32_16x16x32_bf16 a[228:231], v[44:47], v[200:203], a[228:231]\n"
"v_mfma_f32_16x16x32_bf16 a[68:71], v[44:47], v[208:211], a[68:71]\n"
"v_mfma_f32_16x16x32_bf16 a[100:103], v[44:47], v[216:219], a[100:103]\n"
"v_mfma_f32_16x16x32_bf16 a[200:203], v[52:55], v[192:195], a[200:203]\n"
"v_mfma_f32_16x16x32_bf16 a[232:235], v[52:55], v[200:203], a[232:235]\n"
"v_mfma_f32_16x16x32_bf16 a[72:75], v[52:55], v[208:211], a[72:75]\n"
"v_mfma_f32_16x16x32_bf16 a[104:107], v[52:55], v[216:219], a[104:107]\n"
"v_mfma_f32_16x16x32_bf16 a[204:207], v[60:63], v[192:195], a[204:207]\n"
"v_mfma_f32_16x16x32_bf16 a[236:239], v[60:63], v[200:203], a[236:239]\n"
"v_mfma_f32_16x16x32_bf16 a[76:79], v[60:63], v[208:211], a[76:79]\n"
"v_mfma_f32_16x16x32_bf16 a[108:111], v[60:63], v[216:219], a[108:111]\n"
"v_mfma_f32_16x16x32_bf16 a[208:211], v[76:79], v[192:195], a[208:211]\n"
"v_mfma_f32_16x16x32_bf16 a[240:243], v[76:79], v[200:203], a[240:243]\n"
"v_mfma_f32_16x16x32_bf16 a[80:83], v[76:79], v[208:211], a[80:83]\n"
"v_mfma_f32_16x16x32_bf16 a[112:115], v[76:79], v[216:219], a[112:115]\n"
"v_mfma_f32_16x16x32_bf16 a[212:215], v[92:95], v[192:195], a[212:215]\n"
"v_mfma_f32_16x16x32_bf16 a[244:247], v[92:95], v[200:203], a[244:247]\n"
"v_mfma_f32_16x16x32_bf16 a[84:87], v[92:95], v[208:211], a[84:87]\n"
"v_mfma_f32_16x16x32_bf16 a[116:119], v[92:95], v[216:219], a[116:119]\n"
"v_mfma_f32_16x16x32_bf16 a[216:219], v[108:111], v[192:195], a[216:219]\n"
"v_mfma_f32_16x16x32_bf16 a[248:251], v[108:111], v[200:203], a[248:251]\n"
"v_mfma_f32_16x16x32_bf16 a[88:91], v[108:111], v[208:211], a[88:91]\n"
"v_mfma_f32_16x16x32_bf16 a[120:123], v[108:111], v[216:219], a[120:123]\n"
"v_mfma_f32_16x16x32_bf16 a[220:223], v[124:127], v[192:195], a[220:223]\n"
"v_mfma_f32_16x16x32_bf16 a[252:255], v[124:127], v[200:203], a[252:255]\n"
"v_mfma_f32_16x16x32_bf16 a[92:95], v[124:127], v[208:211], a[92:95]\n"
"v_mfma_f32_16x16x32_bf16 a[124:127], v[124:127], v[216:219], a[124:127]\n"
"s_nop 7\n"
"s_sub_u32 s93, s93, 1\n"
"s_cmp_lg_u32 s93, 0\n"
"s_cbranch_scc1 L_slowloop%=\n"
"v_mov_b32 v168, v14\n"
"v_mov_b32 v169, v15\n"
"s_nop 0\n"
"v_permlane16_swap_b32 v14, v168\n"
"v_permlane16_swap_b32 v15, v169\n"
"v_add_f32 v14, v14, v168\n"
"v_add_f32 v15, v15, v169\n"
"v_mov_b32 v168, v14\n"
"v_mov_b32 v169, v15\n"
"s_nop 0\n"
"v_permlane32_swap_b32 v14, v168\n"
"v_permlane32_swap_b32 v15, v169\n"
"v_add_f32 v14, v14, v168\n"
"v_add_f32 v15, v15, v169\n"
"s_cmp_eq_u32 s35, 0\n"
"s_cbranch_scc1 L_slowh%=\n"
"v_mov_b32 v168, v12\n"
"v_mov_b32 v169, v13\n"
"s_nop 0\n"
"v_permlane16_swap_b32 v12, v168\n"
"v_permlane16_swap_b32 v13, v169\n"
"v_add_f32 v12, v12, v168\n"
"v_add_f32 v13, v13, v169\n"
"v_mov_b32 v168, v12\n"
"v_mov_b32 v169, v13\n"
"s_nop 0\n"
"v_permlane32_swap_b32 v12, v168\n"
"v_permlane32_swap_b32 v13, v169\n"
"v_add_f32 v12, v12, v168\n"
"v_add_f32 v13, v13, v169\n"
"v_rcp_f32 v12, v12\n"
"v_rcp_f32 v176, v13\n"
"v_mov_b32 v13, v12\n"
"s_sub_u32 s73, s78, 0x100000\n"
"s_lshl_b32 s73, s73, 3\n"
"s_lshl_b32 s74, s65, 12\n"
"s_add_u32 s73, s73, s74\n"
"s_add_u32 s73, s73, 0x2400000\n"
"v_accvgpr_read_b32 v168, a192\n"
"v_accvgpr_read_b32 v169, a193\n"
"v_accvgpr_read_b32 v170, a194\n"
"v_accvgpr_read_b32 v171, a195\n"
"v_pk_mul_f32 v[168:169], v[168:169], v[12:13]\n"
"v_pk_mul_f32 v[170:171], v[170:171], v[12:13]\n"
"v_cvt_pk_bf16_f32 v24, v168, v169\n"
"v_cvt_pk_bf16_f32 v25, v170, v171\n"
"v_accvgpr_read_b32 v168, a196\n"
"v_accvgpr_read_b32 v169, a197\n"
"v_accvgpr_read_b32 v170, a198\n"
"v_accvgpr_read_b32 v171, a199\n"
"v_pk_mul_f32 v[168:169], v[168:169], v[12:13]\n"
"v_pk_mul_f32 v[170:171], v[170:171], v[12:13]\n"
"v_cvt_pk_bf16_f32 v26, v168, v169\n"
"v_cvt_pk_bf16_f32 v27, v170, v171\n"
"buffer_store_dwordx4 v[24:27], v1, s[48:51], s73 offen offset:0 sc1\n"
"v_accvgpr_read_b32 v168, a200\n"
"v_accvgpr_read_b32 v169, a201\n"
"v_accvgpr_read_b32 v170, a202\n"
"v_accvgpr_read_b32 v171, a203\n"
"v_pk_mul_f32 v[168:169], v[168:169], v[12:13]\n"
"v_pk_mul_f32 v[170:171], v[170:171], v[12:13]\n"
"v_cvt_pk_bf16_f32 v28, v168, v169\n"
"v_cvt_pk_bf16_f32 v29, v170, v171\n"
"v_accvgpr_read_b32 v168, a204\n"
"v_accvgpr_read_b32 v169, a205\n"
"v_accvgpr_read_b32 v170, a206\n"
"v_accvgpr_read_b32 v171, a207\n"
"v_pk_mul_f32 v[168:169], v[168:169], v[12:13]\n"
"v_pk_mul_f32 v[170:171], v[170:171], v[12:13]\n"
"v_cvt_pk_bf16_f32 v30, v168, v169\n"
"v_cvt_pk_bf16_f32 v31, v170, v171\n"
"buffer_store_dwordx4 v[28:31], v1, s[48:51], s73 offen offset:1024 sc1\n"
"v_accvgpr_read_b32 v168, a208\n"
"v_accvgpr_read_b32 v169, a209\n"
"v_accvgpr_read_b32 v170, a210\n"
"v_accvgpr_read_b32 v171, a211\n"
"v_pk_mul_f32 v[168:169], v[168:169], v[12:13]\n"
"v_pk_mul_f32 v[170:171], v[170:171], v[12:13]\n"
"v_cvt_pk_bf16_f32 v24, v168, v169\n"
"v_cvt_pk_bf16_f32 v25, v170, v171\n"
"v_accvgpr_read_b32 v168, a212\n"
"v_accvgpr_read_b32 v169, a213\n"
"v_accvgpr_read_b32 v170, a214\n"
"v_accvgpr_read_b32 v171, a215\n"
"v_pk_mul_f32 v[168:169], v[168:169], v[12:13]\n"
"v_pk_mul_f32 v[170:171], v[170:171], v[12:13]\n"
"v_cvt_pk_bf16_f32 v26, v168, v169\n"
"v_cvt_pk_bf16_f32 v27, v170, v171\n"
"buffer_store_dwordx4 v[24:27], v1, s[48:51], s73 offen offset:2048 sc1\n"
"v_accvgpr_read_b32 v168, a216\n"
"v_accvgpr_read_b32 v169, a217\n"
"v_accvgpr_read_b32 v170, a218\n"
"v_accvgpr_read_b32 v171, a219\n"
"v_pk_mul_f32 v[168:169], v[168:169], v[12:13]\n"
"v_pk_mul_f32 v[170:171], v[170:171], v[12:13]\n"
"v_cvt_pk_bf16_f32 v28, v168, v169\n"
"v_cvt_pk_bf16_f32 v29, v170, v171\n"
"v_accvgpr_read_b32 v168, a220\n"
"v_accvgpr_read_b32 v169, a221\n"
"v_accvgpr_read_b32 v170, a222\n"
"v_accvgpr_read_b32 v171, a223\n"
"v_pk_mul_f32 v[168:169], v[168:169], v[12:13]\n"
"v_pk_mul_f32 v[170:171], v[170:171], v[12:13]\n"
"v_cvt_pk_bf16_f32 v30, v168, v169\n"
"v_cvt_pk_bf16_f32 v31, v170, v171\n"
"buffer_store_dwordx4 v[28:31], v1, s[48:51], s73 offen offset:3072 sc1\n"
"v_mov_b32 v12, v176\n"
"v_mov_b32 v13, v176\n"
"s_sub_u32 s73, s79, 0x100000\n"
"s_lshl_b32 s73, s73, 3\n"
"s_lshl_b32 s74, s65, 12\n"
"s_add_u32 s73, s73, s74\n"
"s_add_u32 s73, s73, 0x2400000\n"
"v_accvgpr_read_b32 v168, a224\n"
"v_accvgpr_read_b32 v169, a225\n"
"v_accvgpr_read_b32 v170, a226\n"
"v_accvgpr_read_b32 v171, a227\n"
"v_pk_mul_f32 v[168:169], v[168:169], v[12:13]\n"
"v_pk_mul_f32 v[170:171], v[170:171], v[12:13]\n"
"v_cvt_pk_bf16_f32 v24, v168, v169\n"
"v_cvt_pk_bf16_f32 v25, v170, v171\n"
"v_accvgpr_read_b32 v168, a228\n"
"v_accvgpr_read_b32 v169, a229\n"
"v_accvgpr_read_b32 v170, a230\n"
"v_accvgpr_read_b32 v171, a231\n"
"v_pk_mul_f32 v[168:169], v[168:169], v[12:13]\n"
"v_pk_mul_f32 v[170:171], v[170:171], v[12:13]\n"
"v_cvt_pk_bf16_f32 v26, v168, v169\n"
"v_cvt_pk_bf16_f32 v27, v170, v171\n"
"buffer_store_dwordx4 v[24:27], v1, s[48:51], s73 offen offset:0 sc1\n"
"v_accvgpr_read_b32 v168, a232\n"
"v_accvgpr_read_b32 v169, a233\n"
"v_accvgpr_read_b32 v170, a234\n"
"v_accvgpr_read_b32 v171, a235\n"
"v_pk_mul_f32 v[168:169], v[168:169], v[12:13]\n"
"v_pk_mul_f32 v[170:171], v[170:171], v[12:13]\n"
"v_cvt_pk_bf16_f32 v28, v168, v169\n"
"v_cvt_pk_bf16_f32 v29, v170, v171\n"
"v_accvgpr_read_b32 v168, a236\n"
"v_accvgpr_read_b32 v169, a237\n"
"v_accvgpr_read_b32 v170, a238\n"
"v_accvgpr_read_b32 v171, a239\n"
"v_pk_mul_f32 v[168:169], v[168:169], v[12:13]\n"
"v_pk_mul_f32 v[170:171], v[170:171], v[12:13]\n"
"v_cvt_pk_bf16_f32 v30, v168, v169\n"
"v_cvt_pk_bf16_f32 v31, v170, v171\n"
"buffer_store_dwordx4 v[28:31], v1, s[48:51], s73 offen offset:1024 sc1\n"
"v_accvgpr_read_b32 v168, a240\n"
"v_accvgpr_read_b32 v169, a241\n"
"v_accvgpr_read_b32 v170, a242\n"
"v_accvgpr_read_b32 v171, a243\n"
"v_pk_mul_f32 v[168:169], v[168:169], v[12:13]\n"
"v_pk_mul_f32 v[170:171], v[170:171], v[12:13]\n"
"v_cvt_pk_bf16_f32 v24, v168, v169\n"
"v_cvt_pk_bf16_f32 v25, v170, v171\n"
"v_accvgpr_read_b32 v168, a244\n"
"v_accvgpr_read_b32 v169, a245\n"
"v_accvgpr_read_b32 v170, a246\n"
"v_accvgpr_read_b32 v171, a247\n"
"v_pk_mul_f32 v[168:169], v[168:169], v[12:13]\n"
"v_pk_mul_f32 v[170:171], v[170:171], v[12:13]\n"
"v_cvt_pk_bf16_f32 v26, v168, v169\n"
"v_cvt_pk_bf16_f32 v27, v170, v171\n"
"buffer_store_dwordx4 v[24:27], v1, s[48:51], s73 offen offset:2048 sc1\n"
"v_accvgpr_read_b32 v168, a248\n"
"v_accvgpr_read_b32 v169, a249\n"
"v_accvgpr_read_b32 v170, a250\n"
"v_accvgpr_read_b32 v171, a251\n"
"v_pk_mul_f32 v[168:169], v[168:169], v[12:13]\n"
"v_pk_mul_f32 v[170:171], v[170:171], v[12:13]\n"
"v_cvt_pk_bf16_f32 v28, v168, v169\n"
"v_cvt_pk_bf16_f32 v29, v170, v171\n"
"v_accvgpr_read_b32 v168, a252\n"
"v_accvgpr_read_b32 v169, a253\n"
"v_accvgpr_read_b32 v170, a254\n"
"v_accvgpr_read_b32 v171, a255\n"
"v_pk_mul_f32 v[168:169], v[168:169], v[12:13]\n"
"v_pk_mul_f32 v[170:171], v[170:171], v[12:13]\n"
"v_cvt_pk_bf16_f32 v30, v168, v169\n"
"v_cvt_pk_bf16_f32 v31, v170, v171\n"
"buffer_store_dwordx4 v[28:31], v1, s[48:51], s73 offen offset:3072 sc1\n"
"s_waitcnt vmcnt(0)\n"
"v_cmp_eq_u32 vcc, 0, v21\n"
"v_mov_b32 v27, -1\n"
"s_lshl_b32 s73, s66, 2\n"
"s_add_u32 s73, s73, s64\n"
"v_cndmask_b32 v27, v27, 0, vcc\n"
"s_lshl_b32 s73, s73, 8\n"
"s_lshl_b32 s74, s65, 3\n"
"s_add_u32 s73, s73, s74\n"
"s_add_u32 s73, s73, 0x92000\n"
"v_mov_b32 v30, s68\n"
"v_mov_b32 v31, s69\n"
"buffer_store_dwordx2 v[30:31], v27, s[48:51], s73 offen sc1\n"
"L_slowh%=:\n"
"v_mov_b32 v12, v220\n"
"v_mov_b32 v13, v221\n"
"s_add_u32 s73, s92, 0xa000\n"
"buffer_load_dwordx4 v[136:139], v1, s[48:51], s73 offen offset:0 sc1\n"
"buffer_load_dwordx4 v[144:147], v1, s[48:51], s73 offen offset:1024 sc1\n"
"buffer_load_dwordx4 v[168:171], v1, s[48:51], s73 offen offset:2048 sc1\n"
"buffer_load_dwordx4 v[176:179], v1, s[48:51], s73 offen offset:3072 sc1\n"
"s_add_u32 s73, s92, 0xb000\n"
"buffer_load_dwordx4 v[184:187], v1, s[48:51], s73 offen offset:0 sc1\n"
"buffer_load_dwordx4 v[204:207], v1, s[48:51], s73 offen offset:1024 sc1\n"
"buffer_load_dwordx4 v[220:223], v1, s[48:51], s73 offen offset:2048 sc1\n"
"buffer_load_dwordx4 v[188:191], v1, s[48:51], s73 offen offset:3072 sc1\n"
"s_add_u32 s73, s92, 0xc000\n"
"buffer_load_dwordx4 v[32:35], v1, s[48:51], s73 offen offset:0 sc1\n"
"buffer_load_dwordx4 v[68:71], v1, s[48:51], s73 offen offset:1024 sc1\n"
"buffer_load_dwordx4 v[196:199], v1, s[48:51], s73 offen offset:2048 sc1\n"
"buffer_load_dwordx4 v[132:135], v1, s[48:51], s73 offen offset:3072 sc1\n"
"s_add_u32 s73, s92, 0xd000\n"
"buffer_load_dwordx4 v[40:43], v1, s[48:51], s73 offen offset:0 sc1\n"
"buffer_load_dwordx4 v[84:87], v1, s[48:51], s73 offen offset:1024 sc1\n"
"buffer_load_dwordx4 v[212:215], v1, s[48:51], s73 offen offset:2048 sc1\n"
"buffer_load_dwordx4 v[148:151], v1, s[48:51], s73 offen offset:3072 sc1\n"
"s_add_u32 s73, s92, 0xe000\n"
"buffer_load_dwordx4 v[36:39], v1, s[48:51], s73 offen offset:0 sc1\n"
"buffer_load_dwordx4 v[44:47], v1, s[48:51], s73 offen offset:1024 sc1\n"
"buffer_load_dwordx4 v[52:55], v1, s[48:51], s73 offen offset:2048 sc1\n"
"buffer_load_dwordx4 v[60:63], v1, s[48:51], s73 offen offset:3072 sc1\n"
"s_add_u32 s73, s92, 0xf000\n"
"buffer_load_dwordx4 v[76:79], v1, s[48:51], s73 offen offset:0 sc1\n"
"buffer_load_dwordx4 v[92:95], v1, s[48:51], s73 offen offset:1024 sc1\n"
"buffer_load_dwordx4 v[108:111], v1, s[48:51], s73 offen offset:2048 sc1\n"
"buffer_load_dwordx4 v[124:127], v1, s[48:51], s73 offen offset:3072 sc1\n"
"s_waitcnt vmcnt(0)\n"
"v_mov_b32 v64, 0\n"
"v_mov_b32 v65, 0\n"
"v_mov_b32 v66, 0\n"
"v_mov_b32 v67, 0\n"
"v_mov_b32 v72, 0\n"
"v_mov_b32 v73, 0\n"
"v_mov_b32 v74, 0\n"
"v_mov_b32 v75, 0\n"
"v_mov_b32 v80, 0\n"
"v_mov_b32 v81, 0\n"
"v_mov_b32 v82, 0\n"
"v_mov_b32 v83, 0\n"
"v_mov_b32 v88, 0\n"
"v_mov_b32 v89, 0\n"
"v_mov_b32 v90, 0\n"
"v_mov_b32 v91, 0\n"
"v_mov_b32 v96, 0\n"
"v_mov_b32 v97, 0\n"
"v_mov_b32 v98, 0\n"
"v_mov_b32 v99, 0\n"
"v_mov_b32 v104, 0\n"
"v_mov_b32 v105, 0\n"
"v_mov_b32 v106, 0\n"
"v_mov_b32 v107, 0\n"
"v_mov_b32 v112, 0\n"
"v_mov_b32 v113, 0\n"
"v_mov_b32 v114, 0\n"
"v_mov_b32 v115, 0\n"
"v_mov_b32 v120, 0\n"
"v_mov_b32 v121, 0\n"
"v_mov_b32 v122, 0\n"
"v_mov_b32 v123, 0\n"
"v_mov_b32 v128, 0\n"
"v_mov_b32 v129, 0\n"
"v_mov_b32 v130, 0\n"
"v_mov_b32 v131, 0\n"
"v_mov_b32 v152, 0\n"
"v_mov_b32 v153, 0\n"
"v_mov_b32 v154, 0\n"
"v_mov_b32 v155, 0\n"
"v_mov_b32 v160, 0\n"
"v_mov_b32 v161, 0\n"
"v_mov_b32 v162, 0\n"
"v_mov_b32 v163, 0\n"
"v_mov_b32 v172, 0\n"
"v_mov_b32 v173, 0\n"
"v_mov_b32 v174, 0\n"
"v_mov_b32 v175, 0\n"
"v_mov_b32 v224, 0\n"
"v_mov_b32 v225, 0\n"
"v_mov_b32 v226, 0\n"
"v_mov_b32 v227, 0\n"
"v_mov_b32 v232, 0\n"
"v_mov_b32 v233, 0\n"
"v_mov_b32 v234, 0\n"
"v_mov_b32 v235, 0\n"
"v_mov_b32 v240, 0\n"
"v_mov_b32 v241, 0\n"
"v_mov_b32 v242, 0\n"
"v_mov_b32 v243, 0\n"
"v_mov_b32 v248, 0\n"
"v_mov_b32 v249, 0\n"
"v_mov_b32 v250, 0\n"
"v_mov_b32 v251, 0\n"
"s_nop 7\n"
"s_branch LT_slowret%=\n"
"LT_padend_%=:\n"
".set L_padn0_%=, LT_padend_%=\n"
".set L_padn1_%=, LT_padend_%=\n"
".set L_padn2_%=, LT_padend_%=\n"
".set L_padn3_%=, LT_padend_%=\n"
"L_end_%=:\n"
  :: "{s[8:9]}"(x), "{s[10:11]}"(ckv), "{s[12:13]}"(wq), "{s[14:15]}"(wk), "{s[16:17]}"(wv), "{s[18:19]}"(wo), "{s[20:21]}"(wr), "{s[22:23]}"(wg), "{s[24:25]}"(wu), "{s[26:27]}"(wd), "{s[28:29]}"(out), "{s[30:31]}"(ws), "{s32}"(blockIdx.x), "{v0}"(threadIdx.x), "{s2}"(ldsb)
  : "v1","v2","v3","v4","v5","v6","v7","v8","v9","v10","v11","v12","v13","v14","v15","v16","v17","v18","v19","v20","v21","v22","v23","v24","v25","v26","v27","v28","v29","v30","v31","v32","v33","v34","v35","v36","v37","v38","v39","v40","v41","v42","v43","v44","v45","v46","v47","v48","v49","v50","v51","v52","v53","v54","v55","v56","v57","v58","v59","v60","v61","v62","v63","v64","v65","v66","v67","v68","v69","v70","v71","v72","v73","v74","v75","v76","v77","v78","v79","v80","v81","v82","v83","v84","v85","v86","v87","v88","v89","v90","v91","v92","v93","v94","v95","v96","v97","v98","v99","v100","v101","v102","v103","v104","v105","v106","v107","v108","v109","v110","v111","v112","v113","v114","v115","v116","v117","v118","v119","v120","v121","v122","v123","v124","v125","v126","v127","v128","v129","v130","v131","v132","v133","v134","v135","v136","v137","v138","v139","v140","v141","v142","v143","v144","v145","v146","v147","v148","v149","v150","v151","v152","v153","v154","v155","v156","v157","v158","v159","v160","v161","v162","v163","v164","v165","v166","v167","v168","v169","v170","v171","v172","v173","v174","v175","v176","v177","v178","v179","v180","v181","v182","v183","v184","v185","v186","v187","v188","v189","v190","v191","v192","v193","v194","v195","v196","v197","v198","v199","v200","v201","v202","v203","v204","v205","v206","v207","v208","v209","v210","v211","v212","v213","v214","v215","v216","v217","v218","v219","v220","v221","v222","v223","v224","v225","v226","v227","v228","v229","v230","v231","v232","v233","v234","v235","v236","v237","v238","v239","v240","v241","v242","v243","v244","v245","v246","v247","v248","v249","v250","v251","v252","v253","v254","v255","a0","a1","a2","a3","a4","a5","a6","a7","a8","a9","a10","a11","a12","a13","a14","a15","a16","a17","a18","a19","a20","a21","a22","a23","a24","a25","a26","a27","a28","a29","a30","a31","a32","a33","a34","a35","a36","a37","a38","a39","a40","a41","a42","a43","a44","a45","a46","a47","a48","a49","a50","a51","a52","a53","a54","a55","a56","a57","a58","a59","a60","a61","a62","a63","a64","a65","a66","a67","a68","a69","a70","a71","a72","a73","a74","a75","a76","a77","a78","a79","a80","a81","a82","a83","a84","a85","a86","a87","a88","a89","a90","a91","a92","a93","a94","a95","a96","a97","a98","a99","a100","a101","a102","a103","a104","a105","a106","a107","a108","a109","a110","a111","a112","a113","a114","a115","a116","a117","a118","a119","a120","a121","a122","a123","a124","a125","a126","a127","a128","a129","a130","a131","a132","a133","a134","a135","a136","a137","a138","a139","a140","a141","a142","a143","a144","a145","a146","a147","a148","a149","a150","a151","a152","a153","a154","a155","a156","a157","a158","a159","a160","a161","a162","a163","a164","a165","a166","a167","a168","a169","a170","a171","a172","a173","a174","a175","a176","a177","a178","a179","a180","a181","a182","a183","a184","a185","a186","a187","a188","a189","a190","a191","a192","a193","a194","a195","a196","a197","a198","a199","a200","a201","a202","a203","a204","a205","a206","a207","a208","a209","a210","a211","a212","a213","a214","a215","a216","a217","a218","a219","a220","a221","a222","a223","a224","a225","a226","a227","a228","a229","a230","a231","a232","a233","a234","a235","a236","a237","a238","a239","a240","a241","a242","a243","a244","a245","a246","a247","a248","a249","a250","a251","a252","a253","a254","a255","s0","s1","s3","s4","s5","s6","s7","s33","s34","s35","s36","s37","s38","s39","s40","s41","s42","s43","s44","s45","s46","s47","s48","s49","s50","s51","s52","s53","s54","s55","s56","s57","s58","s59","s60","s61","s62","s63","s64","s65","s66","s67","s68","s69","s70","s71","s72","s73","s74","s75","s76","s77","s78","s79","s80","s81","s82","s83","s84","s85","s86","s87","s88","s89","s90","s91","s92","s93","s94","s95","s96","s97","s98","s99","vcc","memory");
}

extern "C" void kernel_launch(void* const* d_in, const int* in_sizes, int n_in, void* d_out, int out_size, void* d_ws, size_t ws_size,
                              hipStream_t stream) {
  (void)in_sizes; (void)n_in; (void)out_size; (void)ws_size;
  fused<<<256, 256, 0, stream>>>(d_in[0], d_in[1], d_in[2], d_in[3], d_in[4], d_in[5], d_in[7], d_in[8], d_in[9], d_in[10], d_out, d_ws);
}
```
